```python
import torch

def _softmax(x, axis=-1):
    return torch.softmax(x, dim=axis)

def _gelu(x):
    return torch.nn.functional.gelu(x, approximate='tanh')

def _silu(x):
    return torch.nn.functional.silu(x)

def _sigmoid(x):
    return torch.sigmoid(x)

def _layernorm(x, axis=-1, eps=1e-05):
    m = x.mean(dim=axis, keepdim=True)
    v = x.var(dim=axis, keepdim=True, unbiased=False)
    return (x - m) / torch.sqrt(v + eps)

def _scatter_add(tgt, idx, src, axis=0):
    return tgt.clone().index_add_(axis, idx, src)

def _segment_sum(src, seg, n):
    out = torch.zeros((n,) + src.shape[1:], dtype=src.dtype)
    return out.index_add_(0, seg, src)

def _pad(x, pw):
    flat = tuple((v for pair in reversed(pw) for v in pair))
    return torch.nn.functional.pad(x, flat)
kernel_name = 'sequential_preLN__linear_kernel_hedgehog__mixer_gmlp__prompt'

def setup_inputs():

    _w = lambda a: (lambda t: t.to(torch.bfloat16) if t.is_floating_point() else t)(torch.as_tensor(a))

    def _A_setup():
        B, S, D, H = (1, 2048, 1024, 32)
        Dh = D // H
        return {'x': _w((lambda *s: torch.randn(*s, dtype=torch.bfloat16) * 0.05)(B, S, D)), 'wqkv': _w((lambda *s: torch.randn(*s, dtype=torch.bfloat16) * 0.2)(D, 3 * D)), 'w_phi': _w((lambda *s: torch.randn(*s, dtype=torch.bfloat16) * 0.2)(H, Dh, Dh) * Dh ** (-0.5)), 'wo': _w((lambda *s: torch.randn(*s, dtype=torch.bfloat16) * 0.05)(D, D))}
    _A_in = _A_setup()

    def _B_setup():
        B, S, D, F = (1, 2048, 1024, 3584)
        Fh = F // 2
        return {'x': _w((lambda *s: torch.randn(*s, dtype=torch.bfloat16) * 0.05)(B, S, D)), 'w_in': _w((lambda *s: torch.randn(*s, dtype=torch.bfloat16) * 0.2)(D, 2 * Fh)), 'w_sp': _w(0.01 * (lambda *s: torch.randn(*s, dtype=torch.bfloat16) * 0.2)(S, S)), 'b_sp': _w(1.0 + 0.01 * (lambda *s: torch.randn(*s, dtype=torch.bfloat16) * 0.05)(S)), 'w_out': _w((lambda *s: torch.randn(*s, dtype=torch.bfloat16) * 0.05)(Fh, D))}
    _B_in = _B_setup()
    return {'A_x': _A_in['x'], 'A_wqkv': _A_in['wqkv'], 'A_w_phi': _A_in['w_phi'], 'A_wo': _A_in['wo'], 'B_x': _B_in['x'], 'B_w_in': _B_in['w_in'], 'B_w_sp': _B_in['w_sp'], 'B_b_sp': _B_in['b_sp'], 'B_w_out': _B_in['w_out']}

def reference(A_x, A_wqkv, A_w_phi, A_wo, B_x, B_w_in, B_w_sp, B_b_sp, B_w_out):

    def _A_ref(x, wqkv, w_phi, wo):
        B, S, D = x.shape
        H = 32
        Dh = D // H
        qkv = (lambda x, *d: x.permute(*d))((x @ wqkv).reshape(B, S, 3, H, Dh), 2, 0, 3, 1, 4)
        q, k, v = (qkv[0], qkv[1], qkv[2])
        pq = torch.einsum('bhsd,hdf->bhsf', q, w_phi)
        pk = torch.einsum('bhsd,hdf->bhsf', k, w_phi)
        phi_q = torch.concatenate([_softmax(pq, axis=-1), _softmax(-pq, axis=-1)], axis=-1)
        phi_k = torch.concatenate([_softmax(pk, axis=-1), _softmax(-pk, axis=-1)], axis=-1)
        kv = torch.einsum('bhsf,bhsd->bhfd', phi_k, v)
        z = torch.einsum('bhsf->bhf', phi_k)
        num = torch.einsum('bhqf,bhfd->bhqd', phi_q, kv)
        den = torch.einsum('bhqf,bhf->bhq', phi_q, z)[..., None] + 1e-06
        o = num / den
        return (lambda x, *d: x.permute(*d))(o, 0, 2, 1, 3).reshape(B, S, D) @ wo

    def _B_ref(x, w_in, w_sp, b_sp, w_out):
        B, S, D = x.shape
        F = w_in.shape[-1]
        Fh = F // 2
        z = _gelu(torch.einsum('bsd,df->bsf', x, w_in))
        z1 = z[..., :Fh].clone()
        z2 = _layernorm(z[..., Fh:], axis=-1)
        g = torch.einsum('ts,bsf->btf', w_sp, z2) + b_sp[None, :, None]
        return torch.einsum('bsf,fd->bsd', z1 * g, w_out)
    x = A_x
    h = x + _A_ref(_layernorm(x, axis=-1), A_wqkv, A_w_phi, A_wo)
    h2 = h + _B_ref(_layernorm(h, axis=-1), B_w_in, B_w_sp, B_b_sp, B_w_out)
    return h2
```

```cpp
#include <hip/hip_runtime.h>
#include <hip/hip_bf16.h>
#include <stdint.h>
typedef unsigned short u16;
__device__ inline float b2f(u16 b){ return __uint_as_float(((unsigned)b)<<16); }
__device__ inline u16 f2b(float f){ unsigned u=__float_as_uint(f); u += 0x7fffu + ((u>>16)&1u); return (u16)(u>>16); }
constexpr int S=2048, D=1024, H=32, DH=32, F=3584, FH=1792;

__global__ void k_ln(const u16* x, u16* y, int ncols, int ld, int off){
  int r=blockIdx.x; __shared__ float red[256];
  float s=0; for(int c=threadIdx.x;c<ncols;c+=256) s+=b2f(x[(size_t)r*ld+off+c]);
  red[threadIdx.x]=s; __syncthreads(); for(int o=128;o>0;o>>=1){ if(threadIdx.x<o) red[threadIdx.x]+=red[threadIdx.x+o]; __syncthreads(); }
  float m=red[0]/ncols; __syncthreads();
  float q=0; for(int c=threadIdx.x;c<ncols;c+=256){ float d=b2f(x[(size_t)r*ld+off+c])-m; q+=d*d; }
  red[threadIdx.x]=q; __syncthreads(); for(int o=128;o>0;o>>=1){ if(threadIdx.x<o) red[threadIdx.x]+=red[threadIdx.x+o]; __syncthreads(); }
  float rs=1.0f/sqrtf(red[0]/ncols+1e-5f);
  for(int c=threadIdx.x;c<ncols;c+=256) y[(size_t)r*ncols+c]=f2b((b2f(x[(size_t)r*ld+off+c])-m)*rs);
}
__global__ void k_gemm(const u16* A, const u16* B, u16* C, int M,int N,int K, int mode, const u16* aux, const u16* aux2, int ldaux){
  __shared__ float As[16][65]; __shared__ float Bs[16][65];
  int tx=threadIdx.x&15, ty=threadIdx.x>>4; int r0=blockIdx.y*64, c0=blockIdx.x*64;
  float acc[4][4]={};
  for(int k0=0;k0<K;k0+=16){
    for(int i=threadIdx.x;i<64*16;i+=256){ int r=i>>4,k=i&15; As[k][r]=b2f(A[(size_t)(r0+r)*K+k0+k]); }
    for(int i=threadIdx.x;i<64*16;i+=256){ int k=i>>6,c=i&63; Bs[k][c]=b2f(B[(size_t)(k0+k)*N+c0+c]); }
    __syncthreads();
    for(int k=0;k<16;k++){ float a[4],b[4]; for(int i=0;i<4;i++){a[i]=As[k][ty*4+i]; b[i]=Bs[k][tx*4+i];}
      for(int i=0;i<4;i++) for(int j=0;j<4;j++) acc[i][j]+=a[i]*b[j]; }
    __syncthreads();
  }
  for(int i=0;i<4;i++) for(int j=0;j<4;j++){ int r=r0+ty*4+i,c=c0+tx*4+j; float v=acc[i][j];
    if(mode==1) v+=b2f(aux[(size_t)r*N+c]);
    if(mode==2){ float u=0.7978845608f*(v+0.044715f*v*v*v); v=0.5f*v*(1.f+tanhf(u)); }
    if(mode==3){ v=(v+b2f(aux2[r]))*b2f(aux[(size_t)r*ldaux+c]); }
    C[(size_t)r*N+c]=f2b(v); }
}
__global__ void k_phi(const u16* qkv, const u16* wphi, u16* phiq, u16* phik){
  int idx=blockIdx.x*blockDim.x+threadIdx.x;
  int which=idx&1, h=(idx>>1)&31, s=idx>>6;
  float q[32]; for(int d=0;d<32;d++) q[d]=b2f(qkv[(size_t)s*3072+which*1024+h*32+d]);
  float p[32]; float mx=-1e30f,mn=1e30f;
  for(int f=0;f<32;f++){ float a=0; for(int d=0;d<32;d++) a+=q[d]*b2f(wphi[h*1024+d*32+f]); p[f]=a; mx=fmaxf(mx,a); mn=fminf(mn,a);}
  float s1=0,s2=0; for(int f=0;f<32;f++){ s1+=expf(p[f]-mx); s2+=expf(mn-p[f]); }
  u16* o=(which?phik:phiq)+((size_t)s*32+h)*64;
  for(int f=0;f<32;f++){ o[f]=f2b(expf(p[f]-mx)/s1); o[32+f]=f2b(expf(mn-p[f])/s2); }
}
__global__ void k_kv(const u16* phik, const u16* qkv, float* kv, float* z){
  int h=blockIdx.x, f=blockIdx.y; int d=threadIdx.x&31, part=threadIdx.x>>5; __shared__ float red[8][33];
  float a=0, zz=0; for(int s=part;s<S;s+=8){ float p=b2f(phik[((size_t)s*32+h)*64+f]); a+=p*b2f(qkv[(size_t)s*3072+2048+h*32+d]); zz+=p; }
  red[part][d]=a; if(d==0) red[part][32]=zz; __syncthreads();
  if(part==0){ float t=0; for(int i=0;i<8;i++) t+=red[i][d]; kv[(h*64+f)*32+d]=t; if(d==0){ float u=0; for(int i=0;i<8;i++) u+=red[i][32]; z[h*64+f]=u; } }
}
__global__ void k_o(const u16* phiq, const float* kv, const float* z, u16* o){
  int idx=blockIdx.x*blockDim.x+threadIdx.x; int d=idx&31,h=(idx>>5)&31,s=idx>>10;
  float n=0,dn=0; for(int f=0;f<64;f++){ float p=b2f(phiq[((size_t)s*32+h)*64+f]); n+=p*kv[(h*64+f)*32+d]; dn+=p*z[h*64+f]; }
  o[(size_t)s*1024+h*32+d]=f2b(n/(dn+1e-6f));
}
__global__ void k_copycols(const u16* z, u16* z1){ int r=blockIdx.x; for(int c=threadIdx.x;c<FH;c+=256) z1[(size_t)r*FH+c]=z[(size_t)r*F+c]; }

extern "C" void kernel_launch(void* const* d_in, const int* in_sizes, int n_in, void* d_out, int out_size, void* d_ws, size_t ws_size, hipStream_t stream){
  const u16* x=(const u16*)d_in[0]; const u16* wqkv=(const u16*)d_in[1]; const u16* wphi=(const u16*)d_in[2]; const u16* wo=(const u16*)d_in[3];
  const u16* win=(const u16*)d_in[5]; const u16* wsp=(const u16*)d_in[6]; const u16* bsp=(const u16*)d_in[7]; const u16* wout=(const u16*)d_in[8];
  char* w=(char*)d_ws; size_t o=0; auto al=[&](size_t n){ void* p=w+o; o+=(n+255)&~(size_t)255; return p; };
  u16* xn=(u16*)al(S*D*2); u16* qkv=(u16*)al((size_t)S*3072*2); u16* phiq=(u16*)al((size_t)S*2048*2); u16* phik=(u16*)al((size_t)S*2048*2);
  float* kv=(float*)al(32*64*32*4); float* z=(float*)al(32*64*4); u16* ob=(u16*)al(S*D*2); u16* h=(u16*)al(S*D*2); u16* hn=(u16*)al(S*D*2);
  u16* zz=(u16*)al((size_t)S*F*2); u16* z2n=(u16*)al((size_t)S*FH*2); u16* u=(u16*)al((size_t)S*FH*2);
  k_ln<<<S,256,0,stream>>>(x,xn,D,D,0);
  k_gemm<<<dim3(3072/64,S/64),256,0,stream>>>(xn,wqkv,qkv,S,3072,D,0,0,0,0);
  k_phi<<<S*64/256,256,0,stream>>>(qkv,wphi,phiq,phik);
  k_kv<<<dim3(32,64),256,0,stream>>>(phik,qkv,kv,z);
  k_o<<<S*1024/256,256,0,stream>>>(phiq,kv,z,ob);
  k_gemm<<<dim3(D/64,S/64),256,0,stream>>>(ob,wo,h,S,D,D,1,x,0,0);
  k_ln<<<S,256,0,stream>>>(h,hn,D,D,0);
  k_gemm<<<dim3(F/64,S/64),256,0,stream>>>(hn,win,zz,S,F,D,2,0,0,0);
  k_ln<<<S,256,0,stream>>>(zz,z2n,FH,F,FH);
  k_gemm<<<dim3(FH/64,S/64),256,0,stream>>>(wsp,z2n,u,S,FH,S,3,zz,bsp,F);
  k_gemm<<<dim3(D/64,S/64),256,0,stream>>>(u,wout,(u16*)d_out,S,D,FH,1,h,0,0);
}
```

```cpp
#include <hip/hip_runtime.h>
#include <hip/hip_bf16.h>
#include <stdint.h>

#define LAS __attribute__((address_space(3)))
#define DI __device__ __forceinline__
typedef unsigned short u16;
typedef short bf16x8 __attribute__((ext_vector_type(8)));
typedef short s16x4 __attribute__((ext_vector_type(4)));
typedef float f32x4 __attribute__((ext_vector_type(4)));
typedef float f32x2 __attribute__((ext_vector_type(2)));
typedef unsigned u32x4 __attribute__((ext_vector_type(4)));
typedef unsigned u32x2 __attribute__((ext_vector_type(2)));
typedef __bf16 bf2 __attribute__((ext_vector_type(2)));
typedef LAS char* lds_t;

constexpr unsigned OFF_EPOCH = 0, OFF_ERR = 128, OFF_FLAG = 4096;
constexpr unsigned OFF_KVP = 262144;
constexpr unsigned OFF_ZP = OFF_KVP + 2097152;
constexpr unsigned OFF_ST2 = OFF_ZP + 131072;
constexpr unsigned OFF_OF = 4u << 20, OFF_HF = 8u << 20, OFF_Z2 = 12u << 20, OFF_UF = 20u << 20, OFF_TS = 28u << 20;
constexpr unsigned WS_BYTES = 32u << 20;
enum { FG_KV = 0, FG_O = 1, FG_H = 2, FG_ST = 3, FG_Z2 = 4, FG_U = 5 };
constexpr int LDS_BYTES = 163840;
constexpr int SC1 = 122880, L_WPHI = SC1, L_TAB1 = SC1 + 4096, L_TBUF = SC1 + 6144, L_XBUF = SC1 + 22528;
constexpr int SC3 = 147456, L_TAB3 = SC3, L_PST = SC3 + 2048, L_FIN = SC3 + 4096;
constexpr int L_CNT = LDS_BYTES - 64;
constexpr int SC_SC1 = 16;

DI void bar() { asm volatile("" ::: "memory"); __builtin_amdgcn_s_barrier(); asm volatile("" ::: "memory"); }
template <int N> DI void wait_vm() { asm volatile("s_waitcnt vmcnt(%0)" ::"n"(N) : "memory"); }
DI int opaque(int v) { asm volatile("" : "+v"(v)); return v; }
DI void wait_lgkm0() { __builtin_amdgcn_s_waitcnt(0xc07f); }
DI unsigned pk2(float lo, float hi) { f32x2 v = {lo, hi}; return __builtin_bit_cast(unsigned, __builtin_convertvector(v, bf2)); }
DI bf16x8 pk8(const f32x4& a, const f32x4& b) { u32x4 r = {pk2(a[0], a[1]), pk2(a[2], a[3]), pk2(b[0], b[1]), pk2(b[2], b[3])}; return __builtin_bit_cast(bf16x8, r); }
DI float bflo(unsigned w) { return __uint_as_float(w << 16); }
DI float bfhi(unsigned w) { return __uint_as_float(w & 0xffff0000u); }
DI f32x4 mfma(bf16x8 a, bf16x8 b, f32x4 c) { return __builtin_amdgcn_mfma_f32_16x16x32_bf16(a, b, c, 0, 0, 0); }
DI bf16x8 ldsA(lds_t p) { return *(LAS bf16x8*)p; }
DI bf16x8 ldsT(lds_t p, int d) {
  s16x4 lo = __builtin_amdgcn_ds_read_tr16_b64_v4i16((LAS s16x4*)p), hi = __builtin_amdgcn_ds_read_tr16_b64_v4i16((LAS s16x4*)(p + d));
  return __builtin_shufflevector(lo, hi, 0, 1, 2, 3, 4, 5, 6, 7);
}
template <int D1, int D2> DI void tr4_issue(unsigned addr, u32x2 (&r)[4]) {
  asm volatile("ds_read_b64_tr_b16 %0, %4\n\tds_read_b64_tr_b16 %1, %4 offset:%5\n\tds_read_b64_tr_b16 %2, %4 offset:%6\n\tds_read_b64_tr_b16 %3, %4 offset:%7"
               : "=&v"(r[0]), "=&v"(r[1]), "=&v"(r[2]), "=&v"(r[3]) : "v"(addr), "n"(D1), "n"(D2), "n"(D1 + D2) : "memory");
}
DI void tr4_wait(u32x2 (&r)[4]) { asm volatile("s_waitcnt lgkmcnt(0)" : "+v"(r[0]), "+v"(r[1]), "+v"(r[2]), "+v"(r[3])::"memory"); }
DI float sum_g(float x) {
  auto a = __builtin_amdgcn_permlane16_swap(__float_as_uint(x), __float_as_uint(x), false, false);
  x = __uint_as_float(a[0]) + __uint_as_float(a[1]);
  auto b = __builtin_amdgcn_permlane32_swap(__float_as_uint(x), __float_as_uint(x), false, false);
  return __uint_as_float(b[0]) + __uint_as_float(b[1]);
}
DI float max_g(float x) {
  auto a = __builtin_amdgcn_permlane16_swap(__float_as_uint(x), __float_as_uint(x), false, false);
  x = fmaxf(__uint_as_float(a[0]), __uint_as_float(a[1]));
  auto b = __builtin_amdgcn_permlane32_swap(__float_as_uint(x), __float_as_uint(x), false, false);
  return fmaxf(__uint_as_float(b[0]), __uint_as_float(b[1]));
}
template <int CTRL> DI float dpp_add(float x) { return x + __int_as_float(__builtin_amdgcn_update_dpp(0, __float_as_int(x), CTRL, 0xf, 0xf, true)); }
DI float sum_row16(float x) { x = dpp_add<0x128>(x); x = dpp_add<0x124>(x); x = dpp_add<0x122>(x); return dpp_add<0x121>(x); }
DI float dot2(unsigned a, unsigned b, float c) { return __builtin_amdgcn_fdot2_f32_bf16(__builtin_bit_cast(bf2, a), __builtin_bit_cast(bf2, b), c, false); }
constexpr unsigned ONES2 = 0x3f803f80u;
DI float gelu(float x) { const float t = x * fmaf(x * x, -0.1029432397f, -2.302208198f); return x * __builtin_amdgcn_rcpf(1.0f + __builtin_amdgcn_exp2f(t)); }

struct Ctx {
  __amdgpu_buffer_rsrc_t ws;
  char* wsp; lds_t lds; unsigned tag; int lane, wid, b;
};
DI unsigned* flag_ptr(const Ctx& c, int g, int a, int bb) { return (unsigned*)(c.wsp + OFF_FLAG + ((g * 16 + a) * 16 + bb) * 128); }
DI void publish(const Ctx& c, int cnt, int g, int a, int bb) {
  wait_vm<0>();
  if (c.lane == 0) {
    unsigned old = __hip_atomic_fetch_add((LAS unsigned*)(c.lds + L_CNT + cnt * 4), 1u, __ATOMIC_RELAXED, __HIP_MEMORY_SCOPE_WORKGROUP);
    if (old == 3u) __hip_atomic_store(flag_ptr(c, g, a, bb), c.tag, __ATOMIC_RELAXED, __HIP_MEMORY_SCOPE_AGENT);
  }
}
DI void wait_flags(const Ctx& c, int g, int a, int n) {
  const unsigned* p = flag_ptr(c, g, a, c.lane < n ? c.lane : 0);
  const unsigned long long t0 = __builtin_amdgcn_s_memrealtime();
  for (;;) {
    const unsigned v = __hip_atomic_load(p, __ATOMIC_RELAXED, __HIP_MEMORY_SCOPE_AGENT);
    if (__all(v == c.tag)) break;
    if (__builtin_amdgcn_s_memrealtime() - t0 > 300000ull) { if (c.lane == 0) __hip_atomic_store((unsigned*)(c.wsp + OFF_ERR), 0x100u + (unsigned)g, __ATOMIC_RELAXED, __HIP_MEMORY_SCOPE_AGENT); break; }
    __builtin_amdgcn_s_sleep(1);
  }
  asm volatile("" ::: "memory");
}
#define TS(k) do {} while (0)

DI void st16(const Ctx& c, bf16x8 v, int off) { __builtin_amdgcn_raw_buffer_store_b128(__builtin_bit_cast(u32x4, v), c.ws, off, 0, SC_SC1); }
template <int AUX> DI void dma(__amdgpu_buffer_rsrc_t r, lds_t dst, int voff, int soff) { __builtin_amdgcn_raw_ptr_buffer_load_lds(r, (LAS void*)dst, 16, voff, soff, 0, AUX); }

template <int S, int NW, class IA, class IW, class ST, class FIN>
DI void loader_loop(int nt, bool gated_prologue, IA issueA, IW issueW, ST stats, FIN fin) {
  constexpr int PPW = 4 + NW;
  if (gated_prologue) wait_vm<4 * (S - 2)>(); else wait_vm<PPW * (S - 2)>();
  bar();
  int t = 0;
#pragma unroll
  for (int p = 0; p < S - 2; ++p, ++t) {
    issueA(t + S - 1); issueW(t + S - 1); stats(t);
    if (gated_prologue) {
      if (p == 0) wait_vm<4 * (S - 3 > 0 ? S - 3 : 0) + PPW * 1>();
      else if (p == 1) wait_vm<4 * (S - 4 > 0 ? S - 4 : 0) + PPW * 2>();
      else wait_vm<4 * (S - 5 > 0 ? S - 5 : 0) + PPW * 3>();
    } else wait_vm<PPW * (S - 2)>();
    bar();
  }
  for (; t + S - 1 < nt; ++t) { issueA(t + S - 1); issueW(t + S - 1); stats(t); wait_vm<PPW * (S - 2)>(); bar(); }
#pragma unroll
  for (int r = S - 2; r >= 0; --r, ++t) {
    stats(t);
    if (r == 0) fin();
    if (r >= 4) wait_vm<PPW * 3>(); else if (r == 3) wait_vm<PPW * 2>(); else if (r == 2) wait_vm<PPW * 1>(); else wait_vm<0>();
    bar();
  }
}
template <int S, int SB, class RD0, class RD1, class MM0, class MM1, class SCH>
DI void consumer_loop(int nt, RD0 rd0, RD1 rd1, MM0 mm0, MM1 mm1, SCH sched) {
  bar();
  rd0(0);
  int sb = 0;
  for (int t = 0; t < nt; ++t) {
    rd1(sb); mm0(); sched();
    __builtin_amdgcn_sched_barrier(0);
    wait_lgkm0(); bar();
    __builtin_amdgcn_sched_barrier(0);
    sb = (sb == (S - 1) * SB) ? 0 : sb + SB;
    rd0(sb); mm1(); sched();
    __builtin_amdgcn_sched_barrier(0);
  }
}
template <int NM, int NR> DI void interleave() {
#pragma unroll
  for (int i = 0; i < NR; ++i) { __builtin_amdgcn_sched_group_barrier(0x008, 1, 0); __builtin_amdgcn_sched_group_barrier(0x100, 1, 0); }
  __builtin_amdgcn_sched_group_barrier(0x008, NM - NR, 0);
}
template <int NM, int NR> DI void interleave2() {
#pragma unroll
  for (int i = 0; i < NR / 2; ++i) { __builtin_amdgcn_sched_group_barrier(0x008, 1, 0); __builtin_amdgcn_sched_group_barrier(0x100, 2, 0); }
  __builtin_amdgcn_sched_group_barrier(0x008, NM - NR / 2, 0);
}

DI int rowmajor_voff(int lw, int lane, int pitch) {
  const int rr = lane >> 3, slot = 8 * (lw & 1) + rr;
  const int row = 8 * (slot >> 2) + 4 * (lw >> 1) + (slot & 3);
  return row * pitch + (((lane & 7) ^ (slot >> 1)) << 4);
}
DI int true_row(int mt, int i) { return 32 * (mt >> 1) + 8 * (i >> 2) + 4 * (mt & 1) + (i & 3); }

template <int NC> struct Stats { float s[2], q[2], c[NC]; };
template <int NC> DI void stats_zero(Stats<NC>& st) { st.s[0] = st.s[1] = st.q[0] = st.q[1] = 0.f;
#pragma unroll
  for (int i = 0; i < NC; ++i) st.c[i] = 0.f; }
DI void acc_row(float& s, float& q, bf16x8 f) { const u32x4 w = __builtin_bit_cast(u32x4, f);
#pragma unroll
  for (int i = 0; i < 4; ++i) { s = dot2(w[i], ONES2, s); q = dot2(w[i], w[i], q); } }
DI void acc_col(float& s, const u32x2 (&r)[4]) {
#pragma unroll
  for (int i = 0; i < 4; ++i) { s = dot2(r[i][0], ONES2, s); s = dot2(r[i][1], ONES2, s); } }
template <int NC> DI void stats_fin(Stats<NC>& st, lds_t tab, int lw, int lane) {
#pragma unroll
  for (int e = 0; e < 2; ++e) {
    const float s = sum_g(st.s[e]) * (1.0f / 1024.0f), q = sum_g(st.q[e]) * (1.0f / 1024.0f);
    const float rs = __builtin_amdgcn_rsqf(fmaxf(q - s * s, 0.f) + 1e-5f);
    if (lane < 16) { *(LAS float*)(tab + ((2 * lw + e) * 16 + lane) * 4) = s; *(LAS float*)(tab + 512 + ((2 * lw + e) * 16 + lane) * 4) = rs; }
  }
#pragma unroll
  for (int n = 0; n < NC; ++n) { const float s = sum_g(st.c[n]); if (lane < 16) *(LAS float*)(tab + 1024 + ((NC * lw + n) * 16 + lane) * 4) = s; }
  wait_lgkm0();
}

struct Args { const u16 *x, *wqkv, *wphi, *wo, *win, *wsp, *bsp, *wout; u16* out; char* ws; };

DI void loader_main(const Ctx& c, const Args& A, int i1, int j1, int i2, int j2, int i3, int j3, bool act3, int last) {
  const int lane = c.lane, lw = c.wid - 4, g = lane >> 4, q = (lane & 15) >> 2, p = lane & 3, i16 = lane & 15;
  const lds_t lds = c.lds;
  {
    constexpr int S = 3, SB = 40960, NW = 6;
    const __amdgpu_buffer_rsrc_t rA = __builtin_amdgcn_make_buffer_rsrc((void*)(A.x + (size_t)i1 * 128 * 1024), 0, 128 * 2048, 0x00020000);
    const __amdgpu_buffer_rsrc_t rW = __builtin_amdgcn_make_buffer_rsrc((void*)A.wqkv, 0, 1024 * 6144, 0x00020000);
    const __amdgpu_buffer_rsrc_t rP = __builtin_amdgcn_make_buffer_rsrc((void*)A.wphi, 0, 65536, 0x00020000);
    const int voA = rowmajor_voff(lw, lane, 2048);
    int voW[NW];
#pragma unroll
    for (int i = 0; i < NW; ++i) { const int o = (NW * lw + i) * 1024 + lane * 16, r = o / 384, rem = o % 384, seg = rem >> 7, s16 = (rem & 127) >> 4;
      const int c32 = (s16 >> 1) ^ ((r >> 1) & 3), r5 = r & 31, k = (r & 32) + 8 * ((r5 >> 2) & 3) + 4 * (r5 >> 4) + (r5 & 3);
      voW[i] = k * 6144 + seg * 2048 + j1 * 128 + c32 * 32 + (s16 & 1) * 16; }
    const int rot = (i1 + j1) & 15;
    auto issueA = [&](int t) { const lds_t d = lds + (t % S) * SB + lw * 1024; const int so = ((t + rot) & 15) * 128;
#pragma unroll
      for (int i = 0; i < 4; ++i) dma<0>(rA, d + i * 4096, voA, so + i * 32 * 2048); };
    auto issueW = [&](int t) { const lds_t d = lds + (t % S) * SB + 16384 + lw * NW * 1024; const int so = ((t + rot) & 15) * 64 * 6144;
#pragma unroll
      for (int i = 0; i < NW; ++i) dma<0>(rW, d + i * 1024, voW[i], so); };
    dma<0>(rP, lds + L_WPHI + lw * 1024, lane * 16 + lw * 1024, j1 * 4096);
    issueA(0); issueW(0); issueA(1); issueW(1);
    Stats<3> st; stats_zero(st);
    const int sw = (2 * g + (q >> 1)) & 3;
    const int ao0 = (2 * lw * 16 + i16) * 128 + ((g ^ (i16 >> 1)) << 4), ao1 = ao0 ^ 64;
    int wo_[3];
#pragma unroll
    for (int n = 0; n < 3; ++n) { const int nu = 3 * lw + n; wo_[n] = 16384 + (4 * g + q) * 384 + 8 * p + (nu >> 2) * 128 + 32 * ((nu & 3) ^ sw); }
    auto stats = [&](int t) { const lds_t s = lds + (t % S) * SB; u32x2 cr[3][4];
#pragma unroll
      for (int e = 0; e < 2; ++e) { acc_row(st.s[e], st.q[e], ldsA(s + ao0 + e * 2048)); acc_row(st.s[e], st.q[e], ldsA(s + ao1 + e * 2048)); }
#pragma unroll
      for (int n = 0; n < 3; ++n) tr4_issue<16 * 384, 32 * 384>((unsigned)(size_t)(s + wo_[n]), cr[n]);
#pragma unroll
      for (int n = 0; n < 3; ++n) { tr4_wait(cr[n]); acc_col(st.c[n], cr[n]); }
      wait_lgkm0(); };
    auto fin = [&]() { stats_fin(st, lds + L_TAB1, lw, lane); };
    TS(0);
    loader_loop<S, NW>(16, false, issueA, issueW, stats, fin);
    TS(1);
  }
  const __amdgpu_buffer_rsrc_t rWS = c.ws;
  int voW2[2];
#pragma unroll
  for (int i = 0; i < 2; ++i) { const int r = (2 * lw + i) * 8 + (lane >> 3), s16 = lane & 7; voW2[i] = r * 2048 + j2 * 128 + (((s16 >> 1) ^ ((r >> 1) & 3)) << 5) + (s16 & 1) * 16; }
  const int voF = lane * 16 + lw * 1024;
  {
    constexpr int S = 5, SB = 24576, NW = 2;
    const __amdgpu_buffer_rsrc_t rW = __builtin_amdgcn_make_buffer_rsrc((void*)A.wo, 0, 1024 * 2048, 0x00020000);
    auto issueA = [&](int t) { const lds_t d = lds + (t % S) * SB + lw * 1024; const int so = OFF_OF + (i2 * 16 + t) * 16384;
#pragma unroll
      for (int i = 0; i < 4; ++i) dma<SC_SC1>(rWS, d + i * 4096, voF, so + i * 4096); };
    auto issueW = [&](int t) { const lds_t d = lds + (t % S) * SB + 16384 + lw * NW * 1024;
#pragma unroll
      for (int i = 0; i < NW; ++i) dma<0>(rW, d + i * 1024, voW2[i], t * 64 * 2048); };
#pragma unroll
    for (int s = 0; s < S - 1; ++s) issueW(s);
    bar();
    if (lw == 0) wait_flags(c, FG_KV, j1, 16);
    TS(2);
    bar();
    {
      const int tid = c.wid * 64 + lane; f32x4 a0 = {0, 0, 0, 0}, a1 = {0, 0, 0, 0};
      u32x4 v[16];
#pragma unroll
      for (int k = 0; k < 16; ++k) v[k] = __builtin_amdgcn_raw_buffer_load_b128(rWS, tid * 16, OFF_KVP + (j1 * 16 + k) * 8192, SC_SC1);
#pragma unroll
      for (int k = 0; k < 16; ++k) { a0[0] += bflo(v[k][0]); a0[1] += bfhi(v[k][0]); a0[2] += bflo(v[k][1]); a0[3] += bfhi(v[k][1]); a1[0] += bflo(v[k][2]); a1[1] += bfhi(v[k][2]); a1[2] += bflo(v[k][3]); a1[3] += bfhi(v[k][3]); }
      const int w = c.wid, hd = w >> 2, dt = (w >> 1) & 1, fp = w & 1;
      const lds_t tb = lds + L_XBUF + hd * 8192 + (32 * fp + i16) * 128 + (16 * dt + 4 * g) * 2;
      *(LAS u32x2*)tb = (u32x2){pk2(a0[0], a0[1]), pk2(a0[2], a0[3])};
      *(LAS u32x2*)(tb + 16 * 128) = (u32x2){pk2(a1[0], a1[1]), pk2(a1[2], a1[3])};
      wait_lgkm0();
    }
    bar();
    if (last < 2) return;
    if (lw == 0) wait_flags(c, FG_O, i2, 16);
    TS(3);
    bar();
#pragma unroll
    for (int s = 0; s < S - 1; ++s) issueA(s);
    loader_loop<S, NW>(16, true, issueA, issueW, [](int) {}, []() {});
    TS(4);
  }
  if (last < 3) return;
  if (act3) {
    {
      constexpr int S = 3, SB = 49152, NW = 8;
      const __amdgpu_buffer_rsrc_t rW = __builtin_amdgcn_make_buffer_rsrc((void*)A.win, 0, 1024 * 7168, 0x00020000);
      int voW[NW];
#pragma unroll
      for (int i = 0; i < NW; ++i) { const int r = (NW * lw + i) * 2 + (lane >> 5), l5 = lane & 31, seg = l5 >> 4, s16 = l5 & 15;
        voW[i] = r * 7168 + (seg == 0 ? 3584 : 0) + j3 * 256 + (((s16 >> 1) ^ (r & 7)) << 5) + (s16 & 1) * 16; }
      auto issueA = [&](int t) { const lds_t d = lds + (t % S) * SB + lw * 1024; const int so = OFF_HF + (i3 * 16 + t) * 16384;
#pragma unroll
        for (int i = 0; i < 4; ++i) dma<SC_SC1>(rWS, d + i * 4096, voF, so + i * 4096); };
      auto issueW = [&](int t) { const lds_t d = lds + (t % S) * SB + 16384 + lw * NW * 1024;
#pragma unroll
        for (int i = 0; i < NW; ++i) dma<0>(rW, d + i * 1024, voW[i], t * 64 * 7168); };
      issueW(0); issueW(1);
      if (lw == 0) wait_flags(c, FG_H, i3, 16);
      TS(5);
      bar();
      issueA(0); issueA(1);
      Stats<4> st; stats_zero(st);
      const int ao = 2 * lw * 2048 + lane * 16;
      int wo_[4];
#pragma unroll
      for (int n = 0; n < 4; ++n) wo_[n] = 16384 + (4 * g + q) * 512 + 8 * p + (lw >> 1) * 256 + 32 * ((4 * (lw & 1) + n) ^ (4 * (g & 1) + q));
      auto stats = [&](int t) { const lds_t s = lds + (t % S) * SB; u32x2 cr[4][4];
#pragma unroll
        for (int e = 0; e < 2; ++e) { acc_row(st.s[e], st.q[e], ldsA(s + ao + e * 2048)); acc_row(st.s[e], st.q[e], ldsA(s + ao + e * 2048 + 1024)); }
#pragma unroll
        for (int n = 0; n < 4; ++n) tr4_issue<16 * 512, 32 * 512>((unsigned)(size_t)(s + wo_[n]), cr[n]);
#pragma unroll
        for (int n = 0; n < 4; ++n) { tr4_wait(cr[n]); acc_col(st.c[n], cr[n]); }
        wait_lgkm0(); };
      auto fin = [&]() { stats_fin(st, lds + L_TAB3, lw, lane); };
      loader_loop<S, NW>(16, true, issueA, issueW, stats, fin);
      TS(6);
    }
    {
      constexpr int S = 4, SB = 32768, NW = 4;
      const __amdgpu_buffer_rsrc_t rA = __builtin_amdgcn_make_buffer_rsrc((void*)(A.wsp + (size_t)i3 * 128 * 2048), 0, 128 * 4096, 0x00020000);
      const int voA = rowmajor_voff(lw, lane, 4096);
      auto issueW = [&](int t) { const lds_t d = lds + (t % S) * SB + lw * 1024;
#pragma unroll
        for (int i = 0; i < 4; ++i) dma<0>(rA, d + i * 4096, voA, t * 128 + i * 32 * 4096); };
      auto issueA = [&](int t) { const lds_t d = lds + (t % S) * SB + 16384 + lw * 1024; const int so = OFF_Z2 + (j3 * 32 + t) * 16384;
#pragma unroll
        for (int i = 0; i < 4; ++i) dma<SC_SC1>(rWS, d + i * 4096, voF, so + i * 4096); };
#pragma unroll
      for (int s = 0; s < S - 1; ++s) issueW(s);
      bar();
      if (lw == 0) wait_flags(c, FG_ST, i3, 14);
      TS(7);
      bar();
      if (lw < 2) {
        const int R = lw * 64 + lane; float s = 0.f, qq = 0.f; u32x2 v[14];
#pragma unroll
        for (int k = 0; k < 14; ++k) v[k] = __builtin_amdgcn_raw_buffer_load_b64(rWS, R * 8, OFF_ST2 + (i3 * 14 + k) * 1024, SC_SC1);
#pragma unroll
        for (int k = 0; k < 14; ++k) { s += __uint_as_float(v[k][0]); qq += __uint_as_float(v[k][1]); }
        s *= (1.0f / 1792.0f); qq *= (1.0f / 1792.0f);
        *(LAS float*)(lds + L_FIN + R * 4) = s; *(LAS float*)(lds + L_FIN + 512 + R * 4) = __builtin_amdgcn_rsqf(fmaxf(qq - s * s, 0.f) + 1e-5f);
        wait_lgkm0();
      }
      bar();
      if (last < 4) return;
      if (lw == 0) wait_flags(c, FG_Z2, j3, 16);
      TS(8);
      bar();
      if (c.b == 0 && lw == 0 && lane == 0) __hip_atomic_store((unsigned*)(c.wsp + OFF_EPOCH), c.tag, __ATOMIC_RELAXED, __HIP_MEMORY_SCOPE_AGENT);
#pragma unroll
      for (int s = 0; s < S - 1; ++s) issueA(s);
      loader_loop<S, NW>(32, true, issueA, issueW, [](int) {}, []() {});
      TS(9);
    }
  }
  if (last < 5) return;
  {
    constexpr int S = 5, SB = 24576, NW = 2;
    const __amdgpu_buffer_rsrc_t rW = __builtin_amdgcn_make_buffer_rsrc((void*)A.wout, 0, 1792 * 2048, 0x00020000);
    auto issueA = [&](int t) { const lds_t d = lds + (t % S) * SB + lw * 1024; const int so = OFF_UF + (i2 * 28 + t) * 16384;
#pragma unroll
      for (int i = 0; i < 4; ++i) dma<SC_SC1>(rWS, d + i * 4096, voF, so + i * 4096); };
    auto issueW = [&](int t) { const lds_t d = lds + (t % S) * SB + 16384 + lw * NW * 1024;
#pragma unroll
      for (int i = 0; i < NW; ++i) dma<0>(rW, d + i * 1024, voW2[i], t * 64 * 2048); };
#pragma unroll
    for (int s = 0; s < S - 1; ++s) issueW(s);
    if (lw == 0) wait_flags(c, FG_U, i2, 14);
    TS(10);
    bar();
#pragma unroll
    for (int s = 0; s < S - 1; ++s) issueA(s);
    loader_loop<S, NW>(28, true, issueA, issueW, [](int) {}, []() {});
    TS(11);
  }
}

DI void feat(const f32x4& p0, const f32x4& p1, f32x4& e0, f32x4& e1, f32x4& n0, f32x4& n1) {
  float mx = fmaxf(fmaxf(fmaxf(p0[0], p0[1]), fmaxf(p0[2], p0[3])), fmaxf(fmaxf(p1[0], p1[1]), fmaxf(p1[2], p1[3])));
  float mn = fminf(fminf(fminf(p0[0], p0[1]), fminf(p0[2], p0[3])), fminf(fminf(p1[0], p1[1]), fminf(p1[2], p1[3])));
  mx = max_g(mx); mn = -max_g(-mn);
  constexpr float L2E = 1.4426950408889634f;
  const float bx = -mx * L2E, bn = mn * L2E;
  float sp = 0.f, sn = 0.f;
#pragma unroll
  for (int r = 0; r < 4; ++r) {
    e0[r] = __builtin_amdgcn_exp2f(fmaf(p0[r], L2E, bx)); e1[r] = __builtin_amdgcn_exp2f(fmaf(p1[r], L2E, bx));
    n0[r] = __builtin_amdgcn_exp2f(fmaf(p0[r], -L2E, bn)); n1[r] = __builtin_amdgcn_exp2f(fmaf(p1[r], -L2E, bn));
    sp += e0[r] + e1[r]; sn += n0[r] + n1[r];
  }
  sp = __builtin_amdgcn_rcpf(sum_g(sp)); sn = __builtin_amdgcn_rcpf(sum_g(sn));
  e0 *= sp; e1 *= sp; n0 *= sn; n1 *= sn;
}

DI void consumer_main(const Ctx& c, const Args& A, int i1, int j1, int i2, int j2, int i3, int j3, bool act3, int last) {
  const int lane = c.lane, wr = c.wid >> 1, wc = c.wid & 1, g = lane >> 4, q = (lane & 15) >> 2, p = lane & 3, i16 = lane & 15;
  const lds_t lds = c.lds;
  const __amdgpu_buffer_rsrc_t rWS = c.ws;
  const f32x4 Z4 = {0.f, 0.f, 0.f, 0.f};
  {
    constexpr int S = 3, SB = 40960;
    f32x4 aq[4][2], ak[4][2], av[4][2];
#pragma unroll
    for (int m = 0; m < 4; ++m)
#pragma unroll
      for (int n = 0; n < 2; ++n) { aq[m][n] = Z4; ak[m][n] = Z4; av[m][n] = Z4; }
    const int sw = (2 * g + (q >> 1)) & 3;
    const int ao0 = (wr * 64 + i16) * 128 + ((g ^ (i16 >> 1)) << 4), ao1 = ao0 ^ 64;
    const int wl = 16384 + (4 * g + q) * 384 + 8 * p;
    const int wo0 = wl + 32 * ((2 * wc) ^ sw), wo1 = wl + 32 * ((2 * wc + 1) ^ sw);
    bf16x8 a0[4], w0[6], a1[4], w1[6];
    auto rd = [&](bf16x8(&a)[4], bf16x8(&w)[6], int sb, int ks) { const lds_t s = lds + sb;
#pragma unroll
      for (int sg = 0; sg < 3; ++sg) { w[2 * sg] = ldsT(s + wo0 + sg * 128 + ks * 32 * 384, 16 * 384); w[2 * sg + 1] = ldsT(s + wo1 + sg * 128 + ks * 32 * 384, 16 * 384); }
#pragma unroll
      for (int m = 0; m < 4; ++m) a[m] = ldsA(s + (ks ? ao1 : ao0) + m * 2048); };
    auto mm = [&](bf16x8(&a)[4], bf16x8(&w)[6]) {
#pragma unroll
      for (int n = 0; n < 2; ++n)
#pragma unroll
        for (int m = 0; m < 4; ++m) aq[m][n] = mfma(w[n], a[m], aq[m][n]);
#pragma unroll
      for (int n = 0; n < 2; ++n)
#pragma unroll
        for (int m = 0; m < 4; ++m) ak[m][n] = mfma(w[2 + n], a[m], ak[m][n]);
#pragma unroll
      for (int n = 0; n < 2; ++n)
#pragma unroll
        for (int m = 0; m < 4; ++m) av[m][n] = mfma(a[m], w[4 + n], av[m][n]); };
    TS(0);
    consumer_loop<S, SB>(16, [&](int sb) { rd(a0, w0, sb, 0); }, [&](int sb) { rd(a1, w1, sb, 1); }, [&]() { mm(a0, w0); }, [&]() { mm(a1, w1); }, []() { interleave<24, 16>(); });
    TS(1);
    const lds_t tab = lds + L_TAB1;
    bf16x8 qf[4], kf[4], vf[2][2];
    {
      f32x4 cq[2], ck[2]; float cv[2];
#pragma unroll
      for (int n = 0; n < 2; ++n) { cq[n] = *(LAS f32x4*)(tab + 1024 + (32 * wc + 16 * n + 4 * g) * 4); ck[n] = *(LAS f32x4*)(tab + 1024 + (64 + 32 * wc + 16 * n + 4 * g) * 4); cv[n] = *(LAS float*)(tab + 1024 + (128 + 32 * wc + 16 * n + i16) * 4); }
      f32x4 vv[4][2];
#pragma unroll
      for (int m = 0; m < 4; ++m) {
        const int R = 64 * wr + 16 * m;
        const float mu = *(LAS float*)(tab + (R + i16) * 4), rs = *(LAS float*)(tab + 512 + (R + i16) * 4);
        qf[m] = pk8((aq[m][0] - mu * cq[0]) * rs, (aq[m][1] - mu * cq[1]) * rs);
        kf[m] = pk8((ak[m][0] - mu * ck[0]) * rs, (ak[m][1] - mu * ck[1]) * rs);
        const f32x4 mu4 = *(LAS f32x4*)(tab + (R + 4 * g) * 4), rs4 = *(LAS f32x4*)(tab + 512 + (R + 4 * g) * 4);
        vv[m][0] = (av[m][0] - mu4 * cv[0]) * rs4; vv[m][1] = (av[m][1] - mu4 * cv[1]) * rs4;
      }
#pragma unroll
      for (int u = 0; u < 2; ++u) { vf[u][0] = pk8(vv[2 * u][0], vv[2 * u + 1][0]); vf[u][1] = pk8(vv[2 * u][1], vv[2 * u + 1][1]); }
    }
    bf16x8 wT[2];
#pragma unroll
    for (int ft = 0; ft < 2; ++ft) wT[ft] = ldsT(lds + L_WPHI + wc * 2048 + (4 * g + q) * 64 + ft * 32 + 8 * p, 16 * 64);
    f32x4 kv[2][4], zz[4];
#pragma unroll
    for (int f = 0; f < 4; ++f) { kv[0][f] = Z4; kv[1][f] = Z4; zz[f] = Z4; }
    const lds_t tb = lds + L_TBUF + c.wid * 4096;
    const bf16x8 ones = __builtin_bit_cast(bf16x8, (u32x4){ONES2, ONES2, ONES2, ONES2});
#pragma unroll
    for (int u = 0; u < 2; ++u) {
#pragma unroll
      for (int e = 0; e < 2; ++e) {
        const f32x4 p0 = mfma(wT[0], kf[2 * u + e], Z4), p1 = mfma(wT[1], kf[2 * u + e], Z4);
        f32x4 e0, e1, n0, n1; feat(p0, p1, e0, e1, n0, n1);
        const lds_t rw = tb + (16 * e + i16) * 128;
        *(LAS u32x2*)(rw + 8 * ((0 + g) ^ i16)) = (u32x2){pk2(e0[0], e0[1]), pk2(e0[2], e0[3])};
        *(LAS u32x2*)(rw + 8 * ((4 + g) ^ i16)) = (u32x2){pk2(e1[0], e1[1]), pk2(e1[2], e1[3])};
        *(LAS u32x2*)(rw + 8 * ((8 + g) ^ i16)) = (u32x2){pk2(n0[0], n0[1]), pk2(n0[2], n0[3])};
        *(LAS u32x2*)(rw + 8 * ((12 + g) ^ i16)) = (u32x2){pk2(n1[0], n1[1]), pk2(n1[2], n1[3])};
      }
      asm volatile("s_waitcnt lgkmcnt(0)" ::: "memory");
#pragma unroll
      for (int f = 0; f < 4; ++f) {
        const int r0 = 4 * g + q;
        const s16x4 lo = __builtin_amdgcn_ds_read_tr16_b64_v4i16((LAS s16x4*)(tb + r0 * 128 + 8 * ((4 * f + p) ^ r0)));
        const s16x4 hi = __builtin_amdgcn_ds_read_tr16_b64_v4i16((LAS s16x4*)(tb + (16 + r0) * 128 + 8 * ((4 * f + p) ^ r0)));
        const bf16x8 pf = __builtin_shufflevector(lo, hi, 0, 1, 2, 3, 4, 5, 6, 7);
        kv[0][f] = mfma(vf[u][0], pf, kv[0][f]); kv[1][f] = mfma(vf[u][1], pf, kv[1][f]); zz[f] = mfma(ones, pf, zz[f]);
      }
      asm volatile("s_waitcnt lgkmcnt(0)" ::: "memory");
    }
    {
      const lds_t xb = lds + L_XBUF + c.wid * 4096 + lane * 16;
#pragma unroll
      for (int f = 0; f < 4; ++f) *(LAS f32x4*)(xb + f * 1024) = wr ? kv[0][f] : kv[1][f];
      const float zsel = g == 0 ? zz[0][0] : g == 1 ? zz[1][0] : g == 2 ? zz[2][0] : zz[3][0];
      if (wr == 1) *(LAS float*)(tb + lane * 4) = zsel;
      wait_lgkm0(); bar();
      const lds_t xp = lds + L_XBUF + (c.wid ^ 2) * 4096 + lane * 16;
      f32x4 k4[4];
#pragma unroll
      for (int f = 0; f < 4; ++f) k4[f] = (wr ? kv[1][f] : kv[0][f]) + *(LAS f32x4*)(xp + f * 1024);
      const int so = OFF_KVP + (j1 * 16 + i1) * 8192 + wc * 4096 + wr * 2048;
      st16(c, pk8(k4[0], k4[1]), lane * 16 + (so));
      st16(c, pk8(k4[2], k4[3]), lane * 16 + (so + 1024));
      if (wr == 0) { const float zt = zsel + *(LAS float*)(lds + L_TBUF + (c.wid ^ 2) * 4096 + lane * 4);
        __builtin_amdgcn_raw_buffer_store_b32(__float_as_uint(zt), rWS, lane * 4, OFF_ZP + ((j1 * 16 + i1) * 2 + wc) * 256, SC_SC1); }
      publish(c, 0, FG_KV, j1, i1);
    }
    TS(2);
    bf16x8 ph[4][2];
#pragma unroll
    for (int m = 0; m < 4; ++m) {
      const f32x4 p0 = mfma(wT[0], qf[m], Z4), p1 = mfma(wT[1], qf[m], Z4);
      f32x4 e0, e1, n0, n1; feat(p0, p1, e0, e1, n0, n1);
      ph[m][0] = pk8(e0, e1); ph[m][1] = pk8(n0, n1);
    }
    TS(3);
    bar();
    {
      const int tid = c.wid * 64 + lane; f32x4 s0 = Z4, s1 = Z4;
      u32x4 v[16];
#pragma unroll
      for (int k = 0; k < 16; ++k) v[k] = __builtin_amdgcn_raw_buffer_load_b128(rWS, tid * 16, OFF_KVP + (j1 * 16 + k) * 8192, SC_SC1);
      float zs = 0.f;
      if (wr == 0) { unsigned zv[16];
#pragma unroll
        for (int k = 0; k < 16; ++k) zv[k] = __builtin_amdgcn_raw_buffer_load_b32(rWS, lane * 4, OFF_ZP + ((j1 * 16 + k) * 2 + wc) * 256, SC_SC1);
#pragma unroll
        for (int k = 0; k < 16; ++k) zs += __uint_as_float(zv[k]); }
#pragma unroll
      for (int k = 0; k < 16; ++k) { s0[0] += bflo(v[k][0]); s0[1] += bfhi(v[k][0]); s0[2] += bflo(v[k][1]); s0[3] += bfhi(v[k][1]); s1[0] += bflo(v[k][2]); s1[1] += bfhi(v[k][2]); s1[2] += bflo(v[k][3]); s1[3] += bfhi(v[k][3]); }
      const int w = c.wid, hd = w >> 2, dt = (w >> 1) & 1, fp = w & 1;
      const lds_t t2 = lds + L_XBUF + hd * 8192 + (32 * fp + i16) * 128 + (16 * dt + 4 * g) * 2;
      *(LAS u32x2*)t2 = (u32x2){pk2(s0[0], s0[1]), pk2(s0[2], s0[3])};
      *(LAS u32x2*)(t2 + 16 * 128) = (u32x2){pk2(s1[0], s1[1]), pk2(s1[2], s1[3])};
      if (wr == 0) { const unsigned z2 = pk2(zs, zs); const lds_t t3 = lds + L_XBUF + wc * 8192 + lane * 128 + 64;
        *(LAS u32x4*)t3 = (u32x4){z2, z2, z2, z2}; *(LAS u32x4*)(t3 + 16) = (u32x4){z2, z2, z2, z2}; }
      wait_lgkm0();
    }
    bar();
    TS(4);
    {
      bf16x8 kt[3][2];
#pragma unroll
      for (int d = 0; d < 3; ++d)
#pragma unroll
        for (int s = 0; s < 2; ++s) kt[d][s] = ldsT(lds + L_XBUF + wc * 8192 + (32 * s + 4 * g + q) * 128 + 32 * d + 8 * p, 16 * 128);
#pragma unroll
      for (int m = 0; m < 4; ++m) {
        const f32x4 n0 = mfma(kt[0][1], ph[m][1], mfma(kt[0][0], ph[m][0], Z4)), n1 = mfma(kt[1][1], ph[m][1], mfma(kt[1][0], ph[m][0], Z4));
        const f32x4 dn = mfma(kt[2][1], ph[m][1], mfma(kt[2][0], ph[m][0], Z4));
        const float r = __builtin_amdgcn_rcpf(dn[0] + 1e-6f);
        st16(c, pk8(n0 * r, n1 * r), lane * 16 + (OFF_OF + (i1 * 16 + j1) * 16384 + ((4 * wr + m) * 2 + wc) * 1024));
      }
      publish(c, 1, FG_O, i1, j1);
    }
    TS(5);
  }
  if (last < 2) return;
  auto gemm64 = [&](f32x4(&acc)[4][2], int nt) {
    constexpr int S = 5, SB = 24576;
    const int ln = opaque(lane), g = ln >> 4, q = (ln & 15) >> 2, p = ln & 3;
    const int sw2 = (2 * g + (q >> 1)) & 3, ao2 = wr * 8192 + ln * 16;
    const int wl2 = 16384 + (4 * g + q) * 128 + 8 * p, w2o0 = wl2 + 32 * ((2 * wc) ^ sw2), w2o1 = wl2 + 32 * ((2 * wc + 1) ^ sw2);
    bf16x8 a0[4], w0[2], a1[4], w1[2];
    auto rd = [&](bf16x8(&a)[4], bf16x8(&w)[2], int sb, int ks) { const lds_t s = lds + sb;
      w[0] = ldsT(s + w2o0 + ks * 32 * 128, 16 * 128); w[1] = ldsT(s + w2o1 + ks * 32 * 128, 16 * 128);
#pragma unroll
      for (int m = 0; m < 4; ++m) a[m] = ldsA(s + ao2 + m * 2048 + ks * 1024); };
    auto mm = [&](bf16x8(&a)[4], bf16x8(&w)[2]) {
#pragma unroll
      for (int n = 0; n < 2; ++n)
#pragma unroll
        for (int m = 0; m < 4; ++m) acc[m][n] = mfma(w[n], a[m], acc[m][n]); };
    consumer_loop<S, SB>(nt, [&](int sb) { rd(a0, w0, sb, 0); }, [&](int sb) { rd(a1, w1, sb, 1); }, [&]() { mm(a0, w0); }, [&]() { mm(a1, w1); }, []() { interleave<8, 8>(); });
  };
  {
    f32x4 acc[4][2]; u32x2 xr[4][2];
#pragma unroll
    for (int m = 0; m < 4; ++m)
#pragma unroll
      for (int n = 0; n < 2; ++n) { acc[m][n] = Z4; xr[m][n] = *(const u32x2*)(A.x + (size_t)(i2 * 128 + true_row(4 * wr + m, i16)) * 1024 + j2 * 64 + 32 * wc + 16 * n + 4 * g); }
    bar();
    gemm64(acc, 16);
    TS(6);
#pragma unroll
    for (int m = 0; m < 4; ++m) {
      f32x4 h0 = acc[m][0], h1 = acc[m][1];
      h0[0] += bflo(xr[m][0][0]); h0[1] += bfhi(xr[m][0][0]); h0[2] += bflo(xr[m][0][1]); h0[3] += bfhi(xr[m][0][1]);
      h1[0] += bflo(xr[m][1][0]); h1[1] += bfhi(xr[m][1][0]); h1[2] += bflo(xr[m][1][1]); h1[3] += bfhi(xr[m][1][1]);
      st16(c, pk8(h0, h1), lane * 16 + (OFF_HF + (i2 * 16 + j2) * 16384 + ((4 * wr + m) * 2 + wc) * 1024));
    }
    publish(c, 2, FG_H, i2, j2);
    TS(7);
  }
  if (last < 3) return;
  if (act3) {
    u32x2 z1[4][4];
    {
      constexpr int S = 3, SB = 49152;
      f32x4 a2[4][4], a1_[4][4];
#pragma unroll
      for (int m = 0; m < 4; ++m)
#pragma unroll
        for (int n = 0; n < 4; ++n) { a2[m][n] = Z4; a1_[m][n] = Z4; }
      const int ao2 = wr * 8192 + lane * 16;
      int wo_[4];
#pragma unroll
      for (int n = 0; n < 4; ++n) wo_[n] = 16384 + (4 * g + q) * 512 + 8 * p + 32 * ((4 * wc + n) ^ (4 * (g & 1) + q));
      bf16x8 a0[4], w0[8], a1[4], w1[8];
      auto rd = [&](bf16x8(&a)[4], bf16x8(&w)[8], int sb, int ks) { const lds_t s = lds + sb;
#pragma unroll
        for (int n = 0; n < 8; ++n) w[n] = ldsT(s + wo_[n & 3] + (n >> 2) * 256 + ks * 32 * 512, 16 * 512);
#pragma unroll
        for (int m = 0; m < 4; ++m) a[m] = ldsA(s + ao2 + m * 2048 + ks * 1024); };
      auto mm = [&](bf16x8(&a)[4], bf16x8(&w)[8]) {
#pragma unroll
        for (int n = 0; n < 4; ++n)
#pragma unroll
          for (int m = 0; m < 4; ++m) a2[m][n] = mfma(a[m], w[n], a2[m][n]);
#pragma unroll
        for (int n = 0; n < 4; ++n)
#pragma unroll
          for (int m = 0; m < 4; ++m) a1_[m][n] = mfma(w[4 + n], a[m], a1_[m][n]); };
      bar();
      consumer_loop<S, SB>(16, [&](int sb) { rd(a0, w0, sb, 0); }, [&](int sb) { rd(a1, w1, sb, 1); }, [&]() { mm(a0, w0); }, [&]() { mm(a1, w1); }, []() { interleave<32, 20>(); });
      TS(8);
      const lds_t tab = lds + L_TAB3;
      u32x2 z2p[4][4];
      {
        float cs[4];
#pragma unroll
        for (int n = 0; n < 4; ++n) cs[n] = *(LAS float*)(tab + 1024 + (64 * wc + 16 * n + i16) * 4);
#pragma unroll
        for (int m = 0; m < 4; ++m) {
          const int R = 64 * wr + 16 * m + 4 * g;
          const f32x4 mu = *(LAS f32x4*)(tab + R * 4), rs = *(LAS f32x4*)(tab + 512 + R * 4);
          f32x4 s = Z4, qq = Z4;
#pragma unroll
          for (int n = 0; n < 4; ++n) {
            const f32x4 xx = (a2[m][n] - mu * cs[n]) * rs;
            const unsigned lo = pk2(gelu(xx[0]), gelu(xx[1])), hi = pk2(gelu(xx[2]), gelu(xx[3]));
            z2p[m][n] = (u32x2){lo, hi};
            const f32x4 y = {bflo(lo), bfhi(lo), bflo(hi), bfhi(hi)};
            s += y; qq += y * y;
          }
#pragma unroll
          for (int r = 0; r < 4; ++r) { s[r] = sum_row16(s[r]); qq[r] = sum_row16(qq[r]); }
          if (i16 < 4) { const float sv = i16 == 0 ? s[0] : i16 == 1 ? s[1] : i16 == 2 ? s[2] : s[3], qv = i16 == 0 ? qq[0] : i16 == 1 ? qq[1] : i16 == 2 ? qq[2] : qq[3];
            *(LAS f32x2*)(lds + L_PST + wc * 1024 + (R + i16) * 8) = (f32x2){sv, qv}; }
        }
        wait_lgkm0(); bar();
        if (lane < 32) { const int R = c.wid * 32 + lane; const f32x2 v = *(LAS f32x2*)(lds + L_PST + R * 8) + *(LAS f32x2*)(lds + L_PST + 1024 + R * 8);
          __builtin_amdgcn_raw_buffer_store_b64(__builtin_bit_cast(u32x2, v), rWS, R * 8, OFF_ST2 + (i3 * 14 + j3) * 1024, SC_SC1); }
        publish(c, 3, FG_ST, i3, j3);
      }
      TS(9);
#pragma unroll
      for (int m = 0; m < 4; ++m) {
        const int R = 64 * wr + 16 * m + i16;
        const float mu = *(LAS float*)(tab + R * 4), rs = *(LAS float*)(tab + 512 + R * 4);
#pragma unroll
        for (int n = 0; n < 4; ++n) {
          const f32x4 cs = *(LAS f32x4*)(tab + 1024 + (128 + 64 * wc + 16 * n + 4 * g) * 4);
          const f32x4 xx = (a1_[m][n] - mu * cs) * rs;
          z1[m][n] = (u32x2){pk2(gelu(xx[0]), gelu(xx[1])), pk2(gelu(xx[2]), gelu(xx[3]))};
        }
      }
      TS(10);
      bar();
      bar();
#pragma unroll
      for (int u = 0; u < 2; ++u) {
        f32x4 mu[2], rs[2];
#pragma unroll
        for (int e = 0; e < 2; ++e) { const int R = 64 * wr + 16 * (2 * u + e) + 4 * g; mu[e] = *(LAS f32x4*)(lds + L_FIN + R * 4); rs[e] = *(LAS f32x4*)(lds + L_FIN + 512 + R * 4); }
#pragma unroll
        for (int n = 0; n < 4; ++n) {
          f32x4 y[2];
#pragma unroll
          for (int e = 0; e < 2; ++e) { const u32x2 w = z2p[2 * u + e][n]; y[e] = ((f32x4){bflo(w[0]), bfhi(w[0]), bflo(w[1]), bfhi(w[1])} - mu[e]) * rs[e]; }
          st16(c, pk8(y[0], y[1]), lane * 16 + (OFF_Z2 + (j3 * 32 + 2 * i3 + wr) * 16384 + ((4 * wc + n) * 2 + u) * 1024));
        }
      }
      publish(c, 4, FG_Z2, j3, i3);
      TS(11);
    }
    if (last < 4) return;
    {
      constexpr int S = 4, SB = 32768;
      f32x4 acc[4][4]; float bs[4];
#pragma unroll
      for (int m = 0; m < 4; ++m) { bs[m] = bflo((unsigned)A.bsp[i3 * 128 + true_row(4 * wr + m, i16)]);
#pragma unroll
        for (int n = 0; n < 4; ++n) acc[m][n] = Z4; }
      const int ao0 = (wr * 64 + i16) * 128 + ((g ^ (i16 >> 1)) << 4), ao1 = ao0 ^ 64, zo = 16384 + wc * 8192 + lane * 16;
      bf16x8 a0[4], w0[4], a1[4], w1[4];
      auto rd = [&](bf16x8(&a)[4], bf16x8(&w)[4], int sb, int ks) { const lds_t s = lds + sb;
#pragma unroll
        for (int n = 0; n < 4; ++n) { w[n] = ldsA(s + zo + n * 2048 + ks * 1024); a[n] = ldsA(s + (ks ? ao1 : ao0) + n * 2048); } };
      auto mm = [&](bf16x8(&a)[4], bf16x8(&w)[4]) {
#pragma unroll
        for (int n = 0; n < 4; ++n)
#pragma unroll
          for (int m = 0; m < 4; ++m) acc[m][n] = mfma(w[n], a[m], acc[m][n]); };
      bar();
      consumer_loop<S, SB>(32, [&](int sb) { rd(a0, w0, sb, 0); }, [&](int sb) { rd(a1, w1, sb, 1); }, [&]() { mm(a0, w0); }, [&]() { mm(a1, w1); }, []() { interleave<16, 8>(); });
      TS(12);
#pragma unroll
      for (int m = 0; m < 4; ++m)
#pragma unroll
        for (int u = 0; u < 2; ++u) {
          f32x4 y[2];
#pragma unroll
          for (int e = 0; e < 2; ++e) { const u32x2 w = z1[m][2 * u + e]; y[e] = (f32x4){bflo(w[0]), bfhi(w[0]), bflo(w[1]), bfhi(w[1])} * (acc[m][2 * u + e] + bs[m]); }
          st16(c, pk8(y[0], y[1]), lane * 16 + (OFF_UF + (i3 * 28 + 2 * j3 + wc) * 16384 + ((4 * wr + m) * 2 + u) * 1024));
        }
      publish(c, 5, FG_U, i3, j3);
      TS(13);
    }
  }
  if (last < 5) return;
  {
    const int lane = opaque(c.lane), g = lane >> 4, i16 = lane & 15;
    f32x4 acc[4][2]; u32x4 hr[4];
#pragma unroll
    for (int m = 0; m < 4; ++m) { acc[m][0] = Z4; acc[m][1] = Z4;
      hr[m] = __builtin_amdgcn_raw_buffer_load_b128(rWS, lane * 16, OFF_HF + (i2 * 16 + j2) * 16384 + ((4 * wr + m) * 2 + wc) * 1024, SC_SC1); }
    bar();
    gemm64(acc, 28);
    TS(14);
#pragma unroll
    for (int m = 0; m < 4; ++m) {
      u16* o = A.out + (size_t)(i2 * 128 + true_row(4 * wr + m, i16)) * 1024 + j2 * 64 + 32 * wc + 4 * g;
      *(u32x2*)o = (u32x2){pk2(acc[m][0][0] + bflo(hr[m][0]), acc[m][0][1] + bfhi(hr[m][0])), pk2(acc[m][0][2] + bflo(hr[m][1]), acc[m][0][3] + bfhi(hr[m][1]))};
      *(u32x2*)(o + 16) = (u32x2){pk2(acc[m][1][0] + bflo(hr[m][2]), acc[m][1][1] + bfhi(hr[m][2])), pk2(acc[m][1][2] + bflo(hr[m][3]), acc[m][1][3] + bfhi(hr[m][3]))};
    }
    TS(15);
  }
}

#define LAST_PHASE 5
__global__ __launch_bounds__(512, 2) void fused_fwd(const u16* __restrict__ x, const u16* __restrict__ wqkv, const u16* __restrict__ wphi, const u16* __restrict__ wo,
                                                      const u16* __restrict__ win, const u16* __restrict__ wsp, const u16* __restrict__ bsp, const u16* __restrict__ wout,
                                                      u16* __restrict__ out, char* __restrict__ ws) {
  extern __shared__ __attribute__((aligned(16))) char shm[];
  Ctx c;
  c.lds = (lds_t)shm; c.wsp = ws; c.lane = threadIdx.x & 63; c.wid = __builtin_amdgcn_readfirstlane(threadIdx.x >> 6); c.b = blockIdx.x;
  c.ws = __builtin_amdgcn_make_buffer_rsrc((void*)ws, 0, WS_BYTES, 0x00020000);
  c.tag = __builtin_amdgcn_readfirstlane(__hip_atomic_load((const unsigned*)(ws + OFF_EPOCH), __ATOMIC_RELAXED, __HIP_MEMORY_SCOPE_AGENT)) + 1u;
  if (threadIdx.x < 16) *(LAS unsigned*)(c.lds + L_CNT + threadIdx.x * 4) = 0u;
  const int xcd = c.b & 7, jj = c.b >> 3;
  const int i1 = 8 * (xcd & 1) + (jj >> 2), j1 = 4 * (xcd >> 1) + (jj & 3);
  const int i2 = 4 * (xcd >> 1) + (jj >> 3), j2 = 8 * (xcd & 1) + (jj & 7);
  const bool act3 = jj < 28;
  const int i3 = 4 * (xcd >> 1) + (act3 ? jj / 7 : 0), j3 = 7 * (xcd & 1) + (act3 ? jj % 7 : 0);
  Args A{x, wqkv, wphi, wo, win, wsp, bsp, wout, out, ws};
  if (c.wid >= 4) loader_main(c, A, i1, j1, i2, j2, i3, j3, act3, LAST_PHASE);
  else { __builtin_amdgcn_s_setprio(1); consumer_main(c, A, i1, j1, i2, j2, i3, j3, act3, LAST_PHASE); }
}

extern "C" void kernel_launch(void* const* d_in, const int* in_sizes, int n_in, void* d_out, int out_size, void* d_ws, size_t ws_size, hipStream_t stream) {
  (void)in_sizes; (void)n_in; (void)out_size; (void)ws_size;
  (void)hipFuncSetAttribute((const void*)fused_fwd, hipFuncAttributeMaxDynamicSharedMemorySize, LDS_BYTES);
  fused_fwd<<<256, 512, LDS_BYTES, stream>>>((const u16*)d_in[0], (const u16*)d_in[1], (const u16*)d_in[2], (const u16*)d_in[3], (const u16*)d_in[5], (const u16*)d_in[6],
                                             (const u16*)d_in[7], (const u16*)d_in[8], (u16*)d_out, (char*)d_ws);
}
```

```cpp
#include <hip/hip_runtime.h>
#include <hip/hip_bf16.h>
#include <stdint.h>
#include <type_traits>

#define LAS __attribute__((address_space(3)))
#define DI __device__ __forceinline__
typedef unsigned short u16;
typedef short bf16x8 __attribute__((ext_vector_type(8)));
typedef short s16x4 __attribute__((ext_vector_type(4)));
typedef float f32x4 __attribute__((ext_vector_type(4)));
typedef float f32x2 __attribute__((ext_vector_type(2)));
typedef unsigned u32x4 __attribute__((ext_vector_type(4)));
typedef unsigned u32x2 __attribute__((ext_vector_type(2)));
typedef __bf16 bf2 __attribute__((ext_vector_type(2)));
typedef LAS char* lds_t;

constexpr unsigned OFF_EPOCH = 0, OFF_ERR = 128, OFF_FLAG = 4096;
constexpr unsigned OFF_KVP = 262144;
constexpr unsigned OFF_ZP = OFF_KVP + 2097152;
constexpr unsigned OFF_ST2 = OFF_ZP + 131072;
constexpr unsigned OFF_STH = OFF_ST2 + 262144;
constexpr unsigned OFF_GX = OFF_STH + 262144;
constexpr unsigned OFF_GC1 = OFF_GX + 65536;
constexpr unsigned OFF_GC3 = OFF_GC1 + 393216;
constexpr unsigned OFF_OF = 4u << 20, OFF_HF = 8u << 20, OFF_Z2 = 12u << 20, OFF_UF = 20u << 20, OFF_TS = 28u << 20;
constexpr unsigned WS_BYTES = 32u << 20;
enum { FG_KV = 0, FG_O = 1, FG_H = 2, FG_ST = 3, FG_Z2 = 4, FG_U = 5 };
constexpr int LDS_BYTES = 163840;
constexpr int SC1 = 122880, L_WPHI = SC1, L_TAB1 = SC1 + 4096, L_TBUF = SC1 + 6144, L_XBUF = SC1 + 22528;
constexpr int SC3 = 147456, L_TAB3 = SC3, L_PST = SC3 + 2048, L_FIN = SC3 + 4096;
constexpr int L_CNT = LDS_BYTES - 64, L_DUMMY = LDS_BYTES - 512;
constexpr int SC_SC1 = 16;
#define LDAUX 16

DI void bar() { asm volatile("" ::: "memory"); __builtin_amdgcn_s_barrier(); asm volatile("" ::: "memory"); }
template <int N> DI void wait_vm() { asm volatile("s_waitcnt vmcnt(%0)" ::"n"(N) : "memory"); }
DI int opaque(int v) { asm volatile("" : "+v"(v)); return v; }
DI void wait_lgkm0() { __builtin_amdgcn_s_waitcnt(0xc07f); }
DI unsigned pk2(float lo, float hi) { f32x2 v = {lo, hi}; return __builtin_bit_cast(unsigned, __builtin_convertvector(v, bf2)); }
DI bf16x8 pk8(const f32x4& a, const f32x4& b) { u32x4 r = {pk2(a[0], a[1]), pk2(a[2], a[3]), pk2(b[0], b[1]), pk2(b[2], b[3])}; return __builtin_bit_cast(bf16x8, r); }
DI float bflo(unsigned w) { return __uint_as_float(w << 16); }
DI float bfhi(unsigned w) { return __uint_as_float(w & 0xffff0000u); }
DI f32x4 mfma(bf16x8 a, bf16x8 b, f32x4 c) { return __builtin_amdgcn_mfma_f32_16x16x32_bf16(a, b, c, 0, 0, 0); }
DI bf16x8 ldsA(lds_t p) { return *(LAS bf16x8*)p; }
DI bf16x8 ldsT(lds_t p, int d) {
  s16x4 lo = __builtin_amdgcn_ds_read_tr16_b64_v4i16((LAS s16x4*)p), hi = __builtin_amdgcn_ds_read_tr16_b64_v4i16((LAS s16x4*)(p + d));
  return __builtin_shufflevector(lo, hi, 0, 1, 2, 3, 4, 5, 6, 7);
}
template <int D1, int D2> DI void tr4_issue(unsigned addr, u32x2 (&r)[4]) {
  asm volatile("ds_read_b64_tr_b16 %0, %4\n\tds_read_b64_tr_b16 %1, %4 offset:%5\n\tds_read_b64_tr_b16 %2, %4 offset:%6\n\tds_read_b64_tr_b16 %3, %4 offset:%7"
               : "=&v"(r[0]), "=&v"(r[1]), "=&v"(r[2]), "=&v"(r[3]) : "v"(addr), "n"(D1), "n"(D2), "n"(D1 + D2) : "memory");
}
DI void tr4_wait(u32x2 (&r)[4]) { asm volatile("s_waitcnt lgkmcnt(0)" : "+v"(r[0]), "+v"(r[1]), "+v"(r[2]), "+v"(r[3])::"memory"); }
DI float sum_g(float x) {
  auto a = __builtin_amdgcn_permlane16_swap(__float_as_uint(x), __float_as_uint(x), false, false);
  x = __uint_as_float(a[0]) + __uint_as_float(a[1]);
  auto b = __builtin_amdgcn_permlane32_swap(__float_as_uint(x), __float_as_uint(x), false, false);
  return __uint_as_float(b[0]) + __uint_as_float(b[1]);
}
DI float max_g(float x) {
  auto a = __builtin_amdgcn_permlane16_swap(__float_as_uint(x), __float_as_uint(x), false, false);
  x = fmaxf(__uint_as_float(a[0]), __uint_as_float(a[1]));
  auto b = __builtin_amdgcn_permlane32_swap(__float_as_uint(x), __float_as_uint(x), false, false);
  return fmaxf(__uint_as_float(b[0]), __uint_as_float(b[1]));
}
template <int CTRL> DI float dpp_add(float x) { return x + __int_as_float(__builtin_amdgcn_update_dpp(0, __float_as_int(x), CTRL, 0xf, 0xf, true)); }
DI float sum_row16(float x) { x = dpp_add<0x128>(x); x = dpp_add<0x124>(x); x = dpp_add<0x122>(x); return dpp_add<0x121>(x); }
DI float dot2(unsigned a, unsigned b, float c) { return __builtin_amdgcn_fdot2_f32_bf16(__builtin_bit_cast(bf2, a), __builtin_bit_cast(bf2, b), c, false); }
constexpr unsigned ONES2 = 0x3f803f80u;
DI float gelu(float x) { const float t = x * fmaf(x * x, -0.1029432397f, -2.302208198f); return x * __builtin_amdgcn_rcpf(1.0f + __builtin_amdgcn_exp2f(t)); }

struct Ctx {
  __amdgpu_buffer_rsrc_t ws;
  char* wsp; lds_t lds; unsigned tag; int lane, wid, b;
};
DI unsigned* flag_ptr(const Ctx& c, int g, int a, int bb) { return (unsigned*)(c.wsp + OFF_FLAG + ((g * 16 + a) * 16 + bb) * 128); }
template <unsigned NWAVES = 4> DI void publish(const Ctx& c, int cnt, int g, int a, int bb) {
  wait_vm<0>();
  if (c.lane == 0) {
    unsigned old = __hip_atomic_fetch_add((LAS unsigned*)(c.lds + L_CNT + cnt * 4), 1u, __ATOMIC_RELAXED, __HIP_MEMORY_SCOPE_WORKGROUP);
    if (old == NWAVES - 1u) __hip_atomic_store(flag_ptr(c, g, a, bb), c.tag, __ATOMIC_RELAXED, __HIP_MEMORY_SCOPE_AGENT);
  }
}
DI void wait_flags(const Ctx& c, int g, int a, int n) {
  const unsigned* p = flag_ptr(c, g, a, c.lane < n ? c.lane : 0);
  const unsigned long long t0 = __builtin_amdgcn_s_memrealtime();
  for (;;) {
    const unsigned v = __hip_atomic_load(p, __ATOMIC_RELAXED, __HIP_MEMORY_SCOPE_AGENT);
    if (__all(v == c.tag)) break;
    if (__builtin_amdgcn_s_memrealtime() - t0 > 300000ull) { if (c.lane == 0) __hip_atomic_store((unsigned*)(c.wsp + OFF_ERR), 0x100u + (unsigned)g, __ATOMIC_RELAXED, __HIP_MEMORY_SCOPE_AGENT); break; }
    __builtin_amdgcn_s_sleep(1);
  }
  asm volatile("" ::: "memory");
}
#define STEP(t) do {} while (0)
#define TS(k) do {} while (0)

DI void st16(const Ctx& c, bf16x8 v, int off) { __builtin_amdgcn_raw_buffer_store_b128(__builtin_bit_cast(u32x4, v), c.ws, off, 0, SC_SC1); }
DI void dma_asm(__amdgpu_buffer_rsrc_t r, lds_t dst, int voff, int soff) { unsigned keep;
  asm volatile("s_mov_b32 %0, m0\n\ts_mov_b32 m0, %1\n\ts_nop 0\n\tbuffer_load_dwordx4 %2, %3, %4 offen lds\n\ts_mov_b32 m0, %0"
               : "=&s"(keep) : "s"((unsigned)(size_t)dst), "v"(voff), "s"(r), "s"(soff) : "memory"); }
template <int AUX> DI void dma(__amdgpu_buffer_rsrc_t r, lds_t dst, int voff, int soff) { __builtin_amdgcn_raw_ptr_buffer_load_lds(r, (LAS void*)dst, 16, voff, soff, 0, AUX); }

#define EXP 0
template <int S, int NW, class IA, class IW, class MID, class POST>
DI void loader_loop(int nt, bool gated_prologue, IA issueA, IW issueW, MID mid, POST post) {
  constexpr int PPW = 4 + NW;
  if (gated_prologue) wait_vm<4 * (S - 2)>(); else wait_vm<PPW * (S - 2)>();
  bar();
  int t = 0;
#pragma unroll
  for (int p = 0; p < S - 2; ++p, ++t) {
    mid(t); issueA(t + S - 1); issueW(t + S - 1); post(t);
    if (gated_prologue) {
      if (p == 0) wait_vm<4 * (S - 3 > 0 ? S - 3 : 0) + PPW * 1>();
      else if (p == 1) wait_vm<4 * (S - 4 > 0 ? S - 4 : 0) + PPW * 2>();
      else wait_vm<4 * (S - 5 > 0 ? S - 5 : 0) + PPW * 3>();
    } else wait_vm<PPW * (S - 2)>();
    bar();
  }
#pragma unroll 1
  for (; t + S - 1 < nt; ++t) { mid(t); if (!(EXP & 1)) { issueA(t + S - 1); issueW(t + S - 1); } wait_vm<PPW * (S - 2)>(); bar(); }
#pragma unroll
  for (int r = S - 2; r >= 0; --r, ++t) {
    if (r >= 4) wait_vm<PPW * 3>(); else if (r == 3) wait_vm<PPW * 2>(); else if (r == 2) wait_vm<PPW * 1>(); else wait_vm<0>();
    bar();
  }
}
template <int NW, class IA, class IW, class POST, class GA, class FIN>
DI void loader_loop_p1(int lw, IA issueA, IW issueW, POST post, GA gather, FIN fin) {
  constexpr int PPW = 4 + NW;
  wait_vm<PPW>(); bar();
  issueA(2); issueW(2); post(); wait_vm<PPW>(); bar();
#pragma unroll 1
  for (int t = 1; t < 5; ++t) { if (!(EXP & 1)) { issueA(t + 2); issueW(t + 2); } wait_vm<PPW>(); bar(); }
#pragma unroll
  for (int t = 5; t < 13; ++t) {
    issueA(t + 2); issueW(t + 2); gather(t - 5);
    if (t == 5) wait_vm<PPW + 2>();
    else if (lw < 3) wait_vm<PPW + 4>();
    else if (t == 6) wait_vm<PPW + 2>();
    else wait_vm<PPW>();
    bar();
  }
  issueA(15); issueW(15); if (lw < 3) wait_vm<PPW + 2>(); else wait_vm<PPW>(); bar();
  wait_vm<0>(); bar();
  fin(); bar();
}
template <int S, int SB, class RD0, class RD1, class MM0, class MM1, class SCH, class HK, class ST>
DI void consumer_loop(const Ctx& c, int sbase, int nt, RD0 rd0, RD1 rd1, MM0 mm0, MM1 mm1, SCH sched, HK hook, ST stamp) {
  bar();
  stamp();
  rd0(0);
  int sb = 0;
#pragma unroll 1
  for (int t = 0; t < nt; ++t) {
    hook(t); STEP(t);
    __builtin_amdgcn_sched_barrier(0);
    if (!(EXP & 4)) rd1(sb); if (!(EXP & 2)) mm0(); sched();
    __builtin_amdgcn_sched_barrier(0);
    wait_lgkm0(); bar();
    __builtin_amdgcn_sched_barrier(0);
    sb = (sb == (S - 1) * SB) ? 0 : sb + SB;
    if (!(EXP & 4)) rd0(sb); if (!(EXP & 2)) mm1(); sched();
    __builtin_amdgcn_sched_barrier(0);
  }
}
#define TOUCH(name, mod) DI void name(lds_t lds, const void* p) { unsigned keep; const unsigned dst = (unsigned)(size_t)(lds + L_DUMMY); \
  asm volatile("s_mov_b32 %0, m0\n\ts_mov_b32 m0, %2\n\ts_nop 0\n\tglobal_load_lds_dword %1, off" mod "\n\ts_mov_b32 m0, %0" : "=&s"(keep) : "v"(p), "s"(dst) : "memory"); }
TOUCH(touch, "") TOUCH(touch_nt, " nt") TOUCH(touch_sc1, " sc1")
template <int NM, int NR> DI void interleave() {
#pragma unroll
  for (int i = 0; i < NR; ++i) { __builtin_amdgcn_sched_group_barrier(0x008, 1, 0); __builtin_amdgcn_sched_group_barrier(0x100, 1, 0); }
  __builtin_amdgcn_sched_group_barrier(0x008, NM - NR, 0);
}
template <int NM, int NR> DI void interleave2() {
#pragma unroll
  for (int i = 0; i < NR / 2; ++i) { __builtin_amdgcn_sched_group_barrier(0x008, 1, 0); __builtin_amdgcn_sched_group_barrier(0x100, 2, 0); }
  __builtin_amdgcn_sched_group_barrier(0x008, NM - NR / 2, 0);
}

DI int rowmajor_voff(int lw, int lane, int pitch) {
  const int rr = lane >> 3, slot = 8 * (lw & 1) + rr;
  const int row = 8 * (slot >> 2) + 4 * (lw >> 1) + (slot & 3);
  return row * pitch + (((lane & 7) ^ (slot >> 1)) << 4);
}
DI int true_row(int mt, int i) { return 32 * (mt >> 1) + 8 * (i >> 2) + 4 * (mt & 1) + (i & 3); }

DI void acc_row(float& s, float& q, u32x4 w) {
#pragma unroll
  for (int i = 0; i < 4; ++i) { s = dot2(w[i], ONES2, s); q = dot2(w[i], w[i], q); } }
DI float col_sum(const u32x2 (&r)[4]) { float s0 = 0.f, s1 = 0.f;
#pragma unroll
  for (int i = 0; i < 4; ++i) { s0 = dot2(r[i][0], ONES2, s0); s1 = dot2(r[i][1], ONES2, s1); }
  return sum_g(s0 + s1); }
DI void st_granule(const Ctx& c, float v, int off) { __builtin_amdgcn_raw_buffer_store_b64((u32x2){__float_as_uint(v), c.tag}, c.ws, off, 0, SC_SC1); }
DI bool timed_out(const Ctx& c, unsigned long long t0, unsigned code) {
  if (__builtin_amdgcn_s_memrealtime() - t0 <= 300000ull) return false;
  if (c.lane == 0) __hip_atomic_store((unsigned*)(c.wsp + OFF_ERR), code, __ATOMIC_RELAXED, __HIP_MEMORY_SCOPE_AGENT);
  return true;
}
DI float sum_granules16(const Ctx& c, u32x2 (&v)[16], int off, int stride) {
  const unsigned long long t0 = __builtin_amdgcn_s_memrealtime();
  for (;;) {
    bool ok = true;
#pragma unroll
    for (int k = 0; k < 16; ++k) ok &= v[k][1] == c.tag;
    if (__all(ok) || timed_out(c, t0, 0x200u)) break;
    __builtin_amdgcn_s_sleep(2);
#pragma unroll
    for (int k = 0; k < 16; ++k) v[k] = __builtin_amdgcn_raw_buffer_load_b64(c.ws, off + k * stride, 0, SC_SC1);
  }
  float s = 0.f;
#pragma unroll
  for (int k = 0; k < 16; ++k) s += __uint_as_float(v[k][0]);
  return s;
}

#define ROTM 0
#define ROT2(j) (ROTM * 2 * ((j) & 7))
#define ROT5(j) (ROTM * ((7 * ((j) & 7)) >> 1))
#define P4D 0
#define TOUCHW touch
#define PF_T0 2
#define PF_DT 3

struct Args { const u16 *x, *wqkv, *wphi, *wo, *win, *wsp, *bsp, *wout; u16* out; char* ws; };

DI void get_tag(Ctx& c) { unsigned e; asm volatile("s_load_dword %0, %1, 0x0\n\ts_waitcnt lgkmcnt(0)" : "=&s"(e) : "s"(c.wsp) : "memory"); c.tag = e + 1u; }
DI void kv_gather(const Ctx& c, int j1) {
  const int lane = c.lane, g = lane >> 4, i16 = lane & 15, tid = c.wid * 64 + lane;
  f32x4 a0 = {0, 0, 0, 0}, a1 = {0, 0, 0, 0};
  u32x4 v[16];
#pragma unroll
  for (int k = 0; k < 16; ++k) v[k] = __builtin_amdgcn_raw_buffer_load_b128(c.ws, tid * 16, OFF_KVP + (j1 * 16 + k) * 8192, SC_SC1);
  float zs = 0.f;
  if (c.wid < 2) { unsigned zv[16];
#pragma unroll
    for (int k = 0; k < 16; ++k) zv[k] = __builtin_amdgcn_raw_buffer_load_b32(c.ws, lane * 4, OFF_ZP + ((j1 * 16 + k) * 2 + c.wid) * 256, SC_SC1);
#pragma unroll
    for (int k = 0; k < 16; ++k) zs += __uint_as_float(zv[k]); }
#pragma unroll
  for (int k = 0; k < 16; ++k) { a0[0] += bflo(v[k][0]); a0[1] += bfhi(v[k][0]); a0[2] += bflo(v[k][1]); a0[3] += bfhi(v[k][1]); a1[0] += bflo(v[k][2]); a1[1] += bfhi(v[k][2]); a1[2] += bflo(v[k][3]); a1[3] += bfhi(v[k][3]); }
  const int w = c.wid, hd = w >> 2, dt = (w >> 1) & 1, fp = w & 1;
  const lds_t tb = c.lds + L_XBUF + hd * 8192 + (32 * fp + i16) * 128 + (16 * dt + 4 * g) * 2;
  *(LAS u32x2*)tb = (u32x2){pk2(a0[0], a0[1]), pk2(a0[2], a0[3])};
  *(LAS u32x2*)(tb + 16 * 128) = (u32x2){pk2(a1[0], a1[1]), pk2(a1[2], a1[3])};
  if (c.wid < 2) { const unsigned z2 = pk2(zs, zs); const lds_t t3 = c.lds + L_XBUF + c.wid * 8192 + lane * 128 + 64;
    *(LAS u32x4*)t3 = (u32x4){z2, z2, z2, z2}; *(LAS u32x4*)(t3 + 16) = (u32x4){z2, z2, z2, z2}; }
  wait_lgkm0();
}
DI void loader_p1(Ctx& c, const Args& A, int i1, int j1, int i2, int j2, int i3, int j3, bool act3, int last) {
  const int lane = c.lane, lw = c.wid - 4, g = lane >> 4, q = (lane & 15) >> 2, p = lane & 3, i16 = lane & 15;
  const lds_t lds = c.lds;
  const __amdgpu_buffer_rsrc_t rWS = c.ws;
  {
    constexpr int S = 3, SB = 40960, NW = 6;
    const __amdgpu_buffer_rsrc_t rA = __builtin_amdgcn_make_buffer_rsrc((void*)(A.x + (size_t)i1 * 128 * 1024), 0, 128 * 2048, 0x00020000);
    const __amdgpu_buffer_rsrc_t rW = __builtin_amdgcn_make_buffer_rsrc((void*)A.wqkv, 0, 1024 * 6144, 0x00020000);
    const __amdgpu_buffer_rsrc_t rP = __builtin_amdgcn_make_buffer_rsrc((void*)A.wphi, 0, 65536, 0x00020000);
    const int voA = rowmajor_voff(lw, lane, 2048);
    int voW[NW];
#pragma unroll
    for (int i = 0; i < NW; ++i) { const int o = (NW * lw + i) * 1024 + lane * 16, r = o / 384, rem = o % 384, seg = rem >> 7, s16 = (rem & 127) >> 4;
      const int c32 = (s16 >> 1) ^ ((r >> 1) & 3), r5 = r & 31, k = (r & 32) + 8 * ((r5 >> 2) & 3) + 4 * (r5 >> 4) + (r5 & 3);
      voW[i] = k * 6144 + seg * 2048 + j1 * 128 + c32 * 32 + (s16 & 1) * 16; }
    const int rot = (2 * (i1 & 7) + (i1 >> 3) + 4 * (j1 & 3)) & 15;
    auto issueA = [&](int t) { const lds_t d = lds + (t % S) * SB + lw * 1024; const int so = ((t + rot) & 15) * 128;
#pragma unroll
      for (int i = 0; i < 4; ++i) dma<0>(rA, d + i * 4096, voA, so + i * 32 * 2048); };
    auto issueW = [&](int t) { const lds_t d = lds + (t % S) * SB + 16384 + lw * NW * 1024; const int so = ((t + rot) & 15) * 64 * 6144;
#pragma unroll
      for (int i = 0; i < NW; ++i) dma<0>(rW, d + i * 1024, voW[i], so); };
    issueA(0); issueW(0); issueA(1); issueW(1);
    dma<0>(rP, lds + L_WPHI + lw * 1024, lane * 16 + lw * 1024, j1 * 4096);
    get_tag(c);
    TS(0);
    u32x2 gv[16]; u32x4 gx[2];
    const int goff = OFF_GC1 + j1 * 16 * 1536 + (lw * 64 + lane) * 8, xoff = OFF_GX + (i1 * 128 + lane) * 16;
    auto post = [&]() {
      const int sw = (2 * g + (q >> 1)) & 3; u32x2 cr[3][4];
#pragma unroll
      for (int n = 0; n < 3; ++n) { const int nu = 3 * lw + n;
        tr4_issue<16 * 384, 32 * 384>((unsigned)(size_t)(lds + 16384 + (4 * g + q) * 384 + 8 * p + (nu >> 2) * 128 + 32 * ((nu & 3) ^ sw)), cr[n]); }
#pragma unroll
      for (int n = 0; n < 3; ++n) { tr4_wait(cr[n]); const float s = col_sum(cr[n]);
        if (lane < 16) st_granule(c, s, OFF_GC1 + (j1 * 16 + rot) * 1536 + ((3 * lw + n) * 16 + lane) * 8); }
    };
    auto gather = [&](int k) {
      if (lw < 3) { gv[2 * k] = __builtin_amdgcn_raw_buffer_load_b64(rWS, goff + 2 * k * 1536, 0, SC_SC1); gv[2 * k + 1] = __builtin_amdgcn_raw_buffer_load_b64(rWS, goff + (2 * k + 1) * 1536, 0, SC_SC1); }
      else if (k == 0) { gx[0] = __builtin_amdgcn_raw_buffer_load_b128(rWS, xoff, 0, SC_SC1); gx[1] = __builtin_amdgcn_raw_buffer_load_b128(rWS, xoff + 1024, 0, SC_SC1); }
    };
    auto fin = [&]() {
      if (lw < 3) *(LAS float*)(lds + L_TAB1 + 1024 + (lw * 64 + lane) * 4) = sum_granules16(c, gv, goff, 1536);
      else {
        const unsigned long long t0 = __builtin_amdgcn_s_memrealtime();
        for (;;) {
          if (__all(gx[0][1] == c.tag && gx[0][3] == c.tag && gx[1][1] == c.tag && gx[1][3] == c.tag) || timed_out(c, t0, 0x201u)) break;
          __builtin_amdgcn_s_sleep(2);
          gx[0] = __builtin_amdgcn_raw_buffer_load_b128(rWS, xoff, 0, SC_SC1); gx[1] = __builtin_amdgcn_raw_buffer_load_b128(rWS, xoff + 1024, 0, SC_SC1);
        }
#pragma unroll
        for (int e = 0; e < 2; ++e) { *(LAS unsigned*)(lds + L_TAB1 + (lane + 64 * e) * 4) = gx[e][0]; *(LAS unsigned*)(lds + L_TAB1 + 512 + (lane + 64 * e) * 4) = gx[e][2]; }
      }
      wait_lgkm0();
    };
    loader_loop_p1<NW>(lw, issueA, issueW, post, gather, fin);
    TS(1);
  }
  int voW2[2];
#pragma unroll
  for (int i = 0; i < 2; ++i) { const int r = (2 * lw + i) * 8 + (lane >> 3), s16 = lane & 7; voW2[i] = r * 2048 + j2 * 128 + (((s16 >> 1) ^ ((r >> 1) & 3)) << 5) + (s16 & 1) * 16; }
  {
    const __amdgpu_buffer_rsrc_t rW = __builtin_amdgcn_make_buffer_rsrc((void*)A.wo, 0, 1024 * 2048, 0x00020000);
    auto issueW = [&](int t) {
#pragma unroll
      for (int i = 0; i < 2; ++i) dma<0>(rW, lds + t * 8192 + (lw * 2 + i) * 1024, voW2[i], t * 64 * 2048); };
    if (act3) {
      const __amdgpu_buffer_rsrc_t rW3 = __builtin_amdgcn_make_buffer_rsrc((void*)A.win, 0, 1024 * 7168, 0x00020000);
#pragma unroll
      for (int i = 0; i < 8; ++i) { const int r = (8 * lw + i) * 2 + (lane >> 5), l5 = lane & 31, seg = l5 >> 4, s16 = l5 & 15;
        dma<0>(rW3, lds + (8 * lw + i) * 1024, r * 7168 + (seg == 0 ? 3584 : 0) + j3 * 256 + (((s16 >> 1) ^ (r & 7)) << 5) + (s16 & 1) * 16, i3 * 64 * 7168); }
    }
    bar();
    bar();
#pragma unroll
    for (int t = 4; t < 15; ++t) issueW(t);
    if (lw == 0) wait_flags(c, FG_KV, j1, 16);
    TS(2);
    wait_vm<22>();
    bar();
    issueW(15);
    kv_gather(c, j1);
    bar();
    if (act3) {
      u32x2 cr[4][4];
#pragma unroll
      for (int n = 0; n < 4; ++n) tr4_issue<16 * 512, 32 * 512>((unsigned)(size_t)(lds + (4 * g + q) * 512 + 8 * p + (lw >> 1) * 256 + 32 * ((4 * (lw & 1) + n) ^ (4 * (g & 1) + q))), cr[n]);
#pragma unroll
      for (int n = 0; n < 4; ++n) { tr4_wait(cr[n]); const float s = col_sum(cr[n]);
        if (lane < 16) st_granule(c, s, OFF_GC3 + (j3 * 16 + i3) * 2048 + ((4 * lw + n) * 16 + lane) * 8); }
    }
    bar();
#pragma unroll
    for (int t = 0; t < 4; ++t) issueW(t);
    if (last < 2) return;
    if (lw == 0) wait_flags(c, FG_O, i2, 16);
    wait_vm<0>();
    TS(3);
  }
}

DI void loader_p34(Ctx& c, const Args& A, int i3, int j3, int last) {
  const int lane = opaque(c.lane), lw = c.wid - 4, g = lane >> 4, q = (lane & 15) >> 2, p = lane & 3;
  const lds_t lds = c.lds;
  const __amdgpu_buffer_rsrc_t rWS = c.ws;
  const int voF = lane * 16 + lw * 1024;
  {
    {
      constexpr int S = 3, SB = 49152, NW = 8;
      const __amdgpu_buffer_rsrc_t rW = __builtin_amdgcn_make_buffer_rsrc((void*)A.win, 0, 1024 * 7168, 0x00020000);
      int voW[NW];
#pragma unroll
      for (int i = 0; i < NW; ++i) { const int r = (NW * lw + i) * 2 + (lane >> 5), l5 = lane & 31, seg = l5 >> 4, s16 = l5 & 15;
        voW[i] = r * 7168 + (seg == 0 ? 3584 : 0) + j3 * 256 + (((s16 >> 1) ^ (r & 7)) << 5) + (s16 & 1) * 16; }
      auto issueA = [&](int t) { const lds_t d = lds + (t % S) * SB + lw * 1024; const int so = OFF_HF + (i3 * 16 + t) * 16384;
#pragma unroll
        for (int i = 0; i < 4; ++i) dma<LDAUX>(rWS, d + i * 4096, voF, so + i * 4096); };
      auto issueW = [&](int t) { const lds_t d = lds + (t % S) * SB + 16384 + lw * NW * 1024;
#pragma unroll
        for (int i = 0; i < NW; ++i) dma<0>(rW, d + i * 1024, voW[i], t * 64 * 7168); };
      issueW(0); issueW(1);
      {
        u32x2 gv[16]; const int goff = OFF_GC3 + j3 * 16 * 2048 + (lw * 64 + lane) * 8;
#pragma unroll
        for (int k = 0; k < 16; ++k) gv[k] = __builtin_amdgcn_raw_buffer_load_b64(rWS, goff + k * 2048, 0, SC_SC1);
        *(LAS float*)(lds + L_TAB3 + 1024 + (lw * 64 + lane) * 4) = sum_granules16(c, gv, goff, 2048);
        wait_lgkm0();
      }
      if (lw == 0) wait_flags(c, FG_H, i3, 16);
      TS(5);
      bar();
      issueA(0); issueA(1);
      loader_loop<S, NW>(16, true, issueA, issueW, [](int) {}, [](int) {});
      TS(6);
    }
    {
      constexpr int S = 4, SB = 32768, NW = 4;
      const __amdgpu_buffer_rsrc_t rA = __builtin_amdgcn_make_buffer_rsrc((void*)(A.wsp + (size_t)i3 * 128 * 2048), 0, 128 * 4096, 0x00020000);
      const int voA = rowmajor_voff(lw, lane, 4096);
      auto issueW = [&](int t) { const lds_t d = lds + (t % S) * SB + lw * 1024;
#pragma unroll
        for (int i = 0; i < 4; ++i) dma<0>(rA, d + i * 4096, voA, t * 128 + i * 32 * 4096); };
      auto issueA = [&](int t) { const lds_t d = lds + (t % S) * SB + 16384 + lw * 1024; const int so = OFF_Z2 + (j3 * 32 + t) * 16384;
#pragma unroll
        for (int i = 0; i < 4; ++i) dma<LDAUX>(rWS, d + i * 4096, voF, so + i * 4096); };
#pragma unroll
      for (int s = 0; s < S - 1; ++s) issueW(s);
      bar();
      if (lw == 0) wait_flags(c, FG_ST, i3, 14);
      TS(7);
      bar();
      if (lw < 2) {
        const int R = lw * 64 + lane; float s = 0.f, qq = 0.f; u32x2 v[14];
#pragma unroll
        for (int k = 0; k < 14; ++k) v[k] = __builtin_amdgcn_raw_buffer_load_b64(rWS, R * 8, OFF_ST2 + (i3 * 14 + k) * 1024, SC_SC1);
#pragma unroll
        for (int k = 0; k < 14; ++k) { s += __uint_as_float(v[k][0]); qq += __uint_as_float(v[k][1]); }
        s *= (1.0f / 1792.0f); qq *= (1.0f / 1792.0f);
        *(LAS float*)(lds + L_FIN + R * 4) = s; *(LAS float*)(lds + L_FIN + 512 + R * 4) = __builtin_amdgcn_rsqf(fmaxf(qq - s * s, 0.f) + 1e-5f);
        wait_lgkm0();
      }
      bar();
      if (last < 4) return;
      if (lw == 0) wait_flags(c, FG_Z2, j3, 16);
      TS(8);
      bar();
      if (c.b == 0 && lw == 0 && lane == 0) __hip_atomic_store((unsigned*)(c.wsp + OFF_EPOCH), c.tag, __ATOMIC_RELAXED, __HIP_MEMORY_SCOPE_AGENT);
#pragma unroll
      for (int s = 0; s < S - 1; ++s) issueA(s);
      loader_loop<S, NW>(32, true, issueA, issueW, [](int) {}, [](int) {});
      TS(9);
      bar();
    }
    }
}

DI void loader_p5(Ctx& c, const Args& A, int i2, int j2) {
  const int lane = opaque(c.lane), lw = c.wid - 4;
  const __amdgpu_buffer_rsrc_t rW = __builtin_amdgcn_make_buffer_rsrc((void*)A.wout, 0, 1792 * 2048, 0x00020000);
  int vo[2];
#pragma unroll
  for (int i = 0; i < 2; ++i) { const int r = (2 * lw + i) * 8 + (lane >> 3), s16 = lane & 7; vo[i] = r * 2048 + j2 * 128 + (((s16 >> 1) ^ ((r >> 1) & 3)) << 5) + (s16 & 1) * 16; }
#pragma unroll
  for (int t = 0; t < 18; ++t)
#pragma unroll
    for (int i = 0; i < 2; ++i) dma<0>(rW, c.lds + t * 8192 + (lw * 2 + i) * 1024, vo[i], (t < 9 ? t : t + 5) * 64 * 2048);
  if (lw == 0) wait_flags(c, FG_U, i2, 14);
  wait_vm<0>();
  TS(10);
}

DI void feat(const f32x4& p0, const f32x4& p1, f32x4& e0, f32x4& e1, f32x4& n0, f32x4& n1) {
  float mx = fmaxf(fmaxf(fmaxf(p0[0], p0[1]), fmaxf(p0[2], p0[3])), fmaxf(fmaxf(p1[0], p1[1]), fmaxf(p1[2], p1[3])));
  float mn = fminf(fminf(fminf(p0[0], p0[1]), fminf(p0[2], p0[3])), fminf(fminf(p1[0], p1[1]), fminf(p1[2], p1[3])));
  mx = max_g(mx); mn = -max_g(-mn);
  constexpr float L2E = 1.4426950408889634f;
  const float bx = -mx * L2E, bn = mn * L2E;
  float sp = 0.f, sn = 0.f;
#pragma unroll
  for (int r = 0; r < 4; ++r) {
    e0[r] = __builtin_amdgcn_exp2f(fmaf(p0[r], L2E, bx)); e1[r] = __builtin_amdgcn_exp2f(fmaf(p1[r], L2E, bx));
    n0[r] = __builtin_amdgcn_exp2f(fmaf(p0[r], -L2E, bn)); n1[r] = __builtin_amdgcn_exp2f(fmaf(p1[r], -L2E, bn));
    sp += e0[r] + e1[r]; sn += n0[r] + n1[r];
  }
  sp = __builtin_amdgcn_rcpf(sum_g(sp)); sn = __builtin_amdgcn_rcpf(sum_g(sn));
  e0 *= sp; e1 *= sp; n0 *= sn; n1 *= sn;
}

DI void consumer_p1(Ctx& c, const Args& A, int i1, int j1) {
  const int lane = c.lane, wr = c.wid >> 1, wc = c.wid & 1, g = lane >> 4, q = (lane & 15) >> 2, p = lane & 3, i16 = lane & 15;
  const lds_t lds = c.lds;
  const __amdgpu_buffer_rsrc_t rWS = c.ws;
  const f32x4 Z4 = {0.f, 0.f, 0.f, 0.f};
  {
    constexpr int S = 3, SB = 40960;
    f32x4 aq[4][2], ak[4][2], av[4][2];
#pragma unroll
    for (int m = 0; m < 4; ++m)
#pragma unroll
      for (int n = 0; n < 2; ++n) { aq[m][n] = Z4; ak[m][n] = Z4; av[m][n] = Z4; }
    const int sw = (2 * g + (q >> 1)) & 3;
    const int ao0 = (wr * 64 + i16) * 128 + ((g ^ (i16 >> 1)) << 4), ao1 = ao0 ^ 64;
    const int wl = 16384 + (4 * g + q) * 384 + 8 * p;
    const int wo0 = wl + 32 * ((2 * wc) ^ sw), wo1 = wl + 32 * ((2 * wc + 1) ^ sw);
    bf16x8 a0[4], w0[6], a1[4], w1[6];
    auto rd = [&](bf16x8(&a)[4], bf16x8(&w)[6], int sb, int ks) { const lds_t s = lds + sb;
#pragma unroll
      for (int sg = 0; sg < 3; ++sg) { w[2 * sg] = ldsT(s + wo0 + sg * 128 + ks * 32 * 384, 16 * 384); w[2 * sg + 1] = ldsT(s + wo1 + sg * 128 + ks * 32 * 384, 16 * 384); }
#pragma unroll
      for (int m = 0; m < 4; ++m) a[m] = ldsA(s + (ks ? ao1 : ao0) + m * 2048); };
    auto mm = [&](bf16x8(&a)[4], bf16x8(&w)[6]) {
#pragma unroll
      for (int n = 0; n < 2; ++n)
#pragma unroll
        for (int m = 0; m < 4; ++m) aq[m][n] = mfma(w[n], a[m], aq[m][n]);
#pragma unroll
      for (int n = 0; n < 2; ++n)
#pragma unroll
        for (int m = 0; m < 4; ++m) ak[m][n] = mfma(w[2 + n], a[m], ak[m][n]);
#pragma unroll
      for (int n = 0; n < 2; ++n)
#pragma unroll
        for (int m = 0; m < 4; ++m) av[m][n] = mfma(a[m], w[4 + n], av[m][n]); };
    TS(0);
    {
      u32x4 xv[2][2];
#pragma unroll
      for (int e = 0; e < 2; ++e) { const int R = 8 * j1 + 2 * c.wid + e; const u16* xr = A.x + (size_t)(i1 * 128 + true_row(R >> 4, R & 15)) * 1024 + lane * 16;
        xv[e][0] = *(const u32x4*)xr; xv[e][1] = *(const u32x4*)(xr + 8); }
      get_tag(c);
#pragma unroll
      for (int e = 0; e < 2; ++e) { float s = 0.f, qq = 0.f; acc_row(s, qq, xv[e][0]); acc_row(s, qq, xv[e][1]);
        s = sum_g(sum_row16(s)) * (1.0f / 1024.0f); qq = sum_g(sum_row16(qq)) * (1.0f / 1024.0f);
        const float rs = __builtin_amdgcn_rsqf(fmaxf(qq - s * s, 0.f) + 1e-5f);
        if (lane == 0) __builtin_amdgcn_raw_buffer_store_b128((u32x4){__float_as_uint(s), c.tag, __float_as_uint(rs), c.tag}, rWS, OFF_GX + (i1 * 128 + 8 * j1 + 2 * c.wid + e) * 16, 0, SC_SC1); }
    }
    auto hook = [](int) {};
    consumer_loop<S, SB>(c, 0, 16, [&](int sb) { rd(a0, w0, sb, 0); }, [&](int sb) { rd(a1, w1, sb, 1); }, [&]() { mm(a0, w0); }, [&]() { mm(a1, w1); }, []() { interleave<24, 16>(); }, hook, [&]() { TS(16); });
    TS(1);
    const lds_t tab = lds + L_TAB1;
    bf16x8 qf[4], kf[4], vf[2][2];
    {
      f32x4 cq[2], ck[2]; float cv[2];
#pragma unroll
      for (int n = 0; n < 2; ++n) { cq[n] = *(LAS f32x4*)(tab + 1024 + (32 * wc + 16 * n + 4 * g) * 4); ck[n] = *(LAS f32x4*)(tab + 1024 + (64 + 32 * wc + 16 * n + 4 * g) * 4); cv[n] = *(LAS float*)(tab + 1024 + (128 + 32 * wc + 16 * n + i16) * 4); }
      f32x4 vv[4][2];
#pragma unroll
      for (int m = 0; m < 4; ++m) {
        const int R = 64 * wr + 16 * m;
        const float mu = *(LAS float*)(tab + (R + i16) * 4), rs = *(LAS float*)(tab + 512 + (R + i16) * 4);
        qf[m] = pk8((aq[m][0] - mu * cq[0]) * rs, (aq[m][1] - mu * cq[1]) * rs);
        kf[m] = pk8((ak[m][0] - mu * ck[0]) * rs, (ak[m][1] - mu * ck[1]) * rs);
        const f32x4 mu4 = *(LAS f32x4*)(tab + (R + 4 * g) * 4), rs4 = *(LAS f32x4*)(tab + 512 + (R + 4 * g) * 4);
        vv[m][0] = (av[m][0] - mu4 * cv[0]) * rs4; vv[m][1] = (av[m][1] - mu4 * cv[1]) * rs4;
      }
#pragma unroll
      for (int u = 0; u < 2; ++u) { vf[u][0] = pk8(vv[2 * u][0], vv[2 * u + 1][0]); vf[u][1] = pk8(vv[2 * u][1], vv[2 * u + 1][1]); }
    }
    bf16x8 wT[2];
#pragma unroll
    for (int ft = 0; ft < 2; ++ft) wT[ft] = ldsT(lds + L_WPHI + wc * 2048 + (4 * g + q) * 64 + ft * 32 + 8 * p, 16 * 64);
    f32x4 kv[2][4], zz[4];
#pragma unroll
    for (int f = 0; f < 4; ++f) { kv[0][f] = Z4; kv[1][f] = Z4; zz[f] = Z4; }
    const lds_t tb = lds + L_TBUF + c.wid * 4096;
    const bf16x8 ones = __builtin_bit_cast(bf16x8, (u32x4){ONES2, ONES2, ONES2, ONES2});
#pragma unroll
    for (int u = 0; u < 2; ++u) {
#pragma unroll
      for (int e = 0; e < 2; ++e) {
        const f32x4 p0 = mfma(wT[0], kf[2 * u + e], Z4), p1 = mfma(wT[1], kf[2 * u + e], Z4);
        f32x4 e0, e1, n0, n1; feat(p0, p1, e0, e1, n0, n1);
        const lds_t rw = tb + (16 * e + i16) * 128;
        *(LAS u32x2*)(rw + 8 * ((0 + g) ^ i16)) = (u32x2){pk2(e0[0], e0[1]), pk2(e0[2], e0[3])};
        *(LAS u32x2*)(rw + 8 * ((4 + g) ^ i16)) = (u32x2){pk2(e1[0], e1[1]), pk2(e1[2], e1[3])};
        *(LAS u32x2*)(rw + 8 * ((8 + g) ^ i16)) = (u32x2){pk2(n0[0], n0[1]), pk2(n0[2], n0[3])};
        *(LAS u32x2*)(rw + 8 * ((12 + g) ^ i16)) = (u32x2){pk2(n1[0], n1[1]), pk2(n1[2], n1[3])};
      }
      asm volatile("s_waitcnt lgkmcnt(0)" ::: "memory");
#pragma unroll
      for (int f = 0; f < 4; ++f) {
        const int r0 = 4 * g + q;
        const s16x4 lo = __builtin_amdgcn_ds_read_tr16_b64_v4i16((LAS s16x4*)(tb + r0 * 128 + 8 * ((4 * f + p) ^ r0)));
        const s16x4 hi = __builtin_amdgcn_ds_read_tr16_b64_v4i16((LAS s16x4*)(tb + (16 + r0) * 128 + 8 * ((4 * f + p) ^ r0)));
        const bf16x8 pf = __builtin_shufflevector(lo, hi, 0, 1, 2, 3, 4, 5, 6, 7);
        kv[0][f] = mfma(vf[u][0], pf, kv[0][f]); kv[1][f] = mfma(vf[u][1], pf, kv[1][f]); zz[f] = mfma(ones, pf, zz[f]);
      }
      asm volatile("s_waitcnt lgkmcnt(0)" ::: "memory");
    }
    {
      const lds_t xb = lds + L_XBUF + c.wid * 4096 + lane * 16;
#pragma unroll
      for (int f = 0; f < 4; ++f) *(LAS f32x4*)(xb + f * 1024) = wr ? kv[0][f] : kv[1][f];
      const float zsel = g == 0 ? zz[0][0] : g == 1 ? zz[1][0] : g == 2 ? zz[2][0] : zz[3][0];
      if (wr == 1) *(LAS float*)(tb + lane * 4) = zsel;
      wait_lgkm0(); bar();
      const lds_t xp = lds + L_XBUF + (c.wid ^ 2) * 4096 + lane * 16;
      f32x4 k4[4];
#pragma unroll
      for (int f = 0; f < 4; ++f) k4[f] = (wr ? kv[1][f] : kv[0][f]) + *(LAS f32x4*)(xp + f * 1024);
      const int so = OFF_KVP + (j1 * 16 + i1) * 8192 + wc * 4096 + wr * 2048;
      st16(c, pk8(k4[0], k4[1]), lane * 16 + (so));
      st16(c, pk8(k4[2], k4[3]), lane * 16 + (so + 1024));
      if (wr == 0) { const float zt = zsel + *(LAS float*)(lds + L_TBUF + (c.wid ^ 2) * 4096 + lane * 4);
        __builtin_amdgcn_raw_buffer_store_b32(__float_as_uint(zt), rWS, lane * 4, OFF_ZP + ((j1 * 16 + i1) * 2 + wc) * 256, SC_SC1); }
      bar();
      publish(c, 0, FG_KV, j1, i1);
    }
    TS(2);
    bf16x8 ph[4][2];
#pragma unroll
    for (int m = 0; m < 4; ++m) {
      const f32x4 p0 = mfma(wT[0], qf[m], Z4), p1 = mfma(wT[1], qf[m], Z4);
      f32x4 e0, e1, n0, n1; feat(p0, p1, e0, e1, n0, n1);
      ph[m][0] = pk8(e0, e1); ph[m][1] = pk8(n0, n1);
    }
    TS(3);
    bar();
    kv_gather(c, j1);
    bar();
    TS(4);
    {
      bf16x8 kt[3][2];
#pragma unroll
      for (int d = 0; d < 3; ++d)
#pragma unroll
        for (int s = 0; s < 2; ++s) kt[d][s] = ldsT(lds + L_XBUF + wc * 8192 + (32 * s + 4 * g + q) * 128 + 32 * d + 8 * p, 16 * 128);
#pragma unroll
      for (int m = 0; m < 4; ++m) {
        const f32x4 n0 = mfma(kt[0][1], ph[m][1], mfma(kt[0][0], ph[m][0], Z4)), n1 = mfma(kt[1][1], ph[m][1], mfma(kt[1][0], ph[m][0], Z4));
        const f32x4 dn = mfma(kt[2][1], ph[m][1], mfma(kt[2][0], ph[m][0], Z4));
        const float r = __builtin_amdgcn_rcpf(dn[0] + 1e-6f);
        st16(c, pk8(n0 * r, n1 * r), lane * 16 + (OFF_OF + (i1 * 16 + j1) * 16384 + ((4 * wr + m) * 2 + wc) * 1024));
      }
      bar();
      publish(c, 1, FG_O, i1, j1);
    }
    TS(5);
  }
}

DI void consumer_p34(Ctx& c, const Args& A, int i3, int j3, int last) {
  const int lane = opaque(c.lane), wr = c.wid >> 1, wc = c.wid & 1, g = lane >> 4, q = (lane & 15) >> 2, p = lane & 3, i16 = lane & 15;
  const lds_t lds = c.lds;
  const __amdgpu_buffer_rsrc_t rWS = c.ws;
  const f32x4 Z4 = {0.f, 0.f, 0.f, 0.f};
  {
    u32x2 z1[4][4];
    {
      constexpr int S = 3, SB = 49152;
      f32x4 a2[4][4], a1_[4][4];
#pragma unroll
      for (int m = 0; m < 4; ++m)
#pragma unroll
        for (int n = 0; n < 4; ++n) { a2[m][n] = Z4; a1_[m][n] = Z4; }
      const int ao2 = wr * 8192 + lane * 16;
      int wo_[4];
#pragma unroll
      for (int n = 0; n < 4; ++n) wo_[n] = 16384 + (4 * g + q) * 512 + 8 * p + 32 * ((4 * wc + n) ^ (4 * (g & 1) + q));
      bf16x8 a0[4], w0[8], a1[4], w1[8];
      auto rd = [&](bf16x8(&a)[4], bf16x8(&w)[8], int sb, int ks) { const lds_t s = lds + sb;
#pragma unroll
        for (int n = 0; n < 8; ++n) w[n] = ldsT(s + wo_[n & 3] + (n >> 2) * 256 + ks * 32 * 512, 16 * 512);
#pragma unroll
        for (int m = 0; m < 4; ++m) a[m] = ldsA(s + ao2 + m * 2048 + ks * 1024); };
      auto mm = [&](bf16x8(&a)[4], bf16x8(&w)[8]) {
#pragma unroll
        for (int n = 0; n < 4; ++n)
#pragma unroll
          for (int m = 0; m < 4; ++m) a2[m][n] = mfma(a[m], w[n], a2[m][n]);
#pragma unroll
        for (int n = 0; n < 4; ++n)
#pragma unroll
          for (int m = 0; m < 4; ++m) a1_[m][n] = mfma(w[4 + n], a[m], a1_[m][n]); };
      bar();
      if (wc == 0) {
        const int R = 64 * wr + lane; u32x2 hv[16]; float s = 0.f, qq = 0.f;
#pragma unroll
        for (int k = 0; k < 16; ++k) hv[k] = __builtin_amdgcn_raw_buffer_load_b64(rWS, R * 8, OFF_STH + (i3 * 16 + k) * 1024, SC_SC1);
#pragma unroll
        for (int k = 0; k < 16; ++k) { s += __uint_as_float(hv[k][0]); qq += __uint_as_float(hv[k][1]); }
        s *= (1.0f / 1024.0f); qq *= (1.0f / 1024.0f);
        *(LAS float*)(lds + L_TAB3 + R * 4) = s; *(LAS float*)(lds + L_TAB3 + 512 + R * 4) = __builtin_amdgcn_rsqf(fmaxf(qq - s * s, 0.f) + 1e-5f);
        wait_lgkm0();
      }
      consumer_loop<S, SB>(c, 16, 16, [&](int sb) { rd(a0, w0, sb, 0); }, [&](int sb) { rd(a1, w1, sb, 1); }, [&]() { mm(a0, w0); }, [&]() { mm(a1, w1); }, []() { interleave<32, 20>(); }, [](int) {}, [&]() { TS(17); });
      TS(8);
      const lds_t tab = lds + L_TAB3;
      u32x2 z2p[4][4];
      {
        float cs[4];
#pragma unroll
        for (int n = 0; n < 4; ++n) cs[n] = *(LAS float*)(tab + 1024 + (64 * wc + 16 * n + i16) * 4);
#pragma unroll
        for (int m = 0; m < 4; ++m) {
          const int R = 64 * wr + 16 * m + 4 * g;
          const f32x4 mu = *(LAS f32x4*)(tab + R * 4), rs = *(LAS f32x4*)(tab + 512 + R * 4);
          f32x4 s = Z4, qq = Z4;
#pragma unroll
          for (int n = 0; n < 4; ++n) {
            const f32x4 xx = (a2[m][n] - mu * cs[n]) * rs;
            const unsigned lo = pk2(gelu(xx[0]), gelu(xx[1])), hi = pk2(gelu(xx[2]), gelu(xx[3]));
            z2p[m][n] = (u32x2){lo, hi};
            const f32x4 y = {bflo(lo), bfhi(lo), bflo(hi), bfhi(hi)};
            s += y; qq += y * y;
          }
#pragma unroll
          for (int r = 0; r < 4; ++r) { s[r] = sum_row16(s[r]); qq[r] = sum_row16(qq[r]); }
          if (i16 < 4) { const float sv = i16 == 0 ? s[0] : i16 == 1 ? s[1] : i16 == 2 ? s[2] : s[3], qv = i16 == 0 ? qq[0] : i16 == 1 ? qq[1] : i16 == 2 ? qq[2] : qq[3];
            *(LAS f32x2*)(lds + L_PST + wc * 1024 + (R + i16) * 8) = (f32x2){sv, qv}; }
        }
        wait_lgkm0(); bar();
        if (lane < 32) { const int R = c.wid * 32 + lane; const f32x2 v = *(LAS f32x2*)(lds + L_PST + R * 8) + *(LAS f32x2*)(lds + L_PST + 1024 + R * 8);
          __builtin_amdgcn_raw_buffer_store_b64(__builtin_bit_cast(u32x2, v), rWS, R * 8, OFF_ST2 + (i3 * 14 + j3) * 1024, SC_SC1); }
        publish(c, 3, FG_ST, i3, j3);
      }
      TS(9);
#pragma unroll
      for (int m = 0; m < 4; ++m) asm volatile("" : "+v"(a1_[m][0]), "+v"(a1_[m][1]), "+v"(a1_[m][2]), "+v"(a1_[m][3]));
#pragma unroll
      for (int m = 0; m < 4; ++m) {
        const int R = 64 * wr + 16 * m + i16;
        const float mu = *(LAS float*)(tab + R * 4), rs = *(LAS float*)(tab + 512 + R * 4);
#pragma unroll
        for (int n = 0; n < 4; ++n) {
          const f32x4 cs = *(LAS f32x4*)(tab + 1024 + (128 + 64 * wc + 16 * n + 4 * g) * 4);
          const f32x4 xx = (a1_[m][n] - mu * cs) * rs;
          z1[m][n] = (u32x2){pk2(gelu(xx[0]), gelu(xx[1])), pk2(gelu(xx[2]), gelu(xx[3]))};
        }
      }
      TS(10);
      bar();
      bar();
#pragma unroll
      for (int u = 0; u < 2; ++u) {
        f32x4 mu[2], rs[2];
#pragma unroll
        for (int e = 0; e < 2; ++e) { const int R = 64 * wr + 16 * (2 * u + e) + 4 * g; mu[e] = *(LAS f32x4*)(lds + L_FIN + R * 4); rs[e] = *(LAS f32x4*)(lds + L_FIN + 512 + R * 4); }
#pragma unroll
        for (int n = 0; n < 4; ++n) {
          f32x4 y[2];
#pragma unroll
          for (int e = 0; e < 2; ++e) { const u32x2 w = z2p[2 * u + e][n]; y[e] = ((f32x4){bflo(w[0]), bfhi(w[0]), bflo(w[1]), bfhi(w[1])} - mu[e]) * rs[e]; }
          st16(c, pk8(y[0], y[1]), lane * 16 + (OFF_Z2 + (j3 * 32 + 2 * i3 + wr) * 16384 + ((4 * wc + n) * 2 + u) * 1024));
        }
      }
      publish(c, 4, FG_Z2, j3, i3);
      TS(11);
    }
    if (last < 4) return;
    {
      constexpr int S = 4, SB = 32768;
      f32x4 acc[4][4]; float bs[4];
#pragma unroll
      for (int m = 0; m < 4; ++m) { bs[m] = bflo((unsigned)A.bsp[i3 * 128 + true_row(4 * wr + m, i16)]);
#pragma unroll
        for (int n = 0; n < 4; ++n) acc[m][n] = Z4; }
      const int ao0 = (wr * 64 + i16) * 128 + ((g ^ (i16 >> 1)) << 4), ao1 = ao0 ^ 64, zo = 16384 + wc * 8192 + lane * 16;
      bf16x8 a0[4], w0[4], a1[4], w1[4];
      auto rd = [&](bf16x8(&a)[4], bf16x8(&w)[4], int sb, int ks) { const lds_t s = lds + sb;
#pragma unroll
        for (int n = 0; n < 4; ++n) { w[n] = ldsA(s + zo + n * 2048 + ks * 1024); a[n] = ldsA(s + (ks ? ao1 : ao0) + n * 2048); } };
      auto mm = [&](bf16x8(&a)[4], bf16x8(&w)[4]) {
#pragma unroll
        for (int n = 0; n < 4; ++n)
#pragma unroll
          for (int m = 0; m < 4; ++m) acc[m][n] = mfma(w[n], a[m], acc[m][n]); };
      bar();
      const char* tp; int tstep;
      { const int l5 = lane & 31, r7 = ((c.b >> 3) % 7) * 19 + l5;
        tp = lane < 32 ? c.wsp + OFF_Z2 + j3 * 524288 + ((i3 & 3) * 32 + l5) * 128 : (const char*)A.wsp + (size_t)(i3 * 128 + (r7 < 128 && l5 < 19 ? r7 : 0)) * 4096;
        tstep = lane < 32 ? 16384 : 128; tp += P4D * tstep; }
      auto hook4 = [&](int t) {
      };
      consumer_loop<S, SB>(c, 32, 32, [&](int sb) { rd(a0, w0, sb, 0); }, [&](int sb) { rd(a1, w1, sb, 1); }, [&]() { mm(a0, w0); }, [&]() { mm(a1, w1); }, []() { interleave<16, 8>(); }, hook4, [&]() { TS(18); });
      TS(12);
#pragma unroll
      for (int m = 0; m < 4; ++m)
#pragma unroll
        for (int u = 0; u < 2; ++u) {
          f32x4 y[2];
#pragma unroll
          for (int e = 0; e < 2; ++e) { const u32x2 w = z1[m][2 * u + e]; y[e] = (f32x4){bflo(w[0]), bfhi(w[0]), bflo(w[1]), bfhi(w[1])} * (acc[m][2 * u + e] + bs[m]); }
          st16(c, pk8(y[0], y[1]), lane * 16 + (OFF_UF + (i3 * 28 + 2 * j3 + wc) * 16384 + ((4 * wr + m) * 2 + u) * 1024));
        }
      bar();
      publish(c, 5, FG_U, i3, j3);
      TS(13);
    }
    }
}

constexpr int L_RED = 98304;
#define STG1 0
#define STG2 2
#define R5 6
#define R2 6
template <int NSH, int R, bool REFILL>
DI void gemm_split(const Ctx& c, f32x4 (&res)[4], int aoff, __amdgpu_buffer_rsrc_t rW, int j2) {
  const int ln = opaque(c.lane), g = ln >> 4, q = (ln & 15) >> 2, p = ln & 3, rw = c.wid & 3, kh = c.wid >> 2;
  const lds_t lds = c.lds;
  const int sw2 = (2 * g + (q >> 1)) & 3, wl2 = (4 * g + q) * 128 + 8 * p + kh * (REFILL ? 9 : 8) * 8192, va = ln * 16 + rw * 4096;
  const int ao = aoff + kh * (NSH / 2) * 16384;
  int wo2[4];
#pragma unroll
  for (int n = 0; n < 4; ++n) wo2[n] = wl2 + 32 * (n ^ sw2);
  f32x4 acc[2][4];
#pragma unroll
  for (int n = 0; n < 4; ++n) { acc[0][n] = (f32x4){0.f, 0.f, 0.f, 0.f}; acc[1][n] = acc[0][n]; }
  u32x4 a[R][2]; bf16x8 w0[4], w1[4];
  auto lda = [&](int s) {
#pragma unroll
    for (int m = 0; m < 2; ++m) a[s % R][m] = __builtin_amdgcn_raw_buffer_load_b128(c.ws, va + m * 2048, ao + (s >> 1) * 16384 + (s & 1) * 1024, LDAUX); };
  auto ldw = [&](bf16x8(&d)[4], int s) { const int lt = s >> 1, slot = (REFILL && lt >= 9) ? lt - 9 : lt;
#pragma unroll
    for (int n = 0; n < 4; ++n) d[n] = ldsT(lds + wo2[n] + slot * 8192 + (s & 1) * 4096, 16 * 128); };
  TS(REFILL ? 19 : 24);
#pragma unroll
  for (int s = 0; s < R; ++s) { lda(s); if (s == STG1 || s == STG2) bar(); }
  ldw(w0, 0);
#pragma unroll
  for (int s = 0; s < NSH; ++s) {
    if (s == 1) TS(REFILL ? 20 : 25);
    if (s == 9) TS(REFILL ? 21 : 26);
    if (s == 15) TS(REFILL ? 22 : 27);
    if (REFILL && s == 18) TS(23);
    if (REFILL && s == 10) {
      wait_lgkm0(); bar();
      const int r = 8 * c.wid + (ln >> 3), s16 = ln & 7, vo = r * 2048 + j2 * 128 + (((s16 >> 1) ^ ((r >> 1) & 3)) << 5) + (s16 & 1) * 16;
#pragma unroll
      for (int k = 0; k < 10; ++k) dma_asm(rW, lds + ((k / 5) * 9 + (k % 5)) * 8192 + c.wid * 1024, vo, ((k / 5) * 14 + 9 + (k % 5)) * 64 * 2048);
      __builtin_amdgcn_sched_barrier(0);
    }
    if (REFILL && s == 17) { wait_vm<2 * ((NSH - R < 17 ? NSH - R : 17) - 10)>(); wait_lgkm0(); bar(); __builtin_amdgcn_sched_barrier(0); }
    if (s + 1 < NSH) { if (s & 1) ldw(w0, s + 1); else ldw(w1, s + 1); }
#pragma unroll
    for (int n = 0; n < 4; ++n)
#pragma unroll
      for (int m = 0; m < 2; ++m) acc[m][n] = mfma((s & 1) ? w1[n] : w0[n], __builtin_bit_cast(bf16x8, a[s % R][m]), acc[m][n]);
    if (s + R < NSH) lda(s + R);
    interleave<8, 8>();
    __builtin_amdgcn_sched_barrier(0);
  }
  TS(REFILL ? 29 : 28);
  wait_lgkm0(); bar();
#pragma unroll
  for (int n = 0; n < 4; ++n) *(LAS f32x4*)(lds + L_RED + c.wid * 4096 + n * 1024 + ln * 16) = kh ? acc[0][n] : acc[1][n];
  wait_lgkm0(); bar();
#pragma unroll
  for (int n = 0; n < 4; ++n) res[n] = (kh ? acc[1][n] : acc[0][n]) + *(LAS f32x4*)(lds + L_RED + (c.wid ^ 4) * 4096 + n * 1024 + ln * 16);
}

DI void phase2(Ctx& c, const Args& A, int i2, int j2) {
  const int lane = opaque(c.lane), g = lane >> 4, i16 = lane & 15, mt = 2 * (c.wid & 3) + (c.wid >> 2);
  u32x2 xr[4];
#pragma unroll
  for (int n = 0; n < 4; ++n) xr[n] = *(const u32x2*)(A.x + (size_t)(i2 * 128 + true_row(mt, i16)) * 1024 + j2 * 64 + 16 * n + 4 * g);
  bar();
  f32x4 acc[4];
  gemm_split<16, R2, false>(c, acc, OFF_OF + i2 * 262144, c.ws, j2);
  TS(6);
  float s = 0.f, qq = 0.f;
#pragma unroll
  for (int u = 0; u < 2; ++u) {
    f32x4 h0 = acc[2 * u], h1 = acc[2 * u + 1]; const u32x2 x0 = xr[2 * u], x1 = xr[2 * u + 1];
    h0[0] += bflo(x0[0]); h0[1] += bfhi(x0[0]); h0[2] += bflo(x0[1]); h0[3] += bfhi(x0[1]);
    h1[0] += bflo(x1[0]); h1[1] += bfhi(x1[0]); h1[2] += bflo(x1[1]); h1[3] += bfhi(x1[1]);
    const bf16x8 hb = pk8(h0, h1);
    st16(c, hb, lane * 16 + (OFF_HF + (i2 * 16 + j2) * 16384 + (mt * 2 + u) * 1024));
    acc_row(s, qq, __builtin_bit_cast(u32x4, hb));
  }
  s = sum_g(s); qq = sum_g(qq);
  if (lane < 16) __builtin_amdgcn_raw_buffer_store_b64((u32x2){__float_as_uint(s), __float_as_uint(qq)}, c.ws, OFF_STH + (i2 * 16 + j2) * 1024 + (mt * 16 + lane) * 8, 0, SC_SC1);
  publish<8>(c, 2, FG_H, i2, j2);
  TS(7);
}

DI void phase5(Ctx& c, const Args& A, int i2, int j2) {
  const int lane = opaque(c.lane), g = lane >> 4, i16 = lane & 15, mt = 2 * (c.wid & 3) + (c.wid >> 2);
  u32x4 hr[2];
#pragma unroll
  for (int u = 0; u < 2; ++u) hr[u] = __builtin_amdgcn_raw_buffer_load_b128(c.ws, lane * 16, OFF_HF + (i2 * 16 + j2) * 16384 + (mt * 2 + u) * 1024, SC_SC1);
  const __amdgpu_buffer_rsrc_t rW = __builtin_amdgcn_make_buffer_rsrc((void*)A.wout, 0, 1792 * 2048, 0x00020000);
  bar();
  f32x4 acc[4];
  gemm_split<28, R5, true>(c, acc, OFF_UF + i2 * 458752, rW, j2);
  TS(14);
  u16* o = A.out + (size_t)(i2 * 128 + true_row(mt, i16)) * 1024 + j2 * 64 + 4 * g;
#pragma unroll
  for (int n = 0; n < 4; ++n) { const unsigned r0 = hr[n >> 1][2 * (n & 1)], r1 = hr[n >> 1][2 * (n & 1) + 1];
    *(u32x2*)(o + 16 * n) = (u32x2){pk2(acc[n][0] + bflo(r0), acc[n][1] + bfhi(r0)), pk2(acc[n][2] + bflo(r1), acc[n][3] + bfhi(r1))}; }
  TS(15);
}

#define LAST_PHASE 5
__global__ __launch_bounds__(512, 2) void fused_fwd(const u16* __restrict__ x, const u16* __restrict__ wqkv, const u16* __restrict__ wphi, const u16* __restrict__ wo,
                                                      const u16* __restrict__ win, const u16* __restrict__ wsp, const u16* __restrict__ bsp, const u16* __restrict__ wout,
                                                      u16* __restrict__ out, char* __restrict__ ws) {
  extern __shared__ __attribute__((aligned(16))) char shm[];
  Ctx c;
  c.lds = (lds_t)shm; c.wsp = ws; c.lane = threadIdx.x & 63; c.wid = __builtin_amdgcn_readfirstlane(threadIdx.x >> 6); c.b = blockIdx.x;
  c.ws = __builtin_amdgcn_make_buffer_rsrc((void*)ws, 0, WS_BYTES, 0x00020000);
  c.tag = 0u;
  if (threadIdx.x < 16) *(LAS unsigned*)(c.lds + L_CNT + threadIdx.x * 4) = 0u;
  const int xcd = c.b & 7, jj = c.b >> 3;
  const int i1 = 8 * (xcd & 1) + (jj >> 2), j1 = 4 * (xcd >> 1) + (jj & 3);
  const int i2 = 4 * (xcd >> 1) + (jj >> 3), j2 = 8 * (xcd & 1) + (jj & 7);
  const bool act3 = jj < 28;
  const int i3 = 4 * (xcd >> 1) + (act3 ? jj / 7 : 0), j3 = 7 * (xcd & 1) + (act3 ? jj % 7 : 0);
  Args A{x, wqkv, wphi, wo, win, wsp, bsp, wout, out, ws};
  constexpr int last = LAST_PHASE;
  if (c.wid >= 4) loader_p1(c, A, i1, j1, i2, j2, i3, j3, act3, last);
  else { __builtin_amdgcn_s_setprio(1); consumer_p1(c, A, i1, j1); __builtin_amdgcn_s_setprio(0); }
  if (last < 2) return;
  phase2(c, A, i2, j2);
  if (last < 3) return;
  if (act3) {
    if (c.wid >= 4) loader_p34(c, A, i3, j3, last);
    else { __builtin_amdgcn_s_setprio(1); consumer_p34(c, A, i3, j3, last); __builtin_amdgcn_s_setprio(0); }
  } else {
    bar();
    if (c.wid < 4) { const int wv = ((c.b & 7) * 4 + ((c.b >> 3) - 28)) * 4 + c.wid;
#pragma unroll 1
      for (int k = 0; k < 12; ++k) { const int L = (k * 128 + wv) * 64 + c.lane;
        touch(c.lds, L < 65536 ? (const char*)A.wsp + (size_t)L * 128 : (const char*)A.wout + (size_t)((L < 94208 ? L : 94207) - 65536) * 128); } }
  }
  if (last < 5) return;
  if (c.wid >= 4) loader_p5(c, A, i2, j2);
  phase5(c, A, i2, j2);
}

extern "C" void kernel_launch(void* const* d_in, const int* in_sizes, int n_in, void* d_out, int out_size, void* d_ws, size_t ws_size, hipStream_t stream) {
  (void)in_sizes; (void)n_in; (void)out_size; (void)ws_size;
  (void)hipFuncSetAttribute((const void*)fused_fwd, hipFuncAttributeMaxDynamicSharedMemorySize, LDS_BYTES);
  fused_fwd<<<256, 512, LDS_BYTES, stream>>>((const u16*)d_in[0], (const u16*)d_in[1], (const u16*)d_in[2], (const u16*)d_in[3], (const u16*)d_in[5], (const u16*)d_in[6],
                                             (const u16*)d_in[7], (const u16*)d_in[8], (u16*)d_out, (char*)d_ws);
}
```

```cpp
#include <hip/hip_runtime.h>
#include <hip/hip_bf16.h>
#include <stdint.h>
#include <type_traits>

#define LAS __attribute__((address_space(3)))
#define DI __device__ __forceinline__
typedef unsigned short u16;
typedef short bf16x8 __attribute__((ext_vector_type(8)));
typedef short s16x4 __attribute__((ext_vector_type(4)));
typedef float f32x4 __attribute__((ext_vector_type(4)));
typedef float f32x2 __attribute__((ext_vector_type(2)));
typedef unsigned u32x4 __attribute__((ext_vector_type(4)));
typedef unsigned u32x2 __attribute__((ext_vector_type(2)));
typedef __bf16 bf2 __attribute__((ext_vector_type(2)));
typedef LAS char* lds_t;

constexpr unsigned OFF_EPOCH = 0, OFF_ERR = 128, OFF_FLAG = 4096;
constexpr unsigned OFF_KVP = 262144;
constexpr unsigned OFF_ZP = OFF_KVP + 2097152;
constexpr unsigned OFF_ST2 = OFF_ZP + 131072;
constexpr unsigned OFF_STH = OFF_ST2 + 262144;
constexpr unsigned OFF_GX = OFF_STH + 262144;
constexpr unsigned OFF_GC1 = OFF_GX + 65536;
constexpr unsigned OFF_GC3 = OFF_GC1 + 393216;
constexpr unsigned OFF_OF = 4u << 20, OFF_HF = 8u << 20, OFF_Z2 = 12u << 20, OFF_UF = 20u << 20, OFF_TS = 28u << 20;
constexpr unsigned WS_BYTES = 32u << 20;
enum { FG_KV = 0, FG_O = 1, FG_H = 2, FG_ST = 3, FG_Z2 = 4, FG_U = 5 };
constexpr int LDS_BYTES = 163840;
constexpr int SC1 = 122880, L_WPHI = SC1, L_TAB1 = SC1 + 4096, L_XBUF = SC1 + 22528;
constexpr int L_XF1 = 65536, L_TB = 32768, L_KR = 65536, L_ZR = 131072;
constexpr int SC3 = 147456, L_TAB3 = SC3, L_PST = SC3 + 2048, L_FIN = SC3 + 4096;
constexpr int L_CNT = LDS_BYTES - 64, L_DUMMY = LDS_BYTES - 512;
constexpr int SC_SC1 = 16;
#define LDAUX 16

DI void bar() { asm volatile("" ::: "memory"); __builtin_amdgcn_s_barrier(); asm volatile("" ::: "memory"); }
template <int N> DI void wait_vm() { asm volatile("s_waitcnt vmcnt(%0)" ::"n"(N) : "memory"); }
DI int opaque(int v) { asm volatile("" : "+v"(v)); return v; }
DI void wait_lgkm0() { __builtin_amdgcn_s_waitcnt(0xc07f); }
DI unsigned pk2(float lo, float hi) { f32x2 v = {lo, hi}; return __builtin_bit_cast(unsigned, __builtin_convertvector(v, bf2)); }
DI bf16x8 pk8(const f32x4& a, const f32x4& b) { u32x4 r = {pk2(a[0], a[1]), pk2(a[2], a[3]), pk2(b[0], b[1]), pk2(b[2], b[3])}; return __builtin_bit_cast(bf16x8, r); }
DI float bflo(unsigned w) { return __uint_as_float(w << 16); }
DI float bfhi(unsigned w) { return __uint_as_float(w & 0xffff0000u); }
DI f32x4 mfma(bf16x8 a, bf16x8 b, f32x4 c) { return __builtin_amdgcn_mfma_f32_16x16x32_bf16(a, b, c, 0, 0, 0); }
DI bf16x8 ldsA(lds_t p) { return *(LAS bf16x8*)p; }
DI bf16x8 ldsT(lds_t p, int d) {
  s16x4 lo = __builtin_amdgcn_ds_read_tr16_b64_v4i16((LAS s16x4*)p), hi = __builtin_amdgcn_ds_read_tr16_b64_v4i16((LAS s16x4*)(p + d));
  return __builtin_shufflevector(lo, hi, 0, 1, 2, 3, 4, 5, 6, 7);
}
template <int D1, int D2> DI void tr4_issue(unsigned addr, u32x2 (&r)[4]) {
  asm volatile("ds_read_b64_tr_b16 %0, %4\n\tds_read_b64_tr_b16 %1, %4 offset:%5\n\tds_read_b64_tr_b16 %2, %4 offset:%6\n\tds_read_b64_tr_b16 %3, %4 offset:%7"
               : "=&v"(r[0]), "=&v"(r[1]), "=&v"(r[2]), "=&v"(r[3]) : "v"(addr), "n"(D1), "n"(D2), "n"(D1 + D2) : "memory");
}
DI void tr4_wait(u32x2 (&r)[4]) { asm volatile("s_waitcnt lgkmcnt(0)" : "+v"(r[0]), "+v"(r[1]), "+v"(r[2]), "+v"(r[3])::"memory"); }
DI float sum_g(float x) {
  auto a = __builtin_amdgcn_permlane16_swap(__float_as_uint(x), __float_as_uint(x), false, false);
  x = __uint_as_float(a[0]) + __uint_as_float(a[1]);
  auto b = __builtin_amdgcn_permlane32_swap(__float_as_uint(x), __float_as_uint(x), false, false);
  return __uint_as_float(b[0]) + __uint_as_float(b[1]);
}
DI float max_g(float x) {
  auto a = __builtin_amdgcn_permlane16_swap(__float_as_uint(x), __float_as_uint(x), false, false);
  x = fmaxf(__uint_as_float(a[0]), __uint_as_float(a[1]));
  auto b = __builtin_amdgcn_permlane32_swap(__float_as_uint(x), __float_as_uint(x), false, false);
  return fmaxf(__uint_as_float(b[0]), __uint_as_float(b[1]));
}
template <int CTRL> DI float dpp_add(float x) { return x + __int_as_float(__builtin_amdgcn_update_dpp(0, __float_as_int(x), CTRL, 0xf, 0xf, true)); }
DI float sum_row16(float x) { x = dpp_add<0x128>(x); x = dpp_add<0x124>(x); x = dpp_add<0x122>(x); return dpp_add<0x121>(x); }
DI void sum_row16x8(f32x4& a, f32x4& b) {
#define RR(n) "v_add_f32_dpp %0, %0, %0 row_ror:" n " row_mask:0xf bank_mask:0xf\n\tv_add_f32_dpp %1, %1, %1 row_ror:" n " row_mask:0xf bank_mask:0xf\n\t" \
  "v_add_f32_dpp %2, %2, %2 row_ror:" n " row_mask:0xf bank_mask:0xf\n\tv_add_f32_dpp %3, %3, %3 row_ror:" n " row_mask:0xf bank_mask:0xf\n\t" \
  "v_add_f32_dpp %4, %4, %4 row_ror:" n " row_mask:0xf bank_mask:0xf\n\tv_add_f32_dpp %5, %5, %5 row_ror:" n " row_mask:0xf bank_mask:0xf\n\t" \
  "v_add_f32_dpp %6, %6, %6 row_ror:" n " row_mask:0xf bank_mask:0xf\n\tv_add_f32_dpp %7, %7, %7 row_ror:" n " row_mask:0xf bank_mask:0xf\n\t"
  float a0 = a[0], a1 = a[1], a2 = a[2], a3 = a[3], b0 = b[0], b1 = b[1], b2 = b[2], b3 = b[3];
  asm volatile("s_nop 1\n\t" RR("8") RR("4") RR("2") RR("1") "s_nop 0" : "+v"(a0), "+v"(a1), "+v"(a2), "+v"(a3), "+v"(b0), "+v"(b1), "+v"(b2), "+v"(b3));
#undef RR
  a = (f32x4){a0, a1, a2, a3}; b = (f32x4){b0, b1, b2, b3};
}
DI float dot2(unsigned a, unsigned b, float c) { return __builtin_amdgcn_fdot2_f32_bf16(__builtin_bit_cast(bf2, a), __builtin_bit_cast(bf2, b), c, false); }
constexpr unsigned ONES2 = 0x3f803f80u;
DI f32x4 gelu4(f32x4 x) {
  const f32x4 t = x * (x * x * -0.1029432397f + -2.302208198f);
  f32x4 d, r;
#pragma unroll
  for (int i = 0; i < 4; ++i) d[i] = __builtin_amdgcn_exp2f(t[i]);
  d += 1.0f;
#pragma unroll
  for (int i = 0; i < 4; ++i) r[i] = __builtin_amdgcn_rcpf(d[i]);
  return x * r;
}
DI u32x2 pk4(const f32x4& a) { return (u32x2){pk2(a[0], a[1]), pk2(a[2], a[3])}; }
DI f32x4 up4(u32x2 w) { return (f32x4){bflo(w[0]), bfhi(w[0]), bflo(w[1]), bfhi(w[1])}; }
DI float gelu(float x) { const float t = x * fmaf(x * x, -0.1029432397f, -2.302208198f); return x * __builtin_amdgcn_rcpf(1.0f + __builtin_amdgcn_exp2f(t)); }

struct Ctx {
  __amdgpu_buffer_rsrc_t ws;
  char* wsp; lds_t lds; unsigned tag; int lane, wid, b; unsigned ts;
};
DI unsigned* flag_ptr(const Ctx& c, int g, int a, int bb) { return (unsigned*)(c.wsp + OFF_FLAG + ((g * 16 + a) * 16 + bb) * 128); }
template <unsigned NWAVES = 4, int YOUNGER = 0> DI void publish(const Ctx& c, int cnt, int g, int a, int bb) {
  wait_vm<YOUNGER>();
  if (c.lane == 0) {
    unsigned old = __hip_atomic_fetch_add((LAS unsigned*)(c.lds + L_CNT + cnt * 4), 1u, __ATOMIC_RELAXED, __HIP_MEMORY_SCOPE_WORKGROUP);
    if (old == NWAVES - 1u) __hip_atomic_store(flag_ptr(c, g, a, bb), c.tag, __ATOMIC_RELAXED, __HIP_MEMORY_SCOPE_AGENT);
  }
}
DI void wait_flags(const Ctx& c, int g, int a, int n) {
  const unsigned* p = flag_ptr(c, g, a, c.lane < n ? c.lane : 0);
  const unsigned long long t0 = __builtin_amdgcn_s_memrealtime();
  for (;;) {
    const unsigned v = __hip_atomic_load(p, __ATOMIC_RELAXED, __HIP_MEMORY_SCOPE_AGENT);
    if (__all(v == c.tag)) break;
    if (__builtin_amdgcn_s_memrealtime() - t0 > 300000ull) { if (c.lane == 0) __hip_atomic_store((unsigned*)(c.wsp + OFF_ERR), 0x100u + (unsigned)g, __ATOMIC_RELAXED, __HIP_MEMORY_SCOPE_AGENT); break; }
    __builtin_amdgcn_s_sleep(1);
  }
  asm volatile("" ::: "memory");
}
#define STEP(t) do {} while (0)
#define TS(k) do {} while (0)

DI void st16(const Ctx& c, bf16x8 v, int off) { __builtin_amdgcn_raw_buffer_store_b128(__builtin_bit_cast(u32x4, v), c.ws, off, 0, SC_SC1); }
template <int AUX> DI void dma(__amdgpu_buffer_rsrc_t r, lds_t dst, int voff, int soff) {
  if (AUX) asm volatile("s_mov_b32 m0, %0\n\ts_nop 0\n\tbuffer_load_dwordx4 %1, %2, %3 offen sc1 lds" ::"s"((unsigned)(size_t)dst), "v"(voff), "s"(r), "s"(soff) : "memory");
  else asm volatile("s_mov_b32 m0, %0\n\ts_nop 0\n\tbuffer_load_dwordx4 %1, %2, %3 offen lds" ::"s"((unsigned)(size_t)dst), "v"(voff), "s"(r), "s"(soff) : "memory");
}

#define EXP 0
template <int S, int NW, class IA, class IW, class MID, class POST>
DI void loader_loop(int nt, bool gated_prologue, IA issueA, IW issueW, MID mid, POST post) {
  constexpr int PPW = 4 + NW;
  if (gated_prologue) wait_vm<4 * (S - 2)>(); else wait_vm<PPW * (S - 2)>();
  bar();
  int t = 0;
#pragma unroll
  for (int p = 0; p < S - 2; ++p, ++t) {
    mid(t); issueA(t + S - 1); issueW(t + S - 1); post(t);
    if (gated_prologue) {
      if (p == 0) wait_vm<4 * (S - 3 > 0 ? S - 3 : 0) + PPW * 1>();
      else if (p == 1) wait_vm<4 * (S - 4 > 0 ? S - 4 : 0) + PPW * 2>();
      else wait_vm<4 * (S - 5 > 0 ? S - 5 : 0) + PPW * 3>();
    } else wait_vm<PPW * (S - 2)>();
    bar();
  }
#pragma unroll 1
  for (; t + S - 1 < nt; ++t) { mid(t); if (!(EXP & 1)) { issueA(t + S - 1); issueW(t + S - 1); } wait_vm<PPW * (S - 2)>(); bar(); }
#pragma unroll
  for (int r = S - 2; r >= 0; --r, ++t) {
    if (r >= 4) wait_vm<PPW * 3>(); else if (r == 3) wait_vm<PPW * 2>(); else if (r == 2) wait_vm<PPW * 1>(); else wait_vm<0>();
    bar();
  }
}
template <int NW, bool GATED, class IA, class IW, class POST, class GA, class FIN>
DI void loader_loop16(bool full, bool once, IA issueA, IW issueW, POST post, GA gather, FIN fin) {
  constexpr int PPW = 4 + NW;
  if (GATED) wait_vm<4>(); else wait_vm<PPW>();
  bar();
  issueA(2); issueW(2); post(0); wait_vm<PPW>(); bar();
#pragma unroll 1
  for (int t = 1; t < 5; ++t) { if (!(EXP & 1)) { issueA(t + 2); issueW(t + 2); } post(t); wait_vm<PPW>(); bar(); }
#pragma unroll
  for (int t = 5; t < 13; ++t) {
    issueA(t + 2); issueW(t + 2); gather(t - 5);
    if (full) { if (t == 5) wait_vm<PPW + 2>(); else wait_vm<PPW + 4>(); }
    else if (once && t <= 6) wait_vm<PPW + 2>();
    else wait_vm<PPW>();
    bar();
  }
  issueA(15); issueW(15); if (full) wait_vm<PPW + 2>(); else wait_vm<PPW>(); bar();
  wait_vm<0>(); bar();
  fin(); bar();
}
template <int S, int SB, class RD0, class RD1, class MM0, class MM1, class SCH, class HK, class ST>
DI void consumer_loop(Ctx& c, int sbase, int nt, RD0 rd0, RD1 rd1, MM0 mm0, MM1 mm1, SCH sched, HK hook, ST stamp) {
  bar();
  stamp();
  rd0(0);
  int sb = 0;
#pragma unroll 1
  for (int t = 0; t < nt; ++t) {
    hook(t); STEP(t);
    __builtin_amdgcn_sched_barrier(0);
    if (!(EXP & 4)) rd1(sb); if (!(EXP & 2)) mm0(); sched();
    __builtin_amdgcn_sched_barrier(0);
    wait_lgkm0(); bar();
    __builtin_amdgcn_sched_barrier(0);
    sb = (sb == (S - 1) * SB) ? 0 : sb + SB;
    if (!(EXP & 4)) rd0(sb); if (!(EXP & 2)) mm1(); sched();
    __builtin_amdgcn_sched_barrier(0);
  }
}
#define TOUCH(name, mod) DI void name(lds_t lds, const void* p) { \
  asm volatile("s_mov_b32 m0, %1\n\ts_nop 0\n\tglobal_load_lds_dword %0, off" mod ::"v"(p), "s"((unsigned)(size_t)(lds + L_DUMMY)) : "memory"); }
TOUCH(touch, "") TOUCH(touch_nt, " nt") TOUCH(touch_sc1, " sc1")
template <int NM, int NR> DI void interleave() {
#pragma unroll
  for (int i = 0; i < NR; ++i) { __builtin_amdgcn_sched_group_barrier(0x008, 1, 0); __builtin_amdgcn_sched_group_barrier(0x100, 1, 0); }
  __builtin_amdgcn_sched_group_barrier(0x008, NM - NR, 0);
}
template <int NM, int NR> DI void interleave2() {
#pragma unroll
  for (int i = 0; i < NR / 2; ++i) { __builtin_amdgcn_sched_group_barrier(0x008, 1, 0); __builtin_amdgcn_sched_group_barrier(0x100, 2, 0); }
  __builtin_amdgcn_sched_group_barrier(0x008, NM - NR / 2, 0);
}

DI int rowmajor_voff(int lw, int lane, int pitch) {
  const int rr = lane >> 3, slot = 8 * (lw & 1) + rr;
  const int row = 8 * (slot >> 2) + 4 * (lw >> 1) + (slot & 3);
  return row * pitch + (((lane & 7) ^ (slot >> 1)) << 4);
}
DI int true_row(int mt, int i) { return 32 * (mt >> 1) + 8 * (i >> 2) + 4 * (mt & 1) + (i & 3); }

DI void acc_row(float& s, float& q, u32x4 w) {
#pragma unroll
  for (int i = 0; i < 4; ++i) { s = dot2(w[i], ONES2, s); q = dot2(w[i], w[i], q); } }
DI float col_sum(const u32x2 (&r)[4]) { float s0 = 0.f, s1 = 0.f;
#pragma unroll
  for (int i = 0; i < 4; ++i) { s0 = dot2(r[i][0], ONES2, s0); s1 = dot2(r[i][1], ONES2, s1); }
  return sum_g(s0 + s1); }
DI void st_granule(const Ctx& c, float v, int off) { __builtin_amdgcn_raw_buffer_store_b64((u32x2){__float_as_uint(v), c.tag}, c.ws, off, 0, SC_SC1); }
DI bool timed_out(const Ctx& c, unsigned long long t0, unsigned code) {
  if (__builtin_amdgcn_s_memrealtime() - t0 <= 300000ull) return false;
  if (c.lane == 0) __hip_atomic_store((unsigned*)(c.wsp + OFF_ERR), code, __ATOMIC_RELAXED, __HIP_MEMORY_SCOPE_AGENT);
  return true;
}
DI float sum_granules16(const Ctx& c, u32x2 (&v)[16], int off, int stride) {
  const unsigned long long t0 = __builtin_amdgcn_s_memrealtime();
  for (;;) {
    bool ok = true;
#pragma unroll
    for (int k = 0; k < 16; ++k) ok &= v[k][1] == c.tag;
    if (__all(ok) || timed_out(c, t0, 0x200u)) break;
    __builtin_amdgcn_s_sleep(2);
#pragma unroll
    for (int k = 0; k < 16; ++k) v[k] = __builtin_amdgcn_raw_buffer_load_b64(c.ws, off + k * stride, 0, SC_SC1);
  }
  float s = 0.f;
#pragma unroll
  for (int k = 0; k < 16; ++k) s += __uint_as_float(v[k][0]);
  return s;
}

#define ROTM 0
#define ROT2(j) (ROTM * 2 * ((j) & 7))
#define ROT5(j) (ROTM * ((7 * ((j) & 7)) >> 1))
#define P4D 0
#define TOUCHW touch
#define PF_T0 2
#define PF_DT 3

struct Args { const u16 *x, *wqkv, *wphi, *wo, *win, *wsp, *bsp, *wout; u16* out; char* ws; };

DI void get_tag(Ctx& c) { unsigned e; asm volatile("s_load_dword %0, %1, 0x0\n\ts_waitcnt lgkmcnt(0)" : "=&s"(e) : "s"(c.wsp) : "memory"); c.tag = e + 1u; }
DI void kv_gather(const Ctx& c, int j1) {
  const int lane = c.lane, g = lane >> 4, i16 = lane & 15, tid = c.wid * 64 + lane;
  f32x4 a0 = {0, 0, 0, 0}, a1 = {0, 0, 0, 0};
  u32x4 v[16];
#pragma unroll
  for (int k = 0; k < 16; ++k) v[k] = __builtin_amdgcn_raw_buffer_load_b128(c.ws, tid * 16, OFF_KVP + (j1 * 16 + k) * 8192, SC_SC1);
  float zs = 0.f;
  if (c.wid < 2) { unsigned zv[16];
#pragma unroll
    for (int k = 0; k < 16; ++k) zv[k] = __builtin_amdgcn_raw_buffer_load_b32(c.ws, lane * 4, OFF_ZP + ((j1 * 16 + k) * 2 + c.wid) * 256, SC_SC1);
#pragma unroll
    for (int k = 0; k < 16; ++k) zs += __uint_as_float(zv[k]); }
#pragma unroll
  for (int k = 0; k < 16; ++k) { a0[0] += bflo(v[k][0]); a0[1] += bfhi(v[k][0]); a0[2] += bflo(v[k][1]); a0[3] += bfhi(v[k][1]); a1[0] += bflo(v[k][2]); a1[1] += bfhi(v[k][2]); a1[2] += bflo(v[k][3]); a1[3] += bfhi(v[k][3]); }
  const int w = c.wid, hd = w >> 2, dt = (w >> 1) & 1, fp = w & 1;
  const lds_t tb = c.lds + L_XBUF + hd * 8192 + (32 * fp + i16) * 128 + (16 * dt + 4 * g) * 2;
  *(LAS u32x2*)tb = (u32x2){pk2(a0[0], a0[1]), pk2(a0[2], a0[3])};
  *(LAS u32x2*)(tb + 16 * 128) = (u32x2){pk2(a1[0], a1[1]), pk2(a1[2], a1[3])};
  if (c.wid < 2) { const unsigned z2 = pk2(zs, zs); const lds_t t3 = c.lds + L_XBUF + c.wid * 8192 + lane * 128 + 64;
    *(LAS u32x4*)t3 = (u32x4){z2, z2, z2, z2}; *(LAS u32x4*)(t3 + 16) = (u32x4){z2, z2, z2, z2}; }
  wait_lgkm0();
}
struct Map { int i1, j1, i2, j2, i3, j3; bool act3; };
DI void p1_load(Ctx& c, const Args& A, int i1, int j1) {
  const int lane = c.lane, lw = c.wid - 4, g = lane >> 4, q = (lane & 15) >> 2, p = lane & 3;
  const lds_t lds = c.lds;
  const __amdgpu_buffer_rsrc_t rWS = c.ws;
  constexpr int S = 3, SB = 40960, NW = 6;
  const __amdgpu_buffer_rsrc_t rA = __builtin_amdgcn_make_buffer_rsrc((void*)(A.x + (size_t)i1 * 128 * 1024), 0, 128 * 2048, 0x00020000);
  const __amdgpu_buffer_rsrc_t rW = __builtin_amdgcn_make_buffer_rsrc((void*)A.wqkv, 0, 1024 * 6144, 0x00020000);
  const __amdgpu_buffer_rsrc_t rP = __builtin_amdgcn_make_buffer_rsrc((void*)A.wphi, 0, 65536, 0x00020000);
  const int voA = rowmajor_voff(lw, lane, 2048);
  int voW[NW];
#pragma unroll
  for (int i = 0; i < NW; ++i) { const int o = (NW * lw + i) * 1024 + lane * 16, r = o / 384, rem = o % 384, seg = rem >> 7, s16 = (rem & 127) >> 4;
    const int c32 = (s16 >> 1) ^ ((r >> 1) & 3), r5 = r & 31, k = (r & 32) + 8 * ((r5 >> 2) & 3) + 4 * (r5 >> 4) + (r5 & 3);
    voW[i] = k * 6144 + seg * 2048 + j1 * 128 + c32 * 32 + (s16 & 1) * 16; }
#define ROTMODE 1
  const int rot = ROTMODE == 2 ? (2 * (i1 & 7) + (i1 >> 3) + 4 * (j1 & 3)) & 15 : ROTMODE == 1 ? 4 * (i1 & 3) : ROTMODE == 3 ? 8 * (i1 & 1) : 0;
  auto issueA = [&](int t) { const lds_t d = lds + (t % S) * SB + lw * 1024; const int so = ((t + rot) & 15) * 128;
#pragma unroll
    for (int i = 0; i < 4; ++i) dma<0>(rA, d + i * 4096, voA, so + i * 32 * 2048); };
  auto issueW = [&](int t) { const lds_t d = lds + (t % S) * SB + 16384 + lw * NW * 1024; const int so = ((t + rot) & 15) * 64 * 6144;
#pragma unroll
    for (int i = 0; i < NW; ++i) dma<0>(rW, d + i * 1024, voW[i], so); };
  issueA(0); issueW(0); issueA(1); issueW(1);
  dma<0>(rP, lds + L_WPHI + lw * 1024, lane * 16 + lw * 1024, j1 * 4096);
  get_tag(c);
  TS(0);
  u32x2 gv[16]; u32x4 gx[2];
  const int goff = OFF_GC1 + j1 * 16 * 1536 + (lw * 64 + lane) * 8, xoff = OFF_GX + (i1 * 128 + lane) * 16;
  const int tau = ROTMODE == 2 ? 0 : ROTMODE == 1 ? i1 >> 2 : ROTMODE == 3 ? 0 : 0;
  auto post = [&](int t) {
    if (t != tau) return;
    const int sw = (2 * g + (q >> 1)) & 3; u32x2 cr[3][4];
#pragma unroll
    for (int n = 0; n < 3; ++n) { const int nu = 3 * lw + n;
      tr4_issue<16 * 384, 32 * 384>((unsigned)(size_t)(lds + (t % S) * SB + 16384 + (4 * g + q) * 384 + 8 * p + (nu >> 2) * 128 + 32 * ((nu & 3) ^ sw)), cr[n]); }
#pragma unroll
    for (int n = 0; n < 3; ++n) { tr4_wait(cr[n]); const float s = col_sum(cr[n]);
      if (lane < 16) st_granule(c, s, OFF_GC1 + (j1 * 16 + rot + tau) * 1536 + ((3 * lw + n) * 16 + lane) * 8); }
  };
  auto gather = [&](int k) {
    if (lw < 3) { gv[2 * k] = __builtin_amdgcn_raw_buffer_load_b64(rWS, goff + 2 * k * 1536, 0, SC_SC1); gv[2 * k + 1] = __builtin_amdgcn_raw_buffer_load_b64(rWS, goff + (2 * k + 1) * 1536, 0, SC_SC1); }
    else if (k == 0) { gx[0] = __builtin_amdgcn_raw_buffer_load_b128(rWS, xoff, 0, SC_SC1); gx[1] = __builtin_amdgcn_raw_buffer_load_b128(rWS, xoff + 1024, 0, SC_SC1); }
  };
  auto fin = [&]() {
    if (lw < 3) *(LAS float*)(lds + L_TAB1 + 1024 + (lw * 64 + lane) * 4) = sum_granules16(c, gv, goff, 1536);
    else {
      const unsigned long long t0 = __builtin_amdgcn_s_memrealtime();
      for (;;) {
        if (__all(gx[0][1] == c.tag && gx[0][3] == c.tag && gx[1][1] == c.tag && gx[1][3] == c.tag) || timed_out(c, t0, 0x201u)) break;
        __builtin_amdgcn_s_sleep(2);
        gx[0] = __builtin_amdgcn_raw_buffer_load_b128(rWS, xoff, 0, SC_SC1); gx[1] = __builtin_amdgcn_raw_buffer_load_b128(rWS, xoff + 1024, 0, SC_SC1);
      }
#pragma unroll
      for (int e = 0; e < 2; ++e) { *(LAS unsigned*)(lds + L_TAB1 + (lane + 64 * e) * 4) = gx[e][0]; *(LAS unsigned*)(lds + L_TAB1 + 512 + (lane + 64 * e) * 4) = gx[e][2]; }
    }
    wait_lgkm0();
  };
  loader_loop16<NW, false>(lw < 3, lw == 3, issueA, issueW, post, gather, fin);
  TS(1);
}

struct Acc1 { f32x4 q[2][2], k[2][2], v[2][2]; };
DI void p1_mma(Ctx& c, const Args& A, int i1, int j1, Acc1& e) {
  const int lane = c.lane, wr = c.wid >> 1, wc = c.wid & 1, g = lane >> 4, q = (lane & 15) >> 2, p = lane & 3, i16 = lane & 15;
  const lds_t lds = c.lds;
  const f32x4 Z4 = {0.f, 0.f, 0.f, 0.f};
  constexpr int S = 3, SB = 40960;
  f32x4 aq[4][2], ak[4][2], av[4][2];
#pragma unroll
  for (int m = 0; m < 4; ++m)
#pragma unroll
    for (int n = 0; n < 2; ++n) { aq[m][n] = Z4; ak[m][n] = Z4; av[m][n] = Z4; asm volatile("" : "+v"(aq[m][n]), "+v"(ak[m][n]), "+v"(av[m][n])); }
  const int sw = (2 * g + (q >> 1)) & 3;
  const int ao0 = (wr * 64 + i16) * 128 + ((g ^ (i16 >> 1)) << 4), ao1 = ao0 ^ 64;
  const int wl = 16384 + (4 * g + q) * 384 + 8 * p;
  const int wo0 = wl + 32 * ((2 * wc) ^ sw), wo1 = wl + 32 * ((2 * wc + 1) ^ sw);
  bf16x8 a0[4], w0[6], a1[4], w1[6];
  auto rd = [&](bf16x8(&a)[4], bf16x8(&w)[6], int sb, int ks) { const lds_t s = lds + sb;
#pragma unroll
    for (int sg = 0; sg < 3; ++sg) { w[2 * sg] = ldsT(s + wo0 + sg * 128 + ks * 32 * 384, 16 * 384); w[2 * sg + 1] = ldsT(s + wo1 + sg * 128 + ks * 32 * 384, 16 * 384); }
#pragma unroll
    for (int m = 0; m < 4; ++m) a[m] = ldsA(s + (ks ? ao1 : ao0) + m * 2048); };
  auto mm = [&](bf16x8(&a)[4], bf16x8(&w)[6]) {
#pragma unroll
    for (int n = 0; n < 2; ++n)
#pragma unroll
      for (int m = 0; m < 4; ++m) aq[m][n] = mfma(w[n], a[m], aq[m][n]);
#pragma unroll
    for (int n = 0; n < 2; ++n)
#pragma unroll
      for (int m = 0; m < 4; ++m) ak[m][n] = mfma(w[2 + n], a[m], ak[m][n]);
#pragma unroll
    for (int n = 0; n < 2; ++n)
#pragma unroll
      for (int m = 0; m < 4; ++m) av[m][n] = mfma(a[m], w[4 + n], av[m][n]); };
  TS(0);
  {
    u32x4 xv[2][2];
#pragma unroll
    for (int e2 = 0; e2 < 2; ++e2) { const int R = 8 * j1 + 2 * c.wid + e2; const u16* xr = A.x + (size_t)(i1 * 128 + true_row(R >> 4, R & 15)) * 1024 + lane * 16;
      xv[e2][0] = *(const u32x4*)xr; xv[e2][1] = *(const u32x4*)(xr + 8); }
    get_tag(c);
#pragma unroll
    for (int e2 = 0; e2 < 2; ++e2) { float s = 0.f, qq = 0.f; acc_row(s, qq, xv[e2][0]); acc_row(s, qq, xv[e2][1]);
      s = sum_g(sum_row16(s)) * (1.0f / 1024.0f); qq = sum_g(sum_row16(qq)) * (1.0f / 1024.0f);
      const float rs = __builtin_amdgcn_rsqf(fmaxf(qq - s * s, 0.f) + 1e-5f);
      if (lane == 0) __builtin_amdgcn_raw_buffer_store_b128((u32x4){__float_as_uint(s), c.tag, __float_as_uint(rs), c.tag}, c.ws, OFF_GX + (i1 * 128 + 8 * j1 + 2 * c.wid + e2) * 16, 0, SC_SC1); }
  }
  consumer_loop<S, SB>(c, 0, 16, [&](int sb) { rd(a0, w0, sb, 0); }, [&](int sb) { rd(a1, w1, sb, 1); }, [&]() { mm(a0, w0); }, [&]() { mm(a1, w1); }, []() { interleave<24, 16>(); }, [](int) {}, [&]() { TS(16); });
  TS(1);
  const lds_t xf = lds + L_XF1 + c.wid * 12288 + lane * 16;
#pragma unroll
  for (int m = 0; m < 2; ++m)
#pragma unroll
    for (int n = 0; n < 2; ++n) {
      *(LAS f32x4*)(xf + (m * 6 + n) * 1024) = aq[2 + m][n]; *(LAS f32x4*)(xf + (m * 6 + 2 + n) * 1024) = ak[2 + m][n]; *(LAS f32x4*)(xf + (m * 6 + 4 + n) * 1024) = av[2 + m][n];
      e.q[m][n] = aq[m][n]; e.k[m][n] = ak[m][n]; e.v[m][n] = av[m][n];
    }
  wait_lgkm0();
}

DI void both_g_max(float& a, float& b) {
  auto r = __builtin_amdgcn_permlane16_swap(__float_as_uint(a), __float_as_uint(b), false, false);
  const float x = fmaxf(__uint_as_float(r[0]), __uint_as_float(r[1]));
  auto s = __builtin_amdgcn_permlane32_swap(__float_as_uint(x), __float_as_uint(x), false, false);
  const float y = fmaxf(__uint_as_float(s[0]), __uint_as_float(s[1]));
  auto t = __builtin_amdgcn_permlane16_swap(__float_as_uint(y), __float_as_uint(y), false, false);
  a = __uint_as_float(t[0]); b = __uint_as_float(t[1]);
}
DI void both_g_sum(float& a, float& b) {
  auto r = __builtin_amdgcn_permlane16_swap(__float_as_uint(a), __float_as_uint(b), false, false);
  const float x = __uint_as_float(r[0]) + __uint_as_float(r[1]);
  auto s = __builtin_amdgcn_permlane32_swap(__float_as_uint(x), __float_as_uint(x), false, false);
  const float y = __uint_as_float(s[0]) + __uint_as_float(s[1]);
  auto t = __builtin_amdgcn_permlane16_swap(__float_as_uint(y), __float_as_uint(y), false, false);
  a = __uint_as_float(t[0]); b = __uint_as_float(t[1]);
}
DI void feat(const f32x4& p0, const f32x4& p1, f32x4& e0, f32x4& e1, f32x4& n0, f32x4& n1) {
  constexpr float L2E = 1.4426950408889634f;
  const f32x4 t0 = p0 * L2E, t1 = p1 * L2E;
  float mx = fmaxf(fmaxf(fmaxf(t0[0], t0[1]), t0[2]), fmaxf(fmaxf(fmaxf(t0[3], t1[0]), t1[1]), fmaxf(t1[2], t1[3])));
  float mn = -fminf(fminf(fminf(t0[0], t0[1]), t0[2]), fminf(fminf(fminf(t0[3], t1[0]), t1[1]), fminf(t1[2], t1[3])));
  both_g_max(mx, mn);
  const f32x4 a0 = t0 - mx, a1 = t1 - mx, b0 = -t0 - mn, b1 = -t1 - mn;
#pragma unroll
  for (int r = 0; r < 4; ++r) { e0[r] = __builtin_amdgcn_exp2f(a0[r]); e1[r] = __builtin_amdgcn_exp2f(a1[r]); n0[r] = __builtin_amdgcn_exp2f(b0[r]); n1[r] = __builtin_amdgcn_exp2f(b1[r]); }
  const f32x4 se = e0 + e1, sn4 = n0 + n1;
  float sp = (se[0] + se[1]) + (se[2] + se[3]), sn = (sn4[0] + sn4[1]) + (sn4[2] + sn4[3]);
  both_g_sum(sp, sn);
  sp = __builtin_amdgcn_rcpf(sp); sn = __builtin_amdgcn_rcpf(sn);
  e0 *= sp; e1 *= sp; n0 *= sn; n1 *= sn;
}

DI void p1_epilogue(Ctx& c, const Args& A, const Map& M, Acc1& e, int last) {
  const int lane = opaque(c.lane), cw = c.wid & 3, hf = c.wid >> 2, wr = cw >> 1, wc = cw & 1, g = lane >> 4, q = (lane & 15) >> 2, p = lane & 3, i16 = lane & 15;
  const int lw = cw, i1 = M.i1, j1 = M.j1;
  const lds_t lds = c.lds;
  const f32x4 Z4 = {0.f, 0.f, 0.f, 0.f};
  bar();
  TS(20);
  if (hf) { const lds_t xf = lds + L_XF1 + cw * 12288 + lane * 16;
#pragma unroll
    for (int m = 0; m < 2; ++m)
#pragma unroll
      for (int n = 0; n < 2; ++n) { e.q[m][n] = *(LAS f32x4*)(xf + (m * 6 + n) * 1024); e.k[m][n] = *(LAS f32x4*)(xf + (m * 6 + 2 + n) * 1024); e.v[m][n] = *(LAS f32x4*)(xf + (m * 6 + 4 + n) * 1024); } }
  const lds_t tab = lds + L_TAB1;
  bf16x8 qf[2], kf[2], vf[2], wT[2];
  {
    f32x4 vv[2][2];
#pragma unroll
    for (int m = 0; m < 2; ++m) {
      const int R = 64 * wr + 32 * hf + 16 * m;
      const float mu = *(LAS float*)(tab + (R + i16) * 4), rs = *(LAS float*)(tab + 512 + (R + i16) * 4), nm = -mu * rs;
      const f32x4 mu4 = *(LAS f32x4*)(tab + (R + 4 * g) * 4), rs4 = *(LAS f32x4*)(tab + 512 + (R + 4 * g) * 4), nm4 = -mu4 * rs4;
      f32x4 qq[2], kk[2];
#pragma unroll
      for (int n = 0; n < 2; ++n) {
        const f32x4 cq = *(LAS f32x4*)(tab + 1024 + (32 * wc + 16 * n + 4 * g) * 4), ck = *(LAS f32x4*)(tab + 1024 + (64 + 32 * wc + 16 * n + 4 * g) * 4);
        const float cv = *(LAS float*)(tab + 1024 + (128 + 32 * wc + 16 * n + i16) * 4);
        qq[n] = e.q[m][n] * rs + cq * nm; kk[n] = e.k[m][n] * rs + ck * nm; vv[m][n] = e.v[m][n] * rs4 + nm4 * cv;
      }
      qf[m] = pk8(qq[0], qq[1]); kf[m] = pk8(kk[0], kk[1]);
    }
    vf[0] = pk8(vv[0][0], vv[1][0]); vf[1] = pk8(vv[0][1], vv[1][1]);
#pragma unroll
    for (int ft = 0; ft < 2; ++ft) wT[ft] = ldsT(lds + L_WPHI + wc * 2048 + (4 * g + q) * 64 + ft * 32 + 8 * p, 16 * 64);
  }
  wait_lgkm0(); bar();
  TS(21);
  {
    const lds_t tb = lds + L_TB + c.wid * 4096;
#pragma unroll
    for (int m = 0; m < 2; ++m) {
      const f32x4 p0 = mfma(wT[0], kf[m], Z4), p1 = mfma(wT[1], kf[m], Z4);
      f32x4 e0, e1, n0, n1; feat(p0, p1, e0, e1, n0, n1);
      const lds_t rw = tb + (16 * m + i16) * 128;
      *(LAS u32x2*)(rw + 8 * ((0 + g) ^ i16)) = pk4(e0); *(LAS u32x2*)(rw + 8 * ((4 + g) ^ i16)) = pk4(e1);
      *(LAS u32x2*)(rw + 8 * ((8 + g) ^ i16)) = pk4(n0); *(LAS u32x2*)(rw + 8 * ((12 + g) ^ i16)) = pk4(n1);
    }
    asm volatile("s_waitcnt lgkmcnt(0)" ::: "memory");
    const bf16x8 ones = __builtin_bit_cast(bf16x8, (u32x4){ONES2, ONES2, ONES2, ONES2});
    const lds_t kr = lds + L_KR + c.wid * 8192 + lane * 16;
    float zf[4];
#pragma unroll
    for (int f = 0; f < 4; ++f) {
      const int r0 = 4 * g + q;
      const s16x4 lo = __builtin_amdgcn_ds_read_tr16_b64_v4i16((LAS s16x4*)(tb + r0 * 128 + 8 * ((4 * f + p) ^ r0)));
      const s16x4 hi = __builtin_amdgcn_ds_read_tr16_b64_v4i16((LAS s16x4*)(tb + (16 + r0) * 128 + 8 * ((4 * f + p) ^ r0)));
      const bf16x8 pf = __builtin_shufflevector(lo, hi, 0, 1, 2, 3, 4, 5, 6, 7);
      *(LAS f32x4*)(kr + f * 1024) = mfma(vf[0], pf, Z4); *(LAS f32x4*)(kr + (4 + f) * 1024) = mfma(vf[1], pf, Z4);
      zf[f] = mfma(ones, pf, Z4)[0];
    }
    *(LAS float*)(lds + L_ZR + c.wid * 256 + lane * 4) = g == 0 ? zf[0] : g == 1 ? zf[1] : g == 2 ? zf[2] : zf[3];
    TS(22);
    wait_lgkm0(); bar();
    f32x4 k0 = Z4, k1 = Z4;
#pragma unroll
    for (int w = 0; w < 4; ++w) { const lds_t s = lds + L_KR + (wc + 2 * w) * 8192 + (4 * wr + 2 * hf) * 1024 + lane * 16; k0 += *(LAS f32x4*)s; k1 += *(LAS f32x4*)(s + 1024); }
    st16(c, pk8(k0, k1), lane * 16 + (OFF_KVP + (j1 * 16 + i1) * 8192 + wc * 4096 + wr * 2048 + hf * 1024));
    if (c.wid < 2) { float zt = 0.f;
#pragma unroll
      for (int w = 0; w < 4; ++w) zt += *(LAS float*)(lds + L_ZR + (wc + 2 * w) * 256 + lane * 4);
      __builtin_amdgcn_raw_buffer_store_b32(__float_as_uint(zt), c.ws, lane * 4, OFF_ZP + ((j1 * 16 + i1) * 2 + wc) * 256, SC_SC1); }
    wait_lgkm0(); bar();
    TS(23);
    publish<8>(c, 0, FG_KV, j1, i1);
  }
  TS(2);
  const __amdgpu_buffer_rsrc_t rW2 = __builtin_amdgcn_make_buffer_rsrc((void*)A.wo, 0, 1024 * 2048, 0x00020000);
  int voW2[2];
#pragma unroll
  for (int i = 0; i < 2; ++i) { const int r = (2 * lw + i) * 8 + (lane >> 3), s16 = lane & 7; voW2[i] = r * 2048 + M.j2 * 128 + (((s16 >> 1) ^ ((r >> 1) & 3)) << 5) + (s16 & 1) * 16; }
  auto issueW2 = [&](int t) {
#pragma unroll
    for (int i = 0; i < 2; ++i) dma<0>(rW2, lds + t * 8192 + (lw * 2 + i) * 1024, voW2[i], t * 64 * 2048); };
  bf16x8 ph[2][2];
#pragma unroll
  for (int m = 0; m < 2; ++m) {
    const f32x4 p0 = mfma(wT[0], qf[m], Z4), p1 = mfma(wT[1], qf[m], Z4);
    f32x4 e0, e1, n0, n1; feat(p0, p1, e0, e1, n0, n1);
    ph[m][0] = pk8(e0, e1); ph[m][1] = pk8(n0, n1);
  }
  asm volatile("" : "+v"(ph[0][0]), "+v"(ph[0][1]), "+v"(ph[1][0]), "+v"(ph[1][1]));
  TS(3);
  if (hf) {
    if (M.act3) {
      const __amdgpu_buffer_rsrc_t rW3 = __builtin_amdgcn_make_buffer_rsrc((void*)A.win, 0, 1024 * 7168, 0x00020000);
#pragma unroll
      for (int i = 0; i < 8; ++i) { const int r = (8 * lw + i) * 2 + (lane >> 5), l5 = lane & 31, seg = l5 >> 4, s16 = l5 & 15;
        dma<0>(rW3, lds + 32768 + (8 * lw + i) * 1024, r * 7168 + (seg == 0 ? 3584 : 0) + M.j3 * 256 + (((s16 >> 1) ^ (r & 7)) << 5) + (s16 & 1) * 16, M.i3 * 64 * 7168); }
    }
#pragma unroll
    for (int t = 0; t < 3; ++t) { issueW2(t); issueW2(8 + t); }
  } else if (c.wid == 0) wait_flags(c, FG_KV, j1, 16);
  bar();
  TS(27);
  kv_gather(c, j1);
  bar();
  TS(4);
  {
    bf16x8 kt[3][2];
#pragma unroll
    for (int d = 0; d < 3; ++d)
#pragma unroll
      for (int s = 0; s < 2; ++s) kt[d][s] = ldsT(lds + L_XBUF + wc * 8192 + (32 * s + 4 * g + q) * 128 + 32 * d + 8 * p, 16 * 128);
#pragma unroll
    for (int m = 0; m < 2; ++m) {
      const f32x4 n0 = mfma(kt[0][1], ph[m][1], mfma(kt[0][0], ph[m][0], Z4)), n1 = mfma(kt[1][1], ph[m][1], mfma(kt[1][0], ph[m][0], Z4));
      const f32x4 dn = mfma(kt[2][1], ph[m][1], mfma(kt[2][0], ph[m][0], Z4));
      const float r = __builtin_amdgcn_rcpf(dn[0] + 1e-6f);
      st16(c, pk8(n0 * r, n1 * r), lane * 16 + (OFF_OF + (i1 * 16 + j1) * 16384 + ((4 * wr + 2 * hf + m) * 2 + wc) * 1024));
    }
  }
  if (hf && M.act3) {
    u32x2 cr[4][4];
#pragma unroll
    for (int n = 0; n < 4; ++n) tr4_issue<16 * 512, 32 * 512>((unsigned)(size_t)(lds + 32768 + (4 * g + q) * 512 + 8 * p + (lw >> 1) * 256 + 32 * ((4 * (lw & 1) + n) ^ (4 * (g & 1) + q))), cr[n]);
#pragma unroll
    for (int n = 0; n < 4; ++n) { tr4_wait(cr[n]); const float s = col_sum(cr[n]);
      if (lane < 16) st_granule(c, s, OFF_GC3 + (M.j3 * 16 + M.i3) * 2048 + ((4 * lw + n) * 16 + lane) * 8); }
  }
  bar();
  publish<8>(c, 1, FG_O, i1, j1);
  TS(5);
  if (hf) {
#pragma unroll
    for (int t = 3; t < 8; ++t) { issueW2(t); issueW2(8 + t); }
  } else if (c.wid == 0 && last >= 2) wait_flags(c, FG_O, M.i2, 16);
}

struct Ld3 { __amdgpu_buffer_rsrc_t rW; int voW[8]; };
DI void p3_setup(const Ctx& c, const Args& A, int j3, Ld3& L) {
  const int lane = opaque(c.lane), lw = c.wid & 3;
  L.rW = __builtin_amdgcn_make_buffer_rsrc((void*)A.win, 0, 1024 * 7168, 0x00020000);
#pragma unroll
  for (int i = 0; i < 8; ++i) { const int r = (8 * lw + i) * 2 + (lane >> 5), l5 = lane & 31, seg = l5 >> 4, s16 = l5 & 15;
    L.voW[i] = r * 7168 + (seg == 0 ? 3584 : 0) + j3 * 256 + (((s16 >> 1) ^ (r & 7)) << 5) + (s16 & 1) * 16; }
}
DI void p3_issueW(const Ctx& c, const Ld3& L, int t) { const lds_t d = c.lds + (t % 3) * 49152 + 16384 + (c.wid & 3) * 8192;
#pragma unroll
  for (int i = 0; i < 8; ++i) dma<0>(L.rW, d + i * 1024, L.voW[i], t * 64 * 7168); }
DI void p3_load(Ctx& c, const Ld3& L, int i3) {
  const int lane = opaque(c.lane), lw = c.wid - 4;
  const lds_t lds = c.lds;
  constexpr int S = 3, SB = 49152;
  const int voF = lane * 16 + lw * 1024;
  auto issueA = [&](int t) { const lds_t d = lds + (t % S) * SB + lw * 1024; const int so = OFF_HF + (i3 * 16 + t) * 16384;
#pragma unroll
    for (int i = 0; i < 4; ++i) dma<LDAUX>(c.ws, d + i * 4096, voF, so + i * 4096); };
  auto issueW = [&](int t) { p3_issueW(c, L, t); };
  bar();
  issueA(0); issueA(1);
  u32x2 hv[16]; const int R = (lw & 1) * 64 + lane;
  auto gather = [&](int k) { if (lw < 2) {
      hv[2 * k] = __builtin_amdgcn_raw_buffer_load_b64(c.ws, R * 8, OFF_STH + (i3 * 16 + 2 * k) * 1024, SC_SC1);
      hv[2 * k + 1] = __builtin_amdgcn_raw_buffer_load_b64(c.ws, R * 8, OFF_STH + (i3 * 16 + 2 * k + 1) * 1024, SC_SC1); } };
  auto fin = [&]() { if (lw < 2) { float s = 0.f, qq = 0.f;
#pragma unroll
      for (int k = 0; k < 16; ++k) { s += __uint_as_float(hv[k][0]); qq += __uint_as_float(hv[k][1]); }
      s *= (1.0f / 1024.0f); qq *= (1.0f / 1024.0f);
      *(LAS float*)(lds + L_TAB3 + R * 4) = s; *(LAS float*)(lds + L_TAB3 + 512 + R * 4) = __builtin_amdgcn_rsqf(fmaxf(qq - s * s, 0.f) + 1e-5f);
      wait_lgkm0(); } };
  loader_loop16<8, true>(lw < 2, false, issueA, issueW, [](int) {}, gather, fin);
  TS(6);
}
struct Acc3 { f32x4 z2[2][4], z1[2][4]; };
DI int xf3_base(int cw) { return cw < 3 ? 16384 + cw * 32768 : 98304; }
DI void p3_mma(Ctx& c, int i3, int j3, Acc3& e) {
  const int lane = opaque(c.lane), wr = c.wid >> 1, wc = c.wid & 1, g = lane >> 4, q = (lane & 15) >> 2, p = lane & 3;
  const lds_t lds = c.lds;
  const __amdgpu_buffer_rsrc_t rWS = c.ws;
  const f32x4 Z4 = {0.f, 0.f, 0.f, 0.f};
  constexpr int S = 3, SB = 49152;
  if (wc == 1) {
    const int off = OFF_GC3 + j3 * 16 * 2048 + (128 * wr + 2 * lane) * 8; u32x4 gv[16];
    const unsigned long long t0 = __builtin_amdgcn_s_memrealtime();
    for (;;) {
      bool ok = true;
#pragma unroll
      for (int k = 0; k < 16; ++k) gv[k] = __builtin_amdgcn_raw_buffer_load_b128(rWS, off + k * 2048, 0, SC_SC1);
#pragma unroll
      for (int k = 0; k < 16; ++k) ok &= gv[k][1] == c.tag && gv[k][3] == c.tag;
      if (__all(ok) || timed_out(c, t0, 0x202u)) break;
      __builtin_amdgcn_s_sleep(2);
    }
    f32x2 cs = {0.f, 0.f};
#pragma unroll
    for (int k = 0; k < 16; ++k) { cs[0] += __uint_as_float(gv[k][0]); cs[1] += __uint_as_float(gv[k][2]); }
    *(LAS f32x2*)(lds + L_TAB3 + 1024 + (128 * wr + 2 * lane) * 4) = cs;
    wait_lgkm0();
  } else if (c.wid == 0) wait_flags(c, FG_H, i3, 16);
  f32x4 a2[4][4], a1_[4][4];
#pragma unroll
  for (int m = 0; m < 4; ++m)
#pragma unroll
    for (int n = 0; n < 4; ++n) { a2[m][n] = Z4; a1_[m][n] = Z4; }
#pragma unroll
  for (int m = 0; m < 4; ++m) asm volatile("" : "+v"(a2[m][0]), "+v"(a2[m][1]), "+v"(a2[m][2]), "+v"(a2[m][3]), "+v"(a1_[m][0]), "+v"(a1_[m][1]), "+v"(a1_[m][2]), "+v"(a1_[m][3]));
  bar();
  const int ao2 = wr * 8192 + lane * 16;
  int wo_[4];
#pragma unroll
  for (int n = 0; n < 4; ++n) wo_[n] = 16384 + (4 * g + q) * 512 + 8 * p + 32 * ((4 * wc + n) ^ (4 * (g & 1) + q));
  bf16x8 a0[4], w0[8], a1[4], w1[8];
  auto rd = [&](bf16x8(&a)[4], bf16x8(&w)[8], int sb, int ks) { const lds_t s = lds + sb;
#pragma unroll
    for (int n = 0; n < 8; ++n) w[n] = ldsT(s + wo_[n & 3] + (n >> 2) * 256 + ks * 32 * 512, 16 * 512);
#pragma unroll
    for (int m = 0; m < 4; ++m) a[m] = ldsA(s + ao2 + m * 2048 + ks * 1024); };
  auto mm = [&](bf16x8(&a)[4], bf16x8(&w)[8]) {
#pragma unroll
    for (int n = 0; n < 4; ++n)
#pragma unroll
      for (int m = 0; m < 4; ++m) a2[m][n] = mfma(a[m], w[n], a2[m][n]);
#pragma unroll
    for (int n = 0; n < 4; ++n)
#pragma unroll
      for (int m = 0; m < 4; ++m) a1_[m][n] = mfma(w[4 + n], a[m], a1_[m][n]); };
  consumer_loop<S, SB>(c, 16, 16, [&](int sb) { rd(a0, w0, sb, 0); }, [&](int sb) { rd(a1, w1, sb, 1); }, [&]() { mm(a0, w0); }, [&]() { mm(a1, w1); }, []() { interleave<32, 20>(); }, [](int) {}, [&]() { TS(17); });
  TS(8);
  const lds_t xf = lds + xf3_base(c.wid) + lane * 16;
#pragma unroll
  for (int m = 0; m < 2; ++m)
#pragma unroll
    for (int n = 0; n < 4; ++n) { *(LAS f32x4*)(xf + (m * 8 + n) * 1024) = a2[2 + m][n]; *(LAS f32x4*)(xf + (m * 8 + 4 + n) * 1024) = a1_[2 + m][n]; e.z2[m][n] = a2[m][n]; e.z1[m][n] = a1_[m][n]; }
  wait_lgkm0();
}
struct Ld4 { __amdgpu_buffer_rsrc_t rA; int voA, voF; };
DI void p4_issueW(const Ctx& c, const Ld4& L, int t) { const int lw = c.wid - 4; const lds_t d = c.lds + (t & 3) * 32768 + lw * 1024;
#pragma unroll
  for (int i = 0; i < 4; ++i) dma<0>(L.rA, d + i * 4096, L.voA, t * 128 + i * 32 * 4096); }
DI void p3_epilogue(Ctx& c, const Ld4& L, int i3, int j3, Acc3& e, u32x2 (&z1)[2][4]) {
  const int lane = opaque(c.lane), cw = c.wid & 3, hf = c.wid >> 2, wr = cw >> 1, wc = cw & 1, g = lane >> 4, i16 = lane & 15;
  const lds_t lds = c.lds, tab = lds + L_TAB3;
  const __amdgpu_buffer_rsrc_t rWS = c.ws;
  const f32x4 Z4 = {0.f, 0.f, 0.f, 0.f};
  bar();
  TS(25);
  if (hf) { const lds_t xf = lds + xf3_base(cw) + lane * 16;
#pragma unroll
    for (int m = 0; m < 2; ++m)
#pragma unroll
      for (int n = 0; n < 4; ++n) { e.z2[m][n] = *(LAS f32x4*)(xf + (m * 8 + n) * 1024); e.z1[m][n] = *(LAS f32x4*)(xf + (m * 8 + 4 + n) * 1024); } }
  {
    float cs[4];
#pragma unroll
    for (int n = 0; n < 4; ++n) cs[n] = *(LAS float*)(tab + 1024 + (64 * wc + 16 * n + i16) * 4);
#pragma unroll
    for (int m = 0; m < 2; ++m) {
      const int R = 64 * wr + 32 * hf + 16 * m + 4 * g;
      const f32x4 mu = *(LAS f32x4*)(tab + R * 4), rs = *(LAS f32x4*)(tab + 512 + R * 4), nm = -mu * rs;
      f32x4 s = Z4, qq = Z4;
#pragma unroll
      for (int n = 0; n < 4; ++n) { const f32x4 y = gelu4(e.z2[m][n] * rs + nm * cs[n]); e.z2[m][n] = y; s += y; qq += y * y; }
      sum_row16x8(s, qq);
      if (i16 < 4) { const float sv = i16 == 0 ? s[0] : i16 == 1 ? s[1] : i16 == 2 ? s[2] : s[3], qv = i16 == 0 ? qq[0] : i16 == 1 ? qq[1] : i16 == 2 ? qq[2] : qq[3];
        *(LAS f32x2*)(lds + L_PST + wc * 1024 + (R + i16) * 8) = (f32x2){sv, qv}; }
    }
  }
  TS(26);
  wait_lgkm0(); bar();
  if (!hf) {
    if (lane < 32) { const int R = c.wid * 32 + lane; const f32x2 v = *(LAS f32x2*)(lds + L_PST + R * 8) + *(LAS f32x2*)(lds + L_PST + 1024 + R * 8);
      __builtin_amdgcn_raw_buffer_store_b64(__builtin_bit_cast(u32x2, v), rWS, R * 8, OFF_ST2 + (i3 * 14 + j3) * 1024, SC_SC1); }
    publish<4>(c, 3, FG_ST, i3, j3);
  } else {
#pragma unroll
    for (int s = 0; s < 3; ++s) p4_issueW(c, L, s);
  }
  TS(9);
#pragma unroll
  for (int m = 0; m < 2; ++m) asm volatile("" : "+v"(e.z1[m][0]), "+v"(e.z1[m][1]), "+v"(e.z1[m][2]), "+v"(e.z1[m][3]));
#pragma unroll
  for (int m = 0; m < 2; ++m) {
    const int R = 64 * wr + 32 * hf + 16 * m + i16;
    const float mu = *(LAS float*)(tab + R * 4), rs = *(LAS float*)(tab + 512 + R * 4), nm = -mu * rs;
#pragma unroll
    for (int n = 0; n < 4; ++n) { const f32x4 cs = *(LAS f32x4*)(tab + 1024 + (128 + 64 * wc + 16 * n + 4 * g) * 4); z1[m][n] = pk4(gelu4(e.z1[m][n] * rs + cs * nm)); }
    asm volatile("" : "+v"(z1[m][0]), "+v"(z1[m][1]), "+v"(z1[m][2]), "+v"(z1[m][3]));
  }
  TS(10);
  if (c.wid == 0) wait_flags(c, FG_ST, i3, 14);
  bar();
  TS(30);
  if (c.wid < 2) {
    const int R = c.wid * 64 + lane; float s = 0.f, qq = 0.f; u32x2 v[14];
#pragma unroll
    for (int k = 0; k < 14; ++k) v[k] = __builtin_amdgcn_raw_buffer_load_b64(rWS, R * 8, OFF_ST2 + (i3 * 14 + k) * 1024, SC_SC1);
#pragma unroll
    for (int k = 0; k < 14; ++k) { s += __uint_as_float(v[k][0]); qq += __uint_as_float(v[k][1]); }
    s *= (1.0f / 1792.0f); qq *= (1.0f / 1792.0f);
    *(LAS float*)(lds + L_FIN + R * 4) = s; *(LAS float*)(lds + L_FIN + 512 + R * 4) = __builtin_amdgcn_rsqf(fmaxf(qq - s * s, 0.f) + 1e-5f);
    wait_lgkm0();
  }
  bar();
  TS(31);
  {
    f32x4 rs[2], nm[2];
#pragma unroll
    for (int m = 0; m < 2; ++m) { const int R = 64 * wr + 32 * hf + 16 * m + 4 * g; rs[m] = *(LAS f32x4*)(lds + L_FIN + 512 + R * 4); nm[m] = -*(LAS f32x4*)(lds + L_FIN + R * 4) * rs[m]; }
#pragma unroll
    for (int n = 0; n < 4; ++n)
      st16(c, pk8(e.z2[0][n] * rs[0] + nm[0], e.z2[1][n] * rs[1] + nm[1]), lane * 16 + (OFF_Z2 + (j3 * 32 + 2 * i3 + wr) * 16384 + ((4 * wc + n) * 2 + hf) * 1024));
  }
  publish<8>(c, 4, FG_Z2, j3, i3);
  TS(11);
}
DI void p4_load(Ctx& c, const Ld4& L, int j3) {
  const int lw = c.wid - 4;
  auto issueW = [&](int t) { p4_issueW(c, L, t); };
  auto issueA = [&](int t) { const lds_t d = c.lds + (t & 3) * 32768 + 16384 + lw * 1024; const int so = OFF_Z2 + (j3 * 32 + t) * 16384;
#pragma unroll
    for (int i = 0; i < 4; ++i) dma<LDAUX>(c.ws, d + i * 4096, L.voF, so + i * 4096); };
  bar();
  if (c.b == 0 && lw == 0 && c.lane == 0) __hip_atomic_store((unsigned*)(c.wsp + OFF_EPOCH), c.tag, __ATOMIC_RELAXED, __HIP_MEMORY_SCOPE_AGENT);
#pragma unroll
  for (int s = 0; s < 3; ++s) issueA(s);
  loader_loop<4, 4>(32, true, issueA, issueW, [](int) {}, [](int) {});
}
DI void p4_mma(Ctx& c, int j3, f32x4 (&e)[2][4]) {
  const int lane = opaque(c.lane), wr = c.wid >> 1, wc = c.wid & 1, g = lane >> 4, i16 = lane & 15;
  const lds_t lds = c.lds;
  constexpr int S = 4, SB = 32768;
  f32x4 acc[4][4];
#pragma unroll
  for (int m = 0; m < 4; ++m)
#pragma unroll
    for (int n = 0; n < 4; ++n) { acc[m][n] = (f32x4){0.f, 0.f, 0.f, 0.f}; asm volatile("" : "+v"(acc[m][n])); }
  const int ao0 = (wr * 64 + i16) * 128 + ((g ^ (i16 >> 1)) << 4), ao1 = ao0 ^ 64, zo = 16384 + wc * 8192 + lane * 16;
  bf16x8 a0[4], w0[4], a1[4], w1[4];
  auto rd = [&](bf16x8(&a)[4], bf16x8(&w)[4], int sb, int ks) { const lds_t s = lds + sb;
#pragma unroll
    for (int n = 0; n < 4; ++n) { w[n] = ldsA(s + zo + n * 2048 + ks * 1024); a[n] = ldsA(s + (ks ? ao1 : ao0) + n * 2048); } };
  auto mm = [&](bf16x8(&a)[4], bf16x8(&w)[4]) {
#pragma unroll
    for (int n = 0; n < 4; ++n)
#pragma unroll
      for (int m = 0; m < 4; ++m) acc[m][n] = mfma(w[n], a[m], acc[m][n]); };
  if (c.wid == 0) wait_flags(c, FG_Z2, j3, 16);
  bar();
  consumer_loop<S, SB>(c, 32, 32, [&](int sb) { rd(a0, w0, sb, 0); }, [&](int sb) { rd(a1, w1, sb, 1); }, [&]() { mm(a0, w0); }, [&]() { mm(a1, w1); }, []() { interleave<16, 8>(); }, [](int) {}, [&]() { TS(18); });
  TS(12);
  const lds_t xf = lds + c.wid * 8192 + lane * 16;
#pragma unroll
  for (int m = 0; m < 2; ++m)
#pragma unroll
    for (int n = 0; n < 4; ++n) { *(LAS f32x4*)(xf + (m * 4 + n) * 1024) = acc[2 + m][n]; e[m][n] = acc[m][n]; }
  wait_lgkm0();
}
DI void p5_issueW(const Ctx& c, const Args& A, int j2);
DI void p4_epilogue(Ctx& c, const Args& A, int i3, int j3, int j2, f32x4 (&e)[2][4], const u32x2 (&z1)[2][4]) {
  const int lane = opaque(c.lane), cw = c.wid & 3, hf = c.wid >> 2, wr = cw >> 1, wc = cw & 1, i16 = lane & 15;
  float bs[2];
#pragma unroll
  for (int m = 0; m < 2; ++m) bs[m] = bflo((unsigned)A.bsp[i3 * 128 + true_row(4 * wr + 2 * hf + m, i16)]);
  bar();
  if (hf) { const lds_t xf = c.lds + cw * 8192 + lane * 16;
#pragma unroll
    for (int m = 0; m < 2; ++m)
#pragma unroll
      for (int n = 0; n < 4; ++n) e[m][n] = *(LAS f32x4*)(xf + (m * 4 + n) * 1024); }
#pragma unroll
  for (int m = 0; m < 2; ++m)
#pragma unroll
    for (int u = 0; u < 2; ++u)
      st16(c, pk8(up4(z1[m][2 * u]) * (e[m][2 * u] + bs[m]), up4(z1[m][2 * u + 1]) * (e[m][2 * u + 1] + bs[m])),
           lane * 16 + (OFF_UF + (i3 * 28 + 2 * j3 + wc) * 16384 + ((4 * wr + 2 * hf + m) * 2 + u) * 1024));
  wait_lgkm0(); bar();
  publish<8>(c, 5, FG_U, i3, j3);
  if (hf) p5_issueW(c, A, j2);
  TS(13);
}

DI void p5_issueW(const Ctx& c, const Args& A, int j2) {
  const int lane = opaque(c.lane), lw = c.wid - 4;
  const __amdgpu_buffer_rsrc_t rW = __builtin_amdgcn_make_buffer_rsrc((void*)A.wout, 0, 1792 * 2048, 0x00020000);
  int vo[2];
#pragma unroll
  for (int i = 0; i < 2; ++i) { const int r = (2 * lw + i) * 8 + (lane >> 3), s16 = lane & 7; vo[i] = r * 2048 + j2 * 128 + (((s16 >> 1) ^ ((r >> 1) & 3)) << 5) + (s16 & 1) * 16; }
#pragma unroll
  for (int t = 0; t < 18; ++t)
#pragma unroll
    for (int i = 0; i < 2; ++i) dma<0>(rW, c.lds + ((t & 1) * 9 + (t >> 1)) * 8192 + (lw * 2 + i) * 1024, vo[i], ((t & 1) * 14 + (t >> 1)) * 64 * 2048);
}

constexpr int L_RED = 98304;
#define STG1 0
#define STG2 2
#define R5 6
#define R2 6
template <int NSH, int R, bool REFILL>
DI void gemm_split(Ctx& c, f32x4 (&res)[4], int aoff, __amdgpu_buffer_rsrc_t rW, int j2) {
  const int ln = opaque(c.lane), g = ln >> 4, q = (ln & 15) >> 2, p = ln & 3, rw = c.wid & 3, kh = c.wid >> 2;
  const lds_t lds = c.lds;
  const int sw2 = (2 * g + (q >> 1)) & 3, wl2 = (4 * g + q) * 128 + 8 * p + kh * (REFILL ? 9 : 8) * 8192, va = ln * 16 + rw * 4096;
  const int ao = aoff + kh * (NSH / 2) * 16384;
  int wo2[4];
#pragma unroll
  for (int n = 0; n < 4; ++n) wo2[n] = wl2 + 32 * (n ^ sw2);
  f32x4 acc[2][4];
#pragma unroll
  for (int n = 0; n < 4; ++n) { acc[0][n] = (f32x4){0.f, 0.f, 0.f, 0.f}; acc[1][n] = acc[0][n]; }
  u32x4 a[R][2]; bf16x8 w0[4], w1[4];
  auto lda = [&](int s) {
#pragma unroll
    for (int m = 0; m < 2; ++m) a[s % R][m] = __builtin_amdgcn_raw_buffer_load_b128(c.ws, va + m * 2048, ao + (s >> 1) * 16384 + (s & 1) * 1024, LDAUX); };
  auto ldw = [&](bf16x8(&d)[4], int s) { const int lt = s >> 1, slot = (REFILL && lt >= 9) ? lt - 9 : lt;
#pragma unroll
    for (int n = 0; n < 4; ++n) d[n] = ldsT(lds + wo2[n] + slot * 8192 + (s & 1) * 4096, 16 * 128); };
  TS(REFILL ? 19 : 24);
#pragma unroll
  for (int s = 0; s < R; ++s) { lda(s); if (s == STG1 || s == STG2) bar(); }
  ldw(w0, 0);
#pragma unroll
  for (int s = 0; s < NSH; ++s) {
    if (s == 5) {
      if (kh) wait_vm<2 * R + 10>();
      bar(); __builtin_amdgcn_sched_barrier(0);
    }
    if (REFILL && s == 10) {
      wait_lgkm0(); bar();
      const int r = 8 * c.wid + (ln >> 3), s16 = ln & 7, vo = r * 2048 + j2 * 128 + (((s16 >> 1) ^ ((r >> 1) & 3)) << 5) + (s16 & 1) * 16;
#pragma unroll
      for (int k = 0; k < 10; ++k) dma<0>(rW, lds + ((k / 5) * 9 + (k % 5)) * 8192 + c.wid * 1024, vo, ((k / 5) * 14 + 9 + (k % 5)) * 64 * 2048);
      __builtin_amdgcn_sched_barrier(0);
    }
    if (REFILL && s == 17) { wait_vm<2 * ((NSH - R < 17 ? NSH - R : 17) - 10)>(); wait_lgkm0(); bar(); __builtin_amdgcn_sched_barrier(0); }
    if (s + 1 < NSH) { if (s & 1) ldw(w0, s + 1); else ldw(w1, s + 1); }
#pragma unroll
    for (int n = 0; n < 4; ++n)
#pragma unroll
      for (int m = 0; m < 2; ++m) acc[m][n] = mfma((s & 1) ? w1[n] : w0[n], __builtin_bit_cast(bf16x8, a[s % R][m]), acc[m][n]);
    if (s + R < NSH) lda(s + R);
    interleave<8, 8>();
    __builtin_amdgcn_sched_barrier(0);
  }
  TS(REFILL ? 29 : 28);
  wait_lgkm0(); bar();
#pragma unroll
  for (int n = 0; n < 4; ++n) *(LAS f32x4*)(lds + L_RED + c.wid * 4096 + n * 1024 + ln * 16) = kh ? acc[0][n] : acc[1][n];
  wait_lgkm0(); bar();
#pragma unroll
  for (int n = 0; n < 4; ++n) res[n] = (kh ? acc[1][n] : acc[0][n]) + *(LAS f32x4*)(lds + L_RED + (c.wid ^ 4) * 4096 + n * 1024 + ln * 16);
}

template <class PRE> DI void phase2(Ctx& c, const Args& A, int i2, int j2, bool pre_on, PRE pre) {
  const int lane = opaque(c.lane), g = lane >> 4, i16 = lane & 15, mt = 2 * (c.wid & 3) + (c.wid >> 2);
  u32x2 xr[4];
#pragma unroll
  for (int n = 0; n < 4; ++n) xr[n] = *(const u32x2*)(A.x + (size_t)(i2 * 128 + true_row(mt, i16)) * 1024 + j2 * 64 + 16 * n + 4 * g);
  bar();
  f32x4 acc[4];
  gemm_split<16, R2, false>(c, acc, OFF_OF + i2 * 262144, c.ws, j2);
  TS(6);
  float s = 0.f, qq = 0.f;
#pragma unroll
  for (int u = 0; u < 2; ++u) {
    f32x4 h0 = acc[2 * u], h1 = acc[2 * u + 1]; const u32x2 x0 = xr[2 * u], x1 = xr[2 * u + 1];
    h0[0] += bflo(x0[0]); h0[1] += bfhi(x0[0]); h0[2] += bflo(x0[1]); h0[3] += bfhi(x0[1]);
    h1[0] += bflo(x1[0]); h1[1] += bfhi(x1[0]); h1[2] += bflo(x1[1]); h1[3] += bfhi(x1[1]);
    const bf16x8 hb = pk8(h0, h1);
    st16(c, hb, lane * 16 + (OFF_HF + (i2 * 16 + j2) * 16384 + (mt * 2 + u) * 1024));
    acc_row(s, qq, __builtin_bit_cast(u32x4, hb));
  }
  s = sum_g(s); qq = sum_g(qq);
  if (lane < 16) __builtin_amdgcn_raw_buffer_store_b64((u32x2){__float_as_uint(s), __float_as_uint(qq)}, c.ws, OFF_STH + (i2 * 16 + j2) * 1024 + (mt * 16 + lane) * 8, 0, SC_SC1);
  publish<8>(c, 2, FG_H, i2, j2);
  if (pre_on) pre();
  TS(7);
}

DI void phase5(Ctx& c, const Args& A, int i2, int j2) {
  const int lane = opaque(c.lane), g = lane >> 4, i16 = lane & 15, mt = 2 * (c.wid & 3) + (c.wid >> 2);
  u32x4 hr[2];
#pragma unroll
  for (int u = 0; u < 2; ++u) hr[u] = __builtin_amdgcn_raw_buffer_load_b128(c.ws, lane * 16, OFF_HF + (i2 * 16 + j2) * 16384 + (mt * 2 + u) * 1024, SC_SC1);
  const __amdgpu_buffer_rsrc_t rW = __builtin_amdgcn_make_buffer_rsrc((void*)A.wout, 0, 1792 * 2048, 0x00020000);
  if (c.wid == 0) wait_flags(c, FG_U, i2, 14);
  if (c.wid >= 4) wait_vm<24>();
  bar();
  f32x4 acc[4];
  gemm_split<28, R5, true>(c, acc, OFF_UF + i2 * 458752, rW, j2);
  TS(14);
  u16* o = A.out + (size_t)(i2 * 128 + true_row(mt, i16)) * 1024 + j2 * 64 + 4 * g;
#pragma unroll
  for (int n = 0; n < 4; ++n) { const unsigned r0 = hr[n >> 1][2 * (n & 1)], r1 = hr[n >> 1][2 * (n & 1) + 1];
    *(u32x2*)(o + 16 * n) = (u32x2){pk2(acc[n][0] + bflo(r0), acc[n][1] + bfhi(r0)), pk2(acc[n][2] + bflo(r1), acc[n][3] + bfhi(r1))}; }
  TS(15);
}

#define LAST_PHASE 5
__global__ __launch_bounds__(512, 2) void fused_fwd(const u16* __restrict__ x, const u16* __restrict__ wqkv, const u16* __restrict__ wphi, const u16* __restrict__ wo,
                                                      const u16* __restrict__ win, const u16* __restrict__ wsp, const u16* __restrict__ bsp, const u16* __restrict__ wout,
                                                      u16* __restrict__ out, char* __restrict__ ws) {
  extern __shared__ __attribute__((aligned(16))) char shm[];
  Ctx c;
  c.lds = (lds_t)shm; c.wsp = ws; c.lane = threadIdx.x & 63; c.wid = __builtin_amdgcn_readfirstlane(threadIdx.x >> 6); c.b = blockIdx.x;
  c.ws = __builtin_amdgcn_make_buffer_rsrc((void*)ws, 0, WS_BYTES, 0x00020000);
  c.tag = 0u; c.ts = 0u;
  if (threadIdx.x < 16) *(LAS unsigned*)(c.lds + L_CNT + threadIdx.x * 4) = 0u;
  const int xcd = c.b & 7, jj = c.b >> 3;
  Map M;
  M.i1 = 8 * (xcd & 1) + (jj >> 2); M.j1 = 4 * (xcd >> 1) + (jj & 3);
  M.i2 = 4 * (xcd >> 1) + (jj >> 3); M.j2 = 8 * (xcd & 1) + (jj & 7);
  M.act3 = jj < 28;
  M.i3 = 4 * (xcd >> 1) + (M.act3 ? jj / 7 : 0); M.j3 = 7 * (xcd & 1) + (M.act3 ? jj % 7 : 0);
  const Args A{x, wqkv, wphi, wo, win, wsp, bsp, wout, out, ws};
  constexpr int last = LAST_PHASE;
  const bool ld = c.wid >= 4;
  {
    Acc1 e;
    if (ld) p1_load(c, A, M.i1, M.j1);
    else { __builtin_amdgcn_s_setprio(1); p1_mma(c, A, M.i1, M.j1, e); __builtin_amdgcn_s_setprio(0); }
    p1_epilogue(c, A, M, e, last);
  }
  if (last < 2) return;
  Ld3 L3;
  p3_setup(c, A, M.j3, L3);
  phase2(c, A, M.i2, M.j2, ld && M.act3, [&]() { p3_issueW(c, L3, 0); p3_issueW(c, L3, 1); });
  if (last < 3) return;
  if (M.act3) {
    u32x2 z1[2][4];
    Ld4 L;
    L.rA = __builtin_amdgcn_make_buffer_rsrc((void*)(A.wsp + (size_t)M.i3 * 128 * 2048), 0, 128 * 4096, 0x00020000);
    {
      Acc3 e;
      if (ld) p3_load(c, L3, M.i3);
      else { __builtin_amdgcn_s_setprio(1); p3_mma(c, M.i3, M.j3, e); __builtin_amdgcn_s_setprio(0); }
      const int ln = opaque(c.lane);
      L.voA = rowmajor_voff(c.wid & 3, ln, 4096); L.voF = ln * 16 + (c.wid & 3) * 1024;
      p3_epilogue(c, L, M.i3, M.j3, e, z1);
    }
    if (last < 4) return;
    f32x4 e4[2][4];
    if (ld) p4_load(c, L, M.j3);
    else { __builtin_amdgcn_s_setprio(1); p4_mma(c, M.j3, e4); __builtin_amdgcn_s_setprio(0); }
    p4_epilogue(c, A, M.i3, M.j3, M.j2, e4, z1);
  } else {
    bar();
    if (!ld) { const int wv = ((c.b & 7) * 4 + ((c.b >> 3) - 28)) * 4 + c.wid;
#pragma unroll 1
      for (int k = 0; k < 12; ++k) { const int Li = (k * 128 + wv) * 64 + c.lane;
        touch(c.lds, Li < 65536 ? (const char*)A.wsp + (size_t)Li * 128 : (const char*)A.wout + (size_t)((Li < 94208 ? Li : 94207) - 65536) * 128); } }
    if (ld) p5_issueW(c, A, M.j2);
  }
  if (last < 5) return;
  phase5(c, A, M.i2, M.j2);
}

extern "C" void kernel_launch(void* const* d_in, const int* in_sizes, int n_in, void* d_out, int out_size, void* d_ws, size_t ws_size, hipStream_t stream) {
  (void)in_sizes; (void)n_in; (void)out_size; (void)ws_size;
  (void)hipFuncSetAttribute((const void*)fused_fwd, hipFuncAttributeMaxDynamicSharedMemorySize, LDS_BYTES);
  fused_fwd<<<256, 512, LDS_BYTES, stream>>>((const u16*)d_in[0], (const u16*)d_in[1], (const u16*)d_in[2], (const u16*)d_in[3], (const u16*)d_in[5], (const u16*)d_in[6],
                                             (const u16*)d_in[7], (const u16*)d_in[8], (u16*)d_out, (char*)d_ws);
}
```

```cpp
#include <hip/hip_runtime.h>
#include <hip/hip_bf16.h>
#include <stdint.h>
#include <type_traits>

#define LAS __attribute__((address_space(3)))
#define DI __device__ __forceinline__
typedef unsigned short u16;
typedef short bf16x8 __attribute__((ext_vector_type(8)));
typedef short s16x4 __attribute__((ext_vector_type(4)));
typedef float f32x4 __attribute__((ext_vector_type(4)));
typedef float f32x2 __attribute__((ext_vector_type(2)));
typedef unsigned u32x4 __attribute__((ext_vector_type(4)));
typedef unsigned u32x2 __attribute__((ext_vector_type(2)));
typedef __bf16 bf2 __attribute__((ext_vector_type(2)));
typedef LAS char* lds_t;

constexpr unsigned OFF_EPOCH = 0, OFF_ERR = 128, OFF_FLAG = 4096;
constexpr unsigned OFF_KVP = 262144;
constexpr unsigned OFF_ZP = OFF_KVP + 2097152;
constexpr unsigned OFF_ST2 = 27u << 20 | 0;
constexpr unsigned OFF_STH = OFF_ZP + 131072 + 262144;
constexpr unsigned OFF_GX = OFF_STH + 262144;
constexpr unsigned OFF_GC1 = OFF_GX + 65536;
constexpr unsigned OFF_GC3 = OFF_GC1 + 393216;
constexpr unsigned OFF_OF = 4u << 20, OFF_HF = 8u << 20, OFF_Z2 = 12u << 20, OFF_UF = 20u << 20, OFF_TS = 28u << 20;
constexpr unsigned WS_BYTES = 32u << 20;
enum { FG_KV = 0, FG_O = 1, FG_H = 2, FG_ST = 3, FG_Z2 = 4, FG_U = 5 };
constexpr int LDS_BYTES = 163840;
constexpr int SC1 = 122880, L_WPHI = SC1, L_TAB1 = SC1 + 4096, L_XBUF = SC1 + 22528;
constexpr int L_XF1 = 65536, L_TB = 32768, L_KR = 65536, L_ZR = 131072;
constexpr int SC3 = 147456, L_TAB3 = SC3, L_FIN = SC3 + 4096;
constexpr int L_CNT = LDS_BYTES - 64, L_DUMMY = LDS_BYTES - 512;
constexpr int SC_SC1 = 16;
#define PSLEEP 4
#define OUTAUX 16
#define LDAUX 16

DI void bar() { asm volatile("" ::: "memory"); __builtin_amdgcn_s_barrier(); asm volatile("" ::: "memory"); }
template <int N> DI void wait_vm() { asm volatile("s_waitcnt vmcnt(%0)" ::"n"(N) : "memory"); }
DI int opaque(int v) { asm volatile("" : "+v"(v)); return v; }
DI void wait_lgkm0() { __builtin_amdgcn_s_waitcnt(0xc07f); }
DI unsigned pk2(float lo, float hi) { f32x2 v = {lo, hi}; return __builtin_bit_cast(unsigned, __builtin_convertvector(v, bf2)); }
DI bf16x8 pk8(const f32x4& a, const f32x4& b) { u32x4 r = {pk2(a[0], a[1]), pk2(a[2], a[3]), pk2(b[0], b[1]), pk2(b[2], b[3])}; return __builtin_bit_cast(bf16x8, r); }
DI float bflo(unsigned w) { return __uint_as_float(w << 16); }
DI float bfhi(unsigned w) { return __uint_as_float(w & 0xffff0000u); }
DI f32x4 mfma(bf16x8 a, bf16x8 b, f32x4 c) { return __builtin_amdgcn_mfma_f32_16x16x32_bf16(a, b, c, 0, 0, 0); }
DI bf16x8 ldsA(lds_t p) { return *(LAS bf16x8*)p; }
DI bf16x8 ldsT(lds_t p, int d) {
  s16x4 lo = __builtin_amdgcn_ds_read_tr16_b64_v4i16((LAS s16x4*)p), hi = __builtin_amdgcn_ds_read_tr16_b64_v4i16((LAS s16x4*)(p + d));
  return __builtin_shufflevector(lo, hi, 0, 1, 2, 3, 4, 5, 6, 7);
}
template <int D1, int D2> DI void tr4_issue(unsigned addr, u32x2 (&r)[4]) {
  asm volatile("ds_read_b64_tr_b16 %0, %4\n\tds_read_b64_tr_b16 %1, %4 offset:%5\n\tds_read_b64_tr_b16 %2, %4 offset:%6\n\tds_read_b64_tr_b16 %3, %4 offset:%7"
               : "=&v"(r[0]), "=&v"(r[1]), "=&v"(r[2]), "=&v"(r[3]) : "v"(addr), "n"(D1), "n"(D2), "n"(D1 + D2) : "memory");
}
DI void tr4_wait(u32x2 (&r)[4]) { asm volatile("s_waitcnt lgkmcnt(0)" : "+v"(r[0]), "+v"(r[1]), "+v"(r[2]), "+v"(r[3])::"memory"); }
DI float sum_g(float x) {
  auto a = __builtin_amdgcn_permlane16_swap(__float_as_uint(x), __float_as_uint(x), false, false);
  x = __uint_as_float(a[0]) + __uint_as_float(a[1]);
  auto b = __builtin_amdgcn_permlane32_swap(__float_as_uint(x), __float_as_uint(x), false, false);
  return __uint_as_float(b[0]) + __uint_as_float(b[1]);
}
DI float max_g(float x) {
  auto a = __builtin_amdgcn_permlane16_swap(__float_as_uint(x), __float_as_uint(x), false, false);
  x = fmaxf(__uint_as_float(a[0]), __uint_as_float(a[1]));
  auto b = __builtin_amdgcn_permlane32_swap(__float_as_uint(x), __float_as_uint(x), false, false);
  return fmaxf(__uint_as_float(b[0]), __uint_as_float(b[1]));
}
template <int CTRL> DI float dpp_add(float x) { return x + __int_as_float(__builtin_amdgcn_update_dpp(0, __float_as_int(x), CTRL, 0xf, 0xf, true)); }
DI float sum_row16(float x) { x = dpp_add<0x128>(x); x = dpp_add<0x124>(x); x = dpp_add<0x122>(x); return dpp_add<0x121>(x); }
DI void sum_row16x8(f32x4& a, f32x4& b) {
#define RR(n) "v_add_f32_dpp %0, %0, %0 row_ror:" n " row_mask:0xf bank_mask:0xf\n\tv_add_f32_dpp %1, %1, %1 row_ror:" n " row_mask:0xf bank_mask:0xf\n\t" \
  "v_add_f32_dpp %2, %2, %2 row_ror:" n " row_mask:0xf bank_mask:0xf\n\tv_add_f32_dpp %3, %3, %3 row_ror:" n " row_mask:0xf bank_mask:0xf\n\t" \
  "v_add_f32_dpp %4, %4, %4 row_ror:" n " row_mask:0xf bank_mask:0xf\n\tv_add_f32_dpp %5, %5, %5 row_ror:" n " row_mask:0xf bank_mask:0xf\n\t" \
  "v_add_f32_dpp %6, %6, %6 row_ror:" n " row_mask:0xf bank_mask:0xf\n\tv_add_f32_dpp %7, %7, %7 row_ror:" n " row_mask:0xf bank_mask:0xf\n\t"
  float a0 = a[0], a1 = a[1], a2 = a[2], a3 = a[3], b0 = b[0], b1 = b[1], b2 = b[2], b3 = b[3];
  asm volatile("s_nop 1\n\t" RR("8") RR("4") RR("2") RR("1") "s_nop 0" : "+v"(a0), "+v"(a1), "+v"(a2), "+v"(a3), "+v"(b0), "+v"(b1), "+v"(b2), "+v"(b3));
#undef RR
  a = (f32x4){a0, a1, a2, a3}; b = (f32x4){b0, b1, b2, b3};
}
DI float dot2(unsigned a, unsigned b, float c) { return __builtin_amdgcn_fdot2_f32_bf16(__builtin_bit_cast(bf2, a), __builtin_bit_cast(bf2, b), c, false); }
constexpr unsigned ONES2 = 0x3f803f80u;
DI f32x4 gelu4(f32x4 x) {
  const f32x4 t = x * (x * x * -0.1029432397f + -2.302208198f);
  f32x4 d, r;
#pragma unroll
  for (int i = 0; i < 4; ++i) d[i] = __builtin_amdgcn_exp2f(t[i]);
  d += 1.0f;
#pragma unroll
  for (int i = 0; i < 4; ++i) r[i] = __builtin_amdgcn_rcpf(d[i]);
  return x * r;
}
DI u32x2 pk4(const f32x4& a) { return (u32x2){pk2(a[0], a[1]), pk2(a[2], a[3])}; }
DI f32x4 up4(u32x2 w) { return (f32x4){bflo(w[0]), bfhi(w[0]), bflo(w[1]), bfhi(w[1])}; }
DI float gelu(float x) { const float t = x * fmaf(x * x, -0.1029432397f, -2.302208198f); return x * __builtin_amdgcn_rcpf(1.0f + __builtin_amdgcn_exp2f(t)); }

struct Ctx {
  __amdgpu_buffer_rsrc_t ws;
  char* wsp; lds_t lds; unsigned tag; int lane, wid, b; unsigned ts;
};
DI unsigned* flag_ptr(const Ctx& c, int g, int a, int bb) { return (unsigned*)(c.wsp + OFF_FLAG + ((g * 16 + a) * 16 + bb) * 128); }
template <unsigned NWAVES = 4, int YOUNGER = 0> DI void publish(const Ctx& c, int cnt, int g, int a, int bb) {
  wait_vm<YOUNGER>();
  if (c.lane == 0) {
    unsigned old = __hip_atomic_fetch_add((LAS unsigned*)(c.lds + L_CNT + cnt * 4), 1u, __ATOMIC_RELAXED, __HIP_MEMORY_SCOPE_WORKGROUP);
    if (old == NWAVES - 1u) __hip_atomic_store(flag_ptr(c, g, a, bb), c.tag, __ATOMIC_RELAXED, __HIP_MEMORY_SCOPE_AGENT);
  }
}
DI void wait_flags(const Ctx& c, int g, int a, int n) {
  const unsigned* p = flag_ptr(c, g, a, c.lane < n ? c.lane : 0);
  const unsigned long long t0 = __builtin_amdgcn_s_memrealtime();
  for (;;) {
    const unsigned v = __hip_atomic_load(p, __ATOMIC_RELAXED, __HIP_MEMORY_SCOPE_AGENT);
    if (__all(v == c.tag)) break;
    if (__builtin_amdgcn_s_memrealtime() - t0 > 300000ull) { if (c.lane == 0) __hip_atomic_store((unsigned*)(c.wsp + OFF_ERR), 0x100u + (unsigned)g, __ATOMIC_RELAXED, __HIP_MEMORY_SCOPE_AGENT); break; }
    __builtin_amdgcn_s_sleep(1);
  }
  asm volatile("" ::: "memory");
}
#define STEP(t) do {} while (0)
#define TS(k) do {} while (0)

DI void st16(const Ctx& c, bf16x8 v, int off) { __builtin_amdgcn_raw_buffer_store_b128(__builtin_bit_cast(u32x4, v), c.ws, off, 0, SC_SC1); }
template <int AUX> DI void dma(__amdgpu_buffer_rsrc_t r, lds_t dst, int voff, int soff) {
  if (AUX) asm volatile("s_mov_b32 m0, %0\n\ts_nop 0\n\tbuffer_load_dwordx4 %1, %2, %3 offen sc1 lds" ::"s"((unsigned)(size_t)dst), "v"(voff), "s"(r), "s"(soff) : "memory");
  else asm volatile("s_mov_b32 m0, %0\n\ts_nop 0\n\tbuffer_load_dwordx4 %1, %2, %3 offen lds" ::"s"((unsigned)(size_t)dst), "v"(voff), "s"(r), "s"(soff) : "memory");
}

#define EXP 0
template <int S, int NW, class IA, class IW, class MID, class POST>
DI void loader_loop(int nt, bool gated_prologue, IA issueA, IW issueW, MID mid, POST post) {
  constexpr int PPW = 4 + NW;
  if (gated_prologue) wait_vm<4 * (S - 2)>(); else wait_vm<PPW * (S - 2)>();
  bar();
  int t = 0;
#pragma unroll
  for (int p = 0; p < S - 2; ++p, ++t) {
    mid(t); issueA(t + S - 1); issueW(t + S - 1); post(t);
    if (gated_prologue) {
      if (p == 0) wait_vm<4 * (S - 3 > 0 ? S - 3 : 0) + PPW * 1>();
      else if (p == 1) wait_vm<4 * (S - 4 > 0 ? S - 4 : 0) + PPW * 2>();
      else wait_vm<4 * (S - 5 > 0 ? S - 5 : 0) + PPW * 3>();
    } else wait_vm<PPW * (S - 2)>();
    bar();
  }
#pragma unroll 1
  for (; t + S - 1 < nt; ++t) { mid(t); if (!(EXP & 1)) { issueA(t + S - 1); issueW(t + S - 1); } wait_vm<PPW * (S - 2)>(); bar(); }
#pragma unroll
  for (int r = S - 2; r >= 0; --r, ++t) {
    if (r >= 4) wait_vm<PPW * 3>(); else if (r == 3) wait_vm<PPW * 2>(); else if (r == 2) wait_vm<PPW * 1>(); else wait_vm<0>();
    bar();
  }
}
template <int NW, bool GATED, class IA, class IW, class POST, class GA, class FIN>
DI void loader_loop16(bool full, bool once, IA issueA, IW issueW, POST post, GA gather, FIN fin) {
  constexpr int PPW = 4 + NW;
  if (GATED) wait_vm<4>(); else wait_vm<PPW>();
  bar();
  issueA(2); issueW(2); post(0); wait_vm<PPW>(); bar();
#pragma unroll 1
  for (int t = 1; t < 5; ++t) { if (!(EXP & 1)) { issueA(t + 2); issueW(t + 2); } post(t); wait_vm<PPW>(); bar(); }
#pragma unroll
  for (int t = 5; t < 13; ++t) {
    issueA(t + 2); issueW(t + 2); gather(t - 5);
    if (full) { if (t == 5) wait_vm<PPW + 2>(); else wait_vm<PPW + 4>(); }
    else if (once && t <= 6) wait_vm<PPW + 2>();
    else wait_vm<PPW>();
    bar();
  }
  issueA(15); issueW(15); if (full) wait_vm<PPW + 2>(); else wait_vm<PPW>(); bar();
  wait_vm<0>(); bar();
  fin(); bar();
}
template <int S, int SB, class RD0, class RD1, class MM0, class MM1, class SCH, class HK, class ST>
DI void consumer_loop(Ctx& c, int sbase, int nt, RD0 rd0, RD1 rd1, MM0 mm0, MM1 mm1, SCH sched, HK hook, ST stamp) {
  bar();
  stamp();
  rd0(0);
  int sb = 0;
#pragma unroll 1
  for (int t = 0; t < nt; ++t) {
    hook(t); STEP(t);
    __builtin_amdgcn_sched_barrier(0);
    if (!(EXP & 4)) rd1(sb); if (!(EXP & 2)) mm0(); sched();
    __builtin_amdgcn_sched_barrier(0);
    wait_lgkm0(); bar();
    __builtin_amdgcn_sched_barrier(0);
    sb = (sb == (S - 1) * SB) ? 0 : sb + SB;
    if (!(EXP & 4)) rd0(sb); if (!(EXP & 2)) mm1(); sched();
    __builtin_amdgcn_sched_barrier(0);
  }
}
#define TOUCH(name, mod) DI void name(lds_t lds, const void* p) { \
  asm volatile("s_mov_b32 m0, %1\n\ts_nop 0\n\tglobal_load_lds_dword %0, off" mod ::"v"(p), "s"((unsigned)(size_t)(lds + L_DUMMY)) : "memory"); }
TOUCH(touch, "") TOUCH(touch_nt, " nt") TOUCH(touch_sc1, " sc1")
template <int NM, int NR> DI void interleave() {
#pragma unroll
  for (int i = 0; i < NR; ++i) { __builtin_amdgcn_sched_group_barrier(0x008, 1, 0); __builtin_amdgcn_sched_group_barrier(0x100, 1, 0); }
  __builtin_amdgcn_sched_group_barrier(0x008, NM - NR, 0);
}
template <int NM, int NR> DI void interleave2() {
#pragma unroll
  for (int i = 0; i < NR / 2; ++i) { __builtin_amdgcn_sched_group_barrier(0x008, 1, 0); __builtin_amdgcn_sched_group_barrier(0x100, 2, 0); }
  __builtin_amdgcn_sched_group_barrier(0x008, NM - NR / 2, 0);
}

DI int rowmajor_voff(int lw, int lane, int pitch) {
  const int rr = lane >> 3, slot = 8 * (lw & 1) + rr;
  const int row = 8 * (slot >> 2) + 4 * (lw >> 1) + (slot & 3);
  return row * pitch + (((lane & 7) ^ (slot >> 1)) << 4);
}
DI int true_row(int mt, int i) { return 32 * (mt >> 1) + 8 * (i >> 2) + 4 * (mt & 1) + (i & 3); }

DI void acc_row(float& s, float& q, u32x4 w) {
#pragma unroll
  for (int i = 0; i < 4; ++i) { s = dot2(w[i], ONES2, s); q = dot2(w[i], w[i], q); } }
DI float col_sum(const u32x2 (&r)[4]) { float s0 = 0.f, s1 = 0.f;
#pragma unroll
  for (int i = 0; i < 4; ++i) { s0 = dot2(r[i][0], ONES2, s0); s1 = dot2(r[i][1], ONES2, s1); }
  return sum_g(s0 + s1); }
DI void st_granule(const Ctx& c, float v, int off) { __builtin_amdgcn_raw_buffer_store_b64((u32x2){__float_as_uint(v), c.tag}, c.ws, off, 0, SC_SC1); }
DI bool timed_out(const Ctx& c, unsigned long long t0, unsigned code) {
  if (__builtin_amdgcn_s_memrealtime() - t0 <= 300000ull) return false;
  if (c.lane == 0) __hip_atomic_store((unsigned*)(c.wsp + OFF_ERR), code, __ATOMIC_RELAXED, __HIP_MEMORY_SCOPE_AGENT);
  return true;
}
DI float sum_granules16(const Ctx& c, u32x2 (&v)[16], int off, int stride) {
  const unsigned long long t0 = __builtin_amdgcn_s_memrealtime();
  for (;;) {
    bool ok = true;
#pragma unroll
    for (int k = 0; k < 16; ++k) ok &= v[k][1] == c.tag;
    if (__all(ok) || timed_out(c, t0, 0x200u)) break;
    __builtin_amdgcn_s_sleep(2);
#pragma unroll
    for (int k = 0; k < 16; ++k) v[k] = __builtin_amdgcn_raw_buffer_load_b64(c.ws, off + k * stride, 0, SC_SC1);
  }
  float s = 0.f;
#pragma unroll
  for (int k = 0; k < 16; ++k) s += __uint_as_float(v[k][0]);
  return s;
}

#define ROTM 0
#define ROT2(j) (ROTM * 2 * ((j) & 7))
#define ROT5(j) (ROTM * ((7 * ((j) & 7)) >> 1))
#define P4D 0
#define TOUCHW touch
#define PF_T0 2
#define PF_DT 3

struct Args { const u16 *x, *wqkv, *wphi, *wo, *win, *wsp, *bsp, *wout; u16* out; char* ws; };

DI void get_tag(Ctx& c) { unsigned e; asm volatile("s_load_dword %0, %1, 0x0\n\ts_waitcnt lgkmcnt(0)" : "=&s"(e) : "s"(c.wsp) : "memory"); c.tag = e + 1u; }
DI void kv_gather(const Ctx& c, int j1) {
  const int lane = c.lane, g = lane >> 4, i16 = lane & 15, tid = c.wid * 64 + lane;
  f32x4 a0 = {0, 0, 0, 0}, a1 = {0, 0, 0, 0};
  u32x4 v[16];
#pragma unroll
  for (int k = 0; k < 16; ++k) v[k] = __builtin_amdgcn_raw_buffer_load_b128(c.ws, tid * 16, OFF_KVP + (j1 * 16 + k) * 8192, SC_SC1);
  float zs = 0.f;
  if (c.wid < 2) { unsigned zv[16];
#pragma unroll
    for (int k = 0; k < 16; ++k) zv[k] = __builtin_amdgcn_raw_buffer_load_b32(c.ws, lane * 4, OFF_ZP + ((j1 * 16 + k) * 2 + c.wid) * 256, SC_SC1);
#pragma unroll
    for (int k = 0; k < 16; ++k) zs += __uint_as_float(zv[k]); }
#pragma unroll
  for (int k = 0; k < 16; ++k) { a0[0] += bflo(v[k][0]); a0[1] += bfhi(v[k][0]); a0[2] += bflo(v[k][1]); a0[3] += bfhi(v[k][1]); a1[0] += bflo(v[k][2]); a1[1] += bfhi(v[k][2]); a1[2] += bflo(v[k][3]); a1[3] += bfhi(v[k][3]); }
  const int w = c.wid, hd = w >> 2, dt = (w >> 1) & 1, fp = w & 1;
  const lds_t tb = c.lds + L_XBUF + hd * 8192 + (32 * fp + i16) * 128 + (16 * dt + 4 * g) * 2;
  *(LAS u32x2*)tb = (u32x2){pk2(a0[0], a0[1]), pk2(a0[2], a0[3])};
  *(LAS u32x2*)(tb + 16 * 128) = (u32x2){pk2(a1[0], a1[1]), pk2(a1[2], a1[3])};
  if (c.wid < 2) { const unsigned z2 = pk2(zs, zs); const lds_t t3 = c.lds + L_XBUF + c.wid * 8192 + lane * 128 + 64;
    *(LAS u32x4*)t3 = (u32x4){z2, z2, z2, z2}; *(LAS u32x4*)(t3 + 16) = (u32x4){z2, z2, z2, z2}; }
  wait_lgkm0();
}
struct Map { int i1, j1, i2, j2, i3, j3; bool act3; };
DI void p1_load(Ctx& c, const Args& A, int i1, int j1) {
  const int lane = c.lane, lw = c.wid - 4, g = lane >> 4, q = (lane & 15) >> 2, p = lane & 3;
  const lds_t lds = c.lds;
  const __amdgpu_buffer_rsrc_t rWS = c.ws;
  constexpr int S = 3, SB = 40960, NW = 6;
  const __amdgpu_buffer_rsrc_t rA = __builtin_amdgcn_make_buffer_rsrc((void*)(A.x + (size_t)i1 * 128 * 1024), 0, 128 * 2048, 0x00020000);
  const __amdgpu_buffer_rsrc_t rW = __builtin_amdgcn_make_buffer_rsrc((void*)A.wqkv, 0, 1024 * 6144, 0x00020000);
  const __amdgpu_buffer_rsrc_t rP = __builtin_amdgcn_make_buffer_rsrc((void*)A.wphi, 0, 65536, 0x00020000);
  const int voA = rowmajor_voff(lw, lane, 2048);
  int voW[NW];
#pragma unroll
  for (int i = 0; i < NW; ++i) { const int o = (NW * lw + i) * 1024 + lane * 16, r = o / 384, rem = o % 384, seg = rem >> 7, s16 = (rem & 127) >> 4;
    const int c32 = (s16 >> 1) ^ ((r >> 1) & 3), r5 = r & 31, k = (r & 32) + 8 * ((r5 >> 2) & 3) + 4 * (r5 >> 4) + (r5 & 3);
    voW[i] = k * 6144 + seg * 2048 + j1 * 128 + c32 * 32 + (s16 & 1) * 16; }
#define ROTMODE 1
  const int rot = ROTMODE == 2 ? (2 * (i1 & 7) + (i1 >> 3) + 4 * (j1 & 3)) & 15 : ROTMODE == 1 ? 4 * (i1 & 3) : ROTMODE == 3 ? 8 * (i1 & 1) : 0;
  auto issueA = [&](int t) { const lds_t d = lds + (t % S) * SB + lw * 1024; const int so = ((t + rot) & 15) * 128;
#pragma unroll
    for (int i = 0; i < 4; ++i) dma<0>(rA, d + i * 4096, voA, so + i * 32 * 2048); };
  auto issueW = [&](int t) { const lds_t d = lds + (t % S) * SB + 16384 + lw * NW * 1024; const int so = ((t + rot) & 15) * 64 * 6144;
#pragma unroll
    for (int i = 0; i < NW; ++i) dma<0>(rW, d + i * 1024, voW[i], so); };
  issueA(0); issueW(0); issueA(1); issueW(1);
  dma<0>(rP, lds + L_WPHI + lw * 1024, lane * 16 + lw * 1024, j1 * 4096);
  get_tag(c);
  TS(0);
  u32x2 gv[16]; u32x4 gx[2];
  const int goff = OFF_GC1 + j1 * 16 * 1536 + (lw * 64 + lane) * 8, xoff = OFF_GX + (i1 * 128 + lane) * 16;
  const int tau = ROTMODE == 2 ? 0 : ROTMODE == 1 ? i1 >> 2 : ROTMODE == 3 ? 0 : 0;
  auto post = [&](int t) {
    if (t != tau) return;
    const int sw = (2 * g + (q >> 1)) & 3; u32x2 cr[3][4];
#pragma unroll
    for (int n = 0; n < 3; ++n) { const int nu = 3 * lw + n;
      tr4_issue<16 * 384, 32 * 384>((unsigned)(size_t)(lds + (t % S) * SB + 16384 + (4 * g + q) * 384 + 8 * p + (nu >> 2) * 128 + 32 * ((nu & 3) ^ sw)), cr[n]); }
#pragma unroll
    for (int n = 0; n < 3; ++n) { tr4_wait(cr[n]); const float s = col_sum(cr[n]);
      if (lane < 16) st_granule(c, s, OFF_GC1 + (j1 * 16 + rot + tau) * 1536 + ((3 * lw + n) * 16 + lane) * 8); }
  };
  auto gather = [&](int k) {
    if (lw < 3) { gv[2 * k] = __builtin_amdgcn_raw_buffer_load_b64(rWS, goff + 2 * k * 1536, 0, SC_SC1); gv[2 * k + 1] = __builtin_amdgcn_raw_buffer_load_b64(rWS, goff + (2 * k + 1) * 1536, 0, SC_SC1); }
    else if (k == 0) { gx[0] = __builtin_amdgcn_raw_buffer_load_b128(rWS, xoff, 0, SC_SC1); gx[1] = __builtin_amdgcn_raw_buffer_load_b128(rWS, xoff + 1024, 0, SC_SC1); }
  };
  auto fin = [&]() {
    if (lw < 3) *(LAS float*)(lds + L_TAB1 + 1024 + (lw * 64 + lane) * 4) = sum_granules16(c, gv, goff, 1536);
    else {
      const unsigned long long t0 = __builtin_amdgcn_s_memrealtime();
      for (;;) {
        if (__all(gx[0][1] == c.tag && gx[0][3] == c.tag && gx[1][1] == c.tag && gx[1][3] == c.tag) || timed_out(c, t0, 0x201u)) break;
        __builtin_amdgcn_s_sleep(2);
        gx[0] = __builtin_amdgcn_raw_buffer_load_b128(rWS, xoff, 0, SC_SC1); gx[1] = __builtin_amdgcn_raw_buffer_load_b128(rWS, xoff + 1024, 0, SC_SC1);
      }
#pragma unroll
      for (int e = 0; e < 2; ++e) { *(LAS unsigned*)(lds + L_TAB1 + (lane + 64 * e) * 4) = gx[e][0]; *(LAS unsigned*)(lds + L_TAB1 + 512 + (lane + 64 * e) * 4) = gx[e][2]; }
    }
    wait_lgkm0();
  };
  loader_loop16<NW, false>(lw < 3, lw == 3, issueA, issueW, post, gather, fin);
  TS(1);
}

struct Acc1 { f32x4 q[2][2], k[2][2], v[2][2]; };
DI void p1_mma(Ctx& c, const Args& A, int i1, int j1, Acc1& e) {
  const int lane = c.lane, wr = c.wid >> 1, wc = c.wid & 1, g = lane >> 4, q = (lane & 15) >> 2, p = lane & 3, i16 = lane & 15;
  const lds_t lds = c.lds;
  const f32x4 Z4 = {0.f, 0.f, 0.f, 0.f};
  constexpr int S = 3, SB = 40960;
  f32x4 aq[4][2], ak[4][2], av[4][2];
#pragma unroll
  for (int m = 0; m < 4; ++m)
#pragma unroll
    for (int n = 0; n < 2; ++n) { aq[m][n] = Z4; ak[m][n] = Z4; av[m][n] = Z4; asm volatile("" : "+v"(aq[m][n]), "+v"(ak[m][n]), "+v"(av[m][n])); }
  const int sw = (2 * g + (q >> 1)) & 3;
  const int ao0 = (wr * 64 + i16) * 128 + ((g ^ (i16 >> 1)) << 4), ao1 = ao0 ^ 64;
  const int wl = 16384 + (4 * g + q) * 384 + 8 * p;
  const int wo0 = wl + 32 * ((2 * wc) ^ sw), wo1 = wl + 32 * ((2 * wc + 1) ^ sw);
  bf16x8 a0[4], w0[6], a1[4], w1[6];
  auto rd = [&](bf16x8(&a)[4], bf16x8(&w)[6], int sb, int ks) { const lds_t s = lds + sb;
#pragma unroll
    for (int sg = 0; sg < 3; ++sg) { w[2 * sg] = ldsT(s + wo0 + sg * 128 + ks * 32 * 384, 16 * 384); w[2 * sg + 1] = ldsT(s + wo1 + sg * 128 + ks * 32 * 384, 16 * 384); }
#pragma unroll
    for (int m = 0; m < 4; ++m) a[m] = ldsA(s + (ks ? ao1 : ao0) + m * 2048); };
  auto mm = [&](bf16x8(&a)[4], bf16x8(&w)[6]) {
#pragma unroll
    for (int n = 0; n < 2; ++n)
#pragma unroll
      for (int m = 0; m < 4; ++m) aq[m][n] = mfma(w[n], a[m], aq[m][n]);
#pragma unroll
    for (int n = 0; n < 2; ++n)
#pragma unroll
      for (int m = 0; m < 4; ++m) ak[m][n] = mfma(w[2 + n], a[m], ak[m][n]);
#pragma unroll
    for (int n = 0; n < 2; ++n)
#pragma unroll
      for (int m = 0; m < 4; ++m) av[m][n] = mfma(a[m], w[4 + n], av[m][n]); };
  TS(0);
  {
    u32x4 xv[2][2];
#pragma unroll
    for (int e2 = 0; e2 < 2; ++e2) { const int R = 8 * j1 + 2 * c.wid + e2; const u16* xr = A.x + (size_t)(i1 * 128 + true_row(R >> 4, R & 15)) * 1024 + lane * 16;
      xv[e2][0] = *(const u32x4*)xr; xv[e2][1] = *(const u32x4*)(xr + 8); }
    get_tag(c);
#pragma unroll
    for (int e2 = 0; e2 < 2; ++e2) { float s = 0.f, qq = 0.f; acc_row(s, qq, xv[e2][0]); acc_row(s, qq, xv[e2][1]);
      s = sum_g(sum_row16(s)) * (1.0f / 1024.0f); qq = sum_g(sum_row16(qq)) * (1.0f / 1024.0f);
      const float rs = __builtin_amdgcn_rsqf(fmaxf(qq - s * s, 0.f) + 1e-5f);
      if (lane == 0) __builtin_amdgcn_raw_buffer_store_b128((u32x4){__float_as_uint(s), c.tag, __float_as_uint(rs), c.tag}, c.ws, OFF_GX + (i1 * 128 + 8 * j1 + 2 * c.wid + e2) * 16, 0, SC_SC1); }
  }
  consumer_loop<S, SB>(c, 0, 16, [&](int sb) { rd(a0, w0, sb, 0); }, [&](int sb) { rd(a1, w1, sb, 1); }, [&]() { mm(a0, w0); }, [&]() { mm(a1, w1); }, []() { interleave<24, 16>(); }, [](int) {}, [&]() { TS(16); });
  TS(1);
  const lds_t xf = lds + L_XF1 + c.wid * 12288 + lane * 16;
#pragma unroll
  for (int m = 0; m < 2; ++m)
#pragma unroll
    for (int n = 0; n < 2; ++n) {
      *(LAS f32x4*)(xf + (m * 6 + n) * 1024) = aq[2 + m][n]; *(LAS f32x4*)(xf + (m * 6 + 2 + n) * 1024) = ak[2 + m][n]; *(LAS f32x4*)(xf + (m * 6 + 4 + n) * 1024) = av[2 + m][n];
      e.q[m][n] = aq[m][n]; e.k[m][n] = ak[m][n]; e.v[m][n] = av[m][n];
    }
  wait_lgkm0();
}

DI void both_g_max(float& a, float& b) {
  auto r = __builtin_amdgcn_permlane16_swap(__float_as_uint(a), __float_as_uint(b), false, false);
  const float x = fmaxf(__uint_as_float(r[0]), __uint_as_float(r[1]));
  auto s = __builtin_amdgcn_permlane32_swap(__float_as_uint(x), __float_as_uint(x), false, false);
  const float y = fmaxf(__uint_as_float(s[0]), __uint_as_float(s[1]));
  auto t = __builtin_amdgcn_permlane16_swap(__float_as_uint(y), __float_as_uint(y), false, false);
  a = __uint_as_float(t[0]); b = __uint_as_float(t[1]);
}
DI void both_g_sum(float& a, float& b) {
  auto r = __builtin_amdgcn_permlane16_swap(__float_as_uint(a), __float_as_uint(b), false, false);
  const float x = __uint_as_float(r[0]) + __uint_as_float(r[1]);
  auto s = __builtin_amdgcn_permlane32_swap(__float_as_uint(x), __float_as_uint(x), false, false);
  const float y = __uint_as_float(s[0]) + __uint_as_float(s[1]);
  auto t = __builtin_amdgcn_permlane16_swap(__float_as_uint(y), __float_as_uint(y), false, false);
  a = __uint_as_float(t[0]); b = __uint_as_float(t[1]);
}
DI void feat(const f32x4& p0, const f32x4& p1, f32x4& e0, f32x4& e1, f32x4& n0, f32x4& n1) {
  constexpr float L2E = 1.4426950408889634f;
  const f32x4 t0 = p0 * L2E, t1 = p1 * L2E;
  float mx = fmaxf(fmaxf(fmaxf(t0[0], t0[1]), t0[2]), fmaxf(fmaxf(fmaxf(t0[3], t1[0]), t1[1]), fmaxf(t1[2], t1[3])));
  float mn = -fminf(fminf(fminf(t0[0], t0[1]), t0[2]), fminf(fminf(fminf(t0[3], t1[0]), t1[1]), fminf(t1[2], t1[3])));
  both_g_max(mx, mn);
  const f32x4 a0 = t0 - mx, a1 = t1 - mx, b0 = -t0 - mn, b1 = -t1 - mn;
#pragma unroll
  for (int r = 0; r < 4; ++r) { e0[r] = __builtin_amdgcn_exp2f(a0[r]); e1[r] = __builtin_amdgcn_exp2f(a1[r]); n0[r] = __builtin_amdgcn_exp2f(b0[r]); n1[r] = __builtin_amdgcn_exp2f(b1[r]); }
  const f32x4 se = e0 + e1, sn4 = n0 + n1;
  float sp = (se[0] + se[1]) + (se[2] + se[3]), sn = (sn4[0] + sn4[1]) + (sn4[2] + sn4[3]);
  both_g_sum(sp, sn);
  sp = __builtin_amdgcn_rcpf(sp); sn = __builtin_amdgcn_rcpf(sn);
  e0 *= sp; e1 *= sp; n0 *= sn; n1 *= sn;
}

DI void p1_epilogue(Ctx& c, const Args& A, const Map& M, Acc1& e, int last) {
  const int lane = opaque(c.lane), cw = c.wid & 3, hf = c.wid >> 2, wr = cw >> 1, wc = cw & 1, g = lane >> 4, q = (lane & 15) >> 2, p = lane & 3, i16 = lane & 15;
  const int lw = cw, i1 = M.i1, j1 = M.j1;
  const lds_t lds = c.lds;
  const f32x4 Z4 = {0.f, 0.f, 0.f, 0.f};
  bar();
  TS(20);
  if (hf) { const lds_t xf = lds + L_XF1 + cw * 12288 + lane * 16;
#pragma unroll
    for (int m = 0; m < 2; ++m)
#pragma unroll
      for (int n = 0; n < 2; ++n) { e.q[m][n] = *(LAS f32x4*)(xf + (m * 6 + n) * 1024); e.k[m][n] = *(LAS f32x4*)(xf + (m * 6 + 2 + n) * 1024); e.v[m][n] = *(LAS f32x4*)(xf + (m * 6 + 4 + n) * 1024); } }
  const lds_t tab = lds + L_TAB1;
  bf16x8 qf[2], kf[2], vf[2], wT[2];
  {
    f32x4 vv[2][2];
#pragma unroll
    for (int m = 0; m < 2; ++m) {
      const int R = 64 * wr + 32 * hf + 16 * m;
      const float mu = *(LAS float*)(tab + (R + i16) * 4), rs = *(LAS float*)(tab + 512 + (R + i16) * 4), nm = -mu * rs;
      const f32x4 mu4 = *(LAS f32x4*)(tab + (R + 4 * g) * 4), rs4 = *(LAS f32x4*)(tab + 512 + (R + 4 * g) * 4), nm4 = -mu4 * rs4;
      f32x4 qq[2], kk[2];
#pragma unroll
      for (int n = 0; n < 2; ++n) {
        const f32x4 cq = *(LAS f32x4*)(tab + 1024 + (32 * wc + 16 * n + 4 * g) * 4), ck = *(LAS f32x4*)(tab + 1024 + (64 + 32 * wc + 16 * n + 4 * g) * 4);
        const float cv = *(LAS float*)(tab + 1024 + (128 + 32 * wc + 16 * n + i16) * 4);
        qq[n] = e.q[m][n] * rs + cq * nm; kk[n] = e.k[m][n] * rs + ck * nm; vv[m][n] = e.v[m][n] * rs4 + nm4 * cv;
      }
      qf[m] = pk8(qq[0], qq[1]); kf[m] = pk8(kk[0], kk[1]);
    }
    vf[0] = pk8(vv[0][0], vv[1][0]); vf[1] = pk8(vv[0][1], vv[1][1]);
#pragma unroll
    for (int ft = 0; ft < 2; ++ft) wT[ft] = ldsT(lds + L_WPHI + wc * 2048 + (4 * g + q) * 64 + ft * 32 + 8 * p, 16 * 64);
  }
  wait_lgkm0(); bar();
  TS(21);
  {
    const lds_t tb = lds + L_TB + c.wid * 4096;
#pragma unroll
    for (int m = 0; m < 2; ++m) {
      const f32x4 p0 = mfma(wT[0], kf[m], Z4), p1 = mfma(wT[1], kf[m], Z4);
      f32x4 e0, e1, n0, n1; feat(p0, p1, e0, e1, n0, n1);
      const lds_t rw = tb + (16 * m + i16) * 128;
      *(LAS u32x2*)(rw + 8 * ((0 + g) ^ i16)) = pk4(e0); *(LAS u32x2*)(rw + 8 * ((4 + g) ^ i16)) = pk4(e1);
      *(LAS u32x2*)(rw + 8 * ((8 + g) ^ i16)) = pk4(n0); *(LAS u32x2*)(rw + 8 * ((12 + g) ^ i16)) = pk4(n1);
    }
    asm volatile("s_waitcnt lgkmcnt(0)" ::: "memory");
    const bf16x8 ones = __builtin_bit_cast(bf16x8, (u32x4){ONES2, ONES2, ONES2, ONES2});
    const lds_t kr = lds + L_KR + c.wid * 8192 + lane * 16;
    float zf[4];
#pragma unroll
    for (int f = 0; f < 4; ++f) {
      const int r0 = 4 * g + q;
      const s16x4 lo = __builtin_amdgcn_ds_read_tr16_b64_v4i16((LAS s16x4*)(tb + r0 * 128 + 8 * ((4 * f + p) ^ r0)));
      const s16x4 hi = __builtin_amdgcn_ds_read_tr16_b64_v4i16((LAS s16x4*)(tb + (16 + r0) * 128 + 8 * ((4 * f + p) ^ r0)));
      const bf16x8 pf = __builtin_shufflevector(lo, hi, 0, 1, 2, 3, 4, 5, 6, 7);
      *(LAS f32x4*)(kr + f * 1024) = mfma(vf[0], pf, Z4); *(LAS f32x4*)(kr + (4 + f) * 1024) = mfma(vf[1], pf, Z4);
      zf[f] = mfma(ones, pf, Z4)[0];
    }
    *(LAS float*)(lds + L_ZR + c.wid * 256 + lane * 4) = g == 0 ? zf[0] : g == 1 ? zf[1] : g == 2 ? zf[2] : zf[3];
    TS(22);
    wait_lgkm0(); bar();
    f32x4 k0 = Z4, k1 = Z4;
#pragma unroll
    for (int w = 0; w < 4; ++w) { const lds_t s = lds + L_KR + (wc + 2 * w) * 8192 + (4 * wr + 2 * hf) * 1024 + lane * 16; k0 += *(LAS f32x4*)s; k1 += *(LAS f32x4*)(s + 1024); }
    st16(c, pk8(k0, k1), lane * 16 + (OFF_KVP + (j1 * 16 + i1) * 8192 + wc * 4096 + wr * 2048 + hf * 1024));
    if (c.wid < 2) { float zt = 0.f;
#pragma unroll
      for (int w = 0; w < 4; ++w) zt += *(LAS float*)(lds + L_ZR + (wc + 2 * w) * 256 + lane * 4);
      __builtin_amdgcn_raw_buffer_store_b32(__float_as_uint(zt), c.ws, lane * 4, OFF_ZP + ((j1 * 16 + i1) * 2 + wc) * 256, SC_SC1); }
    wait_lgkm0(); bar();
    TS(23);
    publish<8>(c, 0, FG_KV, j1, i1);
  }
  TS(2);
  const __amdgpu_buffer_rsrc_t rW2 = __builtin_amdgcn_make_buffer_rsrc((void*)A.wo, 0, 1024 * 2048, 0x00020000);
  int voW2[2];
#pragma unroll
  for (int i = 0; i < 2; ++i) { const int r = (2 * lw + i) * 8 + (lane >> 3), s16 = lane & 7; voW2[i] = r * 2048 + M.j2 * 128 + (((s16 >> 1) ^ ((r >> 1) & 3)) << 5) + (s16 & 1) * 16; }
  auto issueW2 = [&](int t) {
#pragma unroll
    for (int i = 0; i < 2; ++i) dma<0>(rW2, lds + t * 8192 + (lw * 2 + i) * 1024, voW2[i], t * 64 * 2048); };
  bf16x8 ph[2][2];
#pragma unroll
  for (int m = 0; m < 2; ++m) {
    const f32x4 p0 = mfma(wT[0], qf[m], Z4), p1 = mfma(wT[1], qf[m], Z4);
    f32x4 e0, e1, n0, n1; feat(p0, p1, e0, e1, n0, n1);
    ph[m][0] = pk8(e0, e1); ph[m][1] = pk8(n0, n1);
  }
  asm volatile("" : "+v"(ph[0][0]), "+v"(ph[0][1]), "+v"(ph[1][0]), "+v"(ph[1][1]));
  TS(3);
  if (hf) {
    if (M.act3) {
      const __amdgpu_buffer_rsrc_t rW3 = __builtin_amdgcn_make_buffer_rsrc((void*)A.win, 0, 1024 * 7168, 0x00020000);
#pragma unroll
      for (int i = 0; i < 8; ++i) { const int r = (8 * lw + i) * 2 + (lane >> 5), l5 = lane & 31, seg = l5 >> 4, s16 = l5 & 15;
        dma<0>(rW3, lds + 32768 + (8 * lw + i) * 1024, r * 7168 + (seg == 0 ? 3584 : 0) + M.j3 * 256 + (((s16 >> 1) ^ (r & 7)) << 5) + (s16 & 1) * 16, M.i3 * 64 * 7168); }
    }
#pragma unroll
    for (int t = 0; t < 3; ++t) { issueW2(t); issueW2(8 + t); }
  } else if (c.wid == 0) wait_flags(c, FG_KV, j1, 16);
  bar();
  TS(27);
  kv_gather(c, j1);
  bar();
  TS(4);
  {
    bf16x8 kt[3][2];
#pragma unroll
    for (int d = 0; d < 3; ++d)
#pragma unroll
      for (int s = 0; s < 2; ++s) kt[d][s] = ldsT(lds + L_XBUF + wc * 8192 + (32 * s + 4 * g + q) * 128 + 32 * d + 8 * p, 16 * 128);
#pragma unroll
    for (int m = 0; m < 2; ++m) {
      const f32x4 n0 = mfma(kt[0][1], ph[m][1], mfma(kt[0][0], ph[m][0], Z4)), n1 = mfma(kt[1][1], ph[m][1], mfma(kt[1][0], ph[m][0], Z4));
      const f32x4 dn = mfma(kt[2][1], ph[m][1], mfma(kt[2][0], ph[m][0], Z4));
      const float r = __builtin_amdgcn_rcpf(dn[0] + 1e-6f);
      st16(c, pk8(n0 * r, n1 * r), lane * 16 + (OFF_OF + (i1 * 16 + j1) * 16384 + ((4 * wr + 2 * hf + m) * 2 + wc) * 1024));
    }
  }
  publish<8>(c, 1, FG_O, i1, j1);
  TS(5);
  if (hf && M.act3) {
    u32x2 cr[4][4];
#pragma unroll
    for (int n = 0; n < 4; ++n) tr4_issue<16 * 512, 32 * 512>((unsigned)(size_t)(lds + 32768 + (4 * g + q) * 512 + 8 * p + (lw >> 1) * 256 + 32 * ((4 * (lw & 1) + n) ^ (4 * (g & 1) + q))), cr[n]);
#pragma unroll
    for (int n = 0; n < 4; ++n) { tr4_wait(cr[n]); const float s = col_sum(cr[n]);
      if (lane < 16) st_granule(c, s, OFF_GC3 + (M.j3 * 16 + M.i3) * 2048 + ((4 * lw + n) * 16 + lane) * 8); }
  }
  if (hf) {
    issueW2(3);
#pragma unroll
    for (int t = 11; t < 16; ++t) issueW2(t);
  } else if (c.wid == 0 && last >= 2) wait_flags(c, FG_O, M.i2, 16);
}

struct Ld3 { __amdgpu_buffer_rsrc_t rW; int voW[8]; };
DI void p3_setup(const Ctx& c, const Args& A, int j3, Ld3& L) {
  const int lane = opaque(c.lane), lw = c.wid & 3;
  L.rW = __builtin_amdgcn_make_buffer_rsrc((void*)A.win, 0, 1024 * 7168, 0x00020000);
#pragma unroll
  for (int i = 0; i < 8; ++i) { const int r = (8 * lw + i) * 2 + (lane >> 5), l5 = lane & 31, seg = l5 >> 4, s16 = l5 & 15;
    L.voW[i] = r * 7168 + (seg == 0 ? 3584 : 0) + j3 * 256 + (((s16 >> 1) ^ (r & 7)) << 5) + (s16 & 1) * 16; }
}
DI void p3_issueW(const Ctx& c, const Ld3& L, int t) { const lds_t d = c.lds + (t % 3) * 49152 + 16384 + (c.wid & 3) * 8192;
#pragma unroll
  for (int i = 0; i < 8; ++i) dma<0>(L.rW, d + i * 1024, L.voW[i], t * 64 * 7168); }
DI void p3_load(Ctx& c, const Ld3& L, int i3) {
  const int lane = opaque(c.lane), lw = c.wid - 4;
  const lds_t lds = c.lds;
  constexpr int S = 3, SB = 49152;
  const int voF = lane * 16 + lw * 1024;
  auto issueA = [&](int t) { const lds_t d = lds + (t % S) * SB + lw * 1024; const int so = OFF_HF + (i3 * 16 + t) * 16384;
#pragma unroll
    for (int i = 0; i < 4; ++i) dma<LDAUX>(c.ws, d + i * 4096, voF, so + i * 4096); };
  auto issueW = [&](int t) { p3_issueW(c, L, t); };
  bar();
  issueA(0); issueA(1);
  u32x2 hv[16]; const int R = (lw & 1) * 64 + lane;
  auto gather = [&](int k) { if (lw < 2) {
      hv[2 * k] = __builtin_amdgcn_raw_buffer_load_b64(c.ws, R * 8, OFF_STH + (i3 * 16 + 2 * k) * 1024, SC_SC1);
      hv[2 * k + 1] = __builtin_amdgcn_raw_buffer_load_b64(c.ws, R * 8, OFF_STH + (i3 * 16 + 2 * k + 1) * 1024, SC_SC1); } };
  auto fin = [&]() { if (lw < 2) { float s = 0.f, qq = 0.f;
#pragma unroll
      for (int k = 0; k < 16; ++k) { s += __uint_as_float(hv[k][0]); qq += __uint_as_float(hv[k][1]); }
      s *= (1.0f / 1024.0f); qq *= (1.0f / 1024.0f);
      *(LAS float*)(lds + L_TAB3 + R * 4) = s; *(LAS float*)(lds + L_TAB3 + 512 + R * 4) = __builtin_amdgcn_rsqf(fmaxf(qq - s * s, 0.f) + 1e-5f);
      wait_lgkm0(); } };
  loader_loop16<8, true>(lw < 2, false, issueA, issueW, [](int) {}, gather, fin);
  TS(6);
}
struct Acc3 { f32x4 z2[2][4], z1[2][4]; };
DI int xf3_base(int cw) { return cw < 3 ? 16384 + cw * 32768 : 98304; }
DI void p3_mma(Ctx& c, int i3, int j3, Acc3& e) {
  const int lane = opaque(c.lane), wr = c.wid >> 1, wc = c.wid & 1, g = lane >> 4, q = (lane & 15) >> 2, p = lane & 3;
  const lds_t lds = c.lds;
  const __amdgpu_buffer_rsrc_t rWS = c.ws;
  const f32x4 Z4 = {0.f, 0.f, 0.f, 0.f};
  constexpr int S = 3, SB = 49152;
  if (wc == 1) {
    const int off = OFF_GC3 + j3 * 16 * 2048 + (128 * wr + 2 * lane) * 8; u32x4 gv[16];
    const unsigned long long t0 = __builtin_amdgcn_s_memrealtime();
    for (;;) {
      bool ok = true;
#pragma unroll
      for (int k = 0; k < 16; ++k) gv[k] = __builtin_amdgcn_raw_buffer_load_b128(rWS, off + k * 2048, 0, SC_SC1);
#pragma unroll
      for (int k = 0; k < 16; ++k) ok &= gv[k][1] == c.tag && gv[k][3] == c.tag;
      if (__all(ok) || timed_out(c, t0, 0x202u)) break;
      __builtin_amdgcn_s_sleep(2);
    }
    f32x2 cs = {0.f, 0.f};
#pragma unroll
    for (int k = 0; k < 16; ++k) { cs[0] += __uint_as_float(gv[k][0]); cs[1] += __uint_as_float(gv[k][2]); }
    *(LAS f32x2*)(lds + L_TAB3 + 1024 + (128 * wr + 2 * lane) * 4) = cs;
    wait_lgkm0();
  } else if (c.wid == 0) wait_flags(c, FG_H, i3, 16);
  f32x4 a2[4][4], a1_[4][4];
#pragma unroll
  for (int m = 0; m < 4; ++m)
#pragma unroll
    for (int n = 0; n < 4; ++n) { a2[m][n] = Z4; a1_[m][n] = Z4; }
#pragma unroll
  for (int m = 0; m < 4; ++m) asm volatile("" : "+v"(a2[m][0]), "+v"(a2[m][1]), "+v"(a2[m][2]), "+v"(a2[m][3]), "+v"(a1_[m][0]), "+v"(a1_[m][1]), "+v"(a1_[m][2]), "+v"(a1_[m][3]));
  bar();
  const int ao2 = wr * 8192 + lane * 16;
  int wo_[4];
#pragma unroll
  for (int n = 0; n < 4; ++n) wo_[n] = 16384 + (4 * g + q) * 512 + 8 * p + 32 * ((4 * wc + n) ^ (4 * (g & 1) + q));
  bf16x8 a0[4], w0[8], a1[4], w1[8];
  auto rd = [&](bf16x8(&a)[4], bf16x8(&w)[8], int sb, int ks) { const lds_t s = lds + sb;
#pragma unroll
    for (int n = 0; n < 8; ++n) w[n] = ldsT(s + wo_[n & 3] + (n >> 2) * 256 + ks * 32 * 512, 16 * 512);
#pragma unroll
    for (int m = 0; m < 4; ++m) a[m] = ldsA(s + ao2 + m * 2048 + ks * 1024); };
  auto mm = [&](bf16x8(&a)[4], bf16x8(&w)[8]) {
#pragma unroll
    for (int n = 0; n < 4; ++n)
#pragma unroll
      for (int m = 0; m < 4; ++m) a2[m][n] = mfma(a[m], w[n], a2[m][n]);
#pragma unroll
    for (int n = 0; n < 4; ++n)
#pragma unroll
      for (int m = 0; m < 4; ++m) a1_[m][n] = mfma(w[4 + n], a[m], a1_[m][n]); };
  consumer_loop<S, SB>(c, 16, 16, [&](int sb) { rd(a0, w0, sb, 0); }, [&](int sb) { rd(a1, w1, sb, 1); }, [&]() { mm(a0, w0); }, [&]() { mm(a1, w1); }, []() { interleave<32, 20>(); }, [](int) {}, [&]() { TS(17); });
  TS(8);
  const lds_t xf = lds + xf3_base(c.wid) + lane * 16;
#pragma unroll
  for (int m = 0; m < 2; ++m)
#pragma unroll
    for (int n = 0; n < 4; ++n) { *(LAS f32x4*)(xf + (m * 8 + n) * 1024) = a2[2 + m][n]; *(LAS f32x4*)(xf + (m * 8 + 4 + n) * 1024) = a1_[2 + m][n]; e.z2[m][n] = a2[m][n]; e.z1[m][n] = a1_[m][n]; }
  wait_lgkm0();
}
struct Ld4 { __amdgpu_buffer_rsrc_t rA; int voA, voF; };
DI void p4_issueW(const Ctx& c, const Ld4& L, int t) { const int lw = c.wid & 3; const lds_t d = c.lds + (t & 3) * 32768 + lw * 1024;
#pragma unroll
  for (int i = 0; i < 4; ++i) dma<0>(L.rA, d + i * 4096, L.voA, t * 128 + i * 32 * 4096); }
DI void p3_epilogue(Ctx& c, const Ld4& L, int i3, int j3, Acc3& e, u32x2 (&z1)[2][4]) {
  const int lane = opaque(c.lane), cw = c.wid & 3, hf = c.wid >> 2, wr = cw >> 1, wc = cw & 1, g = lane >> 4, i16 = lane & 15;
  const lds_t lds = c.lds, tab = lds + L_TAB3;
  const __amdgpu_buffer_rsrc_t rWS = c.ws;
  const f32x4 Z4 = {0.f, 0.f, 0.f, 0.f};
  bar();
  TS(25);
  __builtin_amdgcn_s_setprio(1);
  if (hf) { const lds_t xf = lds + xf3_base(cw) + lane * 16;
#pragma unroll
    for (int m = 0; m < 2; ++m)
#pragma unroll
      for (int n = 0; n < 4; ++n) { e.z2[m][n] = *(LAS f32x4*)(xf + (m * 8 + n) * 1024); e.z1[m][n] = *(LAS f32x4*)(xf + (m * 8 + 4 + n) * 1024); } }
  {
    float cs[4];
#pragma unroll
    for (int n = 0; n < 4; ++n) cs[n] = *(LAS float*)(tab + 1024 + (64 * wc + 16 * n + i16) * 4);
#pragma unroll
    for (int m = 0; m < 2; ++m) {
      const int R = 64 * wr + 32 * hf + 16 * m + 4 * g;
      const f32x4 mu = *(LAS f32x4*)(tab + R * 4), rs = *(LAS f32x4*)(tab + 512 + R * 4), nm = -mu * rs;
      f32x4 s = Z4, qq = Z4;
#pragma unroll
      for (int n = 0; n < 4; ++n) { const f32x4 y = gelu4(e.z2[m][n] * rs + nm * cs[n]); e.z2[m][n] = y; s += y; qq += y * y; }
      sum_row16x8(s, qq);
      if (i16 < 4) { const float sv = i16 == 0 ? s[0] : i16 == 1 ? s[1] : i16 == 2 ? s[2] : s[3], qv = i16 == 0 ? qq[0] : i16 == 1 ? qq[1] : i16 == 2 ? qq[2] : qq[3];
        __builtin_amdgcn_raw_buffer_store_b128((u32x4){__float_as_uint(sv), c.tag, __float_as_uint(qv), c.tag}, rWS, OFF_ST2 + ((i3 * 14 + j3) * 2 + wc) * 2048 + (R + i16) * 16, 0, SC_SC1); }
    }
  }
  TS(9);
  __builtin_amdgcn_s_setprio(0);
  if (!hf) {
#pragma unroll
    for (int s = 0; s < 3; ++s) p4_issueW(c, L, s);
  }
  {
    const int R0 = 64 * wr + 32 * hf, off = OFF_ST2 + (i3 * 28 + (lane >> 5)) * 2048 + (R0 + (lane & 31)) * 16;
    u32x4 v[14];
    const unsigned long long t0 = __builtin_amdgcn_s_memrealtime();
    for (;;) {
      bool ok = true;
#pragma unroll
      for (int k = 0; k < 14; ++k) v[k] = __builtin_amdgcn_raw_buffer_load_b128(rWS, off + k * 4096, 0, SC_SC1);
#pragma unroll
      for (int k = 0; k < 14; ++k) ok &= v[k][1] == c.tag && v[k][3] == c.tag;
      if (__all(ok) || timed_out(c, t0, 0x203u)) break;
      __builtin_amdgcn_s_sleep(PSLEEP);
    }
    __builtin_amdgcn_s_setprio(1);
    float s = 0.f, qq = 0.f;
#pragma unroll
    for (int k = 0; k < 14; ++k) { s += __uint_as_float(v[k][0]); qq += __uint_as_float(v[k][2]); }
    { auto a = __builtin_amdgcn_permlane32_swap(__float_as_uint(s), __float_as_uint(s), false, false); s = (__uint_as_float(a[0]) + __uint_as_float(a[1])) * (1.0f / 1792.0f);
      auto b = __builtin_amdgcn_permlane32_swap(__float_as_uint(qq), __float_as_uint(qq), false, false); qq = (__uint_as_float(b[0]) + __uint_as_float(b[1])) * (1.0f / 1792.0f); }
    const float rsv = __builtin_amdgcn_rsqf(fmaxf(qq - s * s, 0.f) + 1e-5f);
    const lds_t ft = lds + L_FIN + c.wid * 256;
    if (lane < 32) { *(LAS float*)(ft + lane * 4) = rsv; *(LAS float*)(ft + 128 + lane * 4) = -s * rsv; }
    asm volatile("s_waitcnt lgkmcnt(0)" ::: "memory");
    TS(31);
    f32x4 rs[2], nm[2];
#pragma unroll
    for (int m = 0; m < 2; ++m) { rs[m] = *(LAS f32x4*)(ft + (16 * m + 4 * g) * 4); nm[m] = *(LAS f32x4*)(ft + 128 + (16 * m + 4 * g) * 4); }
#pragma unroll
    for (int n = 0; n < 4; ++n)
      st16(c, pk8(e.z2[0][n] * rs[0] + nm[0], e.z2[1][n] * rs[1] + nm[1]), lane * 16 + (OFF_Z2 + (j3 * 32 + 2 * i3 + wr) * 16384 + ((4 * wc + n) * 2 + hf) * 1024));
  }
  __builtin_amdgcn_s_setprio(0);
  publish<8>(c, 4, FG_Z2, j3, i3);
  TS(11);
#pragma unroll
  for (int m = 0; m < 2; ++m) asm volatile("" : "+v"(e.z1[m][0]), "+v"(e.z1[m][1]), "+v"(e.z1[m][2]), "+v"(e.z1[m][3]));
#pragma unroll
  for (int m = 0; m < 2; ++m) {
    const int R = 64 * wr + 32 * hf + 16 * m + i16;
    const float mu = *(LAS float*)(tab + R * 4), rs = *(LAS float*)(tab + 512 + R * 4), nm = -mu * rs;
#pragma unroll
    for (int n = 0; n < 4; ++n) { const f32x4 cs = *(LAS f32x4*)(tab + 1024 + (128 + 64 * wc + 16 * n + 4 * g) * 4); z1[m][n] = pk4(gelu4(e.z1[m][n] * rs + cs * nm)); }
    asm volatile("" : "+v"(z1[m][0]), "+v"(z1[m][1]), "+v"(z1[m][2]), "+v"(z1[m][3]));
  }
  TS(10);
}
DI void p4_load(Ctx& c, const Ld4& L, int j3) {
  const int lw = c.wid - 4;
  auto issueW = [&](int t) { p4_issueW(c, L, t); };
  auto issueA = [&](int t) { const lds_t d = c.lds + (t & 3) * 32768 + 16384 + lw * 1024; const int so = OFF_Z2 + (j3 * 32 + t) * 16384;
#pragma unroll
    for (int i = 0; i < 4; ++i) dma<LDAUX>(c.ws, d + i * 4096, L.voF, so + i * 4096); };
  bar();
  if (c.b == 0 && lw == 0 && c.lane == 0) __hip_atomic_store((unsigned*)(c.wsp + OFF_EPOCH), c.tag, __ATOMIC_RELAXED, __HIP_MEMORY_SCOPE_AGENT);
#pragma unroll
  for (int s = 0; s < 3; ++s) issueA(s);
  loader_loop<4, 4>(32, true, issueA, issueW, [](int) {}, [](int) {});
}
DI void p4_mma(Ctx& c, int j3, f32x4 (&e)[2][4]) {
  const int lane = opaque(c.lane), wr = c.wid >> 1, wc = c.wid & 1, g = lane >> 4, i16 = lane & 15;
  const lds_t lds = c.lds;
  constexpr int S = 4, SB = 32768;
  f32x4 acc[4][4];
#pragma unroll
  for (int m = 0; m < 4; ++m)
#pragma unroll
    for (int n = 0; n < 4; ++n) { acc[m][n] = (f32x4){0.f, 0.f, 0.f, 0.f}; asm volatile("" : "+v"(acc[m][n])); }
  const int ao0 = (wr * 64 + i16) * 128 + ((g ^ (i16 >> 1)) << 4), ao1 = ao0 ^ 64, zo = 16384 + wc * 8192 + lane * 16;
  bf16x8 a0[4], w0[4], a1[4], w1[4];
  auto rd = [&](bf16x8(&a)[4], bf16x8(&w)[4], int sb, int ks) { const lds_t s = lds + sb;
#pragma unroll
    for (int n = 0; n < 4; ++n) { w[n] = ldsA(s + zo + n * 2048 + ks * 1024); a[n] = ldsA(s + (ks ? ao1 : ao0) + n * 2048); } };
  auto mm = [&](bf16x8(&a)[4], bf16x8(&w)[4]) {
#pragma unroll
    for (int n = 0; n < 4; ++n)
#pragma unroll
      for (int m = 0; m < 4; ++m) acc[m][n] = mfma(w[n], a[m], acc[m][n]); };
  if (c.wid == 0) wait_flags(c, FG_Z2, j3, 16);
  bar();
  consumer_loop<S, SB>(c, 32, 32, [&](int sb) { rd(a0, w0, sb, 0); }, [&](int sb) { rd(a1, w1, sb, 1); }, [&]() { mm(a0, w0); }, [&]() { mm(a1, w1); }, []() { interleave<16, 8>(); }, [](int) {}, [&]() { TS(18); });
  TS(12);
  const lds_t xf = lds + c.wid * 8192 + lane * 16;
#pragma unroll
  for (int m = 0; m < 2; ++m)
#pragma unroll
    for (int n = 0; n < 4; ++n) { *(LAS f32x4*)(xf + (m * 4 + n) * 1024) = acc[2 + m][n]; e[m][n] = acc[m][n]; }
  wait_lgkm0();
}
DI void p5_issueW(const Ctx& c, const Args& A, int j2);
DI void p4_epilogue(Ctx& c, const Args& A, int i3, int j3, int j2, f32x4 (&e)[2][4], const u32x2 (&z1)[2][4]) {
  const int lane = opaque(c.lane), cw = c.wid & 3, hf = c.wid >> 2, wr = cw >> 1, wc = cw & 1, i16 = lane & 15;
  float bs[2];
#pragma unroll
  for (int m = 0; m < 2; ++m) bs[m] = bflo((unsigned)A.bsp[i3 * 128 + true_row(4 * wr + 2 * hf + m, i16)]);
  bar();
  if (hf) { const lds_t xf = c.lds + cw * 8192 + lane * 16;
#pragma unroll
    for (int m = 0; m < 2; ++m)
#pragma unroll
      for (int n = 0; n < 4; ++n) e[m][n] = *(LAS f32x4*)(xf + (m * 4 + n) * 1024); }
#pragma unroll
  for (int m = 0; m < 2; ++m)
#pragma unroll
    for (int u = 0; u < 2; ++u)
      st16(c, pk8(up4(z1[m][2 * u]) * (e[m][2 * u] + bs[m]), up4(z1[m][2 * u + 1]) * (e[m][2 * u + 1] + bs[m])),
           lane * 16 + (OFF_UF + (i3 * 28 + 2 * j3 + wc) * 16384 + ((4 * wr + 2 * hf + m) * 2 + u) * 1024));
  wait_lgkm0(); bar();
  publish<8>(c, 5, FG_U, i3, j3);
  if (hf) p5_issueW(c, A, j2);
  TS(13);
}

DI void p5_issueW(const Ctx& c, const Args& A, int j2) {
  const int lane = opaque(c.lane), lw = c.wid - 4;
  const __amdgpu_buffer_rsrc_t rW = __builtin_amdgcn_make_buffer_rsrc((void*)A.wout, 0, 1792 * 2048, 0x00020000);
  int vo[2];
#pragma unroll
  for (int i = 0; i < 2; ++i) { const int r = (2 * lw + i) * 8 + (lane >> 3), s16 = lane & 7; vo[i] = r * 2048 + j2 * 128 + (((s16 >> 1) ^ ((r >> 1) & 3)) << 5) + (s16 & 1) * 16; }
#pragma unroll
  for (int t = 0; t < 18; ++t)
#pragma unroll
    for (int i = 0; i < 2; ++i) dma<0>(rW, c.lds + ((t & 1) * 9 + (t >> 1)) * 8192 + (lw * 2 + i) * 1024, vo[i], ((t & 1) * 14 + (t >> 1)) * 64 * 2048);
}

constexpr int L_RED = 98304;
#define STG1 0
#define STG2 2
#define R5 6
#define R2 6
template <int NSH, int R, bool REFILL>
DI void gemm_split(Ctx& c, f32x4 (&res)[4], int aoff, __amdgpu_buffer_rsrc_t rW, int j2) {
  const int ln = opaque(c.lane), g = ln >> 4, q = (ln & 15) >> 2, p = ln & 3, rw = c.wid & 3, kh = c.wid >> 2;
  const lds_t lds = c.lds;
  const int sw2 = (2 * g + (q >> 1)) & 3, wl2 = (4 * g + q) * 128 + 8 * p + kh * (REFILL ? 9 : 8) * 8192, va = ln * 16 + rw * 4096;
  const int ao = aoff + kh * (NSH / 2) * 16384;
  int wo2[4];
#pragma unroll
  for (int n = 0; n < 4; ++n) wo2[n] = wl2 + 32 * (n ^ sw2);
  f32x4 acc[2][4];
#pragma unroll
  for (int n = 0; n < 4; ++n) { acc[0][n] = (f32x4){0.f, 0.f, 0.f, 0.f}; acc[1][n] = acc[0][n]; }
  u32x4 a[R][2]; bf16x8 w0[4], w1[4];
  auto lda = [&](int s) {
#pragma unroll
    for (int m = 0; m < 2; ++m) a[s % R][m] = __builtin_amdgcn_raw_buffer_load_b128(c.ws, va + m * 2048, ao + (s >> 1) * 16384 + (s & 1) * 1024, LDAUX); };
  auto ldw = [&](bf16x8(&d)[4], int s) { const int lt = s >> 1, slot = (REFILL && lt >= 9) ? lt - 9 : lt;
#pragma unroll
    for (int n = 0; n < 4; ++n) d[n] = ldsT(lds + wo2[n] + slot * 8192 + (s & 1) * 4096, 16 * 128); };
  TS(REFILL ? 19 : 24);
#pragma unroll
  for (int s = 0; s < R; ++s) { lda(s); if (s == STG1 || s == STG2) bar(); }
  if (!REFILL && kh) {
#pragma unroll
    for (int i = 0; i < 2; ++i) { const int r = (2 * rw + i) * 8 + (ln >> 3), s16 = ln & 7, vo = r * 2048 + j2 * 128 + (((s16 >> 1) ^ ((r >> 1) & 3)) << 5) + (s16 & 1) * 16;
#pragma unroll
      for (int t = 4; t < 8; ++t) dma<0>(rW, lds + t * 8192 + (rw * 2 + i) * 1024, vo, t * 64 * 2048); }
  }
  ldw(w0, 0);
#pragma unroll
  for (int s = 0; s < NSH; ++s) {
    if (s == 5) {
      if (kh) { if (REFILL) wait_vm<2 * R + 10>(); else wait_vm<10>(); }
      bar(); __builtin_amdgcn_sched_barrier(0);
    }
    if (REFILL && s == 10) {
      wait_lgkm0(); bar();
      const int r = 8 * c.wid + (ln >> 3), s16 = ln & 7, vo = r * 2048 + j2 * 128 + (((s16 >> 1) ^ ((r >> 1) & 3)) << 5) + (s16 & 1) * 16;
#pragma unroll
      for (int k = 0; k < 10; ++k) dma<0>(rW, lds + ((k / 5) * 9 + (k % 5)) * 8192 + c.wid * 1024, vo, ((k / 5) * 14 + 9 + (k % 5)) * 64 * 2048);
      __builtin_amdgcn_sched_barrier(0);
    }
    if (REFILL && s == 17) { wait_vm<2 * ((NSH - R < 17 ? NSH - R : 17) - 10)>(); wait_lgkm0(); bar(); __builtin_amdgcn_sched_barrier(0); }
    if (s + 1 < NSH) { if (s & 1) ldw(w0, s + 1); else ldw(w1, s + 1); }
#pragma unroll
    for (int n = 0; n < 4; ++n)
#pragma unroll
      for (int m = 0; m < 2; ++m) acc[m][n] = mfma((s & 1) ? w1[n] : w0[n], __builtin_bit_cast(bf16x8, a[s % R][m]), acc[m][n]);
    if (s + R < NSH) lda(s + R);
    interleave<8, 8>();
    __builtin_amdgcn_sched_barrier(0);
  }
  TS(REFILL ? 29 : 28);
  wait_lgkm0(); bar();
#pragma unroll
  for (int n = 0; n < 4; ++n) *(LAS f32x4*)(lds + L_RED + c.wid * 4096 + n * 1024 + ln * 16) = kh ? acc[0][n] : acc[1][n];
  wait_lgkm0(); bar();
#pragma unroll
  for (int n = 0; n < 4; ++n) res[n] = (kh ? acc[1][n] : acc[0][n]) + *(LAS f32x4*)(lds + L_RED + (c.wid ^ 4) * 4096 + n * 1024 + ln * 16);
}

template <class PRE> DI void phase2(Ctx& c, const Args& A, int i2, int j2, bool pre_on, PRE pre) {
  const __amdgpu_buffer_rsrc_t rW2 = __builtin_amdgcn_make_buffer_rsrc((void*)A.wo, 0, 1024 * 2048, 0x00020000);
  const int lane = opaque(c.lane), g = lane >> 4, i16 = lane & 15, mt = 2 * (c.wid & 3) + (c.wid >> 2);
  u32x2 xr[4];
#pragma unroll
  for (int n = 0; n < 4; ++n) xr[n] = *(const u32x2*)(A.x + (size_t)(i2 * 128 + true_row(mt, i16)) * 1024 + j2 * 64 + 16 * n + 4 * g);
  wait_lgkm0(); bar();
  f32x4 acc[4];
  gemm_split<16, R2, false>(c, acc, OFF_OF + i2 * 262144, rW2, j2);
  TS(6);
  float s = 0.f, qq = 0.f;
#pragma unroll
  for (int u = 0; u < 2; ++u) {
    f32x4 h0 = acc[2 * u], h1 = acc[2 * u + 1]; const u32x2 x0 = xr[2 * u], x1 = xr[2 * u + 1];
    h0[0] += bflo(x0[0]); h0[1] += bfhi(x0[0]); h0[2] += bflo(x0[1]); h0[3] += bfhi(x0[1]);
    h1[0] += bflo(x1[0]); h1[1] += bfhi(x1[0]); h1[2] += bflo(x1[1]); h1[3] += bfhi(x1[1]);
    const bf16x8 hb = pk8(h0, h1);
    st16(c, hb, lane * 16 + (OFF_HF + (i2 * 16 + j2) * 16384 + (mt * 2 + u) * 1024));
    acc_row(s, qq, __builtin_bit_cast(u32x4, hb));
  }
  s = sum_g(s); qq = sum_g(qq);
  if (lane < 16) __builtin_amdgcn_raw_buffer_store_b64((u32x2){__float_as_uint(s), __float_as_uint(qq)}, c.ws, OFF_STH + (i2 * 16 + j2) * 1024 + (mt * 16 + lane) * 8, 0, SC_SC1);
  publish<8>(c, 2, FG_H, i2, j2);
  if (pre_on) pre();
  TS(7);
}

DI void phase5(Ctx& c, const Args& A, int i2, int j2) {
  const int lane = opaque(c.lane), g = lane >> 4, i16 = lane & 15, mt = 2 * (c.wid & 3) + (c.wid >> 2);
  u32x4 hr[2];
#pragma unroll
  for (int u = 0; u < 2; ++u) hr[u] = __builtin_amdgcn_raw_buffer_load_b128(c.ws, lane * 16, OFF_HF + (i2 * 16 + j2) * 16384 + (mt * 2 + u) * 1024, SC_SC1);
  const __amdgpu_buffer_rsrc_t rW = __builtin_amdgcn_make_buffer_rsrc((void*)A.wout, 0, 1792 * 2048, 0x00020000);
  if (c.wid == 0) wait_flags(c, FG_U, i2, 14);
  if (c.wid >= 4) wait_vm<24>();
  bar();
  f32x4 acc[4];
  gemm_split<28, R5, true>(c, acc, OFF_UF + i2 * 458752, rW, j2);
  TS(14);
  const lds_t ot = c.lds + c.wid * 2048;
#pragma unroll
  for (int n = 0; n < 4; ++n) { const unsigned r0 = hr[n >> 1][2 * (n & 1)], r1 = hr[n >> 1][2 * (n & 1) + 1];
    *(LAS u32x2*)(ot + i16 * 128 + (((2 * n + (g >> 1)) ^ (i16 & 7)) << 4) + (g & 1) * 8) =
        (u32x2){pk2(acc[n][0] + bflo(r0), acc[n][1] + bfhi(r0)), pk2(acc[n][2] + bflo(r1), acc[n][3] + bfhi(r1))}; }
  asm volatile("s_waitcnt lgkmcnt(0)" ::: "memory");
  const __amdgpu_buffer_rsrc_t rO = __builtin_amdgcn_make_buffer_rsrc((void*)A.out, 0, 2048 * 2048, 0x00020000);
#pragma unroll
  for (int u = 0; u < 2; ++u) { const int r = 8 * u + (lane >> 3), ch = lane & 7;
    const u32x4 v = *(LAS u32x4*)(ot + r * 128 + ((ch ^ (r & 7)) << 4));
    __builtin_amdgcn_raw_buffer_store_b128(v, rO, (i2 * 128 + true_row(mt, r)) * 2048 + j2 * 128 + ch * 16, 0, OUTAUX); }
  TS(15);
}

#define LAST_PHASE 5
__global__ __launch_bounds__(512, 2) void fused_fwd(const u16* __restrict__ x, const u16* __restrict__ wqkv, const u16* __restrict__ wphi, const u16* __restrict__ wo,
                                                      const u16* __restrict__ win, const u16* __restrict__ wsp, const u16* __restrict__ bsp, const u16* __restrict__ wout,
                                                      u16* __restrict__ out, char* __restrict__ ws) {
  extern __shared__ __attribute__((aligned(16))) char shm[];
  Ctx c;
  c.lds = (lds_t)shm; c.wsp = ws; c.lane = threadIdx.x & 63; c.wid = __builtin_amdgcn_readfirstlane(threadIdx.x >> 6); c.b = blockIdx.x;
  c.ws = __builtin_amdgcn_make_buffer_rsrc((void*)ws, 0, WS_BYTES, 0x00020000);
  c.tag = 0u; c.ts = 0u;
  if (threadIdx.x < 16) *(LAS unsigned*)(c.lds + L_CNT + threadIdx.x * 4) = 0u;
  const int xcd = c.b & 7, jj = c.b >> 3;
  Map M;
  M.i1 = 8 * (xcd & 1) + (jj >> 2); M.j1 = 4 * (xcd >> 1) + (jj & 3);
  M.i2 = 4 * (xcd >> 1) + (jj >> 3); M.j2 = 8 * (xcd & 1) + (jj & 7);
  M.act3 = jj < 28;
  M.i3 = 4 * (xcd >> 1) + (M.act3 ? jj / 7 : 0); M.j3 = 7 * (xcd & 1) + (M.act3 ? jj % 7 : 0);
  const Args A{x, wqkv, wphi, wo, win, wsp, bsp, wout, out, ws};
  constexpr int last = LAST_PHASE;
  const bool ld = c.wid >= 4;
  {
    Acc1 e;
    if (ld) p1_load(c, A, M.i1, M.j1);
    else { __builtin_amdgcn_s_setprio(1); p1_mma(c, A, M.i1, M.j1, e); __builtin_amdgcn_s_setprio(0); }
    p1_epilogue(c, A, M, e, last);
  }
  if (last < 2) return;
  Ld3 L3;
  p3_setup(c, A, M.j3, L3);
  phase2(c, A, M.i2, M.j2, ld && M.act3, [&]() { p3_issueW(c, L3, 0); p3_issueW(c, L3, 1); });
  if (last < 3) return;
  if (M.act3) {
    u32x2 z1[2][4];
    Ld4 L;
    L.rA = __builtin_amdgcn_make_buffer_rsrc((void*)(A.wsp + (size_t)M.i3 * 128 * 2048), 0, 128 * 4096, 0x00020000);
    {
      Acc3 e;
      if (ld) p3_load(c, L3, M.i3);
      else { __builtin_amdgcn_s_setprio(1); p3_mma(c, M.i3, M.j3, e); __builtin_amdgcn_s_setprio(0); }
      const int ln = opaque(c.lane);
      L.voA = rowmajor_voff(c.wid & 3, ln, 4096); L.voF = ln * 16 + (c.wid & 3) * 1024;
      p3_epilogue(c, L, M.i3, M.j3, e, z1);
    }
    if (last < 4) return;
    f32x4 e4[2][4];
    if (ld) p4_load(c, L, M.j3);
    else { __builtin_amdgcn_s_setprio(1); p4_mma(c, M.j3, e4); __builtin_amdgcn_s_setprio(0); }
    p4_epilogue(c, A, M.i3, M.j3, M.j2, e4, z1);
  } else {
    bar();
    if (!ld) { const int wv = ((c.b & 7) * 4 + ((c.b >> 3) - 28)) * 4 + c.wid;
#pragma unroll 1
      for (int k = 0; k < 12; ++k) { const int Li = (k * 128 + wv) * 64 + c.lane;
        touch(c.lds, Li < 65536 ? (const char*)A.wsp + (size_t)Li * 128 : (const char*)A.wout + (size_t)((Li < 94208 ? Li : 94207) - 65536) * 128); } }
    if (ld) p5_issueW(c, A, M.j2);
  }
  if (last < 5) return;
  phase5(c, A, M.i2, M.j2);
}

extern "C" void kernel_launch(void* const* d_in, const int* in_sizes, int n_in, void* d_out, int out_size, void* d_ws, size_t ws_size, hipStream_t stream) {
  (void)in_sizes; (void)n_in; (void)out_size; (void)ws_size;
  (void)hipFuncSetAttribute((const void*)fused_fwd, hipFuncAttributeMaxDynamicSharedMemorySize, LDS_BYTES);
  fused_fwd<<<256, 512, LDS_BYTES, stream>>>((const u16*)d_in[0], (const u16*)d_in[1], (const u16*)d_in[2], (const u16*)d_in[3], (const u16*)d_in[5], (const u16*)d_in[6],
                                             (const u16*)d_in[7], (const u16*)d_in[8], (u16*)d_out, (char*)d_ws);
}
```

```cpp
#include <hip/hip_runtime.h>
#include <hip/hip_bf16.h>
#include <stdint.h>
#include <type_traits>

#define LAS __attribute__((address_space(3)))
#define DI __device__ __forceinline__
typedef unsigned short u16;
typedef short bf16x8 __attribute__((ext_vector_type(8)));
typedef short s16x4 __attribute__((ext_vector_type(4)));
typedef float f32x4 __attribute__((ext_vector_type(4)));
typedef float f32x2 __attribute__((ext_vector_type(2)));
typedef unsigned u32x4 __attribute__((ext_vector_type(4)));
typedef unsigned u32x2 __attribute__((ext_vector_type(2)));
typedef __bf16 bf2 __attribute__((ext_vector_type(2)));
typedef LAS char* lds_t;

constexpr unsigned OFF_EPOCH = 0, OFF_ERR = 128, OFF_FLAG = 4096;
constexpr unsigned OFF_KVP = 262144;
constexpr unsigned OFF_ZP = OFF_KVP + 2097152;
constexpr unsigned OFF_ST2 = 27u << 20 | 0;
constexpr unsigned OFF_STH = OFF_ZP + 131072 + 262144;
constexpr unsigned OFF_GX = OFF_STH + 262144;
constexpr unsigned OFF_GC1 = OFF_GX + 65536;
constexpr unsigned OFF_GC3 = OFF_GC1 + 393216;
constexpr unsigned OFF_OF = 4u << 20, OFF_HF = 8u << 20, OFF_Z2 = 12u << 20, OFF_UF = 20u << 20, OFF_TS = 28u << 20;
constexpr unsigned WS_BYTES = 32u << 20;
enum { FG_KV = 0, FG_O = 1, FG_H = 2, FG_ST = 3, FG_Z2 = 4, FG_U = 5 };
constexpr int LDS_BYTES = 163840;
constexpr int SC1 = 122880, L_WPHI = SC1, L_TAB1 = SC1 + 4096, L_XBUF = SC1 + 22528;
constexpr int L_XF1 = 65536, L_TB = 32768, L_KR = 65536, L_ZR = 131072;
constexpr int SC3 = 147456, L_TAB3 = SC3, L_FIN = SC3 + 4096;
constexpr int L_CNT = LDS_BYTES - 64, L_DUMMY = LDS_BYTES - 512;
constexpr int SC_SC1 = 16;
#define PSLEEP 4
#define OUTAUX 16
#define LDAUX 16

DI void bar() { asm volatile("" ::: "memory"); __builtin_amdgcn_s_barrier(); asm volatile("" ::: "memory"); }
template <int N> DI void wait_vm() { asm volatile("s_waitcnt vmcnt(%0)" ::"n"(N) : "memory"); }
DI int opaque(int v) { asm volatile("" : "+v"(v)); return v; }
DI void wait_lgkm0() { __builtin_amdgcn_s_waitcnt(0xc07f); }
DI unsigned pk2(float lo, float hi) { f32x2 v = {lo, hi}; return __builtin_bit_cast(unsigned, __builtin_convertvector(v, bf2)); }
DI bf16x8 pk8(const f32x4& a, const f32x4& b) { u32x4 r = {pk2(a[0], a[1]), pk2(a[2], a[3]), pk2(b[0], b[1]), pk2(b[2], b[3])}; return __builtin_bit_cast(bf16x8, r); }
DI float bflo(unsigned w) { return __uint_as_float(w << 16); }
DI float bfhi(unsigned w) { return __uint_as_float(w & 0xffff0000u); }
DI f32x4 mfma(bf16x8 a, bf16x8 b, f32x4 c) { return __builtin_amdgcn_mfma_f32_16x16x32_bf16(a, b, c, 0, 0, 0); }
DI bf16x8 ldsA(lds_t p) { return *(LAS bf16x8*)p; }
DI bf16x8 ldsT(lds_t p, int d) {
  s16x4 lo = __builtin_amdgcn_ds_read_tr16_b64_v4i16((LAS s16x4*)p), hi = __builtin_amdgcn_ds_read_tr16_b64_v4i16((LAS s16x4*)(p + d));
  return __builtin_shufflevector(lo, hi, 0, 1, 2, 3, 4, 5, 6, 7);
}
template <int D1, int D2> DI void tr4_issue(unsigned addr, u32x2 (&r)[4]) {
  asm volatile("ds_read_b64_tr_b16 %0, %4\n\tds_read_b64_tr_b16 %1, %4 offset:%5\n\tds_read_b64_tr_b16 %2, %4 offset:%6\n\tds_read_b64_tr_b16 %3, %4 offset:%7"
               : "=&v"(r[0]), "=&v"(r[1]), "=&v"(r[2]), "=&v"(r[3]) : "v"(addr), "n"(D1), "n"(D2), "n"(D1 + D2) : "memory");
}
DI void tr4_wait(u32x2 (&r)[4]) { asm volatile("s_waitcnt lgkmcnt(0)" : "+v"(r[0]), "+v"(r[1]), "+v"(r[2]), "+v"(r[3])::"memory"); }
DI float sum_g(float x) {
  auto a = __builtin_amdgcn_permlane16_swap(__float_as_uint(x), __float_as_uint(x), false, false);
  x = __uint_as_float(a[0]) + __uint_as_float(a[1]);
  auto b = __builtin_amdgcn_permlane32_swap(__float_as_uint(x), __float_as_uint(x), false, false);
  return __uint_as_float(b[0]) + __uint_as_float(b[1]);
}
DI float max_g(float x) {
  auto a = __builtin_amdgcn_permlane16_swap(__float_as_uint(x), __float_as_uint(x), false, false);
  x = fmaxf(__uint_as_float(a[0]), __uint_as_float(a[1]));
  auto b = __builtin_amdgcn_permlane32_swap(__float_as_uint(x), __float_as_uint(x), false, false);
  return fmaxf(__uint_as_float(b[0]), __uint_as_float(b[1]));
}
template <int CTRL> DI float dpp_add(float x) { return x + __int_as_float(__builtin_amdgcn_update_dpp(0, __float_as_int(x), CTRL, 0xf, 0xf, true)); }
DI float sum_row16(float x) { x = dpp_add<0x128>(x); x = dpp_add<0x124>(x); x = dpp_add<0x122>(x); return dpp_add<0x121>(x); }
DI void sum_row16x8(f32x4& a, f32x4& b) {
#define RR(n) "v_add_f32_dpp %0, %0, %0 row_ror:" n " row_mask:0xf bank_mask:0xf\n\tv_add_f32_dpp %1, %1, %1 row_ror:" n " row_mask:0xf bank_mask:0xf\n\t" \
  "v_add_f32_dpp %2, %2, %2 row_ror:" n " row_mask:0xf bank_mask:0xf\n\tv_add_f32_dpp %3, %3, %3 row_ror:" n " row_mask:0xf bank_mask:0xf\n\t" \
  "v_add_f32_dpp %4, %4, %4 row_ror:" n " row_mask:0xf bank_mask:0xf\n\tv_add_f32_dpp %5, %5, %5 row_ror:" n " row_mask:0xf bank_mask:0xf\n\t" \
  "v_add_f32_dpp %6, %6, %6 row_ror:" n " row_mask:0xf bank_mask:0xf\n\tv_add_f32_dpp %7, %7, %7 row_ror:" n " row_mask:0xf bank_mask:0xf\n\t"
  float a0 = a[0], a1 = a[1], a2 = a[2], a3 = a[3], b0 = b[0], b1 = b[1], b2 = b[2], b3 = b[3];
  asm volatile("s_nop 1\n\t" RR("8") RR("4") RR("2") RR("1") "s_nop 0" : "+v"(a0), "+v"(a1), "+v"(a2), "+v"(a3), "+v"(b0), "+v"(b1), "+v"(b2), "+v"(b3));
#undef RR
  a = (f32x4){a0, a1, a2, a3}; b = (f32x4){b0, b1, b2, b3};
}
DI float dot2(unsigned a, unsigned b, float c) { return __builtin_amdgcn_fdot2_f32_bf16(__builtin_bit_cast(bf2, a), __builtin_bit_cast(bf2, b), c, false); }
constexpr unsigned ONES2 = 0x3f803f80u;
DI f32x4 gelu4(f32x4 x) {
  const f32x4 t = x * (x * x * -0.1029432397f + -2.302208198f);
  f32x4 d, r;
#pragma unroll
  for (int i = 0; i < 4; ++i) d[i] = __builtin_amdgcn_exp2f(t[i]);
  d += 1.0f;
#pragma unroll
  for (int i = 0; i < 4; ++i) r[i] = __builtin_amdgcn_rcpf(d[i]);
  return x * r;
}
DI u32x2 pk4(const f32x4& a) { return (u32x2){pk2(a[0], a[1]), pk2(a[2], a[3])}; }
DI f32x4 up4(u32x2 w) { return (f32x4){bflo(w[0]), bfhi(w[0]), bflo(w[1]), bfhi(w[1])}; }
DI float gelu(float x) { const float t = x * fmaf(x * x, -0.1029432397f, -2.302208198f); return x * __builtin_amdgcn_rcpf(1.0f + __builtin_amdgcn_exp2f(t)); }

struct Ctx {
  __amdgpu_buffer_rsrc_t ws;
  char* wsp; lds_t lds; unsigned tag; int lane, wid, b; unsigned ts;
};
DI unsigned* flag_ptr(const Ctx& c, int g, int a, int bb) { return (unsigned*)(c.wsp + OFF_FLAG + ((g * 16 + a) * 16 + bb) * 128); }
template <unsigned NWAVES = 4, int YOUNGER = 0> DI void publish(const Ctx& c, int cnt, int g, int a, int bb) {
  wait_vm<YOUNGER>();
  if (c.lane == 0) {
    unsigned old = __hip_atomic_fetch_add((LAS unsigned*)(c.lds + L_CNT + cnt * 4), 1u, __ATOMIC_RELAXED, __HIP_MEMORY_SCOPE_WORKGROUP);
    if (old == NWAVES - 1u) __hip_atomic_store(flag_ptr(c, g, a, bb), c.tag, __ATOMIC_RELAXED, __HIP_MEMORY_SCOPE_AGENT);
  }
}
DI void wait_flags(const Ctx& c, int g, int a, int n) {
  const unsigned* p = flag_ptr(c, g, a, c.lane < n ? c.lane : 0);
  const unsigned long long t0 = __builtin_amdgcn_s_memrealtime();
  for (;;) {
    const unsigned v = __hip_atomic_load(p, __ATOMIC_RELAXED, __HIP_MEMORY_SCOPE_AGENT);
    if (__all(v == c.tag)) break;
    if (__builtin_amdgcn_s_memrealtime() - t0 > 300000ull) { if (c.lane == 0) __hip_atomic_store((unsigned*)(c.wsp + OFF_ERR), 0x100u + (unsigned)g, __ATOMIC_RELAXED, __HIP_MEMORY_SCOPE_AGENT); break; }
    __builtin_amdgcn_s_sleep(1);
  }
  asm volatile("" ::: "memory");
}
#define STEP(t) do {} while (0)
#define TS(k) do {} while (0)

DI void st16(const Ctx& c, bf16x8 v, int off) { __builtin_amdgcn_raw_buffer_store_b128(__builtin_bit_cast(u32x4, v), c.ws, off, 0, SC_SC1); }
template <int AUX> DI void dma(__amdgpu_buffer_rsrc_t r, lds_t dst, int voff, int soff) {
  if (AUX) asm volatile("s_mov_b32 m0, %0\n\ts_nop 0\n\tbuffer_load_dwordx4 %1, %2, %3 offen sc1 lds" ::"s"((unsigned)(size_t)dst), "v"(voff), "s"(r), "s"(soff) : "memory");
  else asm volatile("s_mov_b32 m0, %0\n\ts_nop 0\n\tbuffer_load_dwordx4 %1, %2, %3 offen lds" ::"s"((unsigned)(size_t)dst), "v"(voff), "s"(r), "s"(soff) : "memory");
}

#define EXP 0
template <int S, int NW, class IA, class IW, class MID, class POST>
DI void loader_loop(int nt, bool gated_prologue, IA issueA, IW issueW, MID mid, POST post) {
  constexpr int PPW = 4 + NW;
  if (gated_prologue) wait_vm<4 * (S - 2)>(); else wait_vm<PPW * (S - 2)>();
  bar();
  int t = 0;
#pragma unroll
  for (int p = 0; p < S - 2; ++p, ++t) {
    mid(t); issueA(t + S - 1); issueW(t + S - 1); post(t);
    if (gated_prologue) {
      if (p == 0) wait_vm<4 * (S - 3 > 0 ? S - 3 : 0) + PPW * 1>();
      else if (p == 1) wait_vm<4 * (S - 4 > 0 ? S - 4 : 0) + PPW * 2>();
      else wait_vm<4 * (S - 5 > 0 ? S - 5 : 0) + PPW * 3>();
    } else wait_vm<PPW * (S - 2)>();
    bar();
  }
#pragma unroll 1
  for (; t + S - 1 < nt; ++t) { mid(t); if (!(EXP & 1)) { issueA(t + S - 1); issueW(t + S - 1); } wait_vm<PPW * (S - 2)>(); bar(); }
#pragma unroll
  for (int r = S - 2; r >= 0; --r, ++t) {
    if (r >= 4) wait_vm<PPW * 3>(); else if (r == 3) wait_vm<PPW * 2>(); else if (r == 2) wait_vm<PPW * 1>(); else wait_vm<0>();
    bar();
  }
}
template <int NW, bool GATED, class IA, class IW, class POST, class GA, class FIN>
DI void loader_loop16(bool full, bool once, IA issueA, IW issueW, POST post, GA gather, FIN fin) {
  constexpr int PPW = 4 + NW;
  if (GATED) wait_vm<4>(); else wait_vm<PPW>();
  bar();
  issueA(2); issueW(2); post(0); wait_vm<PPW>(); bar();
#pragma unroll 1
  for (int t = 1; t < 5; ++t) { if (!(EXP & 1)) { issueA(t + 2); issueW(t + 2); } post(t); wait_vm<PPW>(); bar(); }
#pragma unroll
  for (int t = 5; t < 13; ++t) {
    issueA(t + 2); issueW(t + 2); gather(t - 5);
    if (full) { if (t == 5) wait_vm<PPW + 2>(); else wait_vm<PPW + 4>(); }
    else if (once && t <= 6) wait_vm<PPW + 2>();
    else wait_vm<PPW>();
    bar();
  }
  issueA(15); issueW(15); if (full) wait_vm<PPW + 2>(); else wait_vm<PPW>(); bar();
  wait_vm<0>(); fin(); bar();
  bar();
}
template <int S, int SB, class RD0, class RD1, class MM0, class MM1, class SCH, class HK, class ST>
DI void consumer_loop(Ctx& c, int sbase, int nt, RD0 rd0, RD1 rd1, MM0 mm0, MM1 mm1, SCH sched, HK hook, ST stamp) {
  bar();
  stamp();
  rd0(0);
  int sb = 0;
#pragma unroll 1
  for (int t = 0; t < nt; ++t) {
    hook(t); STEP(t);
    __builtin_amdgcn_sched_barrier(0);
    if (!(EXP & 4)) rd1(sb); if (!(EXP & 2)) mm0(); sched();
    __builtin_amdgcn_sched_barrier(0);
    wait_lgkm0(); bar();
    __builtin_amdgcn_sched_barrier(0);
    sb = (sb == (S - 1) * SB) ? 0 : sb + SB;
    if (!(EXP & 4)) rd0(sb); if (!(EXP & 2)) mm1(); sched();
    __builtin_amdgcn_sched_barrier(0);
  }
}
#define TOUCH(name, mod) DI void name(lds_t lds, const void* p) { \
  asm volatile("s_mov_b32 m0, %1\n\ts_nop 0\n\tglobal_load_lds_dword %0, off" mod ::"v"(p), "s"((unsigned)(size_t)(lds + L_DUMMY)) : "memory"); }
TOUCH(touch, "") TOUCH(touch_nt, " nt") TOUCH(touch_sc1, " sc1")
template <int NM, int NR> DI void interleave() {
#pragma unroll
  for (int i = 0; i < NR; ++i) { __builtin_amdgcn_sched_group_barrier(0x008, 1, 0); __builtin_amdgcn_sched_group_barrier(0x100, 1, 0); }
  __builtin_amdgcn_sched_group_barrier(0x008, NM - NR, 0);
}
template <int NM, int NR> DI void interleave2() {
#pragma unroll
  for (int i = 0; i < NR / 2; ++i) { __builtin_amdgcn_sched_group_barrier(0x008, 1, 0); __builtin_amdgcn_sched_group_barrier(0x100, 2, 0); }
  __builtin_amdgcn_sched_group_barrier(0x008, NM - NR / 2, 0);
}

DI int rowmajor_voff(int lw, int lane, int pitch) {
  const int rr = lane >> 3, slot = 8 * (lw & 1) + rr;
  const int row = 8 * (slot >> 2) + 4 * (lw >> 1) + (slot & 3);
  return row * pitch + (((lane & 7) ^ (slot >> 1)) << 4);
}
DI int true_row(int mt, int i) { return 32 * (mt >> 1) + 8 * (i >> 2) + 4 * (mt & 1) + (i & 3); }

DI void acc_row(float& s, float& q, u32x4 w) {
#pragma unroll
  for (int i = 0; i < 4; ++i) { s = dot2(w[i], ONES2, s); q = dot2(w[i], w[i], q); } }
DI float col_sum(const u32x2 (&r)[4]) { float s0 = 0.f, s1 = 0.f;
#pragma unroll
  for (int i = 0; i < 4; ++i) { s0 = dot2(r[i][0], ONES2, s0); s1 = dot2(r[i][1], ONES2, s1); }
  return sum_g(s0 + s1); }
DI void st_granule(const Ctx& c, float v, int off) { __builtin_amdgcn_raw_buffer_store_b64((u32x2){__float_as_uint(v), c.tag}, c.ws, off, 0, SC_SC1); }
DI bool timed_out(const Ctx& c, unsigned long long t0, unsigned code) {
  if (__builtin_amdgcn_s_memrealtime() - t0 <= 300000ull) return false;
  if (c.lane == 0) __hip_atomic_store((unsigned*)(c.wsp + OFF_ERR), code, __ATOMIC_RELAXED, __HIP_MEMORY_SCOPE_AGENT);
  return true;
}
DI float sum_granules16(const Ctx& c, u32x2 (&v)[16], int off, int stride) {
  const unsigned long long t0 = __builtin_amdgcn_s_memrealtime();
  for (;;) {
    bool ok = true;
#pragma unroll
    for (int k = 0; k < 16; ++k) ok &= v[k][1] == c.tag;
    if (__all(ok) || timed_out(c, t0, 0x200u)) break;
    __builtin_amdgcn_s_sleep(2);
#pragma unroll
    for (int k = 0; k < 16; ++k) v[k] = __builtin_amdgcn_raw_buffer_load_b64(c.ws, off + k * stride, 0, SC_SC1);
  }
  float s = 0.f;
#pragma unroll
  for (int k = 0; k < 16; ++k) s += __uint_as_float(v[k][0]);
  return s;
}

#define ROTM 0
#define ROT2(j) (ROTM * 2 * ((j) & 7))
#define ROT5(j) (ROTM * ((7 * ((j) & 7)) >> 1))
#define P4D 0
#define TOUCHW touch
#define PF_T0 2
#define PF_DT 3

struct Args { const u16 *x, *wqkv, *wphi, *wo, *win, *wsp, *bsp, *wout; u16* out; char* ws; };

DI void get_tag(Ctx& c) { unsigned e; asm volatile("s_load_dword %0, %1, 0x0\n\ts_waitcnt lgkmcnt(0)" : "=&s"(e) : "s"(c.wsp) : "memory"); c.tag = e + 1u; }
DI void kv_gather(const Ctx& c, int j1) {
  const int lane = c.lane, g = lane >> 4, i16 = lane & 15, tid = c.wid * 64 + lane;
  f32x4 a0 = {0, 0, 0, 0}, a1 = {0, 0, 0, 0};
  u32x4 v[16];
#pragma unroll
  for (int k = 0; k < 16; ++k) v[k] = __builtin_amdgcn_raw_buffer_load_b128(c.ws, tid * 16, OFF_KVP + (j1 * 16 + k) * 8192, SC_SC1);
  float zs = 0.f;
  if (c.wid < 2) { unsigned zv[16];
#pragma unroll
    for (int k = 0; k < 16; ++k) zv[k] = __builtin_amdgcn_raw_buffer_load_b32(c.ws, lane * 4, OFF_ZP + ((j1 * 16 + k) * 2 + c.wid) * 256, SC_SC1);
#pragma unroll
    for (int k = 0; k < 16; ++k) zs += __uint_as_float(zv[k]); }
#pragma unroll
  for (int k = 0; k < 16; ++k) { a0[0] += bflo(v[k][0]); a0[1] += bfhi(v[k][0]); a0[2] += bflo(v[k][1]); a0[3] += bfhi(v[k][1]); a1[0] += bflo(v[k][2]); a1[1] += bfhi(v[k][2]); a1[2] += bflo(v[k][3]); a1[3] += bfhi(v[k][3]); }
  const int w = c.wid, hd = w >> 2, dt = (w >> 1) & 1, fp = w & 1;
  const lds_t tb = c.lds + L_XBUF + hd * 8192 + (32 * fp + i16) * 128 + (16 * dt + 4 * g) * 2;
  *(LAS u32x2*)tb = (u32x2){pk2(a0[0], a0[1]), pk2(a0[2], a0[3])};
  *(LAS u32x2*)(tb + 16 * 128) = (u32x2){pk2(a1[0], a1[1]), pk2(a1[2], a1[3])};
  if (c.wid < 2) { const unsigned z2 = pk2(zs, zs); const lds_t t3 = c.lds + L_XBUF + c.wid * 8192 + lane * 128 + 64;
    *(LAS u32x4*)t3 = (u32x4){z2, z2, z2, z2}; *(LAS u32x4*)(t3 + 16) = (u32x4){z2, z2, z2, z2}; }
  wait_lgkm0();
}
struct Map { int i1, j1, i2, j2, i3, j3; bool act3; };
DI void p1_load(Ctx& c, const Args& A, int i1, int j1) {
  const int lane = c.lane, lw = c.wid - 4, g = lane >> 4, q = (lane & 15) >> 2, p = lane & 3;
  const lds_t lds = c.lds;
  const __amdgpu_buffer_rsrc_t rWS = c.ws;
  constexpr int S = 3, SB = 40960, NW = 6;
  const __amdgpu_buffer_rsrc_t rA = __builtin_amdgcn_make_buffer_rsrc((void*)(A.x + (size_t)i1 * 128 * 1024), 0, 128 * 2048, 0x00020000);
  const __amdgpu_buffer_rsrc_t rW = __builtin_amdgcn_make_buffer_rsrc((void*)A.wqkv, 0, 1024 * 6144, 0x00020000);
  const __amdgpu_buffer_rsrc_t rP = __builtin_amdgcn_make_buffer_rsrc((void*)A.wphi, 0, 65536, 0x00020000);
  const int voA = rowmajor_voff(lw, lane, 2048);
  int voW[NW];
#pragma unroll
  for (int i = 0; i < NW; ++i) { const int o = (NW * lw + i) * 1024 + lane * 16, r = o / 384, rem = o % 384, seg = rem >> 7, s16 = (rem & 127) >> 4;
    const int c32 = (s16 >> 1) ^ ((r >> 1) & 3), r5 = r & 31, k = (r & 32) + 8 * ((r5 >> 2) & 3) + 4 * (r5 >> 4) + (r5 & 3);
    voW[i] = k * 6144 + seg * 2048 + j1 * 128 + c32 * 32 + (s16 & 1) * 16; }
#define ROTMODE 1
  const int rot = ROTMODE == 2 ? (2 * (i1 & 7) + (i1 >> 3) + 4 * (j1 & 3)) & 15 : ROTMODE == 1 ? 4 * (i1 & 3) : ROTMODE == 3 ? 8 * (i1 & 1) : 0;
  auto issueA = [&](int t) { const lds_t d = lds + (t % S) * SB + lw * 1024; const int so = ((t + rot) & 15) * 128;
#pragma unroll
    for (int i = 0; i < 4; ++i) dma<0>(rA, d + i * 4096, voA, so + i * 32 * 2048); };
  auto issueW = [&](int t) { const lds_t d = lds + (t % S) * SB + 16384 + lw * NW * 1024; const int so = ((t + rot) & 15) * 64 * 6144;
#pragma unroll
    for (int i = 0; i < NW; ++i) dma<0>(rW, d + i * 1024, voW[i], so); };
  issueA(0); issueW(0); issueA(1); issueW(1);
  dma<0>(rP, lds + L_WPHI + lw * 1024, lane * 16 + lw * 1024, j1 * 4096);
  get_tag(c);
  TS(0);
  u32x2 gv[16]; u32x4 gx[2];
  const int goff = OFF_GC1 + j1 * 16 * 1536 + (lw * 64 + lane) * 8, xoff = OFF_GX + (i1 * 128 + lane) * 16;
  const int tau = ROTMODE == 2 ? 0 : ROTMODE == 1 ? i1 >> 2 : ROTMODE == 3 ? 0 : 0;
  auto post = [&](int t) {
    if (t != tau) return;
    const int sw = (2 * g + (q >> 1)) & 3; u32x2 cr[3][4];
#pragma unroll
    for (int n = 0; n < 3; ++n) { const int nu = 3 * lw + n;
      tr4_issue<16 * 384, 32 * 384>((unsigned)(size_t)(lds + (t % S) * SB + 16384 + (4 * g + q) * 384 + 8 * p + (nu >> 2) * 128 + 32 * ((nu & 3) ^ sw)), cr[n]); }
#pragma unroll
    for (int n = 0; n < 3; ++n) { tr4_wait(cr[n]); const float s = col_sum(cr[n]);
      if (lane < 16) st_granule(c, s, OFF_GC1 + (j1 * 16 + rot + tau) * 1536 + ((3 * lw + n) * 16 + lane) * 8); }
  };
  auto gather = [&](int k) {
    if (lw < 3) { gv[2 * k] = __builtin_amdgcn_raw_buffer_load_b64(rWS, goff + 2 * k * 1536, 0, SC_SC1); gv[2 * k + 1] = __builtin_amdgcn_raw_buffer_load_b64(rWS, goff + (2 * k + 1) * 1536, 0, SC_SC1); }
    else if (k == 0) { gx[0] = __builtin_amdgcn_raw_buffer_load_b128(rWS, xoff, 0, SC_SC1); gx[1] = __builtin_amdgcn_raw_buffer_load_b128(rWS, xoff + 1024, 0, SC_SC1); }
  };
  auto fin = [&]() {
    if (lw < 3) *(LAS float*)(lds + L_TAB1 + 1024 + (lw * 64 + lane) * 4) = sum_granules16(c, gv, goff, 1536);
    else {
      const unsigned long long t0 = __builtin_amdgcn_s_memrealtime();
      for (;;) {
        if (__all(gx[0][1] == c.tag && gx[0][3] == c.tag && gx[1][1] == c.tag && gx[1][3] == c.tag) || timed_out(c, t0, 0x201u)) break;
        __builtin_amdgcn_s_sleep(2);
        gx[0] = __builtin_amdgcn_raw_buffer_load_b128(rWS, xoff, 0, SC_SC1); gx[1] = __builtin_amdgcn_raw_buffer_load_b128(rWS, xoff + 1024, 0, SC_SC1);
      }
#pragma unroll
      for (int e = 0; e < 2; ++e) { *(LAS unsigned*)(lds + L_TAB1 + (lane + 64 * e) * 4) = gx[e][0]; *(LAS unsigned*)(lds + L_TAB1 + 512 + (lane + 64 * e) * 4) = gx[e][2]; }
    }
    wait_lgkm0();
    __builtin_amdgcn_s_waitcnt(0x0f70);
  {
    const int gw = c.b * 4 + lw;
    if (lane < 56) touch(lds, (const char*)A.win + (size_t)(gw * 56 + lane) * 128);
    if (lane < 16) touch(lds, (const char*)A.wo + (size_t)(gw * 16 + lane) * 128);
  }
  };
  loader_loop16<NW, false>(lw < 3, lw == 3, issueA, issueW, post, gather, fin);
  TS(1);
}

struct Acc1 { f32x4 q[2][2], k[2][2], v[2][2]; };
DI void p1_mma(Ctx& c, const Args& A, int i1, int j1, Acc1& e) {
  const int lane = c.lane, wr = c.wid >> 1, wc = c.wid & 1, g = lane >> 4, q = (lane & 15) >> 2, p = lane & 3, i16 = lane & 15;
  const lds_t lds = c.lds;
  const f32x4 Z4 = {0.f, 0.f, 0.f, 0.f};
  constexpr int S = 3, SB = 40960;
  f32x4 aq[4][2], ak[4][2], av[4][2];
#pragma unroll
  for (int m = 0; m < 4; ++m)
#pragma unroll
    for (int n = 0; n < 2; ++n) { aq[m][n] = Z4; ak[m][n] = Z4; av[m][n] = Z4; asm volatile("" : "+v"(aq[m][n]), "+v"(ak[m][n]), "+v"(av[m][n])); }
  const int sw = (2 * g + (q >> 1)) & 3;
  const int ao0 = (wr * 64 + i16) * 128 + ((g ^ (i16 >> 1)) << 4), ao1 = ao0 ^ 64;
  const int wl = 16384 + (4 * g + q) * 384 + 8 * p;
  const int wo0 = wl + 32 * ((2 * wc) ^ sw), wo1 = wl + 32 * ((2 * wc + 1) ^ sw);
  bf16x8 a0[4], w0[6], a1[4], w1[6];
  auto rd = [&](bf16x8(&a)[4], bf16x8(&w)[6], int sb, int ks) { const lds_t s = lds + sb;
#pragma unroll
    for (int sg = 0; sg < 3; ++sg) { w[2 * sg] = ldsT(s + wo0 + sg * 128 + ks * 32 * 384, 16 * 384); w[2 * sg + 1] = ldsT(s + wo1 + sg * 128 + ks * 32 * 384, 16 * 384); }
#pragma unroll
    for (int m = 0; m < 4; ++m) a[m] = ldsA(s + (ks ? ao1 : ao0) + m * 2048); };
  auto mm = [&](bf16x8(&a)[4], bf16x8(&w)[6]) {
#pragma unroll
    for (int n = 0; n < 2; ++n)
#pragma unroll
      for (int m = 0; m < 4; ++m) aq[m][n] = mfma(w[n], a[m], aq[m][n]);
#pragma unroll
    for (int n = 0; n < 2; ++n)
#pragma unroll
      for (int m = 0; m < 4; ++m) ak[m][n] = mfma(w[2 + n], a[m], ak[m][n]);
#pragma unroll
    for (int n = 0; n < 2; ++n)
#pragma unroll
      for (int m = 0; m < 4; ++m) av[m][n] = mfma(a[m], w[4 + n], av[m][n]); };
  TS(0);
  {
    u32x4 xv[2][2];
#pragma unroll
    for (int e2 = 0; e2 < 2; ++e2) { const int R = 8 * j1 + 2 * c.wid + e2; const u16* xr = A.x + (size_t)(i1 * 128 + true_row(R >> 4, R & 15)) * 1024 + lane * 16;
      xv[e2][0] = *(const u32x4*)xr; xv[e2][1] = *(const u32x4*)(xr + 8); }
    get_tag(c);
#pragma unroll
    for (int e2 = 0; e2 < 2; ++e2) { float s = 0.f, qq = 0.f; acc_row(s, qq, xv[e2][0]); acc_row(s, qq, xv[e2][1]);
      s = sum_g(sum_row16(s)) * (1.0f / 1024.0f); qq = sum_g(sum_row16(qq)) * (1.0f / 1024.0f);
      const float rs = __builtin_amdgcn_rsqf(fmaxf(qq - s * s, 0.f) + 1e-5f);
      if (lane == 0) __builtin_amdgcn_raw_buffer_store_b128((u32x4){__float_as_uint(s), c.tag, __float_as_uint(rs), c.tag}, c.ws, OFF_GX + (i1 * 128 + 8 * j1 + 2 * c.wid + e2) * 16, 0, SC_SC1); }
  }
  consumer_loop<S, SB>(c, 0, 16, [&](int sb) { rd(a0, w0, sb, 0); }, [&](int sb) { rd(a1, w1, sb, 1); }, [&]() { mm(a0, w0); }, [&]() { mm(a1, w1); }, []() { interleave<24, 16>(); }, [](int) {}, [&]() { TS(16); });
  TS(1);
  const lds_t xf = lds + L_XF1 + c.wid * 12288 + lane * 16;
#pragma unroll
  for (int m = 0; m < 2; ++m)
#pragma unroll
    for (int n = 0; n < 2; ++n) {
      *(LAS f32x4*)(xf + (m * 6 + n) * 1024) = aq[2 + m][n]; *(LAS f32x4*)(xf + (m * 6 + 2 + n) * 1024) = ak[2 + m][n]; *(LAS f32x4*)(xf + (m * 6 + 4 + n) * 1024) = av[2 + m][n];
      e.q[m][n] = aq[m][n]; e.k[m][n] = ak[m][n]; e.v[m][n] = av[m][n];
    }
  wait_lgkm0();
}

DI void both_g_max(float& a, float& b) {
  auto r = __builtin_amdgcn_permlane16_swap(__float_as_uint(a), __float_as_uint(b), false, false);
  const float x = fmaxf(__uint_as_float(r[0]), __uint_as_float(r[1]));
  auto s = __builtin_amdgcn_permlane32_swap(__float_as_uint(x), __float_as_uint(x), false, false);
  const float y = fmaxf(__uint_as_float(s[0]), __uint_as_float(s[1]));
  auto t = __builtin_amdgcn_permlane16_swap(__float_as_uint(y), __float_as_uint(y), false, false);
  a = __uint_as_float(t[0]); b = __uint_as_float(t[1]);
}
DI void both_g_sum(float& a, float& b) {
  auto r = __builtin_amdgcn_permlane16_swap(__float_as_uint(a), __float_as_uint(b), false, false);
  const float x = __uint_as_float(r[0]) + __uint_as_float(r[1]);
  auto s = __builtin_amdgcn_permlane32_swap(__float_as_uint(x), __float_as_uint(x), false, false);
  const float y = __uint_as_float(s[0]) + __uint_as_float(s[1]);
  auto t = __builtin_amdgcn_permlane16_swap(__float_as_uint(y), __float_as_uint(y), false, false);
  a = __uint_as_float(t[0]); b = __uint_as_float(t[1]);
}
DI void feat(const f32x4& p0, const f32x4& p1, f32x4& e0, f32x4& e1, f32x4& n0, f32x4& n1) {
  constexpr float L2E = 1.4426950408889634f;
  const f32x4 t0 = p0 * L2E, t1 = p1 * L2E;
  float mx = fmaxf(fmaxf(fmaxf(t0[0], t0[1]), t0[2]), fmaxf(fmaxf(fmaxf(t0[3], t1[0]), t1[1]), fmaxf(t1[2], t1[3])));
  float mn = -fminf(fminf(fminf(t0[0], t0[1]), t0[2]), fminf(fminf(fminf(t0[3], t1[0]), t1[1]), fminf(t1[2], t1[3])));
  both_g_max(mx, mn);
  const f32x4 a0 = t0 - mx, a1 = t1 - mx, b0 = -t0 - mn, b1 = -t1 - mn;
#pragma unroll
  for (int r = 0; r < 4; ++r) { e0[r] = __builtin_amdgcn_exp2f(a0[r]); e1[r] = __builtin_amdgcn_exp2f(a1[r]); n0[r] = __builtin_amdgcn_exp2f(b0[r]); n1[r] = __builtin_amdgcn_exp2f(b1[r]); }
  const f32x4 se = e0 + e1, sn4 = n0 + n1;
  float sp = (se[0] + se[1]) + (se[2] + se[3]), sn = (sn4[0] + sn4[1]) + (sn4[2] + sn4[3]);
  both_g_sum(sp, sn);
  sp = __builtin_amdgcn_rcpf(sp); sn = __builtin_amdgcn_rcpf(sn);
  e0 *= sp; e1 *= sp; n0 *= sn; n1 *= sn;
}

DI void p1_epilogue(Ctx& c, const Args& A, const Map& M, Acc1& e, int last) {
  const int lane = opaque(c.lane), cw = c.wid & 3, hf = c.wid >> 2, wr = cw >> 1, wc = cw & 1, g = lane >> 4, q = (lane & 15) >> 2, p = lane & 3, i16 = lane & 15;
  const int lw = cw, i1 = M.i1, j1 = M.j1;
  const lds_t lds = c.lds;
  const f32x4 Z4 = {0.f, 0.f, 0.f, 0.f};
  bar();
  TS(20);
  if (hf) { const lds_t xf = lds + L_XF1 + cw * 12288 + lane * 16;
#pragma unroll
    for (int m = 0; m < 2; ++m)
#pragma unroll
      for (int n = 0; n < 2; ++n) { e.q[m][n] = *(LAS f32x4*)(xf + (m * 6 + n) * 1024); e.k[m][n] = *(LAS f32x4*)(xf + (m * 6 + 2 + n) * 1024); e.v[m][n] = *(LAS f32x4*)(xf + (m * 6 + 4 + n) * 1024); } }
  const lds_t tab = lds + L_TAB1;
  bf16x8 qf[2], kf[2], vf[2], wT[2];
  {
    f32x4 vv[2][2];
#pragma unroll
    for (int m = 0; m < 2; ++m) {
      const int R = 64 * wr + 32 * hf + 16 * m;
      const float mu = *(LAS float*)(tab + (R + i16) * 4), rs = *(LAS float*)(tab + 512 + (R + i16) * 4), nm = -mu * rs;
      const f32x4 mu4 = *(LAS f32x4*)(tab + (R + 4 * g) * 4), rs4 = *(LAS f32x4*)(tab + 512 + (R + 4 * g) * 4), nm4 = -mu4 * rs4;
      f32x4 qq[2], kk[2];
#pragma unroll
      for (int n = 0; n < 2; ++n) {
        const f32x4 cq = *(LAS f32x4*)(tab + 1024 + (32 * wc + 16 * n + 4 * g) * 4), ck = *(LAS f32x4*)(tab + 1024 + (64 + 32 * wc + 16 * n + 4 * g) * 4);
        const float cv = *(LAS float*)(tab + 1024 + (128 + 32 * wc + 16 * n + i16) * 4);
        qq[n] = e.q[m][n] * rs + cq * nm; kk[n] = e.k[m][n] * rs + ck * nm; vv[m][n] = e.v[m][n] * rs4 + nm4 * cv;
      }
      qf[m] = pk8(qq[0], qq[1]); kf[m] = pk8(kk[0], kk[1]);
    }
    vf[0] = pk8(vv[0][0], vv[1][0]); vf[1] = pk8(vv[0][1], vv[1][1]);
#pragma unroll
    for (int ft = 0; ft < 2; ++ft) wT[ft] = ldsT(lds + L_WPHI + wc * 2048 + (4 * g + q) * 64 + ft * 32 + 8 * p, 16 * 64);
  }
  wait_lgkm0(); bar();
  TS(21);
  {
    const lds_t tb = lds + L_TB + c.wid * 4096;
#pragma unroll
    for (int m = 0; m < 2; ++m) {
      const f32x4 p0 = mfma(wT[0], kf[m], Z4), p1 = mfma(wT[1], kf[m], Z4);
      f32x4 e0, e1, n0, n1; feat(p0, p1, e0, e1, n0, n1);
      const lds_t rw = tb + (16 * m + i16) * 128;
      *(LAS u32x2*)(rw + 8 * ((0 + g) ^ i16)) = pk4(e0); *(LAS u32x2*)(rw + 8 * ((4 + g) ^ i16)) = pk4(e1);
      *(LAS u32x2*)(rw + 8 * ((8 + g) ^ i16)) = pk4(n0); *(LAS u32x2*)(rw + 8 * ((12 + g) ^ i16)) = pk4(n1);
    }
    asm volatile("s_waitcnt lgkmcnt(0)" ::: "memory");
    const bf16x8 ones = __builtin_bit_cast(bf16x8, (u32x4){ONES2, ONES2, ONES2, ONES2});
    const lds_t kr = lds + L_KR + c.wid * 8192 + lane * 16;
    float zf[4];
#pragma unroll
    for (int f = 0; f < 4; ++f) {
      const int r0 = 4 * g + q;
      const s16x4 lo = __builtin_amdgcn_ds_read_tr16_b64_v4i16((LAS s16x4*)(tb + r0 * 128 + 8 * ((4 * f + p) ^ r0)));
      const s16x4 hi = __builtin_amdgcn_ds_read_tr16_b64_v4i16((LAS s16x4*)(tb + (16 + r0) * 128 + 8 * ((4 * f + p) ^ r0)));
      const bf16x8 pf = __builtin_shufflevector(lo, hi, 0, 1, 2, 3, 4, 5, 6, 7);
      *(LAS f32x4*)(kr + f * 1024) = mfma(vf[0], pf, Z4); *(LAS f32x4*)(kr + (4 + f) * 1024) = mfma(vf[1], pf, Z4);
      zf[f] = mfma(ones, pf, Z4)[0];
    }
    *(LAS float*)(lds + L_ZR + c.wid * 256 + lane * 4) = g == 0 ? zf[0] : g == 1 ? zf[1] : g == 2 ? zf[2] : zf[3];
    TS(22);
    wait_lgkm0(); bar();
    f32x4 k0 = Z4, k1 = Z4;
#pragma unroll
    for (int w = 0; w < 4; ++w) { const lds_t s = lds + L_KR + (wc + 2 * w) * 8192 + (4 * wr + 2 * hf) * 1024 + lane * 16; k0 += *(LAS f32x4*)s; k1 += *(LAS f32x4*)(s + 1024); }
    st16(c, pk8(k0, k1), lane * 16 + (OFF_KVP + (j1 * 16 + i1) * 8192 + wc * 4096 + wr * 2048 + hf * 1024));
    if (c.wid < 2) { float zt = 0.f;
#pragma unroll
      for (int w = 0; w < 4; ++w) zt += *(LAS float*)(lds + L_ZR + (wc + 2 * w) * 256 + lane * 4);
      __builtin_amdgcn_raw_buffer_store_b32(__float_as_uint(zt), c.ws, lane * 4, OFF_ZP + ((j1 * 16 + i1) * 2 + wc) * 256, SC_SC1); }
    wait_lgkm0(); bar();
    TS(23);
    publish<8>(c, 0, FG_KV, j1, i1);
  }
  TS(2);
  const __amdgpu_buffer_rsrc_t rW2 = __builtin_amdgcn_make_buffer_rsrc((void*)A.wo, 0, 1024 * 2048, 0x00020000);
  int voW2[2];
#pragma unroll
  for (int i = 0; i < 2; ++i) { const int r = (2 * lw + i) * 8 + (lane >> 3), s16 = lane & 7; voW2[i] = r * 2048 + M.j2 * 128 + (((s16 >> 1) ^ ((r >> 1) & 3)) << 5) + (s16 & 1) * 16; }
  auto issueW2 = [&](int t) {
#pragma unroll
    for (int i = 0; i < 2; ++i) dma<0>(rW2, lds + t * 8192 + (lw * 2 + i) * 1024, voW2[i], t * 64 * 2048); };
  bf16x8 ph[2][2];
#pragma unroll
  for (int m = 0; m < 2; ++m) {
    const f32x4 p0 = mfma(wT[0], qf[m], Z4), p1 = mfma(wT[1], qf[m], Z4);
    f32x4 e0, e1, n0, n1; feat(p0, p1, e0, e1, n0, n1);
    ph[m][0] = pk8(e0, e1); ph[m][1] = pk8(n0, n1);
  }
  asm volatile("" : "+v"(ph[0][0]), "+v"(ph[0][1]), "+v"(ph[1][0]), "+v"(ph[1][1]));
  TS(3);
  if (hf) {
    if (M.act3) {
      const __amdgpu_buffer_rsrc_t rW3 = __builtin_amdgcn_make_buffer_rsrc((void*)A.win, 0, 1024 * 7168, 0x00020000);
#pragma unroll
      for (int i = 0; i < 8; ++i) { const int r = (8 * lw + i) * 2 + (lane >> 5), l5 = lane & 31, seg = l5 >> 4, s16 = l5 & 15;
        dma<0>(rW3, lds + 32768 + (8 * lw + i) * 1024, r * 7168 + (seg == 0 ? 3584 : 0) + M.j3 * 256 + (((s16 >> 1) ^ (r & 7)) << 5) + (s16 & 1) * 16, M.i3 * 64 * 7168); }
    }
#pragma unroll
    for (int t = 0; t < 3; ++t) { issueW2(t); issueW2(8 + t); }
  } else if (c.wid == 0) wait_flags(c, FG_KV, j1, 16);
  bar();
  TS(27);
  kv_gather(c, j1);
  bar();
  TS(4);
  {
    bf16x8 kt[3][2];
#pragma unroll
    for (int d = 0; d < 3; ++d)
#pragma unroll
      for (int s = 0; s < 2; ++s) kt[d][s] = ldsT(lds + L_XBUF + wc * 8192 + (32 * s + 4 * g + q) * 128 + 32 * d + 8 * p, 16 * 128);
#pragma unroll
    for (int m = 0; m < 2; ++m) {
      const f32x4 n0 = mfma(kt[0][1], ph[m][1], mfma(kt[0][0], ph[m][0], Z4)), n1 = mfma(kt[1][1], ph[m][1], mfma(kt[1][0], ph[m][0], Z4));
      const f32x4 dn = mfma(kt[2][1], ph[m][1], mfma(kt[2][0], ph[m][0], Z4));
      const float r = __builtin_amdgcn_rcpf(dn[0] + 1e-6f);
      st16(c, pk8(n0 * r, n1 * r), lane * 16 + (OFF_OF + (i1 * 16 + j1) * 16384 + ((4 * wr + 2 * hf + m) * 2 + wc) * 1024));
    }
  }
  publish<8>(c, 1, FG_O, i1, j1);
  TS(5);
  if (hf && M.act3) {
    u32x2 cr[4][4];
#pragma unroll
    for (int n = 0; n < 4; ++n) tr4_issue<16 * 512, 32 * 512>((unsigned)(size_t)(lds + 32768 + (4 * g + q) * 512 + 8 * p + (lw >> 1) * 256 + 32 * ((4 * (lw & 1) + n) ^ (4 * (g & 1) + q))), cr[n]);
#pragma unroll
    for (int n = 0; n < 4; ++n) { tr4_wait(cr[n]); const float s = col_sum(cr[n]);
      if (lane < 16) st_granule(c, s, OFF_GC3 + (M.j3 * 16 + M.i3) * 2048 + ((4 * lw + n) * 16 + lane) * 8); }
  }
  if (hf) {
    issueW2(3);
#pragma unroll
    for (int t = 11; t < 16; ++t) issueW2(t);
  } else if (c.wid == 0 && last >= 2) wait_flags(c, FG_O, M.i2, 16);
}

struct Ld3 { __amdgpu_buffer_rsrc_t rW; int voW[8]; };
DI void p3_setup(const Ctx& c, const Args& A, int j3, Ld3& L) {
  const int lane = opaque(c.lane), lw = c.wid & 3;
  L.rW = __builtin_amdgcn_make_buffer_rsrc((void*)A.win, 0, 1024 * 7168, 0x00020000);
#pragma unroll
  for (int i = 0; i < 8; ++i) { const int r = (8 * lw + i) * 2 + (lane >> 5), l5 = lane & 31, seg = l5 >> 4, s16 = l5 & 15;
    L.voW[i] = r * 7168 + (seg == 0 ? 3584 : 0) + j3 * 256 + (((s16 >> 1) ^ (r & 7)) << 5) + (s16 & 1) * 16; }
}
DI void p3_issueW(const Ctx& c, const Ld3& L, int t) { const lds_t d = c.lds + (t % 3) * 49152 + 16384 + (c.wid & 3) * 8192;
#pragma unroll
  for (int i = 0; i < 8; ++i) dma<0>(L.rW, d + i * 1024, L.voW[i], t * 64 * 7168); }
DI void p3_load(Ctx& c, const Ld3& L, int i3) {
  const int lane = opaque(c.lane), lw = c.wid - 4;
  const lds_t lds = c.lds;
  constexpr int S = 3, SB = 49152;
  const int voF = lane * 16 + lw * 1024;
  auto issueA = [&](int t) { const lds_t d = lds + (t % S) * SB + lw * 1024; const int so = OFF_HF + (i3 * 16 + t) * 16384;
#pragma unroll
    for (int i = 0; i < 4; ++i) dma<LDAUX>(c.ws, d + i * 4096, voF, so + i * 4096); };
  auto issueW = [&](int t) { p3_issueW(c, L, t); };
  bar();
  issueA(0); issueA(1);
  u32x2 hv[16]; const int R = (lw & 1) * 64 + lane;
  auto gather = [&](int k) { if (lw < 2) {
      hv[2 * k] = __builtin_amdgcn_raw_buffer_load_b64(c.ws, R * 8, OFF_STH + (i3 * 16 + 2 * k) * 1024, SC_SC1);
      hv[2 * k + 1] = __builtin_amdgcn_raw_buffer_load_b64(c.ws, R * 8, OFF_STH + (i3 * 16 + 2 * k + 1) * 1024, SC_SC1); } };
  auto fin = [&]() { if (lw < 2) { float s = 0.f, qq = 0.f;
#pragma unroll
      for (int k = 0; k < 16; ++k) { s += __uint_as_float(hv[k][0]); qq += __uint_as_float(hv[k][1]); }
      s *= (1.0f / 1024.0f); qq *= (1.0f / 1024.0f);
      *(LAS float*)(lds + L_TAB3 + R * 4) = s; *(LAS float*)(lds + L_TAB3 + 512 + R * 4) = __builtin_amdgcn_rsqf(fmaxf(qq - s * s, 0.f) + 1e-5f);
      wait_lgkm0(); } };
  loader_loop16<8, true>(lw < 2, false, issueA, issueW, [](int) {}, gather, fin);
  TS(6);
}
struct Acc3 { f32x4 z2[2][4], z1[2][4]; };
DI int xf3_base(int cw) { return cw < 3 ? 16384 + cw * 32768 : 98304; }
DI void p3_mma(Ctx& c, int i3, int j3, Acc3& e) {
  const int lane = opaque(c.lane), wr = c.wid >> 1, wc = c.wid & 1, g = lane >> 4, q = (lane & 15) >> 2, p = lane & 3;
  const lds_t lds = c.lds;
  const __amdgpu_buffer_rsrc_t rWS = c.ws;
  const f32x4 Z4 = {0.f, 0.f, 0.f, 0.f};
  constexpr int S = 3, SB = 49152;
  if (wc == 1) {
    const int off = OFF_GC3 + j3 * 16 * 2048 + (128 * wr + 2 * lane) * 8; u32x4 gv[16];
    const unsigned long long t0 = __builtin_amdgcn_s_memrealtime();
    for (;;) {
      bool ok = true;
#pragma unroll
      for (int k = 0; k < 16; ++k) gv[k] = __builtin_amdgcn_raw_buffer_load_b128(rWS, off + k * 2048, 0, SC_SC1);
#pragma unroll
      for (int k = 0; k < 16; ++k) ok &= gv[k][1] == c.tag && gv[k][3] == c.tag;
      if (__all(ok) || timed_out(c, t0, 0x202u)) break;
      __builtin_amdgcn_s_sleep(2);
    }
    f32x2 cs = {0.f, 0.f};
#pragma unroll
    for (int k = 0; k < 16; ++k) { cs[0] += __uint_as_float(gv[k][0]); cs[1] += __uint_as_float(gv[k][2]); }
    *(LAS f32x2*)(lds + L_TAB3 + 1024 + (128 * wr + 2 * lane) * 4) = cs;
    wait_lgkm0();
  } else if (c.wid == 0) wait_flags(c, FG_H, i3, 16);
  f32x4 a2[4][4], a1_[4][4];
#pragma unroll
  for (int m = 0; m < 4; ++m)
#pragma unroll
    for (int n = 0; n < 4; ++n) { a2[m][n] = Z4; a1_[m][n] = Z4; }
#pragma unroll
  for (int m = 0; m < 4; ++m) asm volatile("" : "+v"(a2[m][0]), "+v"(a2[m][1]), "+v"(a2[m][2]), "+v"(a2[m][3]), "+v"(a1_[m][0]), "+v"(a1_[m][1]), "+v"(a1_[m][2]), "+v"(a1_[m][3]));
  bar();
  const int ao2 = wr * 8192 + lane * 16;
  int wo_[4];
#pragma unroll
  for (int n = 0; n < 4; ++n) wo_[n] = 16384 + (4 * g + q) * 512 + 8 * p + 32 * ((4 * wc + n) ^ (4 * (g & 1) + q));
  bf16x8 a0[4], w0[8], a1[4], w1[8];
  auto rd = [&](bf16x8(&a)[4], bf16x8(&w)[8], int sb, int ks) { const lds_t s = lds + sb;
#pragma unroll
    for (int n = 0; n < 8; ++n) w[n] = ldsT(s + wo_[n & 3] + (n >> 2) * 256 + ks * 32 * 512, 16 * 512);
#pragma unroll
    for (int m = 0; m < 4; ++m) a[m] = ldsA(s + ao2 + m * 2048 + ks * 1024); };
  auto mm = [&](bf16x8(&a)[4], bf16x8(&w)[8]) {
#pragma unroll
    for (int n = 0; n < 4; ++n)
#pragma unroll
      for (int m = 0; m < 4; ++m) a2[m][n] = mfma(a[m], w[n], a2[m][n]);
#pragma unroll
    for (int n = 0; n < 4; ++n)
#pragma unroll
      for (int m = 0; m < 4; ++m) a1_[m][n] = mfma(w[4 + n], a[m], a1_[m][n]); };
  consumer_loop<S, SB>(c, 16, 16, [&](int sb) { rd(a0, w0, sb, 0); }, [&](int sb) { rd(a1, w1, sb, 1); }, [&]() { mm(a0, w0); }, [&]() { mm(a1, w1); }, []() { interleave<32, 20>(); }, [](int) {}, [&]() { TS(17); });
  TS(8);
  const lds_t xf = lds + xf3_base(c.wid) + lane * 16;
#pragma unroll
  for (int m = 0; m < 2; ++m)
#pragma unroll
    for (int n = 0; n < 4; ++n) { *(LAS f32x4*)(xf + (m * 8 + n) * 1024) = a2[2 + m][n]; *(LAS f32x4*)(xf + (m * 8 + 4 + n) * 1024) = a1_[2 + m][n]; e.z2[m][n] = a2[m][n]; e.z1[m][n] = a1_[m][n]; }
  wait_lgkm0();
}
struct Ld4 { __amdgpu_buffer_rsrc_t rA; int voA, voF; };
DI void p4_issueW(const Ctx& c, const Ld4& L, int t) { const int lw = c.wid & 3; const lds_t d = c.lds + (t & 3) * 32768 + lw * 1024;
#pragma unroll
  for (int i = 0; i < 4; ++i) dma<0>(L.rA, d + i * 4096, L.voA, t * 128 + i * 32 * 4096); }
DI void p3_epilogue(Ctx& c, const Ld4& L, int i3, int j3, Acc3& e, u32x2 (&z1)[2][4]) {
  const int lane = opaque(c.lane), cw = c.wid & 3, hf = c.wid >> 2, wr = cw >> 1, wc = cw & 1, g = lane >> 4, i16 = lane & 15;
  const lds_t lds = c.lds, tab = lds + L_TAB3;
  const __amdgpu_buffer_rsrc_t rWS = c.ws;
  const f32x4 Z4 = {0.f, 0.f, 0.f, 0.f};
  bar();
  TS(25);
  __builtin_amdgcn_s_setprio(1);
  if (hf) { const lds_t xf = lds + xf3_base(cw) + lane * 16;
#pragma unroll
    for (int m = 0; m < 2; ++m)
#pragma unroll
      for (int n = 0; n < 4; ++n) { e.z2[m][n] = *(LAS f32x4*)(xf + (m * 8 + n) * 1024); e.z1[m][n] = *(LAS f32x4*)(xf + (m * 8 + 4 + n) * 1024); } }
  {
    float cs[4];
#pragma unroll
    for (int n = 0; n < 4; ++n) cs[n] = *(LAS float*)(tab + 1024 + (64 * wc + 16 * n + i16) * 4);
#pragma unroll
    for (int m = 0; m < 2; ++m) {
      const int R = 64 * wr + 32 * hf + 16 * m + 4 * g;
      const f32x4 mu = *(LAS f32x4*)(tab + R * 4), rs = *(LAS f32x4*)(tab + 512 + R * 4), nm = -mu * rs;
      f32x4 s = Z4, qq = Z4;
#pragma unroll
      for (int n = 0; n < 4; ++n) { const f32x4 y = gelu4(e.z2[m][n] * rs + nm * cs[n]); e.z2[m][n] = y; s += y; qq += y * y; }
      sum_row16x8(s, qq);
      if (i16 < 4) { const float sv = i16 == 0 ? s[0] : i16 == 1 ? s[1] : i16 == 2 ? s[2] : s[3], qv = i16 == 0 ? qq[0] : i16 == 1 ? qq[1] : i16 == 2 ? qq[2] : qq[3];
        __builtin_amdgcn_raw_buffer_store_b128((u32x4){__float_as_uint(sv), c.tag, __float_as_uint(qv), c.tag}, rWS, OFF_ST2 + ((i3 * 14 + j3) * 2 + wc) * 2048 + (R + i16) * 16, 0, SC_SC1); }
    }
  }
  TS(9);
  __builtin_amdgcn_s_setprio(0);
  if (!hf) {
#pragma unroll
    for (int s = 0; s < 3; ++s) p4_issueW(c, L, s);
  }
  {
    const int R0 = 64 * wr + 32 * hf, off = OFF_ST2 + (i3 * 28 + (lane >> 5)) * 2048 + (R0 + (lane & 31)) * 16;
    u32x4 v[14];
    const unsigned long long t0 = __builtin_amdgcn_s_memrealtime();
    for (;;) {
      bool ok = true;
#pragma unroll
      for (int k = 0; k < 14; ++k) v[k] = __builtin_amdgcn_raw_buffer_load_b128(rWS, off + k * 4096, 0, SC_SC1);
#pragma unroll
      for (int k = 0; k < 14; ++k) ok &= v[k][1] == c.tag && v[k][3] == c.tag;
      if (__all(ok) || timed_out(c, t0, 0x203u)) break;
      __builtin_amdgcn_s_sleep(PSLEEP);
    }
    __builtin_amdgcn_s_setprio(1);
    float s = 0.f, qq = 0.f;
#pragma unroll
    for (int k = 0; k < 14; ++k) { s += __uint_as_float(v[k][0]); qq += __uint_as_float(v[k][2]); }
    { auto a = __builtin_amdgcn_permlane32_swap(__float_as_uint(s), __float_as_uint(s), false, false); s = (__uint_as_float(a[0]) + __uint_as_float(a[1])) * (1.0f / 1792.0f);
      auto b = __builtin_amdgcn_permlane32_swap(__float_as_uint(qq), __float_as_uint(qq), false, false); qq = (__uint_as_float(b[0]) + __uint_as_float(b[1])) * (1.0f / 1792.0f); }
    const float rsv = __builtin_amdgcn_rsqf(fmaxf(qq - s * s, 0.f) + 1e-5f);
    const lds_t ft = lds + L_FIN + c.wid * 256;
    if (lane < 32) { *(LAS float*)(ft + lane * 4) = rsv; *(LAS float*)(ft + 128 + lane * 4) = -s * rsv; }
    asm volatile("s_waitcnt lgkmcnt(0)" ::: "memory");
    TS(31);
    f32x4 rs[2], nm[2];
#pragma unroll
    for (int m = 0; m < 2; ++m) { rs[m] = *(LAS f32x4*)(ft + (16 * m + 4 * g) * 4); nm[m] = *(LAS f32x4*)(ft + 128 + (16 * m + 4 * g) * 4); }
#pragma unroll
    for (int n = 0; n < 4; ++n)
      st16(c, pk8(e.z2[0][n] * rs[0] + nm[0], e.z2[1][n] * rs[1] + nm[1]), lane * 16 + (OFF_Z2 + (j3 * 32 + 2 * i3 + wr) * 16384 + ((4 * wc + n) * 2 + hf) * 1024));
  }
  __builtin_amdgcn_s_setprio(0);
  publish<8>(c, 4, FG_Z2, j3, i3);
  TS(11);
#pragma unroll
  for (int m = 0; m < 2; ++m) asm volatile("" : "+v"(e.z1[m][0]), "+v"(e.z1[m][1]), "+v"(e.z1[m][2]), "+v"(e.z1[m][3]));
#pragma unroll
  for (int m = 0; m < 2; ++m) {
    const int R = 64 * wr + 32 * hf + 16 * m + i16;
    const float mu = *(LAS float*)(tab + R * 4), rs = *(LAS float*)(tab + 512 + R * 4), nm = -mu * rs;
#pragma unroll
    for (int n = 0; n < 4; ++n) { const f32x4 cs = *(LAS f32x4*)(tab + 1024 + (128 + 64 * wc + 16 * n + 4 * g) * 4); z1[m][n] = pk4(gelu4(e.z1[m][n] * rs + cs * nm)); }
    asm volatile("" : "+v"(z1[m][0]), "+v"(z1[m][1]), "+v"(z1[m][2]), "+v"(z1[m][3]));
  }
  TS(10);
}
DI void p4_load(Ctx& c, const Ld4& L, int j3) {
  const int lw = c.wid - 4;
  auto issueW = [&](int t) { p4_issueW(c, L, t); };
  auto issueA = [&](int t) { const lds_t d = c.lds + (t & 3) * 32768 + 16384 + lw * 1024; const int so = OFF_Z2 + (j3 * 32 + t) * 16384;
#pragma unroll
    for (int i = 0; i < 4; ++i) dma<LDAUX>(c.ws, d + i * 4096, L.voF, so + i * 4096); };
  bar();
  if (c.b == 0 && lw == 0 && c.lane == 0) __hip_atomic_store((unsigned*)(c.wsp + OFF_EPOCH), c.tag, __ATOMIC_RELAXED, __HIP_MEMORY_SCOPE_AGENT);
#pragma unroll
  for (int s = 0; s < 3; ++s) issueA(s);
  loader_loop<4, 4>(32, true, issueA, issueW, [](int) {}, [](int) {});
}
DI void p4_mma(Ctx& c, int j3, f32x4 (&e)[2][4]) {
  const int lane = opaque(c.lane), wr = c.wid >> 1, wc = c.wid & 1, g = lane >> 4, i16 = lane & 15;
  const lds_t lds = c.lds;
  constexpr int S = 4, SB = 32768;
  f32x4 acc[4][4];
#pragma unroll
  for (int m = 0; m < 4; ++m)
#pragma unroll
    for (int n = 0; n < 4; ++n) { acc[m][n] = (f32x4){0.f, 0.f, 0.f, 0.f}; asm volatile("" : "+v"(acc[m][n])); }
  const int ao0 = (wr * 64 + i16) * 128 + ((g ^ (i16 >> 1)) << 4), ao1 = ao0 ^ 64, zo = 16384 + wc * 8192 + lane * 16;
  bf16x8 a0[4], w0[4], a1[4], w1[4];
  auto rd = [&](bf16x8(&a)[4], bf16x8(&w)[4], int sb, int ks) { const lds_t s = lds + sb;
#pragma unroll
    for (int n = 0; n < 4; ++n) { w[n] = ldsA(s + zo + n * 2048 + ks * 1024); a[n] = ldsA(s + (ks ? ao1 : ao0) + n * 2048); } };
  auto mm = [&](bf16x8(&a)[4], bf16x8(&w)[4]) {
#pragma unroll
    for (int n = 0; n < 4; ++n)
#pragma unroll
      for (int m = 0; m < 4; ++m) acc[m][n] = mfma(w[n], a[m], acc[m][n]); };
  if (c.wid == 0) wait_flags(c, FG_Z2, j3, 16);
  bar();
  consumer_loop<S, SB>(c, 32, 32, [&](int sb) { rd(a0, w0, sb, 0); }, [&](int sb) { rd(a1, w1, sb, 1); }, [&]() { mm(a0, w0); }, [&]() { mm(a1, w1); }, []() { interleave<16, 8>(); }, [](int) {}, [&]() { TS(18); });
  TS(12);
  const lds_t xf = lds + c.wid * 8192 + lane * 16;
#pragma unroll
  for (int m = 0; m < 2; ++m)
#pragma unroll
    for (int n = 0; n < 4; ++n) { *(LAS f32x4*)(xf + (m * 4 + n) * 1024) = acc[2 + m][n]; e[m][n] = acc[m][n]; }
  wait_lgkm0();
}
DI void p5_issueW(const Ctx& c, const Args& A, int j2);
DI void p4_epilogue(Ctx& c, const Args& A, int i3, int j3, int j2, f32x4 (&e)[2][4], const u32x2 (&z1)[2][4]) {
  const int lane = opaque(c.lane), cw = c.wid & 3, hf = c.wid >> 2, wr = cw >> 1, wc = cw & 1, i16 = lane & 15;
  float bs[2];
#pragma unroll
  for (int m = 0; m < 2; ++m) bs[m] = bflo((unsigned)A.bsp[i3 * 128 + true_row(4 * wr + 2 * hf + m, i16)]);
  bar();
  if (hf) { const lds_t xf = c.lds + cw * 8192 + lane * 16;
#pragma unroll
    for (int m = 0; m < 2; ++m)
#pragma unroll
      for (int n = 0; n < 4; ++n) e[m][n] = *(LAS f32x4*)(xf + (m * 4 + n) * 1024); }
#pragma unroll
  for (int m = 0; m < 2; ++m)
#pragma unroll
    for (int u = 0; u < 2; ++u)
      st16(c, pk8(up4(z1[m][2 * u]) * (e[m][2 * u] + bs[m]), up4(z1[m][2 * u + 1]) * (e[m][2 * u + 1] + bs[m])),
           lane * 16 + (OFF_UF + (i3 * 28 + 2 * j3 + wc) * 16384 + ((4 * wr + 2 * hf + m) * 2 + u) * 1024));
  wait_lgkm0(); bar();
  publish<8>(c, 5, FG_U, i3, j3);
  if (hf) p5_issueW(c, A, j2);
  TS(13);
}

DI void p5_issueW(const Ctx& c, const Args& A, int j2) {
  const int lane = opaque(c.lane), lw = c.wid - 4;
  const __amdgpu_buffer_rsrc_t rW = __builtin_amdgcn_make_buffer_rsrc((void*)A.wout, 0, 1792 * 2048, 0x00020000);
  int vo[2];
#pragma unroll
  for (int i = 0; i < 2; ++i) { const int r = (2 * lw + i) * 8 + (lane >> 3), s16 = lane & 7; vo[i] = r * 2048 + j2 * 128 + (((s16 >> 1) ^ ((r >> 1) & 3)) << 5) + (s16 & 1) * 16; }
#pragma unroll
  for (int t = 0; t < 18; ++t)
#pragma unroll
    for (int i = 0; i < 2; ++i) dma<0>(rW, c.lds + ((t & 1) * 9 + (t >> 1)) * 8192 + (lw * 2 + i) * 1024, vo[i], ((t & 1) * 14 + (t >> 1)) * 64 * 2048);
}

constexpr int L_RED = 98304;
#define STG1 0
#define STG2 2
#define R5 6
#define R2 6
template <int NSH, int R, bool REFILL>
DI void gemm_split(Ctx& c, f32x4 (&res)[4], int aoff, __amdgpu_buffer_rsrc_t rW, int j2) {
  const int ln = opaque(c.lane), g = ln >> 4, q = (ln & 15) >> 2, p = ln & 3, rw = c.wid & 3, kh = c.wid >> 2;
  const lds_t lds = c.lds;
  const int sw2 = (2 * g + (q >> 1)) & 3, wl2 = (4 * g + q) * 128 + 8 * p + kh * (REFILL ? 9 : 8) * 8192, va = ln * 16 + rw * 4096;
  const int ao = aoff + kh * (NSH / 2) * 16384;
  int wo2[4];
#pragma unroll
  for (int n = 0; n < 4; ++n) wo2[n] = wl2 + 32 * (n ^ sw2);
  f32x4 acc[2][4];
#pragma unroll
  for (int n = 0; n < 4; ++n) { acc[0][n] = (f32x4){0.f, 0.f, 0.f, 0.f}; acc[1][n] = acc[0][n]; }
  u32x4 a[R][2]; bf16x8 w0[4], w1[4];
  auto lda = [&](int s) {
#pragma unroll
    for (int m = 0; m < 2; ++m) a[s % R][m] = __builtin_amdgcn_raw_buffer_load_b128(c.ws, va + m * 2048, ao + (s >> 1) * 16384 + (s & 1) * 1024, LDAUX); };
  auto ldw = [&](bf16x8(&d)[4], int s) { const int lt = s >> 1, slot = (REFILL && lt >= 9) ? lt - 9 : lt;
#pragma unroll
    for (int n = 0; n < 4; ++n) d[n] = ldsT(lds + wo2[n] + slot * 8192 + (s & 1) * 4096, 16 * 128); };
  TS(REFILL ? 19 : 24);
#pragma unroll
  for (int s = 0; s < R; ++s) { lda(s); if (s == STG1 || s == STG2) bar(); }
  if (!REFILL && kh) {
#pragma unroll
    for (int i = 0; i < 2; ++i) { const int r = (2 * rw + i) * 8 + (ln >> 3), s16 = ln & 7, vo = r * 2048 + j2 * 128 + (((s16 >> 1) ^ ((r >> 1) & 3)) << 5) + (s16 & 1) * 16;
#pragma unroll
      for (int t = 4; t < 8; ++t) dma<0>(rW, lds + t * 8192 + (rw * 2 + i) * 1024, vo, t * 64 * 2048); }
  }
  ldw(w0, 0);
#pragma unroll
  for (int s = 0; s < NSH; ++s) {
    if (s == 5) {
      if (kh) { if (REFILL) wait_vm<2 * R + 10>(); else wait_vm<10>(); }
      bar(); __builtin_amdgcn_sched_barrier(0);
    }
    if (REFILL && s == 10) {
      wait_lgkm0(); bar();
      const int r = 8 * c.wid + (ln >> 3), s16 = ln & 7, vo = r * 2048 + j2 * 128 + (((s16 >> 1) ^ ((r >> 1) & 3)) << 5) + (s16 & 1) * 16;
#pragma unroll
      for (int k = 0; k < 10; ++k) dma<0>(rW, lds + ((k / 5) * 9 + (k % 5)) * 8192 + c.wid * 1024, vo, ((k / 5) * 14 + 9 + (k % 5)) * 64 * 2048);
      __builtin_amdgcn_sched_barrier(0);
    }
    if (REFILL && s == 17) { wait_vm<2 * ((NSH - R < 17 ? NSH - R : 17) - 10)>(); wait_lgkm0(); bar(); __builtin_amdgcn_sched_barrier(0); }
    if (s + 1 < NSH) { if (s & 1) ldw(w0, s + 1); else ldw(w1, s + 1); }
#pragma unroll
    for (int n = 0; n < 4; ++n)
#pragma unroll
      for (int m = 0; m < 2; ++m) acc[m][n] = mfma((s & 1) ? w1[n] : w0[n], __builtin_bit_cast(bf16x8, a[s % R][m]), acc[m][n]);
    if (s + R < NSH) lda(s + R);
    interleave<8, 8>();
    __builtin_amdgcn_sched_barrier(0);
  }
  TS(REFILL ? 29 : 28);
  wait_lgkm0(); bar();
#pragma unroll
  for (int n = 0; n < 4; ++n) *(LAS f32x4*)(lds + L_RED + c.wid * 4096 + n * 1024 + ln * 16) = kh ? acc[0][n] : acc[1][n];
  wait_lgkm0(); bar();
#pragma unroll
  for (int n = 0; n < 4; ++n) res[n] = (kh ? acc[1][n] : acc[0][n]) + *(LAS f32x4*)(lds + L_RED + (c.wid ^ 4) * 4096 + n * 1024 + ln * 16);
}

template <class PRE> DI void phase2(Ctx& c, const Args& A, int i2, int j2, bool pre_on, PRE pre) {
  const __amdgpu_buffer_rsrc_t rW2 = __builtin_amdgcn_make_buffer_rsrc((void*)A.wo, 0, 1024 * 2048, 0x00020000);
  const int lane = opaque(c.lane), g = lane >> 4, i16 = lane & 15, mt = 2 * (c.wid & 3) + (c.wid >> 2);
  u32x2 xr[4];
#pragma unroll
  for (int n = 0; n < 4; ++n) xr[n] = *(const u32x2*)(A.x + (size_t)(i2 * 128 + true_row(mt, i16)) * 1024 + j2 * 64 + 16 * n + 4 * g);
  wait_lgkm0(); bar();
  f32x4 acc[4];
  gemm_split<16, R2, false>(c, acc, OFF_OF + i2 * 262144, rW2, j2);
  TS(6);
  float s = 0.f, qq = 0.f;
#pragma unroll
  for (int u = 0; u < 2; ++u) {
    f32x4 h0 = acc[2 * u], h1 = acc[2 * u + 1]; const u32x2 x0 = xr[2 * u], x1 = xr[2 * u + 1];
    h0[0] += bflo(x0[0]); h0[1] += bfhi(x0[0]); h0[2] += bflo(x0[1]); h0[3] += bfhi(x0[1]);
    h1[0] += bflo(x1[0]); h1[1] += bfhi(x1[0]); h1[2] += bflo(x1[1]); h1[3] += bfhi(x1[1]);
    const bf16x8 hb = pk8(h0, h1);
    st16(c, hb, lane * 16 + (OFF_HF + (i2 * 16 + j2) * 16384 + (mt * 2 + u) * 1024));
    acc_row(s, qq, __builtin_bit_cast(u32x4, hb));
  }
  s = sum_g(s); qq = sum_g(qq);
  if (lane < 16) __builtin_amdgcn_raw_buffer_store_b64((u32x2){__float_as_uint(s), __float_as_uint(qq)}, c.ws, OFF_STH + (i2 * 16 + j2) * 1024 + (mt * 16 + lane) * 8, 0, SC_SC1);
  publish<8>(c, 2, FG_H, i2, j2);
  if (pre_on) pre();
  TS(7);
}

DI void phase5(Ctx& c, const Args& A, int i2, int j2) {
  const int lane = opaque(c.lane), g = lane >> 4, i16 = lane & 15, mt = 2 * (c.wid & 3) + (c.wid >> 2);
  u32x4 hr[2];
#pragma unroll
  for (int u = 0; u < 2; ++u) hr[u] = __builtin_amdgcn_raw_buffer_load_b128(c.ws, lane * 16, OFF_HF + (i2 * 16 + j2) * 16384 + (mt * 2 + u) * 1024, SC_SC1);
  const __amdgpu_buffer_rsrc_t rW = __builtin_amdgcn_make_buffer_rsrc((void*)A.wout, 0, 1792 * 2048, 0x00020000);
  if (c.wid == 0) wait_flags(c, FG_U, i2, 14);
  if (c.wid >= 4) wait_vm<24>();
  bar();
  f32x4 acc[4];
  gemm_split<28, R5, true>(c, acc, OFF_UF + i2 * 458752, rW, j2);
  TS(14);
  const lds_t ot = c.lds + c.wid * 2048;
#pragma unroll
  for (int n = 0; n < 4; ++n) { const unsigned r0 = hr[n >> 1][2 * (n & 1)], r1 = hr[n >> 1][2 * (n & 1) + 1];
    *(LAS u32x2*)(ot + i16 * 128 + (((2 * n + (g >> 1)) ^ (i16 & 7)) << 4) + (g & 1) * 8) =
        (u32x2){pk2(acc[n][0] + bflo(r0), acc[n][1] + bfhi(r0)), pk2(acc[n][2] + bflo(r1), acc[n][3] + bfhi(r1))}; }
  asm volatile("s_waitcnt lgkmcnt(0)" ::: "memory");
  const __amdgpu_buffer_rsrc_t rO = __builtin_amdgcn_make_buffer_rsrc((void*)A.out, 0, 2048 * 2048, 0x00020000);
#pragma unroll
  for (int u = 0; u < 2; ++u) { const int r = 8 * u + (lane >> 3), ch = lane & 7;
    const u32x4 v = *(LAS u32x4*)(ot + r * 128 + ((ch ^ (r & 7)) << 4));
    __builtin_amdgcn_raw_buffer_store_b128(v, rO, (i2 * 128 + true_row(mt, r)) * 2048 + j2 * 128 + ch * 16, 0, OUTAUX); }
  TS(15);
}

#define LAST_PHASE 5
__global__ __launch_bounds__(512, 2) void fused_fwd(const u16* __restrict__ x, const u16* __restrict__ wqkv, const u16* __restrict__ wphi, const u16* __restrict__ wo,
                                                      const u16* __restrict__ win, const u16* __restrict__ wsp, const u16* __restrict__ bsp, const u16* __restrict__ wout,
                                                      u16* __restrict__ out, char* __restrict__ ws) {
  extern __shared__ __attribute__((aligned(16))) char shm[];
  Ctx c;
  c.lds = (lds_t)shm; c.wsp = ws; c.lane = threadIdx.x & 63; c.wid = __builtin_amdgcn_readfirstlane(threadIdx.x >> 6); c.b = blockIdx.x;
  c.ws = __builtin_amdgcn_make_buffer_rsrc((void*)ws, 0, WS_BYTES, 0x00020000);
  c.tag = 0u; c.ts = 0u;
  if (threadIdx.x < 16) *(LAS unsigned*)(c.lds + L_CNT + threadIdx.x * 4) = 0u;
  const int xcd = c.b & 7, jj = c.b >> 3;
  Map M;
  M.i1 = 8 * (xcd & 1) + (jj >> 2); M.j1 = 4 * (xcd >> 1) + (jj & 3);
  M.i2 = 4 * (xcd >> 1) + (jj >> 3); M.j2 = 8 * (xcd & 1) + (jj & 7);
  M.act3 = jj < 28;
  M.i3 = 4 * (xcd >> 1) + (M.act3 ? jj / 7 : 0); M.j3 = 7 * (xcd & 1) + (M.act3 ? jj % 7 : 0);
  const Args A{x, wqkv, wphi, wo, win, wsp, bsp, wout, out, ws};
  constexpr int last = LAST_PHASE;
  const bool ld = c.wid >= 4;
  {
    Acc1 e;
    if (ld) p1_load(c, A, M.i1, M.j1);
    else { __builtin_amdgcn_s_setprio(1); p1_mma(c, A, M.i1, M.j1, e); __builtin_amdgcn_s_setprio(0); }
    p1_epilogue(c, A, M, e, last);
  }
  if (last < 2) return;
  Ld3 L3;
  p3_setup(c, A, M.j3, L3);
  phase2(c, A, M.i2, M.j2, ld && M.act3, [&]() { p3_issueW(c, L3, 0); p3_issueW(c, L3, 1); });
  if (last < 3) return;
  if (M.act3) {
    u32x2 z1[2][4];
    Ld4 L;
    L.rA = __builtin_amdgcn_make_buffer_rsrc((void*)(A.wsp + (size_t)M.i3 * 128 * 2048), 0, 128 * 4096, 0x00020000);
    {
      Acc3 e;
      if (ld) p3_load(c, L3, M.i3);
      else { __builtin_amdgcn_s_setprio(1); p3_mma(c, M.i3, M.j3, e); __builtin_amdgcn_s_setprio(0); }
      const int ln = opaque(c.lane);
      L.voA = rowmajor_voff(c.wid & 3, ln, 4096); L.voF = ln * 16 + (c.wid & 3) * 1024;
      p3_epilogue(c, L, M.i3, M.j3, e, z1);
    }
    if (last < 4) return;
    f32x4 e4[2][4];
    if (ld) p4_load(c, L, M.j3);
    else { __builtin_amdgcn_s_setprio(1); p4_mma(c, M.j3, e4); __builtin_amdgcn_s_setprio(0); }
    p4_epilogue(c, A, M.i3, M.j3, M.j2, e4, z1);
  } else {
    bar();
    if (!ld) { const int wv = ((c.b & 7) * 4 + ((c.b >> 3) - 28)) * 4 + c.wid;
#pragma unroll 1
      for (int k = 0; k < 12; ++k) { const int Li = (k * 128 + wv) * 64 + c.lane;
        touch(c.lds, Li < 65536 ? (const char*)A.wsp + (size_t)Li * 128 : (const char*)A.wout + (size_t)((Li < 94208 ? Li : 94207) - 65536) * 128); } }
    if (ld) p5_issueW(c, A, M.j2);
  }
  if (last < 5) return;
  phase5(c, A, M.i2, M.j2);
}

extern "C" void kernel_launch(void* const* d_in, const int* in_sizes, int n_in, void* d_out, int out_size, void* d_ws, size_t ws_size, hipStream_t stream) {
  (void)in_sizes; (void)n_in; (void)out_size; (void)ws_size;
  (void)hipFuncSetAttribute((const void*)fused_fwd, hipFuncAttributeMaxDynamicSharedMemorySize, LDS_BYTES);
  fused_fwd<<<256, 512, LDS_BYTES, stream>>>((const u16*)d_in[0], (const u16*)d_in[1], (const u16*)d_in[2], (const u16*)d_in[3], (const u16*)d_in[5], (const u16*)d_in[6],
                                             (const u16*)d_in[7], (const u16*)d_in[8], (u16*)d_out, (char*)d_ws);
}
```

```cpp
#define PROBE 4
#include <hip/hip_runtime.h>
#include <hip/hip_bf16.h>
#include <stdint.h>
#include <type_traits>

#define LAS __attribute__((address_space(3)))
#define DI __device__ __forceinline__
typedef unsigned short u16;
typedef short bf16x8 __attribute__((ext_vector_type(8)));
typedef short s16x4 __attribute__((ext_vector_type(4)));
typedef float f32x4 __attribute__((ext_vector_type(4)));
typedef float f32x2 __attribute__((ext_vector_type(2)));
typedef unsigned u32x4 __attribute__((ext_vector_type(4)));
typedef unsigned u32x2 __attribute__((ext_vector_type(2)));
typedef __bf16 bf2 __attribute__((ext_vector_type(2)));
typedef LAS char* lds_t;

constexpr unsigned OFF_EPOCH = 0, OFF_ERR = 128, OFF_FLAG = 4096;
constexpr unsigned OFF_KVP = 262144;
constexpr unsigned OFF_ZP = OFF_KVP + 2097152;
constexpr unsigned OFF_ST2 = 27u << 20 | 0;
constexpr unsigned OFF_STH = OFF_ZP + 131072 + 262144;
constexpr unsigned OFF_GX = OFF_STH + 262144;
constexpr unsigned OFF_GC1 = OFF_GX + 65536;
constexpr unsigned OFF_GC3 = OFF_GC1 + 393216;
constexpr unsigned OFF_OF = 4u << 20, OFF_HF = 8u << 20, OFF_Z2 = 12u << 20, OFF_UF = 20u << 20, OFF_TS = 28u << 20;
constexpr unsigned WS_BYTES = 32u << 20;
enum { FG_KV = 0, FG_O = 1, FG_H = 2, FG_ST = 3, FG_Z2 = 4, FG_U = 5 };
constexpr int LDS_BYTES = 163840;
constexpr int SC1 = 122880, L_WPHI = SC1, L_TAB1 = SC1 + 4096, L_XBUF = SC1 + 22528;
constexpr int L_XF1 = 65536, L_TB = 32768, L_KR = 65536, L_ZR = 131072;
constexpr int SC3 = 147456, L_TAB3 = SC3, L_FIN = SC3 + 4096;
constexpr int L_CNT = LDS_BYTES - 64, L_DUMMY = LDS_BYTES - 512;
constexpr int SC_SC1 = 16;
#define PSLEEP 4
#define OUTAUX 16
#define LDAUX 16

DI void bar() { asm volatile("" ::: "memory"); __builtin_amdgcn_s_barrier(); asm volatile("" ::: "memory"); }
template <int N> DI void wait_vm() { asm volatile("s_waitcnt vmcnt(%0)" ::"n"(N) : "memory"); }
DI int opaque(int v) { asm volatile("" : "+v"(v)); return v; }
DI void wait_lgkm0() { __builtin_amdgcn_s_waitcnt(0xc07f); }
DI unsigned pk2(float lo, float hi) { f32x2 v = {lo, hi}; return __builtin_bit_cast(unsigned, __builtin_convertvector(v, bf2)); }
DI bf16x8 pk8(const f32x4& a, const f32x4& b) { u32x4 r = {pk2(a[0], a[1]), pk2(a[2], a[3]), pk2(b[0], b[1]), pk2(b[2], b[3])}; return __builtin_bit_cast(bf16x8, r); }
DI float bflo(unsigned w) { return __uint_as_float(w << 16); }
DI float bfhi(unsigned w) { return __uint_as_float(w & 0xffff0000u); }
DI f32x4 mfma(bf16x8 a, bf16x8 b, f32x4 c) { return __builtin_amdgcn_mfma_f32_16x16x32_bf16(a, b, c, 0, 0, 0); }
DI bf16x8 ldsA(lds_t p) { return *(LAS bf16x8*)p; }
DI bf16x8 ldsT(lds_t p, int d) {
  s16x4 lo = __builtin_amdgcn_ds_read_tr16_b64_v4i16((LAS s16x4*)p), hi = __builtin_amdgcn_ds_read_tr16_b64_v4i16((LAS s16x4*)(p + d));
  return __builtin_shufflevector(lo, hi, 0, 1, 2, 3, 4, 5, 6, 7);
}
template <int D1, int D2> DI void tr4_issue(unsigned addr, u32x2 (&r)[4]) {
  asm volatile("ds_read_b64_tr_b16 %0, %4\n\tds_read_b64_tr_b16 %1, %4 offset:%5\n\tds_read_b64_tr_b16 %2, %4 offset:%6\n\tds_read_b64_tr_b16 %3, %4 offset:%7"
               : "=&v"(r[0]), "=&v"(r[1]), "=&v"(r[2]), "=&v"(r[3]) : "v"(addr), "n"(D1), "n"(D2), "n"(D1 + D2) : "memory");
}
DI void tr4_wait(u32x2 (&r)[4]) { asm volatile("s_waitcnt lgkmcnt(0)" : "+v"(r[0]), "+v"(r[1]), "+v"(r[2]), "+v"(r[3])::"memory"); }
DI float sum_g(float x) {
  auto a = __builtin_amdgcn_permlane16_swap(__float_as_uint(x), __float_as_uint(x), false, false);
  x = __uint_as_float(a[0]) + __uint_as_float(a[1]);
  auto b = __builtin_amdgcn_permlane32_swap(__float_as_uint(x), __float_as_uint(x), false, false);
  return __uint_as_float(b[0]) + __uint_as_float(b[1]);
}
DI float max_g(float x) {
  auto a = __builtin_amdgcn_permlane16_swap(__float_as_uint(x), __float_as_uint(x), false, false);
  x = fmaxf(__uint_as_float(a[0]), __uint_as_float(a[1]));
  auto b = __builtin_amdgcn_permlane32_swap(__float_as_uint(x), __float_as_uint(x), false, false);
  return fmaxf(__uint_as_float(b[0]), __uint_as_float(b[1]));
}
template <int CTRL> DI float dpp_add(float x) { return x + __int_as_float(__builtin_amdgcn_update_dpp(0, __float_as_int(x), CTRL, 0xf, 0xf, true)); }
DI float sum_row16(float x) { x = dpp_add<0x128>(x); x = dpp_add<0x124>(x); x = dpp_add<0x122>(x); return dpp_add<0x121>(x); }
DI void sum_row16x8(f32x4& a, f32x4& b) {
#define RR(n) "v_add_f32_dpp %0, %0, %0 row_ror:" n " row_mask:0xf bank_mask:0xf\n\tv_add_f32_dpp %1, %1, %1 row_ror:" n " row_mask:0xf bank_mask:0xf\n\t" \
  "v_add_f32_dpp %2, %2, %2 row_ror:" n " row_mask:0xf bank_mask:0xf\n\tv_add_f32_dpp %3, %3, %3 row_ror:" n " row_mask:0xf bank_mask:0xf\n\t" \
  "v_add_f32_dpp %4, %4, %4 row_ror:" n " row_mask:0xf bank_mask:0xf\n\tv_add_f32_dpp %5, %5, %5 row_ror:" n " row_mask:0xf bank_mask:0xf\n\t" \
  "v_add_f32_dpp %6, %6, %6 row_ror:" n " row_mask:0xf bank_mask:0xf\n\tv_add_f32_dpp %7, %7, %7 row_ror:" n " row_mask:0xf bank_mask:0xf\n\t"
  float a0 = a[0], a1 = a[1], a2 = a[2], a3 = a[3], b0 = b[0], b1 = b[1], b2 = b[2], b3 = b[3];
  asm volatile("s_nop 1\n\t" RR("8") RR("4") RR("2") RR("1") "s_nop 0" : "+v"(a0), "+v"(a1), "+v"(a2), "+v"(a3), "+v"(b0), "+v"(b1), "+v"(b2), "+v"(b3));
#undef RR
  a = (f32x4){a0, a1, a2, a3}; b = (f32x4){b0, b1, b2, b3};
}
DI float dot2(unsigned a, unsigned b, float c) { return __builtin_amdgcn_fdot2_f32_bf16(__builtin_bit_cast(bf2, a), __builtin_bit_cast(bf2, b), c, false); }
constexpr unsigned ONES2 = 0x3f803f80u;
DI f32x4 gelu4(f32x4 x) {
  const f32x4 t = x * (x * x * -0.1029432397f + -2.302208198f);
  f32x4 d, r;
#pragma unroll
  for (int i = 0; i < 4; ++i) d[i] = __builtin_amdgcn_exp2f(t[i]);
  d += 1.0f;
#pragma unroll
  for (int i = 0; i < 4; ++i) r[i] = __builtin_amdgcn_rcpf(d[i]);
  return x * r;
}
DI u32x2 pk4(const f32x4& a) { return (u32x2){pk2(a[0], a[1]), pk2(a[2], a[3])}; }
DI f32x4 up4(u32x2 w) { return (f32x4){bflo(w[0]), bfhi(w[0]), bflo(w[1]), bfhi(w[1])}; }
DI float gelu(float x) { const float t = x * fmaf(x * x, -0.1029432397f, -2.302208198f); return x * __builtin_amdgcn_rcpf(1.0f + __builtin_amdgcn_exp2f(t)); }

struct Ctx {
  __amdgpu_buffer_rsrc_t ws;
  char* wsp; lds_t lds; unsigned tag; int lane, wid, b; unsigned ts; unsigned long long pr0, pc0, pr1, pc1;
};
DI unsigned* flag_ptr(const Ctx& c, int g, int a, int bb) { return (unsigned*)(c.wsp + OFF_FLAG + ((g * 16 + a) * 16 + bb) * 128); }
template <unsigned NWAVES = 4, int YOUNGER = 0> DI void publish(const Ctx& c, int cnt, int g, int a, int bb) {
  wait_vm<YOUNGER>();
  if (c.lane == 0) {
    unsigned old = __hip_atomic_fetch_add((LAS unsigned*)(c.lds + L_CNT + cnt * 4), 1u, __ATOMIC_RELAXED, __HIP_MEMORY_SCOPE_WORKGROUP);
    if (old == NWAVES - 1u) __hip_atomic_store(flag_ptr(c, g, a, bb), c.tag, __ATOMIC_RELAXED, __HIP_MEMORY_SCOPE_AGENT);
  }
}
DI void wait_flags(const Ctx& c, int g, int a, int n) {
  const unsigned* p = flag_ptr(c, g, a, c.lane < n ? c.lane : 0);
  const unsigned long long t0 = __builtin_amdgcn_s_memrealtime();
  for (;;) {
    const unsigned v = __hip_atomic_load(p, __ATOMIC_RELAXED, __HIP_MEMORY_SCOPE_AGENT);
    if (__all(v == c.tag)) break;
    if (__builtin_amdgcn_s_memrealtime() - t0 > 300000ull) { if (c.lane == 0) __hip_atomic_store((unsigned*)(c.wsp + OFF_ERR), 0x100u + (unsigned)g, __ATOMIC_RELAXED, __HIP_MEMORY_SCOPE_AGENT); break; }
    __builtin_amdgcn_s_sleep(1);
  }
  asm volatile("" ::: "memory");
}
#define STEP(t) do {} while (0)
#define TS(k) do {} while (0)

DI void st16(const Ctx& c, bf16x8 v, int off) { __builtin_amdgcn_raw_buffer_store_b128(__builtin_bit_cast(u32x4, v), c.ws, off, 0, SC_SC1); }
template <int AUX> DI void dma(__amdgpu_buffer_rsrc_t r, lds_t dst, int voff, int soff) {
  if (AUX) asm volatile("s_mov_b32 m0, %0\n\ts_nop 0\n\tbuffer_load_dwordx4 %1, %2, %3 offen sc1 lds" ::"s"((unsigned)(size_t)dst), "v"(voff), "s"(r), "s"(soff) : "memory");
  else asm volatile("s_mov_b32 m0, %0\n\ts_nop 0\n\tbuffer_load_dwordx4 %1, %2, %3 offen lds" ::"s"((unsigned)(size_t)dst), "v"(voff), "s"(r), "s"(soff) : "memory");
}

#define EXP 0
template <int S, int NW, class IA, class IW, class MID, class POST>
DI void loader_loop(int nt, bool gated_prologue, IA issueA, IW issueW, MID mid, POST post) {
  constexpr int PPW = 4 + NW;
  if (gated_prologue) wait_vm<4 * (S - 2)>(); else wait_vm<PPW * (S - 2)>();
  bar();
  int t = 0;
#pragma unroll
  for (int p = 0; p < S - 2; ++p, ++t) {
    mid(t); issueA(t + S - 1); issueW(t + S - 1); post(t);
    if (gated_prologue) {
      if (p == 0) wait_vm<4 * (S - 3 > 0 ? S - 3 : 0) + PPW * 1>();
      else if (p == 1) wait_vm<4 * (S - 4 > 0 ? S - 4 : 0) + PPW * 2>();
      else wait_vm<4 * (S - 5 > 0 ? S - 5 : 0) + PPW * 3>();
    } else wait_vm<PPW * (S - 2)>();
    bar();
  }
#pragma unroll 1
  for (; t + S - 1 < nt; ++t) { mid(t); if (!(EXP & 1)) { issueA(t + S - 1); issueW(t + S - 1); } wait_vm<PPW * (S - 2)>(); bar(); }
#pragma unroll
  for (int r = S - 2; r >= 0; --r, ++t) {
    if (r >= 4) wait_vm<PPW * 3>(); else if (r == 3) wait_vm<PPW * 2>(); else if (r == 2) wait_vm<PPW * 1>(); else wait_vm<0>();
    bar();
  }
}
template <int NW, bool GATED, class IA, class IW, class POST, class GA, class FIN>
DI void loader_loop16(bool full, bool once, IA issueA, IW issueW, POST post, GA gather, FIN fin) {
  constexpr int PPW = 4 + NW;
  if (GATED) wait_vm<4>(); else wait_vm<PPW>();
  bar();
  issueA(2); issueW(2); post(0); wait_vm<PPW>(); bar();
#pragma unroll 1
  for (int t = 1; t < 5; ++t) { if (!(EXP & 1)) { issueA(t + 2); issueW(t + 2); } post(t); wait_vm<PPW>(); bar(); }
#pragma unroll
  for (int t = 5; t < 13; ++t) {
    issueA(t + 2); issueW(t + 2); gather(t - 5);
    if (full) { if (t == 5) wait_vm<PPW + 2>(); else wait_vm<PPW + 4>(); }
    else if (once && t <= 6) wait_vm<PPW + 2>();
    else wait_vm<PPW>();
    bar();
  }
  issueA(15); issueW(15); if (full) wait_vm<PPW + 2>(); else wait_vm<PPW>(); bar();
  wait_vm<0>(); fin(); bar();
  bar();
}
template <int S, int SB, class RD0, class RD1, class MM0, class MM1, class SCH, class HK, class ST>
DI void consumer_loop(Ctx& c, int sbase, int nt, RD0 rd0, RD1 rd1, MM0 mm0, MM1 mm1, SCH sched, HK hook, ST stamp) {
  bar();
  stamp();
  rd0(0);
  int sb = 0;
#pragma unroll 1
  for (int t = 0; t < nt; ++t) {
    hook(t); STEP(t);
    __builtin_amdgcn_sched_barrier(0);
    if (!(EXP & 4)) rd1(sb); if (!(EXP & 2)) mm0(); sched();
    __builtin_amdgcn_sched_barrier(0);
    wait_lgkm0(); bar();
    __builtin_amdgcn_sched_barrier(0);
    sb = (sb == (S - 1) * SB) ? 0 : sb + SB;
    if (!(EXP & 4)) rd0(sb); if (!(EXP & 2)) mm1(); sched();
    __builtin_amdgcn_sched_barrier(0);
  }
}
#define TOUCH(name, mod) DI void name(lds_t lds, const void* p) { \
  asm volatile("s_mov_b32 m0, %1\n\ts_nop 0\n\tglobal_load_lds_dword %0, off" mod ::"v"(p), "s"((unsigned)(size_t)(lds + L_DUMMY)) : "memory"); }
TOUCH(touch, "") TOUCH(touch_nt, " nt") TOUCH(touch_sc1, " sc1")
template <int NM, int NR> DI void interleave() {
#pragma unroll
  for (int i = 0; i < NR; ++i) { __builtin_amdgcn_sched_group_barrier(0x008, 1, 0); __builtin_amdgcn_sched_group_barrier(0x100, 1, 0); }
  __builtin_amdgcn_sched_group_barrier(0x008, NM - NR, 0);
}
template <int NM, int NR> DI void interleave2() {
#pragma unroll
  for (int i = 0; i < NR / 2; ++i) { __builtin_amdgcn_sched_group_barrier(0x008, 1, 0); __builtin_amdgcn_sched_group_barrier(0x100, 2, 0); }
  __builtin_amdgcn_sched_group_barrier(0x008, NM - NR / 2, 0);
}

DI int rowmajor_voff(int lw, int lane, int pitch) {
  const int rr = lane >> 3, slot = 8 * (lw & 1) + rr;
  const int row = 8 * (slot >> 2) + 4 * (lw >> 1) + (slot & 3);
  return row * pitch + (((lane & 7) ^ (slot >> 1)) << 4);
}
DI int true_row(int mt, int i) { return 32 * (mt >> 1) + 8 * (i >> 2) + 4 * (mt & 1) + (i & 3); }

DI void acc_row(float& s, float& q, u32x4 w) {
#pragma unroll
  for (int i = 0; i < 4; ++i) { s = dot2(w[i], ONES2, s); q = dot2(w[i], w[i], q); } }
DI float col_sum(const u32x2 (&r)[4]) { float s0 = 0.f, s1 = 0.f;
#pragma unroll
  for (int i = 0; i < 4; ++i) { s0 = dot2(r[i][0], ONES2, s0); s1 = dot2(r[i][1], ONES2, s1); }
  return sum_g(s0 + s1); }
DI void st_granule(const Ctx& c, float v, int off) { __builtin_amdgcn_raw_buffer_store_b64((u32x2){__float_as_uint(v), c.tag}, c.ws, off, 0, SC_SC1); }
DI bool timed_out(const Ctx& c, unsigned long long t0, unsigned code) {
  if (__builtin_amdgcn_s_memrealtime() - t0 <= 300000ull) return false;
  if (c.lane == 0) __hip_atomic_store((unsigned*)(c.wsp + OFF_ERR), code, __ATOMIC_RELAXED, __HIP_MEMORY_SCOPE_AGENT);
  return true;
}
DI float sum_granules16(const Ctx& c, u32x2 (&v)[16], int off, int stride) {
  const unsigned long long t0 = __builtin_amdgcn_s_memrealtime();
  for (;;) {
    bool ok = true;
#pragma unroll
    for (int k = 0; k < 16; ++k) ok &= v[k][1] == c.tag;
    if (__all(ok) || timed_out(c, t0, 0x200u)) break;
    __builtin_amdgcn_s_sleep(2);
#pragma unroll
    for (int k = 0; k < 16; ++k) v[k] = __builtin_amdgcn_raw_buffer_load_b64(c.ws, off + k * stride, 0, SC_SC1);
  }
  float s = 0.f;
#pragma unroll
  for (int k = 0; k < 16; ++k) s += __uint_as_float(v[k][0]);
  return s;
}

#define ROTM 0
#define ROT2(j) (ROTM * 2 * ((j) & 7))
#define ROT5(j) (ROTM * ((7 * ((j) & 7)) >> 1))
#define P4D 0
#define TOUCHW touch
#define PF_T0 2
#define PF_DT 3

struct Args { const u16 *x, *wqkv, *wphi, *wo, *win, *wsp, *bsp, *wout; u16* out; char* ws; };

DI void get_tag(Ctx& c) { unsigned e; asm volatile("s_load_dword %0, %1, 0x0\n\ts_waitcnt lgkmcnt(0)" : "=&s"(e) : "s"(c.wsp) : "memory"); c.tag = e + 1u; }
DI void kv_gather(const Ctx& c, int j1) {
  const int lane = c.lane, g = lane >> 4, i16 = lane & 15, tid = c.wid * 64 + lane;
  f32x4 a0 = {0, 0, 0, 0}, a1 = {0, 0, 0, 0};
  u32x4 v[16];
  const unsigned f0 = __hip_atomic_load(flag_ptr(c, FG_KV, j1, i16), __ATOMIC_RELAXED, __HIP_MEMORY_SCOPE_AGENT);
  const unsigned ready = __builtin_amdgcn_readfirstlane((unsigned)(__ballot(f0 == c.tag) & 0xffffull));
  asm volatile("" ::: "memory");
#pragma unroll
  for (int k = 0; k < 16; ++k) if ((ready >> k) & 1u) v[k] = __builtin_amdgcn_raw_buffer_load_b128(c.ws, tid * 16, OFF_KVP + (j1 * 16 + k) * 8192, SC_SC1);
  if (ready != 0xffffu) {
    wait_flags(c, FG_KV, j1, 16);
#pragma unroll
    for (int k = 0; k < 16; ++k) if (!((ready >> k) & 1u)) v[k] = __builtin_amdgcn_raw_buffer_load_b128(c.ws, tid * 16, OFF_KVP + (j1 * 16 + k) * 8192, SC_SC1);
  }
  float zs = 0.f;
  if (c.wid < 2) { unsigned zv[16];
#pragma unroll
    for (int k = 0; k < 16; ++k) zv[k] = __builtin_amdgcn_raw_buffer_load_b32(c.ws, lane * 4, OFF_ZP + ((j1 * 16 + k) * 2 + c.wid) * 256, SC_SC1);
#pragma unroll
    for (int k = 0; k < 16; ++k) zs += __uint_as_float(zv[k]); }
#pragma unroll
  for (int k = 0; k < 16; ++k) { a0[0] += bflo(v[k][0]); a0[1] += bfhi(v[k][0]); a0[2] += bflo(v[k][1]); a0[3] += bfhi(v[k][1]); a1[0] += bflo(v[k][2]); a1[1] += bfhi(v[k][2]); a1[2] += bflo(v[k][3]); a1[3] += bfhi(v[k][3]); }
  const int w = c.wid, hd = w >> 2, dt = (w >> 1) & 1, fp = w & 1;
  const lds_t tb = c.lds + L_XBUF + hd * 8192 + (32 * fp + i16) * 128 + (16 * dt + 4 * g) * 2;
  *(LAS u32x2*)tb = (u32x2){pk2(a0[0], a0[1]), pk2(a0[2], a0[3])};
  *(LAS u32x2*)(tb + 16 * 128) = (u32x2){pk2(a1[0], a1[1]), pk2(a1[2], a1[3])};
  if (c.wid < 2) { const unsigned z2 = pk2(zs, zs); const lds_t t3 = c.lds + L_XBUF + c.wid * 8192 + lane * 128 + 64;
    *(LAS u32x4*)t3 = (u32x4){z2, z2, z2, z2}; *(LAS u32x4*)(t3 + 16) = (u32x4){z2, z2, z2, z2}; }
  wait_lgkm0();
}
struct Map { int i1, j1, i2, j2, i3, j3; bool act3; };
DI void p1_load(Ctx& c, const Args& A, int i1, int j1) {
  const int lane = c.lane, lw = c.wid - 4, g = lane >> 4, q = (lane & 15) >> 2, p = lane & 3;
  const lds_t lds = c.lds;
  const __amdgpu_buffer_rsrc_t rWS = c.ws;
  constexpr int S = 3, SB = 40960, NW = 6;
  const __amdgpu_buffer_rsrc_t rA = __builtin_amdgcn_make_buffer_rsrc((void*)(A.x + (size_t)i1 * 128 * 1024), 0, 128 * 2048, 0x00020000);
  const __amdgpu_buffer_rsrc_t rW = __builtin_amdgcn_make_buffer_rsrc((void*)A.wqkv, 0, 1024 * 6144, 0x00020000);
  const __amdgpu_buffer_rsrc_t rP = __builtin_amdgcn_make_buffer_rsrc((void*)A.wphi, 0, 65536, 0x00020000);
  const int voA = rowmajor_voff(lw, lane, 2048);
  int voW[NW];
#pragma unroll
  for (int i = 0; i < NW; ++i) { const int o = (NW * lw + i) * 1024 + lane * 16, r = o / 384, rem = o % 384, seg = rem >> 7, s16 = (rem & 127) >> 4;
    const int c32 = (s16 >> 1) ^ ((r >> 1) & 3), r5 = r & 31, k = (r & 32) + 8 * ((r5 >> 2) & 3) + 4 * (r5 >> 4) + (r5 & 3);
    voW[i] = k * 6144 + seg * 2048 + j1 * 128 + c32 * 32 + (s16 & 1) * 16; }
#define ROTMODE 1
  const int rot = ROTMODE == 2 ? (2 * (i1 & 7) + (i1 >> 3) + 4 * (j1 & 3)) & 15 : ROTMODE == 1 ? 4 * (i1 & 3) : ROTMODE == 3 ? 8 * (i1 & 1) : 0;
  auto issueA = [&](int t) { const lds_t d = lds + (t % S) * SB + lw * 1024; const int so = ((t + rot) & 15) * 128;
#pragma unroll
    for (int i = 0; i < 4; ++i) dma<0>(rA, d + i * 4096, voA, so + i * 32 * 2048); };
  auto issueW = [&](int t) { const lds_t d = lds + (t % S) * SB + 16384 + lw * NW * 1024; const int so = ((t + rot) & 15) * 64 * 6144;
#pragma unroll
    for (int i = 0; i < NW; ++i) dma<0>(rW, d + i * 1024, voW[i], so); };
  issueA(0); issueW(0); issueA(1); issueW(1);
  dma<0>(rP, lds + L_WPHI + lw * 1024, lane * 16 + lw * 1024, j1 * 4096);
  get_tag(c);
  TS(0);
  u32x2 gv[16]; u32x4 gx[2];
  const int goff = OFF_GC1 + j1 * 16 * 1536 + (lw * 64 + lane) * 8, xoff = OFF_GX + (i1 * 128 + lane) * 16;
  const int tau = ROTMODE == 2 ? 0 : ROTMODE == 1 ? i1 >> 2 : ROTMODE == 3 ? 0 : 0;
  auto post = [&](int t) {
    if (t != tau) return;
    const int sw = (2 * g + (q >> 1)) & 3; u32x2 cr[3][4];
#pragma unroll
    for (int n = 0; n < 3; ++n) { const int nu = 3 * lw + n;
      tr4_issue<16 * 384, 32 * 384>((unsigned)(size_t)(lds + (t % S) * SB + 16384 + (4 * g + q) * 384 + 8 * p + (nu >> 2) * 128 + 32 * ((nu & 3) ^ sw)), cr[n]); }
#pragma unroll
    for (int n = 0; n < 3; ++n) { tr4_wait(cr[n]); const float s = col_sum(cr[n]);
      if (lane < 16) st_granule(c, s, OFF_GC1 + (j1 * 16 + rot + tau) * 1536 + ((3 * lw + n) * 16 + lane) * 8); }
  };
  auto gather = [&](int k) {
    if (lw < 3) { gv[2 * k] = __builtin_amdgcn_raw_buffer_load_b64(rWS, goff + 2 * k * 1536, 0, SC_SC1); gv[2 * k + 1] = __builtin_amdgcn_raw_buffer_load_b64(rWS, goff + (2 * k + 1) * 1536, 0, SC_SC1); }
    else if (k == 0) { gx[0] = __builtin_amdgcn_raw_buffer_load_b128(rWS, xoff, 0, SC_SC1); gx[1] = __builtin_amdgcn_raw_buffer_load_b128(rWS, xoff + 1024, 0, SC_SC1); }
  };
  auto fin = [&]() {
    if (lw < 3) *(LAS float*)(lds + L_TAB1 + 1024 + (lw * 64 + lane) * 4) = sum_granules16(c, gv, goff, 1536);
    else {
      const unsigned long long t0 = __builtin_amdgcn_s_memrealtime();
      for (;;) {
        if (__all(gx[0][1] == c.tag && gx[0][3] == c.tag && gx[1][1] == c.tag && gx[1][3] == c.tag) || timed_out(c, t0, 0x201u)) break;
        __builtin_amdgcn_s_sleep(2);
        gx[0] = __builtin_amdgcn_raw_buffer_load_b128(rWS, xoff, 0, SC_SC1); gx[1] = __builtin_amdgcn_raw_buffer_load_b128(rWS, xoff + 1024, 0, SC_SC1);
      }
#pragma unroll
      for (int e = 0; e < 2; ++e) { *(LAS unsigned*)(lds + L_TAB1 + (lane + 64 * e) * 4) = gx[e][0]; *(LAS unsigned*)(lds + L_TAB1 + 512 + (lane + 64 * e) * 4) = gx[e][2]; }
    }
    wait_lgkm0();
    __builtin_amdgcn_s_waitcnt(0x0f70);
  {
    const int gw = c.b * 4 + lw;
    if (lane < 56) touch(lds, (const char*)A.win + (size_t)(gw * 56 + lane) * 128);
    if (lane < 16) touch(lds, (const char*)A.wo + (size_t)(gw * 16 + lane) * 128);
  }
  };
  loader_loop16<NW, false>(lw < 3, lw == 3, issueA, issueW, post, gather, fin);
  TS(1);
}

struct Acc1 { f32x4 q[2][2], k[2][2], v[2][2]; };
DI void p1_mma(Ctx& c, const Args& A, int i1, int j1, Acc1& e) {
  const int lane = c.lane, wr = c.wid >> 1, wc = c.wid & 1, g = lane >> 4, q = (lane & 15) >> 2, p = lane & 3, i16 = lane & 15;
  const lds_t lds = c.lds;
  const f32x4 Z4 = {0.f, 0.f, 0.f, 0.f};
  constexpr int S = 3, SB = 40960;
  f32x4 aq[4][2], ak[4][2], av[4][2];
#pragma unroll
  for (int m = 0; m < 4; ++m)
#pragma unroll
    for (int n = 0; n < 2; ++n) { aq[m][n] = Z4; ak[m][n] = Z4; av[m][n] = Z4; asm volatile("" : "+v"(aq[m][n]), "+v"(ak[m][n]), "+v"(av[m][n])); }
  const int sw = (2 * g + (q >> 1)) & 3;
  const int ao0 = (wr * 64 + i16) * 128 + ((g ^ (i16 >> 1)) << 4), ao1 = ao0 ^ 64;
  const int wl = 16384 + (4 * g + q) * 384 + 8 * p;
  const int wo0 = wl + 32 * ((2 * wc) ^ sw), wo1 = wl + 32 * ((2 * wc + 1) ^ sw);
  bf16x8 a0[4], w0[6], a1[4], w1[6];
  auto rd = [&](bf16x8(&a)[4], bf16x8(&w)[6], int sb, int ks) { const lds_t s = lds + sb;
#pragma unroll
    for (int sg = 0; sg < 3; ++sg) { w[2 * sg] = ldsT(s + wo0 + sg * 128 + ks * 32 * 384, 16 * 384); w[2 * sg + 1] = ldsT(s + wo1 + sg * 128 + ks * 32 * 384, 16 * 384); }
#pragma unroll
    for (int m = 0; m < 4; ++m) a[m] = ldsA(s + (ks ? ao1 : ao0) + m * 2048); };
  auto mm = [&](bf16x8(&a)[4], bf16x8(&w)[6]) {
#pragma unroll
    for (int n = 0; n < 2; ++n)
#pragma unroll
      for (int m = 0; m < 4; ++m) aq[m][n] = mfma(w[n], a[m], aq[m][n]);
#pragma unroll
    for (int n = 0; n < 2; ++n)
#pragma unroll
      for (int m = 0; m < 4; ++m) ak[m][n] = mfma(w[2 + n], a[m], ak[m][n]);
#pragma unroll
    for (int n = 0; n < 2; ++n)
#pragma unroll
      for (int m = 0; m < 4; ++m) av[m][n] = mfma(a[m], w[4 + n], av[m][n]); };
  TS(0);
  {
    u32x4 xv[2][2];
#pragma unroll
    for (int e2 = 0; e2 < 2; ++e2) { const int R = 8 * j1 + 2 * c.wid + e2; const u16* xr = A.x + (size_t)(i1 * 128 + true_row(R >> 4, R & 15)) * 1024 + lane * 16;
      xv[e2][0] = *(const u32x4*)xr; xv[e2][1] = *(const u32x4*)(xr + 8); }
    get_tag(c);
#pragma unroll
    for (int e2 = 0; e2 < 2; ++e2) { float s = 0.f, qq = 0.f; acc_row(s, qq, xv[e2][0]); acc_row(s, qq, xv[e2][1]);
      s = sum_g(sum_row16(s)) * (1.0f / 1024.0f); qq = sum_g(sum_row16(qq)) * (1.0f / 1024.0f);
      const float rs = __builtin_amdgcn_rsqf(fmaxf(qq - s * s, 0.f) + 1e-5f);
      if (lane == 0) __builtin_amdgcn_raw_buffer_store_b128((u32x4){__float_as_uint(s), c.tag, __float_as_uint(rs), c.tag}, c.ws, OFF_GX + (i1 * 128 + 8 * j1 + 2 * c.wid + e2) * 16, 0, SC_SC1); }
  }
  consumer_loop<S, SB>(c, 0, 16, [&](int sb) { rd(a0, w0, sb, 0); }, [&](int sb) { rd(a1, w1, sb, 1); }, [&]() { mm(a0, w0); }, [&]() { mm(a1, w1); }, []() { interleave<24, 16>(); }, [](int) {}, [&]() { TS(16); });
  TS(1);
  const lds_t xf = lds + L_XF1 + c.wid * 12288 + lane * 16;
#pragma unroll
  for (int m = 0; m < 2; ++m)
#pragma unroll
    for (int n = 0; n < 2; ++n) {
      *(LAS f32x4*)(xf + (m * 6 + n) * 1024) = aq[2 + m][n]; *(LAS f32x4*)(xf + (m * 6 + 2 + n) * 1024) = ak[2 + m][n]; *(LAS f32x4*)(xf + (m * 6 + 4 + n) * 1024) = av[2 + m][n];
      e.q[m][n] = aq[m][n]; e.k[m][n] = ak[m][n]; e.v[m][n] = av[m][n];
    }
  wait_lgkm0();
}

DI void both_g_max(float& a, float& b) {
  auto r = __builtin_amdgcn_permlane16_swap(__float_as_uint(a), __float_as_uint(b), false, false);
  const float x = fmaxf(__uint_as_float(r[0]), __uint_as_float(r[1]));
  auto s = __builtin_amdgcn_permlane32_swap(__float_as_uint(x), __float_as_uint(x), false, false);
  const float y = fmaxf(__uint_as_float(s[0]), __uint_as_float(s[1]));
  auto t = __builtin_amdgcn_permlane16_swap(__float_as_uint(y), __float_as_uint(y), false, false);
  a = __uint_as_float(t[0]); b = __uint_as_float(t[1]);
}
DI void both_g_sum(float& a, float& b) {
  auto r = __builtin_amdgcn_permlane16_swap(__float_as_uint(a), __float_as_uint(b), false, false);
  const float x = __uint_as_float(r[0]) + __uint_as_float(r[1]);
  auto s = __builtin_amdgcn_permlane32_swap(__float_as_uint(x), __float_as_uint(x), false, false);
  const float y = __uint_as_float(s[0]) + __uint_as_float(s[1]);
  auto t = __builtin_amdgcn_permlane16_swap(__float_as_uint(y), __float_as_uint(y), false, false);
  a = __uint_as_float(t[0]); b = __uint_as_float(t[1]);
}
DI void feat(const f32x4& p0, const f32x4& p1, f32x4& e0, f32x4& e1, f32x4& n0, f32x4& n1) {
  constexpr float L2E = 1.4426950408889634f;
  const f32x4 t0 = p0 * L2E, t1 = p1 * L2E;
  float mx = fmaxf(fmaxf(fmaxf(t0[0], t0[1]), t0[2]), fmaxf(fmaxf(fmaxf(t0[3], t1[0]), t1[1]), fmaxf(t1[2], t1[3])));
  float mn = -fminf(fminf(fminf(t0[0], t0[1]), t0[2]), fminf(fminf(fminf(t0[3], t1[0]), t1[1]), fminf(t1[2], t1[3])));
  both_g_max(mx, mn);
  const f32x4 a0 = t0 - mx, a1 = t1 - mx, b0 = -t0 - mn, b1 = -t1 - mn;
#pragma unroll
  for (int r = 0; r < 4; ++r) { e0[r] = __builtin_amdgcn_exp2f(a0[r]); e1[r] = __builtin_amdgcn_exp2f(a1[r]); n0[r] = __builtin_amdgcn_exp2f(b0[r]); n1[r] = __builtin_amdgcn_exp2f(b1[r]); }
  const f32x4 se = e0 + e1, sn4 = n0 + n1;
  float sp = (se[0] + se[1]) + (se[2] + se[3]), sn = (sn4[0] + sn4[1]) + (sn4[2] + sn4[3]);
  both_g_sum(sp, sn);
  sp = __builtin_amdgcn_rcpf(sp); sn = __builtin_amdgcn_rcpf(sn);
  e0 *= sp; e1 *= sp; n0 *= sn; n1 *= sn;
}

DI void p1_epilogue(Ctx& c, const Args& A, const Map& M, Acc1& e, int last) {
  const int lane = opaque(c.lane), cw = c.wid & 3, hf = c.wid >> 2, wr = cw >> 1, wc = cw & 1, g = lane >> 4, q = (lane & 15) >> 2, p = lane & 3, i16 = lane & 15;
  const int lw = cw, i1 = M.i1, j1 = M.j1;
  const lds_t lds = c.lds;
  const f32x4 Z4 = {0.f, 0.f, 0.f, 0.f};
  bar();
  TS(20);
  if (hf) { const lds_t xf = lds + L_XF1 + cw * 12288 + lane * 16;
#pragma unroll
    for (int m = 0; m < 2; ++m)
#pragma unroll
      for (int n = 0; n < 2; ++n) { e.q[m][n] = *(LAS f32x4*)(xf + (m * 6 + n) * 1024); e.k[m][n] = *(LAS f32x4*)(xf + (m * 6 + 2 + n) * 1024); e.v[m][n] = *(LAS f32x4*)(xf + (m * 6 + 4 + n) * 1024); } }
  const lds_t tab = lds + L_TAB1;
  bf16x8 qf[2], kf[2], vf[2], wT[2];
  {
    f32x4 vv[2][2];
#pragma unroll
    for (int m = 0; m < 2; ++m) {
      const int R = 64 * wr + 32 * hf + 16 * m;
      const float mu = *(LAS float*)(tab + (R + i16) * 4), rs = *(LAS float*)(tab + 512 + (R + i16) * 4), nm = -mu * rs;
      const f32x4 mu4 = *(LAS f32x4*)(tab + (R + 4 * g) * 4), rs4 = *(LAS f32x4*)(tab + 512 + (R + 4 * g) * 4), nm4 = -mu4 * rs4;
      f32x4 qq[2], kk[2];
#pragma unroll
      for (int n = 0; n < 2; ++n) {
        const f32x4 cq = *(LAS f32x4*)(tab + 1024 + (32 * wc + 16 * n + 4 * g) * 4), ck = *(LAS f32x4*)(tab + 1024 + (64 + 32 * wc + 16 * n + 4 * g) * 4);
        const float cv = *(LAS float*)(tab + 1024 + (128 + 32 * wc + 16 * n + i16) * 4);
        qq[n] = e.q[m][n] * rs + cq * nm; kk[n] = e.k[m][n] * rs + ck * nm; vv[m][n] = e.v[m][n] * rs4 + nm4 * cv;
      }
      qf[m] = pk8(qq[0], qq[1]); kf[m] = pk8(kk[0], kk[1]);
    }
    vf[0] = pk8(vv[0][0], vv[1][0]); vf[1] = pk8(vv[0][1], vv[1][1]);
#pragma unroll
    for (int ft = 0; ft < 2; ++ft) wT[ft] = ldsT(lds + L_WPHI + wc * 2048 + (4 * g + q) * 64 + ft * 32 + 8 * p, 16 * 64);
  }
  wait_lgkm0(); bar();
  TS(21);
  {
    const lds_t tb = lds + L_TB + c.wid * 4096;
#pragma unroll
    for (int m = 0; m < 2; ++m) {
      const f32x4 p0 = mfma(wT[0], kf[m], Z4), p1 = mfma(wT[1], kf[m], Z4);
      f32x4 e0, e1, n0, n1; feat(p0, p1, e0, e1, n0, n1);
      const lds_t rw = tb + (16 * m + i16) * 128;
      *(LAS u32x2*)(rw + 8 * ((0 + g) ^ i16)) = pk4(e0); *(LAS u32x2*)(rw + 8 * ((4 + g) ^ i16)) = pk4(e1);
      *(LAS u32x2*)(rw + 8 * ((8 + g) ^ i16)) = pk4(n0); *(LAS u32x2*)(rw + 8 * ((12 + g) ^ i16)) = pk4(n1);
    }
    asm volatile("s_waitcnt lgkmcnt(0)" ::: "memory");
    const bf16x8 ones = __builtin_bit_cast(bf16x8, (u32x4){ONES2, ONES2, ONES2, ONES2});
    const lds_t kr = lds + L_KR + c.wid * 8192 + lane * 16;
    float zf[4];
#pragma unroll
    for (int f = 0; f < 4; ++f) {
      const int r0 = 4 * g + q;
      const s16x4 lo = __builtin_amdgcn_ds_read_tr16_b64_v4i16((LAS s16x4*)(tb + r0 * 128 + 8 * ((4 * f + p) ^ r0)));
      const s16x4 hi = __builtin_amdgcn_ds_read_tr16_b64_v4i16((LAS s16x4*)(tb + (16 + r0) * 128 + 8 * ((4 * f + p) ^ r0)));
      const bf16x8 pf = __builtin_shufflevector(lo, hi, 0, 1, 2, 3, 4, 5, 6, 7);
      *(LAS f32x4*)(kr + f * 1024) = mfma(vf[0], pf, Z4); *(LAS f32x4*)(kr + (4 + f) * 1024) = mfma(vf[1], pf, Z4);
      zf[f] = mfma(ones, pf, Z4)[0];
    }
    *(LAS float*)(lds + L_ZR + c.wid * 256 + lane * 4) = g == 0 ? zf[0] : g == 1 ? zf[1] : g == 2 ? zf[2] : zf[3];
    TS(22);
    wait_lgkm0(); bar();
    f32x4 k0 = Z4, k1 = Z4;
#pragma unroll
    for (int w = 0; w < 4; ++w) { const lds_t s = lds + L_KR + (wc + 2 * w) * 8192 + (4 * wr + 2 * hf) * 1024 + lane * 16; k0 += *(LAS f32x4*)s; k1 += *(LAS f32x4*)(s + 1024); }
    st16(c, pk8(k0, k1), lane * 16 + (OFF_KVP + (j1 * 16 + i1) * 8192 + wc * 4096 + wr * 2048 + hf * 1024));
    if (c.wid < 2) { float zt = 0.f;
#pragma unroll
      for (int w = 0; w < 4; ++w) zt += *(LAS float*)(lds + L_ZR + (wc + 2 * w) * 256 + lane * 4);
      __builtin_amdgcn_raw_buffer_store_b32(__float_as_uint(zt), c.ws, lane * 4, OFF_ZP + ((j1 * 16 + i1) * 2 + wc) * 256, SC_SC1); }
    wait_lgkm0(); bar();
    TS(23);
    publish<8>(c, 0, FG_KV, j1, i1);
  }
  TS(2);
  const __amdgpu_buffer_rsrc_t rW2 = __builtin_amdgcn_make_buffer_rsrc((void*)A.wo, 0, 1024 * 2048, 0x00020000);
  int voW2[2];
#pragma unroll
  for (int i = 0; i < 2; ++i) { const int r = (2 * lw + i) * 8 + (lane >> 3), s16 = lane & 7; voW2[i] = r * 2048 + M.j2 * 128 + (((s16 >> 1) ^ ((r >> 1) & 3)) << 5) + (s16 & 1) * 16; }
  auto issueW2 = [&](int t) {
#pragma unroll
    for (int i = 0; i < 2; ++i) dma<0>(rW2, lds + t * 8192 + (lw * 2 + i) * 1024, voW2[i], t * 64 * 2048); };
  if (hf) {
    if (M.act3) {
      const __amdgpu_buffer_rsrc_t rW3 = __builtin_amdgcn_make_buffer_rsrc((void*)A.win, 0, 1024 * 7168, 0x00020000);
#pragma unroll
      for (int i = 0; i < 8; ++i) { const int r = (8 * lw + i) * 2 + (lane >> 5), l5 = lane & 31, seg = l5 >> 4, s16 = l5 & 15;
        dma<0>(rW3, lds + 32768 + (8 * lw + i) * 1024, r * 7168 + (seg == 0 ? 3584 : 0) + M.j3 * 256 + (((s16 >> 1) ^ (r & 7)) << 5) + (s16 & 1) * 16, M.i3 * 64 * 7168); }
    }
#pragma unroll
    for (int t = 0; t < 3; ++t) { issueW2(t); issueW2(8 + t); }
  }
  bf16x8 ph[2][2];
#pragma unroll
  for (int m = 0; m < 2; ++m) {
    const f32x4 p0 = mfma(wT[0], qf[m], Z4), p1 = mfma(wT[1], qf[m], Z4);
    f32x4 e0, e1, n0, n1; feat(p0, p1, e0, e1, n0, n1);
    ph[m][0] = pk8(e0, e1); ph[m][1] = pk8(n0, n1);
  }
  asm volatile("" : "+v"(ph[0][0]), "+v"(ph[0][1]), "+v"(ph[1][0]), "+v"(ph[1][1]));
  TS(3);
  kv_gather(c, j1);
  bar();
  TS(4);
  {
    bf16x8 kt[3][2];
#pragma unroll
    for (int d = 0; d < 3; ++d)
#pragma unroll
      for (int s = 0; s < 2; ++s) kt[d][s] = ldsT(lds + L_XBUF + wc * 8192 + (32 * s + 4 * g + q) * 128 + 32 * d + 8 * p, 16 * 128);
#pragma unroll
    for (int m = 0; m < 2; ++m) {
      const f32x4 n0 = mfma(kt[0][1], ph[m][1], mfma(kt[0][0], ph[m][0], Z4)), n1 = mfma(kt[1][1], ph[m][1], mfma(kt[1][0], ph[m][0], Z4));
      const f32x4 dn = mfma(kt[2][1], ph[m][1], mfma(kt[2][0], ph[m][0], Z4));
      const float r = __builtin_amdgcn_rcpf(dn[0] + 1e-6f);
      st16(c, pk8(n0 * r, n1 * r), lane * 16 + (OFF_OF + (i1 * 16 + j1) * 16384 + ((4 * wr + 2 * hf + m) * 2 + wc) * 1024));
    }
  }
  publish<8>(c, 1, FG_O, i1, j1);
  TS(5);
  if (hf && M.act3) {
    u32x2 cr[4][4];
#pragma unroll
    for (int n = 0; n < 4; ++n) tr4_issue<16 * 512, 32 * 512>((unsigned)(size_t)(lds + 32768 + (4 * g + q) * 512 + 8 * p + (lw >> 1) * 256 + 32 * ((4 * (lw & 1) + n) ^ (4 * (g & 1) + q))), cr[n]);
#pragma unroll
    for (int n = 0; n < 4; ++n) { tr4_wait(cr[n]); const float s = col_sum(cr[n]);
      if (lane < 16) st_granule(c, s, OFF_GC3 + (M.j3 * 16 + M.i3) * 2048 + ((4 * lw + n) * 16 + lane) * 8); }
  }
  if (hf) {
    issueW2(3);
#pragma unroll
    for (int t = 11; t < 16; ++t) issueW2(t);
  } else if (c.wid == 0 && last >= 2) wait_flags(c, FG_O, M.i2, 16);
}

struct Ld3 { __amdgpu_buffer_rsrc_t rW; int voW[8]; };
DI void p3_setup(const Ctx& c, const Args& A, int j3, Ld3& L) {
  const int lane = opaque(c.lane), lw = c.wid & 3;
  L.rW = __builtin_amdgcn_make_buffer_rsrc((void*)A.win, 0, 1024 * 7168, 0x00020000);
#pragma unroll
  for (int i = 0; i < 8; ++i) { const int r = (8 * lw + i) * 2 + (lane >> 5), l5 = lane & 31, seg = l5 >> 4, s16 = l5 & 15;
    L.voW[i] = r * 7168 + (seg == 0 ? 3584 : 0) + j3 * 256 + (((s16 >> 1) ^ (r & 7)) << 5) + (s16 & 1) * 16; }
}
DI void p3_issueW(const Ctx& c, const Ld3& L, int t) { const lds_t d = c.lds + (t % 3) * 49152 + 16384 + (c.wid & 3) * 8192;
#pragma unroll
  for (int i = 0; i < 8; ++i) dma<0>(L.rW, d + i * 1024, L.voW[i], t * 64 * 7168); }
DI void p3_load(Ctx& c, const Ld3& L, int i3) {
  const int lane = opaque(c.lane), lw = c.wid - 4;
  const lds_t lds = c.lds;
  constexpr int S = 3, SB = 49152;
  const int voF = lane * 16 + lw * 1024;
  auto issueA = [&](int t) { const lds_t d = lds + (t % S) * SB + lw * 1024; const int so = OFF_HF + (i3 * 16 + t) * 16384;
#pragma unroll
    for (int i = 0; i < 4; ++i) dma<LDAUX>(c.ws, d + i * 4096, voF, so + i * 4096); };
  auto issueW = [&](int t) { p3_issueW(c, L, t); };
  bar();
  issueA(0); issueA(1);
  u32x2 hv[16]; const int R = (lw & 1) * 64 + lane;
  auto gather = [&](int k) { if (lw < 2) {
      hv[2 * k] = __builtin_amdgcn_raw_buffer_load_b64(c.ws, R * 8, OFF_STH + (i3 * 16 + 2 * k) * 1024, SC_SC1);
      hv[2 * k + 1] = __builtin_amdgcn_raw_buffer_load_b64(c.ws, R * 8, OFF_STH + (i3 * 16 + 2 * k + 1) * 1024, SC_SC1); } };
  auto fin = [&]() { if (lw < 2) { float s = 0.f, qq = 0.f;
#pragma unroll
      for (int k = 0; k < 16; ++k) { s += __uint_as_float(hv[k][0]); qq += __uint_as_float(hv[k][1]); }
      s *= (1.0f / 1024.0f); qq *= (1.0f / 1024.0f);
      *(LAS float*)(lds + L_TAB3 + R * 4) = s; *(LAS float*)(lds + L_TAB3 + 512 + R * 4) = __builtin_amdgcn_rsqf(fmaxf(qq - s * s, 0.f) + 1e-5f);
      wait_lgkm0(); } };
  loader_loop16<8, true>(lw < 2, false, issueA, issueW, [](int) {}, gather, fin);
  TS(6);
}
struct Acc3 { f32x4 z2[2][4], z1[2][4]; };
DI int xf3_base(int cw) { return cw < 3 ? 16384 + cw * 32768 : 98304; }
DI void p3_mma(Ctx& c, int i3, int j3, Acc3& e) {
  const int lane = opaque(c.lane), wr = c.wid >> 1, wc = c.wid & 1, g = lane >> 4, q = (lane & 15) >> 2, p = lane & 3;
  const lds_t lds = c.lds;
  const __amdgpu_buffer_rsrc_t rWS = c.ws;
  const f32x4 Z4 = {0.f, 0.f, 0.f, 0.f};
  constexpr int S = 3, SB = 49152;
  if (wc == 1) {
    const int off = OFF_GC3 + j3 * 16 * 2048 + (128 * wr + 2 * lane) * 8; u32x4 gv[16];
    const unsigned long long t0 = __builtin_amdgcn_s_memrealtime();
    for (;;) {
      bool ok = true;
#pragma unroll
      for (int k = 0; k < 16; ++k) gv[k] = __builtin_amdgcn_raw_buffer_load_b128(rWS, off + k * 2048, 0, SC_SC1);
#pragma unroll
      for (int k = 0; k < 16; ++k) ok &= gv[k][1] == c.tag && gv[k][3] == c.tag;
      if (__all(ok) || timed_out(c, t0, 0x202u)) break;
      __builtin_amdgcn_s_sleep(2);
    }
    f32x2 cs = {0.f, 0.f};
#pragma unroll
    for (int k = 0; k < 16; ++k) { cs[0] += __uint_as_float(gv[k][0]); cs[1] += __uint_as_float(gv[k][2]); }
    *(LAS f32x2*)(lds + L_TAB3 + 1024 + (128 * wr + 2 * lane) * 4) = cs;
    wait_lgkm0();
  } else if (c.wid == 0) wait_flags(c, FG_H, i3, 16);
  f32x4 a2[4][4], a1_[4][4];
#pragma unroll
  for (int m = 0; m < 4; ++m)
#pragma unroll
    for (int n = 0; n < 4; ++n) { a2[m][n] = Z4; a1_[m][n] = Z4; }
#pragma unroll
  for (int m = 0; m < 4; ++m) asm volatile("" : "+v"(a2[m][0]), "+v"(a2[m][1]), "+v"(a2[m][2]), "+v"(a2[m][3]), "+v"(a1_[m][0]), "+v"(a1_[m][1]), "+v"(a1_[m][2]), "+v"(a1_[m][3]));
  bar();
  const int ao2 = wr * 8192 + lane * 16;
  int wo_[4];
#pragma unroll
  for (int n = 0; n < 4; ++n) wo_[n] = 16384 + (4 * g + q) * 512 + 8 * p + 32 * ((4 * wc + n) ^ (4 * (g & 1) + q));
  bf16x8 a0[4], w0[8], a1[4], w1[8];
  auto rd = [&](bf16x8(&a)[4], bf16x8(&w)[8], int sb, int ks) { const lds_t s = lds + sb;
#pragma unroll
    for (int n = 0; n < 8; ++n) w[n] = ldsT(s + wo_[n & 3] + (n >> 2) * 256 + ks * 32 * 512, 16 * 512);
#pragma unroll
    for (int m = 0; m < 4; ++m) a[m] = ldsA(s + ao2 + m * 2048 + ks * 1024); };
  auto mm = [&](bf16x8(&a)[4], bf16x8(&w)[8]) {
#pragma unroll
    for (int n = 0; n < 4; ++n)
#pragma unroll
      for (int m = 0; m < 4; ++m) a2[m][n] = mfma(a[m], w[n], a2[m][n]);
#pragma unroll
    for (int n = 0; n < 4; ++n)
#pragma unroll
      for (int m = 0; m < 4; ++m) a1_[m][n] = mfma(w[4 + n], a[m], a1_[m][n]); };
  consumer_loop<S, SB>(c, 16, 16, [&](int sb) { rd(a0, w0, sb, 0); }, [&](int sb) { rd(a1, w1, sb, 1); }, [&]() { mm(a0, w0); }, [&]() { mm(a1, w1); }, []() { interleave<32, 20>(); }, [](int) {}, [&]() { TS(17); });
  TS(8);
  const lds_t xf = lds + xf3_base(c.wid) + lane * 16;
#pragma unroll
  for (int m = 0; m < 2; ++m)
#pragma unroll
    for (int n = 0; n < 4; ++n) { *(LAS f32x4*)(xf + (m * 8 + n) * 1024) = a2[2 + m][n]; *(LAS f32x4*)(xf + (m * 8 + 4 + n) * 1024) = a1_[2 + m][n]; e.z2[m][n] = a2[m][n]; e.z1[m][n] = a1_[m][n]; }
  wait_lgkm0();
}
struct Ld4 { __amdgpu_buffer_rsrc_t rA; int voA, voF; };
DI void p4_issueW(const Ctx& c, const Ld4& L, int t) { const int lw = c.wid & 3; const lds_t d = c.lds + (t & 3) * 32768 + lw * 1024;
#pragma unroll
  for (int i = 0; i < 4; ++i) dma<0>(L.rA, d + i * 4096, L.voA, t * 128 + i * 32 * 4096); }
DI void p3_epilogue(Ctx& c, const Ld4& L, int i3, int j3, Acc3& e, u32x2 (&z1)[2][4]) {
  const int lane = opaque(c.lane), cw = c.wid & 3, hf = c.wid >> 2, wr = cw >> 1, wc = cw & 1, g = lane >> 4, i16 = lane & 15;
  const lds_t lds = c.lds, tab = lds + L_TAB3;
  const __amdgpu_buffer_rsrc_t rWS = c.ws;
  const f32x4 Z4 = {0.f, 0.f, 0.f, 0.f};
  bar();
  TS(25);
  __builtin_amdgcn_s_setprio(1);
  if (hf) { const lds_t xf = lds + xf3_base(cw) + lane * 16;
#pragma unroll
    for (int m = 0; m < 2; ++m)
#pragma unroll
      for (int n = 0; n < 4; ++n) { e.z2[m][n] = *(LAS f32x4*)(xf + (m * 8 + n) * 1024); e.z1[m][n] = *(LAS f32x4*)(xf + (m * 8 + 4 + n) * 1024); } }
  {
    float cs[4];
#pragma unroll
    for (int n = 0; n < 4; ++n) cs[n] = *(LAS float*)(tab + 1024 + (64 * wc + 16 * n + i16) * 4);
#pragma unroll
    for (int m = 0; m < 2; ++m) {
      const int R = 64 * wr + 32 * hf + 16 * m + 4 * g;
      const f32x4 mu = *(LAS f32x4*)(tab + R * 4), rs = *(LAS f32x4*)(tab + 512 + R * 4), nm = -mu * rs;
      f32x4 s = Z4, qq = Z4;
#pragma unroll
      for (int n = 0; n < 4; ++n) { const f32x4 y = gelu4(e.z2[m][n] * rs + nm * cs[n]); e.z2[m][n] = y; s += y; qq += y * y; }
      sum_row16x8(s, qq);
      if (i16 < 4) { const float sv = i16 == 0 ? s[0] : i16 == 1 ? s[1] : i16 == 2 ? s[2] : s[3], qv = i16 == 0 ? qq[0] : i16 == 1 ? qq[1] : i16 == 2 ? qq[2] : qq[3];
        __builtin_amdgcn_raw_buffer_store_b128((u32x4){__float_as_uint(sv), c.tag, __float_as_uint(qv), c.tag}, rWS, OFF_ST2 + ((i3 * 14 + j3) * 2 + wc) * 2048 + (R + i16) * 16, 0, SC_SC1); }
    }
  }
  TS(9);
  __builtin_amdgcn_s_setprio(0);
  if (!hf) {
#pragma unroll
    for (int s = 0; s < 3; ++s) p4_issueW(c, L, s);
  }
  {
    const int R0 = 64 * wr + 32 * hf, off = OFF_ST2 + (i3 * 28 + (lane >> 5)) * 2048 + (R0 + (lane & 31)) * 16;
    u32x4 v[14];
    const unsigned long long t0 = __builtin_amdgcn_s_memrealtime();
    for (;;) {
      bool ok = true;
#pragma unroll
      for (int k = 0; k < 14; ++k) v[k] = __builtin_amdgcn_raw_buffer_load_b128(rWS, off + k * 4096, 0, SC_SC1);
#pragma unroll
      for (int k = 0; k < 14; ++k) ok &= v[k][1] == c.tag && v[k][3] == c.tag;
      if (__all(ok) || timed_out(c, t0, 0x203u)) break;
      __builtin_amdgcn_s_sleep(PSLEEP);
    }
    __builtin_amdgcn_s_setprio(1);
    float s = 0.f, qq = 0.f;
#pragma unroll
    for (int k = 0; k < 14; ++k) { s += __uint_as_float(v[k][0]); qq += __uint_as_float(v[k][2]); }
    { auto a = __builtin_amdgcn_permlane32_swap(__float_as_uint(s), __float_as_uint(s), false, false); s = (__uint_as_float(a[0]) + __uint_as_float(a[1])) * (1.0f / 1792.0f);
      auto b = __builtin_amdgcn_permlane32_swap(__float_as_uint(qq), __float_as_uint(qq), false, false); qq = (__uint_as_float(b[0]) + __uint_as_float(b[1])) * (1.0f / 1792.0f); }
    const float rsv = __builtin_amdgcn_rsqf(fmaxf(qq - s * s, 0.f) + 1e-5f);
    const lds_t ft = lds + L_FIN + c.wid * 256;
    if (lane < 32) { *(LAS float*)(ft + lane * 4) = rsv; *(LAS float*)(ft + 128 + lane * 4) = -s * rsv; }
    asm volatile("s_waitcnt lgkmcnt(0)" ::: "memory");
    TS(31);
    f32x4 rs[2], nm[2];
#pragma unroll
    for (int m = 0; m < 2; ++m) { rs[m] = *(LAS f32x4*)(ft + (16 * m + 4 * g) * 4); nm[m] = *(LAS f32x4*)(ft + 128 + (16 * m + 4 * g) * 4); }
#pragma unroll
    for (int n = 0; n < 4; ++n)
      st16(c, pk8(e.z2[0][n] * rs[0] + nm[0], e.z2[1][n] * rs[1] + nm[1]), lane * 16 + (OFF_Z2 + (j3 * 32 + 2 * i3 + wr) * 16384 + ((4 * wc + n) * 2 + hf) * 1024));
  }
  __builtin_amdgcn_s_setprio(0);
  publish<8>(c, 4, FG_Z2, j3, i3);
  TS(11);
#pragma unroll
  for (int m = 0; m < 2; ++m) asm volatile("" : "+v"(e.z1[m][0]), "+v"(e.z1[m][1]), "+v"(e.z1[m][2]), "+v"(e.z1[m][3]));
#pragma unroll
  for (int m = 0; m < 2; ++m) {
    const int R = 64 * wr + 32 * hf + 16 * m + i16;
    const float mu = *(LAS float*)(tab + R * 4), rs = *(LAS float*)(tab + 512 + R * 4), nm = -mu * rs;
#pragma unroll
    for (int n = 0; n < 4; ++n) { const f32x4 cs = *(LAS f32x4*)(tab + 1024 + (128 + 64 * wc + 16 * n + 4 * g) * 4); z1[m][n] = pk4(gelu4(e.z1[m][n] * rs + cs * nm)); }
    asm volatile("" : "+v"(z1[m][0]), "+v"(z1[m][1]), "+v"(z1[m][2]), "+v"(z1[m][3]));
  }
  TS(10);
}
DI void p4_load(Ctx& c, const Ld4& L, int j3) {
  const int lw = c.wid - 4;
  auto issueW = [&](int t) { p4_issueW(c, L, t); };
  auto issueA = [&](int t) { const lds_t d = c.lds + (t & 3) * 32768 + 16384 + lw * 1024; const int so = OFF_Z2 + (j3 * 32 + t) * 16384;
#pragma unroll
    for (int i = 0; i < 4; ++i) dma<LDAUX>(c.ws, d + i * 4096, L.voF, so + i * 4096); };
  bar();
  if (c.b == 0 && lw == 0 && c.lane == 0) __hip_atomic_store((unsigned*)(c.wsp + OFF_EPOCH), c.tag, __ATOMIC_RELAXED, __HIP_MEMORY_SCOPE_AGENT);
#pragma unroll
  for (int s = 0; s < 3; ++s) issueA(s);
  loader_loop<4, 4>(32, true, issueA, issueW, [](int) {}, [](int) {});
}
DI void p4_mma(Ctx& c, int j3, f32x4 (&e)[2][4]) {
  const int lane = opaque(c.lane), wr = c.wid >> 1, wc = c.wid & 1, g = lane >> 4, i16 = lane & 15;
  const lds_t lds = c.lds;
  constexpr int S = 4, SB = 32768;
  f32x4 acc[4][4];
#pragma unroll
  for (int m = 0; m < 4; ++m)
#pragma unroll
    for (int n = 0; n < 4; ++n) { acc[m][n] = (f32x4){0.f, 0.f, 0.f, 0.f}; asm volatile("" : "+v"(acc[m][n])); }
  const int ao0 = (wr * 64 + i16) * 128 + ((g ^ (i16 >> 1)) << 4), ao1 = ao0 ^ 64, zo = 16384 + wc * 8192 + lane * 16;
  bf16x8 a0[4], w0[4], a1[4], w1[4];
  auto rd = [&](bf16x8(&a)[4], bf16x8(&w)[4], int sb, int ks) { const lds_t s = lds + sb;
#pragma unroll
    for (int n = 0; n < 4; ++n) { w[n] = ldsA(s + zo + n * 2048 + ks * 1024); a[n] = ldsA(s + (ks ? ao1 : ao0) + n * 2048); } };
  auto mm = [&](bf16x8(&a)[4], bf16x8(&w)[4]) {
#pragma unroll
    for (int n = 0; n < 4; ++n)
#pragma unroll
      for (int m = 0; m < 4; ++m) acc[m][n] = mfma(w[n], a[m], acc[m][n]); };
  if (c.wid == 0) wait_flags(c, FG_Z2, j3, 16);
  bar();
  c.pr0 = __builtin_amdgcn_s_memrealtime(); c.pc0 = __builtin_amdgcn_s_memtime();
  consumer_loop<S, SB>(c, 32, 32, [&](int sb) { rd(a0, w0, sb, 0); }, [&](int sb) { rd(a1, w1, sb, 1); }, [&]() { mm(a0, w0); }, [&]() { mm(a1, w1); }, []() { interleave<16, 8>(); }, [](int) {}, [&]() { TS(18); });
  c.pr1 = __builtin_amdgcn_s_memrealtime(); c.pc1 = __builtin_amdgcn_s_memtime();
  TS(12);
  const lds_t xf = lds + c.wid * 8192 + lane * 16;
#pragma unroll
  for (int m = 0; m < 2; ++m)
#pragma unroll
    for (int n = 0; n < 4; ++n) { *(LAS f32x4*)(xf + (m * 4 + n) * 1024) = acc[2 + m][n]; e[m][n] = acc[m][n]; }
  wait_lgkm0();
}
DI void p5_issueW(const Ctx& c, const Args& A, int j2);
DI void p4_epilogue(Ctx& c, const Args& A, int i3, int j3, int j2, f32x4 (&e)[2][4], const u32x2 (&z1)[2][4]) {
  const int lane = opaque(c.lane), cw = c.wid & 3, hf = c.wid >> 2, wr = cw >> 1, wc = cw & 1, i16 = lane & 15;
  float bs[2];
#pragma unroll
  for (int m = 0; m < 2; ++m) bs[m] = bflo((unsigned)A.bsp[i3 * 128 + true_row(4 * wr + 2 * hf + m, i16)]);
  bar();
  if (hf) { const lds_t xf = c.lds + cw * 8192 + lane * 16;
#pragma unroll
    for (int m = 0; m < 2; ++m)
#pragma unroll
      for (int n = 0; n < 4; ++n) e[m][n] = *(LAS f32x4*)(xf + (m * 4 + n) * 1024); }
#pragma unroll
  for (int m = 0; m < 2; ++m)
#pragma unroll
    for (int u = 0; u < 2; ++u)
      st16(c, pk8(up4(z1[m][2 * u]) * (e[m][2 * u] + bs[m]), up4(z1[m][2 * u + 1]) * (e[m][2 * u + 1] + bs[m])),
           lane * 16 + (OFF_UF + (i3 * 28 + 2 * j3 + wc) * 16384 + ((4 * wr + 2 * hf + m) * 2 + u) * 1024));
  wait_lgkm0(); bar();
  publish<8>(c, 5, FG_U, i3, j3);
  if (hf) p5_issueW(c, A, j2);
  TS(13);
}

DI void p5_issueW(const Ctx& c, const Args& A, int j2) {
  const int lane = opaque(c.lane), lw = c.wid - 4;
  const __amdgpu_buffer_rsrc_t rW = __builtin_amdgcn_make_buffer_rsrc((void*)A.wout, 0, 1792 * 2048, 0x00020000);
  int vo[2];
#pragma unroll
  for (int i = 0; i < 2; ++i) { const int r = (2 * lw + i) * 8 + (lane >> 3), s16 = lane & 7; vo[i] = r * 2048 + j2 * 128 + (((s16 >> 1) ^ ((r >> 1) & 3)) << 5) + (s16 & 1) * 16; }
#pragma unroll
  for (int t = 0; t < 18; ++t)
#pragma unroll
    for (int i = 0; i < 2; ++i) dma<0>(rW, c.lds + ((t & 1) * 9 + (t >> 1)) * 8192 + (lw * 2 + i) * 1024, vo[i], ((t & 1) * 14 + (t >> 1)) * 64 * 2048);
}

constexpr int L_RED = 98304;
#define STG1 0
#define STG2 2
#define R5 6
#define R2 6
template <int NSH, int R, bool REFILL>
DI void gemm_split(Ctx& c, f32x4 (&res)[4], int aoff, __amdgpu_buffer_rsrc_t rW, int j2) {
  const int ln = opaque(c.lane), g = ln >> 4, q = (ln & 15) >> 2, p = ln & 3, rw = c.wid & 3, kh = c.wid >> 2;
  const lds_t lds = c.lds;
  const int sw2 = (2 * g + (q >> 1)) & 3, wl2 = (4 * g + q) * 128 + 8 * p + kh * (REFILL ? 9 : 8) * 8192, va = ln * 16 + rw * 4096;
  const int ao = aoff + kh * (NSH / 2) * 16384;
  int wo2[4];
#pragma unroll
  for (int n = 0; n < 4; ++n) wo2[n] = wl2 + 32 * (n ^ sw2);
  f32x4 acc[2][4];
#pragma unroll
  for (int n = 0; n < 4; ++n) { acc[0][n] = (f32x4){0.f, 0.f, 0.f, 0.f}; acc[1][n] = acc[0][n]; }
  u32x4 a[R][2]; bf16x8 w0[4], w1[4];
  auto lda = [&](int s) {
#pragma unroll
    for (int m = 0; m < 2; ++m) a[s % R][m] = __builtin_amdgcn_raw_buffer_load_b128(c.ws, va + m * 2048, ao + (s >> 1) * 16384 + (s & 1) * 1024, LDAUX); };
  auto ldw = [&](bf16x8(&d)[4], int s) { const int lt = s >> 1, slot = (REFILL && lt >= 9) ? lt - 9 : lt;
#pragma unroll
    for (int n = 0; n < 4; ++n) d[n] = ldsT(lds + wo2[n] + slot * 8192 + (s & 1) * 4096, 16 * 128); };
  TS(REFILL ? 19 : 24);
#pragma unroll
  for (int s = 0; s < R; ++s) { lda(s); if (s == STG1 || s == STG2) bar(); }
  if (!REFILL && kh) {
#pragma unroll
    for (int i = 0; i < 2; ++i) { const int r = (2 * rw + i) * 8 + (ln >> 3), s16 = ln & 7, vo = r * 2048 + j2 * 128 + (((s16 >> 1) ^ ((r >> 1) & 3)) << 5) + (s16 & 1) * 16;
#pragma unroll
      for (int t = 4; t < 8; ++t) dma<0>(rW, lds + t * 8192 + (rw * 2 + i) * 1024, vo, t * 64 * 2048); }
  }
  ldw(w0, 0);
#pragma unroll
  for (int s = 0; s < NSH; ++s) {
    if (s == 5) {
      if (kh) { if (REFILL) wait_vm<2 * R + 10>(); else wait_vm<10>(); }
      bar(); __builtin_amdgcn_sched_barrier(0);
    }
    if (REFILL && s == 10) {
      wait_lgkm0(); bar();
      const int r = 8 * c.wid + (ln >> 3), s16 = ln & 7, vo = r * 2048 + j2 * 128 + (((s16 >> 1) ^ ((r >> 1) & 3)) << 5) + (s16 & 1) * 16;
#pragma unroll
      for (int k = 0; k < 10; ++k) dma<0>(rW, lds + ((k / 5) * 9 + (k % 5)) * 8192 + c.wid * 1024, vo, ((k / 5) * 14 + 9 + (k % 5)) * 64 * 2048);
      __builtin_amdgcn_sched_barrier(0);
    }
    if (REFILL && s == 17) { wait_vm<2 * ((NSH - R < 17 ? NSH - R : 17) - 10)>(); wait_lgkm0(); bar(); __builtin_amdgcn_sched_barrier(0); }
    if (s + 1 < NSH) { if (s & 1) ldw(w0, s + 1); else ldw(w1, s + 1); }
#pragma unroll
    for (int n = 0; n < 4; ++n)
#pragma unroll
      for (int m = 0; m < 2; ++m) acc[m][n] = mfma((s & 1) ? w1[n] : w0[n], __builtin_bit_cast(bf16x8, a[s % R][m]), acc[m][n]);
    if (s + R < NSH) lda(s + R);
    interleave<8, 8>();
    __builtin_amdgcn_sched_barrier(0);
  }
  TS(REFILL ? 29 : 28);
  wait_lgkm0(); bar();
#pragma unroll
  for (int n = 0; n < 4; ++n) *(LAS f32x4*)(lds + L_RED + c.wid * 4096 + n * 1024 + ln * 16) = kh ? acc[0][n] : acc[1][n];
  wait_lgkm0(); bar();
#pragma unroll
  for (int n = 0; n < 4; ++n) res[n] = (kh ? acc[1][n] : acc[0][n]) + *(LAS f32x4*)(lds + L_RED + (c.wid ^ 4) * 4096 + n * 1024 + ln * 16);
}

template <class PRE> DI void phase2(Ctx& c, const Args& A, int i2, int j2, bool pre_on, PRE pre) {
  const __amdgpu_buffer_rsrc_t rW2 = __builtin_amdgcn_make_buffer_rsrc((void*)A.wo, 0, 1024 * 2048, 0x00020000);
  const int lane = opaque(c.lane), g = lane >> 4, i16 = lane & 15, mt = 2 * (c.wid & 3) + (c.wid >> 2);
  u32x2 xr[4];
#pragma unroll
  for (int n = 0; n < 4; ++n) xr[n] = *(const u32x2*)(A.x + (size_t)(i2 * 128 + true_row(mt, i16)) * 1024 + j2 * 64 + 16 * n + 4 * g);
  wait_lgkm0(); bar();
  f32x4 acc[4];
  gemm_split<16, R2, false>(c, acc, OFF_OF + i2 * 262144, rW2, j2);
  TS(6);
  float s = 0.f, qq = 0.f;
#pragma unroll
  for (int u = 0; u < 2; ++u) {
    f32x4 h0 = acc[2 * u], h1 = acc[2 * u + 1]; const u32x2 x0 = xr[2 * u], x1 = xr[2 * u + 1];
    h0[0] += bflo(x0[0]); h0[1] += bfhi(x0[0]); h0[2] += bflo(x0[1]); h0[3] += bfhi(x0[1]);
    h1[0] += bflo(x1[0]); h1[1] += bfhi(x1[0]); h1[2] += bflo(x1[1]); h1[3] += bfhi(x1[1]);
    const bf16x8 hb = pk8(h0, h1);
    st16(c, hb, lane * 16 + (OFF_HF + (i2 * 16 + j2) * 16384 + (mt * 2 + u) * 1024));
    acc_row(s, qq, __builtin_bit_cast(u32x4, hb));
  }
  s = sum_g(s); qq = sum_g(qq);
  if (lane < 16) __builtin_amdgcn_raw_buffer_store_b64((u32x2){__float_as_uint(s), __float_as_uint(qq)}, c.ws, OFF_STH + (i2 * 16 + j2) * 1024 + (mt * 16 + lane) * 8, 0, SC_SC1);
  publish<8>(c, 2, FG_H, i2, j2);
  if (pre_on) pre();
  TS(7);
}

DI void phase5(Ctx& c, const Args& A, int i2, int j2) {
  const int lane = opaque(c.lane), g = lane >> 4, i16 = lane & 15, mt = 2 * (c.wid & 3) + (c.wid >> 2);
  u32x4 hr[2];
#pragma unroll
  for (int u = 0; u < 2; ++u) hr[u] = __builtin_amdgcn_raw_buffer_load_b128(c.ws, lane * 16, OFF_HF + (i2 * 16 + j2) * 16384 + (mt * 2 + u) * 1024, SC_SC1);
  const __amdgpu_buffer_rsrc_t rW = __builtin_amdgcn_make_buffer_rsrc((void*)A.wout, 0, 1792 * 2048, 0x00020000);
  if (c.wid == 0) wait_flags(c, FG_U, i2, 14);
  if (c.wid >= 4) wait_vm<24>();
  bar();
  f32x4 acc[4];
  gemm_split<28, R5, true>(c, acc, OFF_UF + i2 * 458752, rW, j2);
  TS(14);
  const lds_t ot = c.lds + c.wid * 2048;
#pragma unroll
  for (int n = 0; n < 4; ++n) { const unsigned r0 = hr[n >> 1][2 * (n & 1)], r1 = hr[n >> 1][2 * (n & 1) + 1];
    *(LAS u32x2*)(ot + i16 * 128 + (((2 * n + (g >> 1)) ^ (i16 & 7)) << 4) + (g & 1) * 8) =
        (u32x2){pk2(acc[n][0] + bflo(r0), acc[n][1] + bfhi(r0)), pk2(acc[n][2] + bflo(r1), acc[n][3] + bfhi(r1))}; }
  asm volatile("s_waitcnt lgkmcnt(0)" ::: "memory");
  const __amdgpu_buffer_rsrc_t rO = __builtin_amdgcn_make_buffer_rsrc((void*)A.out, 0, 2048 * 2048, 0x00020000);
#pragma unroll
  for (int u = 0; u < 2; ++u) { const int r = 8 * u + (lane >> 3), ch = lane & 7;
    const u32x4 v = *(LAS u32x4*)(ot + r * 128 + ((ch ^ (r & 7)) << 4));
    __builtin_amdgcn_raw_buffer_store_b128(v, rO, (i2 * 128 + true_row(mt, r)) * 2048 + j2 * 128 + ch * 16, 0, OUTAUX); }
  TS(15);
}

#define LAST_PHASE 5
__global__ __launch_bounds__(512, 2) void fused_fwd(const u16* __restrict__ x, const u16* __restrict__ wqkv, const u16* __restrict__ wphi, const u16* __restrict__ wo,
                                                      const u16* __restrict__ win, const u16* __restrict__ wsp, const u16* __restrict__ bsp, const u16* __restrict__ wout,
                                                      u16* __restrict__ out, char* __restrict__ ws) {
  extern __shared__ __attribute__((aligned(16))) char shm[];
  Ctx c;
  c.lds = (lds_t)shm; c.wsp = ws; c.lane = threadIdx.x & 63; c.wid = __builtin_amdgcn_readfirstlane(threadIdx.x >> 6); c.b = blockIdx.x;
  c.ws = __builtin_amdgcn_make_buffer_rsrc((void*)ws, 0, WS_BYTES, 0x00020000);
  c.tag = 0u; c.ts = 0u; c.pr0 = c.pr1 = c.pc0 = c.pc1 = 0;
  if (threadIdx.x < 16) *(LAS unsigned*)(c.lds + L_CNT + threadIdx.x * 4) = 0u;
  const int xcd = c.b & 7, jj = c.b >> 3;
  Map M;
  M.i1 = 8 * (xcd & 1) + (jj >> 2); M.j1 = 4 * (xcd >> 1) + (jj & 3);
  M.i2 = 4 * (xcd >> 1) + (jj >> 3); M.j2 = 8 * (xcd & 1) + (jj & 7);
  M.act3 = jj < 28;
  M.i3 = 4 * (xcd >> 1) + (M.act3 ? jj / 7 : 0); M.j3 = 7 * (xcd & 1) + (M.act3 ? jj % 7 : 0);
  const Args A{x, wqkv, wphi, wo, win, wsp, bsp, wout, out, ws};
  constexpr int last = LAST_PHASE;
  const bool ld = c.wid >= 4;
  {
    Acc1 e;
    if (ld) p1_load(c, A, M.i1, M.j1);
    else { __builtin_amdgcn_s_setprio(1); p1_mma(c, A, M.i1, M.j1, e); __builtin_amdgcn_s_setprio(0); }
    p1_epilogue(c, A, M, e, last);
  }
  if (last < 2) return;
  Ld3 L3;
  p3_setup(c, A, M.j3, L3);
  phase2(c, A, M.i2, M.j2, ld && M.act3, [&]() { p3_issueW(c, L3, 0); p3_issueW(c, L3, 1); });
  if (last < 3) return;
  if (M.act3) {
    u32x2 z1[2][4];
    Ld4 L;
    L.rA = __builtin_amdgcn_make_buffer_rsrc((void*)(A.wsp + (size_t)M.i3 * 128 * 2048), 0, 128 * 4096, 0x00020000);
    {
      Acc3 e;
      if (ld) p3_load(c, L3, M.i3);
      else { __builtin_amdgcn_s_setprio(1); p3_mma(c, M.i3, M.j3, e); __builtin_amdgcn_s_setprio(0); }
      const int ln = opaque(c.lane);
      L.voA = rowmajor_voff(c.wid & 3, ln, 4096); L.voF = ln * 16 + (c.wid & 3) * 1024;
      p3_epilogue(c, L, M.i3, M.j3, e, z1);
    }
    if (last < 4) return;
    f32x4 e4[2][4];
    if (ld) p4_load(c, L, M.j3);
    else { __builtin_amdgcn_s_setprio(1); p4_mma(c, M.j3, e4); __builtin_amdgcn_s_setprio(0); }
    p4_epilogue(c, A, M.i3, M.j3, M.j2, e4, z1);
  } else {
    bar();
    if (!ld) { const int wv = ((c.b & 7) * 4 + ((c.b >> 3) - 28)) * 4 + c.wid;
#pragma unroll 1
      for (int k = 0; k < 12; ++k) { const int Li = (k * 128 + wv) * 64 + c.lane;
        touch(c.lds, Li < 65536 ? (const char*)A.wsp + (size_t)Li * 128 : (const char*)A.wout + (size_t)((Li < 94208 ? Li : 94207) - 65536) * 128); } }
    if (ld) p5_issueW(c, A, M.j2);
  }
  if (last < 5) return;
  phase5(c, A, M.i2, M.j2);
  if (c.b == 0 && c.wid == 0) {
    const unsigned long long dr = c.pr1 - c.pr0, dc = c.pc1 - c.pc0;
    const unsigned long long w = PROBE == 1 ? dr * 10 : PROBE == 2 ? dc : PROBE == 4 ? 0 : 0;
    if (PROBE == 3) { const unsigned long long c0 = __builtin_amdgcn_s_memtime(); while (__builtin_amdgcn_s_memtime() - c0 < 200000ull) __builtin_amdgcn_s_sleep(1); }
    else { const unsigned long long t0 = __builtin_amdgcn_s_memrealtime(); while (__builtin_amdgcn_s_memrealtime() - t0 < w) __builtin_amdgcn_s_sleep(1); }
  }
}

extern "C" void kernel_launch(void* const* d_in, const int* in_sizes, int n_in, void* d_out, int out_size, void* d_ws, size_t ws_size, hipStream_t stream) {
  (void)in_sizes; (void)n_in; (void)out_size; (void)ws_size;
  (void)hipFuncSetAttribute((const void*)fused_fwd, hipFuncAttributeMaxDynamicSharedMemorySize, LDS_BYTES);
  fused_fwd<<<256, 512, LDS_BYTES, stream>>>((const u16*)d_in[0], (const u16*)d_in[1], (const u16*)d_in[2], (const u16*)d_in[3], (const u16*)d_in[5], (const u16*)d_in[6],
                                             (const u16*)d_in[7], (const u16*)d_in[8], (u16*)d_out, (char*)d_ws);
}
```

```cpp
#include <hip/hip_runtime.h>
#include <hip/hip_bf16.h>
#include <stdint.h>
#include <type_traits>

#define LAS __attribute__((address_space(3)))
#define DI __device__ __forceinline__
typedef unsigned short u16;
typedef short bf16x8 __attribute__((ext_vector_type(8)));
typedef short s16x4 __attribute__((ext_vector_type(4)));
typedef float f32x4 __attribute__((ext_vector_type(4)));
typedef float f32x2 __attribute__((ext_vector_type(2)));
typedef unsigned u32x4 __attribute__((ext_vector_type(4)));
typedef unsigned u32x2 __attribute__((ext_vector_type(2)));
typedef __bf16 bf2 __attribute__((ext_vector_type(2)));
typedef LAS char* lds_t;

constexpr unsigned OFF_EPOCH = 0, OFF_ERR = 128, OFF_FLAG = 4096;
constexpr unsigned OFF_KVP = 262144;
constexpr unsigned OFF_ZP = OFF_KVP + 2097152;
constexpr unsigned OFF_ST2 = 27u << 20 | 0;
constexpr unsigned OFF_STH = OFF_ZP + 131072 + 262144;
constexpr unsigned OFF_GX = OFF_STH + 262144;
constexpr unsigned OFF_GC1 = OFF_GX + 65536;
constexpr unsigned OFF_GC3 = OFF_GC1 + 393216;
constexpr unsigned OFF_OF = 4u << 20, OFF_HF = 8u << 20, OFF_Z2 = 12u << 20, OFF_UF = 20u << 20, OFF_TS = 28u << 20;
constexpr unsigned WS_BYTES = 32u << 20;
enum { FG_KV = 0, FG_O = 1, FG_H = 2, FG_ST = 3, FG_Z2 = 4, FG_U = 5 };
constexpr int LDS_BYTES = 163840;
constexpr int SC1 = 122880, L_WPHI = SC1, L_TAB1 = SC1 + 4096, L_XBUF = SC1 + 22528;
constexpr int L_XF1 = 65536, L_TB = 32768, L_KR = 65536, L_ZR = 131072;
constexpr int SC3 = 147456, L_TAB3 = SC3, L_FIN = SC3 + 4096;
constexpr int L_CNT = LDS_BYTES - 64, L_DUMMY = LDS_BYTES - 512;
constexpr int SC_SC1 = 16;
#define PSLEEP 4
#define OUTAUX 16
#define LDAUX 16

DI void bar() { asm volatile("" ::: "memory"); __builtin_amdgcn_s_barrier(); asm volatile("" ::: "memory"); }
template <int N> DI void wait_vm() { asm volatile("s_waitcnt vmcnt(%0)" ::"n"(N) : "memory"); }
DI int opaque(int v) { asm volatile("" : "+v"(v)); return v; }
DI void wait_lgkm0() { __builtin_amdgcn_s_waitcnt(0xc07f); }
DI unsigned pk2(float lo, float hi) { f32x2 v = {lo, hi}; return __builtin_bit_cast(unsigned, __builtin_convertvector(v, bf2)); }
DI bf16x8 pk8(const f32x4& a, const f32x4& b) { u32x4 r = {pk2(a[0], a[1]), pk2(a[2], a[3]), pk2(b[0], b[1]), pk2(b[2], b[3])}; return __builtin_bit_cast(bf16x8, r); }
DI float bflo(unsigned w) { return __uint_as_float(w << 16); }
DI float bfhi(unsigned w) { return __uint_as_float(w & 0xffff0000u); }
DI f32x4 mfma(bf16x8 a, bf16x8 b, f32x4 c) { return __builtin_amdgcn_mfma_f32_16x16x32_bf16(a, b, c, 0, 0, 0); }
DI bf16x8 ldsA(lds_t p) { return *(LAS bf16x8*)p; }
DI bf16x8 ldsT(lds_t p, int d) {
  s16x4 lo = __builtin_amdgcn_ds_read_tr16_b64_v4i16((LAS s16x4*)p), hi = __builtin_amdgcn_ds_read_tr16_b64_v4i16((LAS s16x4*)(p + d));
  return __builtin_shufflevector(lo, hi, 0, 1, 2, 3, 4, 5, 6, 7);
}
template <int D1, int D2> DI void tr4_issue(unsigned addr, u32x2 (&r)[4]) {
  asm volatile("ds_read_b64_tr_b16 %0, %4\n\tds_read_b64_tr_b16 %1, %4 offset:%5\n\tds_read_b64_tr_b16 %2, %4 offset:%6\n\tds_read_b64_tr_b16 %3, %4 offset:%7"
               : "=&v"(r[0]), "=&v"(r[1]), "=&v"(r[2]), "=&v"(r[3]) : "v"(addr), "n"(D1), "n"(D2), "n"(D1 + D2) : "memory");
}
DI void tr4_wait(u32x2 (&r)[4]) { asm volatile("s_waitcnt lgkmcnt(0)" : "+v"(r[0]), "+v"(r[1]), "+v"(r[2]), "+v"(r[3])::"memory"); }
DI float sum_g(float x) {
  auto a = __builtin_amdgcn_permlane16_swap(__float_as_uint(x), __float_as_uint(x), false, false);
  x = __uint_as_float(a[0]) + __uint_as_float(a[1]);
  auto b = __builtin_amdgcn_permlane32_swap(__float_as_uint(x), __float_as_uint(x), false, false);
  return __uint_as_float(b[0]) + __uint_as_float(b[1]);
}
DI float max_g(float x) {
  auto a = __builtin_amdgcn_permlane16_swap(__float_as_uint(x), __float_as_uint(x), false, false);
  x = fmaxf(__uint_as_float(a[0]), __uint_as_float(a[1]));
  auto b = __builtin_amdgcn_permlane32_swap(__float_as_uint(x), __float_as_uint(x), false, false);
  return fmaxf(__uint_as_float(b[0]), __uint_as_float(b[1]));
}
template <int CTRL> DI float dpp_add(float x) { return x + __int_as_float(__builtin_amdgcn_update_dpp(0, __float_as_int(x), CTRL, 0xf, 0xf, true)); }
DI float sum_row16(float x) { x = dpp_add<0x128>(x); x = dpp_add<0x124>(x); x = dpp_add<0x122>(x); return dpp_add<0x121>(x); }
DI void sum_row16x8(f32x4& a, f32x4& b) {
#define RR(n) "v_add_f32_dpp %0, %0, %0 row_ror:" n " row_mask:0xf bank_mask:0xf\n\tv_add_f32_dpp %1, %1, %1 row_ror:" n " row_mask:0xf bank_mask:0xf\n\t" \
  "v_add_f32_dpp %2, %2, %2 row_ror:" n " row_mask:0xf bank_mask:0xf\n\tv_add_f32_dpp %3, %3, %3 row_ror:" n " row_mask:0xf bank_mask:0xf\n\t" \
  "v_add_f32_dpp %4, %4, %4 row_ror:" n " row_mask:0xf bank_mask:0xf\n\tv_add_f32_dpp %5, %5, %5 row_ror:" n " row_mask:0xf bank_mask:0xf\n\t" \
  "v_add_f32_dpp %6, %6, %6 row_ror:" n " row_mask:0xf bank_mask:0xf\n\tv_add_f32_dpp %7, %7, %7 row_ror:" n " row_mask:0xf bank_mask:0xf\n\t"
  float a0 = a[0], a1 = a[1], a2 = a[2], a3 = a[3], b0 = b[0], b1 = b[1], b2 = b[2], b3 = b[3];
  asm volatile("s_nop 1\n\t" RR("8") RR("4") RR("2") RR("1") "s_nop 0" : "+v"(a0), "+v"(a1), "+v"(a2), "+v"(a3), "+v"(b0), "+v"(b1), "+v"(b2), "+v"(b3));
#undef RR
  a = (f32x4){a0, a1, a2, a3}; b = (f32x4){b0, b1, b2, b3};
}
DI float dot2(unsigned a, unsigned b, float c) { return __builtin_amdgcn_fdot2_f32_bf16(__builtin_bit_cast(bf2, a), __builtin_bit_cast(bf2, b), c, false); }
constexpr unsigned ONES2 = 0x3f803f80u;
DI f32x4 gelu4(f32x4 x) {
  const f32x4 t = x * (x * x * -0.1029432397f + -2.302208198f);
  f32x4 d, r;
#pragma unroll
  for (int i = 0; i < 4; ++i) d[i] = __builtin_amdgcn_exp2f(t[i]);
  d += 1.0f;
#pragma unroll
  for (int i = 0; i < 4; ++i) r[i] = __builtin_amdgcn_rcpf(d[i]);
  return x * r;
}
DI u32x2 pk4(const f32x4& a) { return (u32x2){pk2(a[0], a[1]), pk2(a[2], a[3])}; }
DI f32x4 up4(u32x2 w) { return (f32x4){bflo(w[0]), bfhi(w[0]), bflo(w[1]), bfhi(w[1])}; }
DI float gelu(float x) { const float t = x * fmaf(x * x, -0.1029432397f, -2.302208198f); return x * __builtin_amdgcn_rcpf(1.0f + __builtin_amdgcn_exp2f(t)); }

struct Ctx {
  __amdgpu_buffer_rsrc_t ws;
  char* wsp; lds_t lds; unsigned tag; int lane, wid, b; unsigned ts;
};
DI unsigned* flag_ptr(const Ctx& c, int g, int a, int bb) { return (unsigned*)(c.wsp + OFF_FLAG + ((g * 16 + a) * 16 + bb) * 128); }
template <unsigned NWAVES = 4, int YOUNGER = 0> DI void publish(const Ctx& c, int cnt, int g, int a, int bb) {
  wait_vm<YOUNGER>();
  if (c.lane == 0) {
    unsigned old = __hip_atomic_fetch_add((LAS unsigned*)(c.lds + L_CNT + cnt * 4), 1u, __ATOMIC_RELAXED, __HIP_MEMORY_SCOPE_WORKGROUP);
    if (old == NWAVES - 1u) __hip_atomic_store(flag_ptr(c, g, a, bb), c.tag, __ATOMIC_RELAXED, __HIP_MEMORY_SCOPE_AGENT);
  }
}
DI void wait_flags(const Ctx& c, int g, int a, int n) {
  const unsigned* p = flag_ptr(c, g, a, c.lane < n ? c.lane : 0);
  const unsigned long long t0 = __builtin_amdgcn_s_memrealtime();
  for (;;) {
    const unsigned v = __hip_atomic_load(p, __ATOMIC_RELAXED, __HIP_MEMORY_SCOPE_AGENT);
    if (__all(v == c.tag)) break;
    if (__builtin_amdgcn_s_memrealtime() - t0 > 300000ull) { if (c.lane == 0) __hip_atomic_store((unsigned*)(c.wsp + OFF_ERR), 0x100u + (unsigned)g, __ATOMIC_RELAXED, __HIP_MEMORY_SCOPE_AGENT); break; }
    __builtin_amdgcn_s_sleep(1);
  }
  asm volatile("" ::: "memory");
}
#define STEP(t) do {} while (0)
#define TS(k) do {} while (0)

DI void st16(const Ctx& c, bf16x8 v, int off) { __builtin_amdgcn_raw_buffer_store_b128(__builtin_bit_cast(u32x4, v), c.ws, off, 0, SC_SC1); }
template <int AUX> DI void dma(__amdgpu_buffer_rsrc_t r, lds_t dst, int voff, int soff) {
  if (AUX) asm volatile("s_mov_b32 m0, %0\n\ts_nop 0\n\tbuffer_load_dwordx4 %1, %2, %3 offen sc1 lds" ::"s"((unsigned)(size_t)dst), "v"(voff), "s"(r), "s"(soff) : "memory");
  else asm volatile("s_mov_b32 m0, %0\n\ts_nop 0\n\tbuffer_load_dwordx4 %1, %2, %3 offen lds" ::"s"((unsigned)(size_t)dst), "v"(voff), "s"(r), "s"(soff) : "memory");
}

#define EXP 0
template <int S, int NW, class IA, class IW, class MID, class POST>
DI void loader_loop(int nt, bool gated_prologue, IA issueA, IW issueW, MID mid, POST post) {
  constexpr int PPW = 4 + NW;
  if (gated_prologue) wait_vm<4 * (S - 2)>(); else wait_vm<PPW * (S - 2)>();
  bar();
  int t = 0;
#pragma unroll
  for (int p = 0; p < S - 2; ++p, ++t) {
    mid(t); issueA(t + S - 1); issueW(t + S - 1); post(t);
    if (gated_prologue) {
      if (p == 0) wait_vm<4 * (S - 3 > 0 ? S - 3 : 0) + PPW * 1>();
      else if (p == 1) wait_vm<4 * (S - 4 > 0 ? S - 4 : 0) + PPW * 2>();
      else wait_vm<4 * (S - 5 > 0 ? S - 5 : 0) + PPW * 3>();
    } else wait_vm<PPW * (S - 2)>();
    bar();
  }
#pragma unroll 1
  for (; t + S - 1 < nt; ++t) { mid(t); if (!(EXP & 1)) { issueA(t + S - 1); issueW(t + S - 1); } wait_vm<PPW * (S - 2)>(); bar(); }
#pragma unroll
  for (int r = S - 2; r >= 0; --r, ++t) {
    if (r >= 4) wait_vm<PPW * 3>(); else if (r == 3) wait_vm<PPW * 2>(); else if (r == 2) wait_vm<PPW * 1>(); else wait_vm<0>();
    bar();
  }
}
template <int NW, bool GATED, class IA, class IW, class POST, class GA, class FIN>
DI void loader_loop16(bool full, bool once, IA issueA, IW issueW, POST post, GA gather, FIN fin) {
  constexpr int PPW = 4 + NW;
  if (GATED) wait_vm<4>(); else wait_vm<PPW>();
  bar();
  issueA(2); issueW(2); post(0); wait_vm<PPW>(); bar();
#pragma unroll 1
  for (int t = 1; t < 5; ++t) { if (!(EXP & 1)) { issueA(t + 2); issueW(t + 2); } post(t); wait_vm<PPW>(); bar(); }
#pragma unroll
  for (int t = 5; t < 13; ++t) {
    issueA(t + 2); issueW(t + 2); gather(t - 5);
    if (full) { if (t == 5) wait_vm<PPW + 2>(); else wait_vm<PPW + 4>(); }
    else if (once && t <= 6) wait_vm<PPW + 2>();
    else wait_vm<PPW>();
    bar();
  }
  issueA(15); issueW(15); if (full) wait_vm<PPW + 2>(); else wait_vm<PPW>(); bar();
  wait_vm<0>(); fin(); bar();
  bar();
}
template <int S, int SB, class RD0, class RD1, class MM0, class MM1, class SCH, class HK, class ST>
DI void consumer_loop(Ctx& c, int sbase, int nt, RD0 rd0, RD1 rd1, MM0 mm0, MM1 mm1, SCH sched, HK hook, ST stamp) {
  bar();
  stamp();
  rd0(0);
  int sb = 0;
#pragma unroll 1
  for (int t = 0; t < nt; ++t) {
    hook(t); STEP(t);
    __builtin_amdgcn_sched_barrier(0);
    if (!(EXP & 4)) rd1(sb); if (!(EXP & 2)) mm0(); sched();
    __builtin_amdgcn_sched_barrier(0);
    wait_lgkm0(); bar();
    __builtin_amdgcn_sched_barrier(0);
    sb = (sb == (S - 1) * SB) ? 0 : sb + SB;
    if (!(EXP & 4)) rd0(sb); if (!(EXP & 2)) mm1(); sched();
    __builtin_amdgcn_sched_barrier(0);
  }
}
#define TOUCH(name, mod) DI void name(lds_t lds, const void* p) { \
  asm volatile("s_mov_b32 m0, %1\n\ts_nop 0\n\tglobal_load_lds_dword %0, off" mod ::"v"(p), "s"((unsigned)(size_t)(lds + L_DUMMY)) : "memory"); }
TOUCH(touch, "") TOUCH(touch_nt, " nt") TOUCH(touch_sc1, " sc1")
template <int NM, int NR> DI void interleave() {
#pragma unroll
  for (int i = 0; i < NR; ++i) { __builtin_amdgcn_sched_group_barrier(0x008, 1, 0); __builtin_amdgcn_sched_group_barrier(0x100, 1, 0); }
  __builtin_amdgcn_sched_group_barrier(0x008, NM - NR, 0);
}
template <int NM, int NR> DI void interleave2() {
#pragma unroll
  for (int i = 0; i < NR / 2; ++i) { __builtin_amdgcn_sched_group_barrier(0x008, 1, 0); __builtin_amdgcn_sched_group_barrier(0x100, 2, 0); }
  __builtin_amdgcn_sched_group_barrier(0x008, NM - NR / 2, 0);
}

DI int rowmajor_voff(int lw, int lane, int pitch) {
  const int rr = lane >> 3, slot = 8 * (lw & 1) + rr;
  const int row = 8 * (slot >> 2) + 4 * (lw >> 1) + (slot & 3);
  return row * pitch + (((lane & 7) ^ (slot >> 1)) << 4);
}
DI int true_row(int mt, int i) { return 32 * (mt >> 1) + 8 * (i >> 2) + 4 * (mt & 1) + (i & 3); }

DI void acc_row(float& s, float& q, u32x4 w) {
#pragma unroll
  for (int i = 0; i < 4; ++i) { s = dot2(w[i], ONES2, s); q = dot2(w[i], w[i], q); } }
DI float col_sum(const u32x2 (&r)[4]) { float s0 = 0.f, s1 = 0.f;
#pragma unroll
  for (int i = 0; i < 4; ++i) { s0 = dot2(r[i][0], ONES2, s0); s1 = dot2(r[i][1], ONES2, s1); }
  return sum_g(s0 + s1); }
DI void st_granule(const Ctx& c, float v, int off) { __builtin_amdgcn_raw_buffer_store_b64((u32x2){__float_as_uint(v), c.tag}, c.ws, off, 0, SC_SC1); }
DI bool timed_out(const Ctx& c, unsigned long long t0, unsigned code) {
  if (__builtin_amdgcn_s_memrealtime() - t0 <= 300000ull) return false;
  if (c.lane == 0) __hip_atomic_store((unsigned*)(c.wsp + OFF_ERR), code, __ATOMIC_RELAXED, __HIP_MEMORY_SCOPE_AGENT);
  return true;
}
DI float sum_granules16(const Ctx& c, u32x2 (&v)[16], int off, int stride) {
  const unsigned long long t0 = __builtin_amdgcn_s_memrealtime();
  for (;;) {
    bool ok = true;
#pragma unroll
    for (int k = 0; k < 16; ++k) ok &= v[k][1] == c.tag;
    if (__all(ok) || timed_out(c, t0, 0x200u)) break;
    __builtin_amdgcn_s_sleep(2);
#pragma unroll
    for (int k = 0; k < 16; ++k) v[k] = __builtin_amdgcn_raw_buffer_load_b64(c.ws, off + k * stride, 0, SC_SC1);
  }
  float s = 0.f;
#pragma unroll
  for (int k = 0; k < 16; ++k) s += __uint_as_float(v[k][0]);
  return s;
}

#define ROTM 0
#define ROT2(j) (ROTM * 2 * ((j) & 7))
#define ROT5(j) (ROTM * ((7 * ((j) & 7)) >> 1))
#define P4D 0
#define TOUCHW touch
#define PF_T0 2
#define PF_DT 3

struct Args { const u16 *x, *wqkv, *wphi, *wo, *win, *wsp, *bsp, *wout; u16* out; char* ws; };

DI void get_tag(Ctx& c) { unsigned e; asm volatile("s_load_dword %0, %1, 0x0\n\ts_waitcnt lgkmcnt(0)" : "=&s"(e) : "s"(c.wsp) : "memory"); c.tag = e + 1u; }
DI void kv_gather(const Ctx& c, int j1) {
  const int lane = c.lane, g = lane >> 4, i16 = lane & 15, tid = c.wid * 64 + lane;
  f32x4 a0 = {0, 0, 0, 0}, a1 = {0, 0, 0, 0};
  u32x4 v[16];
  const unsigned f0 = __hip_atomic_load(flag_ptr(c, FG_KV, j1, i16), __ATOMIC_RELAXED, __HIP_MEMORY_SCOPE_AGENT);
  const unsigned ready = __builtin_amdgcn_readfirstlane((unsigned)(__ballot(f0 == c.tag) & 0xffffull));
  asm volatile("" ::: "memory");
#pragma unroll
  for (int k = 0; k < 16; ++k) if ((ready >> k) & 1u) v[k] = __builtin_amdgcn_raw_buffer_load_b128(c.ws, tid * 16, OFF_KVP + (j1 * 16 + k) * 8192, SC_SC1);
  if (ready != 0xffffu) {
    wait_flags(c, FG_KV, j1, 16);
#pragma unroll
    for (int k = 0; k < 16; ++k) if (!((ready >> k) & 1u)) v[k] = __builtin_amdgcn_raw_buffer_load_b128(c.ws, tid * 16, OFF_KVP + (j1 * 16 + k) * 8192, SC_SC1);
  }
  float zs = 0.f;
  if (c.wid < 2) { unsigned zv[16];
#pragma unroll
    for (int k = 0; k < 16; ++k) zv[k] = __builtin_amdgcn_raw_buffer_load_b32(c.ws, lane * 4, OFF_ZP + ((j1 * 16 + k) * 2 + c.wid) * 256, SC_SC1);
#pragma unroll
    for (int k = 0; k < 16; ++k) zs += __uint_as_float(zv[k]); }
#pragma unroll
  for (int k = 0; k < 16; ++k) { a0[0] += bflo(v[k][0]); a0[1] += bfhi(v[k][0]); a0[2] += bflo(v[k][1]); a0[3] += bfhi(v[k][1]); a1[0] += bflo(v[k][2]); a1[1] += bfhi(v[k][2]); a1[2] += bflo(v[k][3]); a1[3] += bfhi(v[k][3]); }
  const int w = c.wid, hd = w >> 2, dt = (w >> 1) & 1, fp = w & 1;
  const lds_t tb = c.lds + L_XBUF + hd * 8192 + (32 * fp + i16) * 128 + (16 * dt + 4 * g) * 2;
  *(LAS u32x2*)tb = (u32x2){pk2(a0[0], a0[1]), pk2(a0[2], a0[3])};
  *(LAS u32x2*)(tb + 16 * 128) = (u32x2){pk2(a1[0], a1[1]), pk2(a1[2], a1[3])};
  if (c.wid < 2) { const unsigned z2 = pk2(zs, zs); const lds_t t3 = c.lds + L_XBUF + c.wid * 8192 + lane * 128 + 64;
    *(LAS u32x4*)t3 = (u32x4){z2, z2, z2, z2}; *(LAS u32x4*)(t3 + 16) = (u32x4){z2, z2, z2, z2}; }
  wait_lgkm0();
}
struct Map { int i1, j1, i2, j2, i3, j3; bool act3; };
DI void p1_load(Ctx& c, const Args& A, int i1, int j1) {
  const int lane = c.lane, lw = c.wid - 4, g = lane >> 4, q = (lane & 15) >> 2, p = lane & 3;
  const lds_t lds = c.lds;
  const __amdgpu_buffer_rsrc_t rWS = c.ws;
  constexpr int S = 3, SB = 40960, NW = 6;
  const __amdgpu_buffer_rsrc_t rA = __builtin_amdgcn_make_buffer_rsrc((void*)(A.x + (size_t)i1 * 128 * 1024), 0, 128 * 2048, 0x00020000);
  const __amdgpu_buffer_rsrc_t rW = __builtin_amdgcn_make_buffer_rsrc((void*)A.wqkv, 0, 1024 * 6144, 0x00020000);
  const __amdgpu_buffer_rsrc_t rP = __builtin_amdgcn_make_buffer_rsrc((void*)A.wphi, 0, 65536, 0x00020000);
  const int voA = rowmajor_voff(lw, lane, 2048);
  int voW[NW];
#pragma unroll
  for (int i = 0; i < NW; ++i) { const int o = (NW * lw + i) * 1024 + lane * 16, r = o / 384, rem = o % 384, seg = rem >> 7, s16 = (rem & 127) >> 4;
    const int c32 = (s16 >> 1) ^ ((r >> 1) & 3), r5 = r & 31, k = (r & 32) + 8 * ((r5 >> 2) & 3) + 4 * (r5 >> 4) + (r5 & 3);
    voW[i] = k * 6144 + seg * 2048 + j1 * 128 + c32 * 32 + (s16 & 1) * 16; }
#define ROTMODE 1
  const int rot = ROTMODE == 2 ? (2 * (i1 & 7) + (i1 >> 3) + 4 * (j1 & 3)) & 15 : ROTMODE == 1 ? 4 * (i1 & 3) : ROTMODE == 3 ? 8 * (i1 & 1) : 0;
  auto issueA = [&](int t) { const lds_t d = lds + (t % S) * SB + lw * 1024; const int so = ((t + rot) & 15) * 128;
#pragma unroll
    for (int i = 0; i < 4; ++i) dma<0>(rA, d + i * 4096, voA, so + i * 32 * 2048); };
  auto issueW = [&](int t) { const lds_t d = lds + (t % S) * SB + 16384 + lw * NW * 1024; const int so = ((t + rot) & 15) * 64 * 6144;
#pragma unroll
    for (int i = 0; i < NW; ++i) dma<0>(rW, d + i * 1024, voW[i], so); };
  issueA(0); issueW(0); issueA(1); issueW(1);
  dma<0>(rP, lds + L_WPHI + lw * 1024, lane * 16 + lw * 1024, j1 * 4096);
  get_tag(c);
  TS(0);
  u32x2 gv[16]; u32x4 gx[2];
  const int goff = OFF_GC1 + j1 * 16 * 1536 + (lw * 64 + lane) * 8, xoff = OFF_GX + (i1 * 128 + lane) * 16;
  const int tau = ROTMODE == 2 ? 0 : ROTMODE == 1 ? i1 >> 2 : ROTMODE == 3 ? 0 : 0;
  auto post = [&](int t) {
    if (t != tau) return;
    const int sw = (2 * g + (q >> 1)) & 3; u32x2 cr[3][4];
#pragma unroll
    for (int n = 0; n < 3; ++n) { const int nu = 3 * lw + n;
      tr4_issue<16 * 384, 32 * 384>((unsigned)(size_t)(lds + (t % S) * SB + 16384 + (4 * g + q) * 384 + 8 * p + (nu >> 2) * 128 + 32 * ((nu & 3) ^ sw)), cr[n]); }
#pragma unroll
    for (int n = 0; n < 3; ++n) { tr4_wait(cr[n]); const float s = col_sum(cr[n]);
      if (lane < 16) st_granule(c, s, OFF_GC1 + (j1 * 16 + rot + tau) * 1536 + ((3 * lw + n) * 16 + lane) * 8); }
  };
  auto gather = [&](int k) {
    if (lw < 3) { gv[2 * k] = __builtin_amdgcn_raw_buffer_load_b64(rWS, goff + 2 * k * 1536, 0, SC_SC1); gv[2 * k + 1] = __builtin_amdgcn_raw_buffer_load_b64(rWS, goff + (2 * k + 1) * 1536, 0, SC_SC1); }
    else if (k == 0) { gx[0] = __builtin_amdgcn_raw_buffer_load_b128(rWS, xoff, 0, SC_SC1); gx[1] = __builtin_amdgcn_raw_buffer_load_b128(rWS, xoff + 1024, 0, SC_SC1); }
  };
  auto fin = [&]() {
    if (lw < 3) *(LAS float*)(lds + L_TAB1 + 1024 + (lw * 64 + lane) * 4) = sum_granules16(c, gv, goff, 1536);
    else {
      const unsigned long long t0 = __builtin_amdgcn_s_memrealtime();
      for (;;) {
        if (__all(gx[0][1] == c.tag && gx[0][3] == c.tag && gx[1][1] == c.tag && gx[1][3] == c.tag) || timed_out(c, t0, 0x201u)) break;
        __builtin_amdgcn_s_sleep(2);
        gx[0] = __builtin_amdgcn_raw_buffer_load_b128(rWS, xoff, 0, SC_SC1); gx[1] = __builtin_amdgcn_raw_buffer_load_b128(rWS, xoff + 1024, 0, SC_SC1);
      }
#pragma unroll
      for (int e = 0; e < 2; ++e) { *(LAS unsigned*)(lds + L_TAB1 + (lane + 64 * e) * 4) = gx[e][0]; *(LAS unsigned*)(lds + L_TAB1 + 512 + (lane + 64 * e) * 4) = gx[e][2]; }
    }
    wait_lgkm0();
    __builtin_amdgcn_s_waitcnt(0x0f70);
  {
    const int gw = c.b * 4 + lw;
    if (lane < 56) touch(lds, (const char*)A.win + (size_t)(gw * 56 + lane) * 128);
    if (lane < 16) touch(lds, (const char*)A.wo + (size_t)(gw * 16 + lane) * 128);
  }
  };
  loader_loop16<NW, false>(lw < 3, lw == 3, issueA, issueW, post, gather, fin);
  TS(1);
}

struct Acc1 { f32x4 q[2][2], k[2][2], v[2][2]; };
DI void p1_mma(Ctx& c, const Args& A, int i1, int j1, Acc1& e) {
  const int lane = c.lane, wr = c.wid >> 1, wc = c.wid & 1, g = lane >> 4, q = (lane & 15) >> 2, p = lane & 3, i16 = lane & 15;
  const lds_t lds = c.lds;
  const f32x4 Z4 = {0.f, 0.f, 0.f, 0.f};
  constexpr int S = 3, SB = 40960;
  f32x4 aq[4][2], ak[4][2], av[4][2];
#pragma unroll
  for (int m = 0; m < 4; ++m)
#pragma unroll
    for (int n = 0; n < 2; ++n) { aq[m][n] = Z4; ak[m][n] = Z4; av[m][n] = Z4; asm volatile("" : "+v"(aq[m][n]), "+v"(ak[m][n]), "+v"(av[m][n])); }
  const int sw = (2 * g + (q >> 1)) & 3;
  const int ao0 = (wr * 64 + i16) * 128 + ((g ^ (i16 >> 1)) << 4), ao1 = ao0 ^ 64;
  const int wl = 16384 + (4 * g + q) * 384 + 8 * p;
  const int wo0 = wl + 32 * ((2 * wc) ^ sw), wo1 = wl + 32 * ((2 * wc + 1) ^ sw);
  bf16x8 a0[4], w0[6], a1[4], w1[6];
  auto rd = [&](bf16x8(&a)[4], bf16x8(&w)[6], int sb, int ks) { const lds_t s = lds + sb;
#pragma unroll
    for (int sg = 0; sg < 3; ++sg) { w[2 * sg] = ldsT(s + wo0 + sg * 128 + ks * 32 * 384, 16 * 384); w[2 * sg + 1] = ldsT(s + wo1 + sg * 128 + ks * 32 * 384, 16 * 384); }
#pragma unroll
    for (int m = 0; m < 4; ++m) a[m] = ldsA(s + (ks ? ao1 : ao0) + m * 2048); };
  auto mm = [&](bf16x8(&a)[4], bf16x8(&w)[6]) {
#pragma unroll
    for (int n = 0; n < 2; ++n)
#pragma unroll
      for (int m = 0; m < 4; ++m) aq[m][n] = mfma(w[n], a[m], aq[m][n]);
#pragma unroll
    for (int n = 0; n < 2; ++n)
#pragma unroll
      for (int m = 0; m < 4; ++m) ak[m][n] = mfma(w[2 + n], a[m], ak[m][n]);
#pragma unroll
    for (int n = 0; n < 2; ++n)
#pragma unroll
      for (int m = 0; m < 4; ++m) av[m][n] = mfma(a[m], w[4 + n], av[m][n]); };
  TS(0);
  {
    u32x4 xv[2][2];
#pragma unroll
    for (int e2 = 0; e2 < 2; ++e2) { const int R = 8 * j1 + 2 * c.wid + e2; const u16* xr = A.x + (size_t)(i1 * 128 + true_row(R >> 4, R & 15)) * 1024 + lane * 16;
      xv[e2][0] = *(const u32x4*)xr; xv[e2][1] = *(const u32x4*)(xr + 8); }
    get_tag(c);
#pragma unroll
    for (int e2 = 0; e2 < 2; ++e2) { float s = 0.f, qq = 0.f; acc_row(s, qq, xv[e2][0]); acc_row(s, qq, xv[e2][1]);
      s = sum_g(sum_row16(s)) * (1.0f / 1024.0f); qq = sum_g(sum_row16(qq)) * (1.0f / 1024.0f);
      const float rs = __builtin_amdgcn_rsqf(fmaxf(qq - s * s, 0.f) + 1e-5f);
      if (lane == 0) __builtin_amdgcn_raw_buffer_store_b128((u32x4){__float_as_uint(s), c.tag, __float_as_uint(rs), c.tag}, c.ws, OFF_GX + (i1 * 128 + 8 * j1 + 2 * c.wid + e2) * 16, 0, SC_SC1); }
  }
  consumer_loop<S, SB>(c, 0, 16, [&](int sb) { rd(a0, w0, sb, 0); }, [&](int sb) { rd(a1, w1, sb, 1); }, [&]() { mm(a0, w0); }, [&]() { mm(a1, w1); }, []() { interleave<24, 16>(); }, [](int) {}, [&]() { TS(16); });
  TS(1);
  const lds_t xf = lds + L_XF1 + c.wid * 12288 + lane * 16;
#pragma unroll
  for (int m = 0; m < 2; ++m)
#pragma unroll
    for (int n = 0; n < 2; ++n) {
      *(LAS f32x4*)(xf + (m * 6 + n) * 1024) = aq[2 + m][n]; *(LAS f32x4*)(xf + (m * 6 + 2 + n) * 1024) = ak[2 + m][n]; *(LAS f32x4*)(xf + (m * 6 + 4 + n) * 1024) = av[2 + m][n];
      e.q[m][n] = aq[m][n]; e.k[m][n] = ak[m][n]; e.v[m][n] = av[m][n];
    }
  wait_lgkm0();
}

DI void both_g_max(float& a, float& b) {
  auto r = __builtin_amdgcn_permlane16_swap(__float_as_uint(a), __float_as_uint(b), false, false);
  const float x = fmaxf(__uint_as_float(r[0]), __uint_as_float(r[1]));
  auto s = __builtin_amdgcn_permlane32_swap(__float_as_uint(x), __float_as_uint(x), false, false);
  const float y = fmaxf(__uint_as_float(s[0]), __uint_as_float(s[1]));
  auto t = __builtin_amdgcn_permlane16_swap(__float_as_uint(y), __float_as_uint(y), false, false);
  a = __uint_as_float(t[0]); b = __uint_as_float(t[1]);
}
DI void both_g_sum(float& a, float& b) {
  auto r = __builtin_amdgcn_permlane16_swap(__float_as_uint(a), __float_as_uint(b), false, false);
  const float x = __uint_as_float(r[0]) + __uint_as_float(r[1]);
  auto s = __builtin_amdgcn_permlane32_swap(__float_as_uint(x), __float_as_uint(x), false, false);
  const float y = __uint_as_float(s[0]) + __uint_as_float(s[1]);
  auto t = __builtin_amdgcn_permlane16_swap(__float_as_uint(y), __float_as_uint(y), false, false);
  a = __uint_as_float(t[0]); b = __uint_as_float(t[1]);
}
DI void feat(const f32x4& p0, const f32x4& p1, f32x4& e0, f32x4& e1, f32x4& n0, f32x4& n1) {
  constexpr float L2E = 1.4426950408889634f;
  const f32x4 t0 = p0 * L2E, t1 = p1 * L2E;
  float mx = fmaxf(fmaxf(fmaxf(t0[0], t0[1]), t0[2]), fmaxf(fmaxf(fmaxf(t0[3], t1[0]), t1[1]), fmaxf(t1[2], t1[3])));
  float mn = -fminf(fminf(fminf(t0[0], t0[1]), t0[2]), fminf(fminf(fminf(t0[3], t1[0]), t1[1]), fminf(t1[2], t1[3])));
  both_g_max(mx, mn);
  const f32x4 a0 = t0 - mx, a1 = t1 - mx, b0 = -t0 - mn, b1 = -t1 - mn;
#pragma unroll
  for (int r = 0; r < 4; ++r) { e0[r] = __builtin_amdgcn_exp2f(a0[r]); e1[r] = __builtin_amdgcn_exp2f(a1[r]); n0[r] = __builtin_amdgcn_exp2f(b0[r]); n1[r] = __builtin_amdgcn_exp2f(b1[r]); }
  const f32x4 se = e0 + e1, sn4 = n0 + n1;
  float sp = (se[0] + se[1]) + (se[2] + se[3]), sn = (sn4[0] + sn4[1]) + (sn4[2] + sn4[3]);
  both_g_sum(sp, sn);
  sp = __builtin_amdgcn_rcpf(sp); sn = __builtin_amdgcn_rcpf(sn);
  e0 *= sp; e1 *= sp; n0 *= sn; n1 *= sn;
}

DI void p1_epilogue(Ctx& c, const Args& A, const Map& M, Acc1& e, int last) {
  const int lane = opaque(c.lane), cw = c.wid & 3, hf = c.wid >> 2, wr = cw >> 1, wc = cw & 1, g = lane >> 4, q = (lane & 15) >> 2, p = lane & 3, i16 = lane & 15;
  const int lw = cw, i1 = M.i1, j1 = M.j1;
  const lds_t lds = c.lds;
  const f32x4 Z4 = {0.f, 0.f, 0.f, 0.f};
  bar();
  TS(20);
  if (hf) { const lds_t xf = lds + L_XF1 + cw * 12288 + lane * 16;
#pragma unroll
    for (int m = 0; m < 2; ++m)
#pragma unroll
      for (int n = 0; n < 2; ++n) { e.q[m][n] = *(LAS f32x4*)(xf + (m * 6 + n) * 1024); e.k[m][n] = *(LAS f32x4*)(xf + (m * 6 + 2 + n) * 1024); e.v[m][n] = *(LAS f32x4*)(xf + (m * 6 + 4 + n) * 1024); } }
  const lds_t tab = lds + L_TAB1;
  bf16x8 qf[2], kf[2], vf[2], wT[2];
  {
    f32x4 vv[2][2];
#pragma unroll
    for (int m = 0; m < 2; ++m) {
      const int R = 64 * wr + 32 * hf + 16 * m;
      const float mu = *(LAS float*)(tab + (R + i16) * 4), rs = *(LAS float*)(tab + 512 + (R + i16) * 4), nm = -mu * rs;
      const f32x4 mu4 = *(LAS f32x4*)(tab + (R + 4 * g) * 4), rs4 = *(LAS f32x4*)(tab + 512 + (R + 4 * g) * 4), nm4 = -mu4 * rs4;
      f32x4 qq[2], kk[2];
#pragma unroll
      for (int n = 0; n < 2; ++n) {
        const f32x4 cq = *(LAS f32x4*)(tab + 1024 + (32 * wc + 16 * n + 4 * g) * 4), ck = *(LAS f32x4*)(tab + 1024 + (64 + 32 * wc + 16 * n + 4 * g) * 4);
        const float cv = *(LAS float*)(tab + 1024 + (128 + 32 * wc + 16 * n + i16) * 4);
        qq[n] = e.q[m][n] * rs + cq * nm; kk[n] = e.k[m][n] * rs + ck * nm; vv[m][n] = e.v[m][n] * rs4 + nm4 * cv;
      }
      qf[m] = pk8(qq[0], qq[1]); kf[m] = pk8(kk[0], kk[1]);
    }
    vf[0] = pk8(vv[0][0], vv[1][0]); vf[1] = pk8(vv[0][1], vv[1][1]);
#pragma unroll
    for (int ft = 0; ft < 2; ++ft) wT[ft] = ldsT(lds + L_WPHI + wc * 2048 + (4 * g + q) * 64 + ft * 32 + 8 * p, 16 * 64);
  }
  wait_lgkm0(); bar();
  TS(21);
  {
    const lds_t tb = lds + L_TB + c.wid * 4096;
#pragma unroll
    for (int m = 0; m < 2; ++m) {
      const f32x4 p0 = mfma(wT[0], kf[m], Z4), p1 = mfma(wT[1], kf[m], Z4);
      f32x4 e0, e1, n0, n1; feat(p0, p1, e0, e1, n0, n1);
      const lds_t rw = tb + (16 * m + i16) * 128;
      *(LAS u32x2*)(rw + 8 * ((0 + g) ^ i16)) = pk4(e0); *(LAS u32x2*)(rw + 8 * ((4 + g) ^ i16)) = pk4(e1);
      *(LAS u32x2*)(rw + 8 * ((8 + g) ^ i16)) = pk4(n0); *(LAS u32x2*)(rw + 8 * ((12 + g) ^ i16)) = pk4(n1);
    }
    asm volatile("s_waitcnt lgkmcnt(0)" ::: "memory");
    const bf16x8 ones = __builtin_bit_cast(bf16x8, (u32x4){ONES2, ONES2, ONES2, ONES2});
    const lds_t kr = lds + L_KR + c.wid * 8192 + lane * 16;
    float zf[4];
#pragma unroll
    for (int f = 0; f < 4; ++f) {
      const int r0 = 4 * g + q;
      const s16x4 lo = __builtin_amdgcn_ds_read_tr16_b64_v4i16((LAS s16x4*)(tb + r0 * 128 + 8 * ((4 * f + p) ^ r0)));
      const s16x4 hi = __builtin_amdgcn_ds_read_tr16_b64_v4i16((LAS s16x4*)(tb + (16 + r0) * 128 + 8 * ((4 * f + p) ^ r0)));
      const bf16x8 pf = __builtin_shufflevector(lo, hi, 0, 1, 2, 3, 4, 5, 6, 7);
      *(LAS f32x4*)(kr + f * 1024) = mfma(vf[0], pf, Z4); *(LAS f32x4*)(kr + (4 + f) * 1024) = mfma(vf[1], pf, Z4);
      zf[f] = mfma(ones, pf, Z4)[0];
    }
    *(LAS float*)(lds + L_ZR + c.wid * 256 + lane * 4) = g == 0 ? zf[0] : g == 1 ? zf[1] : g == 2 ? zf[2] : zf[3];
    TS(22);
    wait_lgkm0(); bar();
    f32x4 k0 = Z4, k1 = Z4;
#pragma unroll
    for (int w = 0; w < 4; ++w) { const lds_t s = lds + L_KR + (wc + 2 * w) * 8192 + (4 * wr + 2 * hf) * 1024 + lane * 16; k0 += *(LAS f32x4*)s; k1 += *(LAS f32x4*)(s + 1024); }
    st16(c, pk8(k0, k1), lane * 16 + (OFF_KVP + (j1 * 16 + i1) * 8192 + wc * 4096 + wr * 2048 + hf * 1024));
    if (c.wid < 2) { float zt = 0.f;
#pragma unroll
      for (int w = 0; w < 4; ++w) zt += *(LAS float*)(lds + L_ZR + (wc + 2 * w) * 256 + lane * 4);
      __builtin_amdgcn_raw_buffer_store_b32(__float_as_uint(zt), c.ws, lane * 4, OFF_ZP + ((j1 * 16 + i1) * 2 + wc) * 256, SC_SC1); }
    wait_lgkm0(); bar();
    TS(23);
    publish<8>(c, 0, FG_KV, j1, i1);
  }
  TS(2);
  const __amdgpu_buffer_rsrc_t rW2 = __builtin_amdgcn_make_buffer_rsrc((void*)A.wo, 0, 1024 * 2048, 0x00020000);
  int voW2[2];
#pragma unroll
  for (int i = 0; i < 2; ++i) { const int r = (2 * lw + i) * 8 + (lane >> 3), s16 = lane & 7; voW2[i] = r * 2048 + M.j2 * 128 + (((s16 >> 1) ^ ((r >> 1) & 3)) << 5) + (s16 & 1) * 16; }
  auto issueW2 = [&](int t) {
#pragma unroll
    for (int i = 0; i < 2; ++i) dma<0>(rW2, lds + t * 8192 + (lw * 2 + i) * 1024, voW2[i], t * 64 * 2048); };
  if (hf) {
    if (M.act3) {
      const __amdgpu_buffer_rsrc_t rW3 = __builtin_amdgcn_make_buffer_rsrc((void*)A.win, 0, 1024 * 7168, 0x00020000);
#pragma unroll
      for (int i = 0; i < 8; ++i) { const int r = (8 * lw + i) * 2 + (lane >> 5), l5 = lane & 31, seg = l5 >> 4, s16 = l5 & 15;
        dma<0>(rW3, lds + 32768 + (8 * lw + i) * 1024, r * 7168 + (seg == 0 ? 3584 : 0) + M.j3 * 256 + (((s16 >> 1) ^ (r & 7)) << 5) + (s16 & 1) * 16, M.i3 * 64 * 7168); }
    }
#pragma unroll
    for (int t = 0; t < 3; ++t) { issueW2(t); issueW2(8 + t); }
  }
  bf16x8 ph[2][2];
#pragma unroll
  for (int m = 0; m < 2; ++m) {
    const f32x4 p0 = mfma(wT[0], qf[m], Z4), p1 = mfma(wT[1], qf[m], Z4);
    f32x4 e0, e1, n0, n1; feat(p0, p1, e0, e1, n0, n1);
    ph[m][0] = pk8(e0, e1); ph[m][1] = pk8(n0, n1);
  }
  asm volatile("" : "+v"(ph[0][0]), "+v"(ph[0][1]), "+v"(ph[1][0]), "+v"(ph[1][1]));
  TS(3);
  kv_gather(c, j1);
  bar();
  TS(4);
  {
    bf16x8 kt[3][2];
#pragma unroll
    for (int d = 0; d < 3; ++d)
#pragma unroll
      for (int s = 0; s < 2; ++s) kt[d][s] = ldsT(lds + L_XBUF + wc * 8192 + (32 * s + 4 * g + q) * 128 + 32 * d + 8 * p, 16 * 128);
#pragma unroll
    for (int m = 0; m < 2; ++m) {
      const f32x4 n0 = mfma(kt[0][1], ph[m][1], mfma(kt[0][0], ph[m][0], Z4)), n1 = mfma(kt[1][1], ph[m][1], mfma(kt[1][0], ph[m][0], Z4));
      const f32x4 dn = mfma(kt[2][1], ph[m][1], mfma(kt[2][0], ph[m][0], Z4));
      const float r = __builtin_amdgcn_rcpf(dn[0] + 1e-6f);
      st16(c, pk8(n0 * r, n1 * r), lane * 16 + (OFF_OF + (i1 * 16 + j1) * 16384 + ((4 * wr + 2 * hf + m) * 2 + wc) * 1024));
    }
  }
  publish<8>(c, 1, FG_O, i1, j1);
  TS(5);
  if (hf && M.act3) {
    u32x2 cr[4][4];
#pragma unroll
    for (int n = 0; n < 4; ++n) tr4_issue<16 * 512, 32 * 512>((unsigned)(size_t)(lds + 32768 + (4 * g + q) * 512 + 8 * p + (lw >> 1) * 256 + 32 * ((4 * (lw & 1) + n) ^ (4 * (g & 1) + q))), cr[n]);
#pragma unroll
    for (int n = 0; n < 4; ++n) { tr4_wait(cr[n]); const float s = col_sum(cr[n]);
      if (lane < 16) st_granule(c, s, OFF_GC3 + (M.j3 * 16 + M.i3) * 2048 + ((4 * lw + n) * 16 + lane) * 8); }
  }
  if (hf) {
    issueW2(3);
#pragma unroll
    for (int t = 11; t < 16; ++t) issueW2(t);
  } else if (c.wid == 0 && last >= 2) wait_flags(c, FG_O, M.i2, 16);
}

struct Ld3 { __amdgpu_buffer_rsrc_t rW; int voW[8]; };
DI void p3_setup(const Ctx& c, const Args& A, int j3, Ld3& L) {
  const int lane = opaque(c.lane), lw = c.wid & 3;
  L.rW = __builtin_amdgcn_make_buffer_rsrc((void*)A.win, 0, 1024 * 7168, 0x00020000);
#pragma unroll
  for (int i = 0; i < 8; ++i) { const int r = (8 * lw + i) * 2 + (lane >> 5), l5 = lane & 31, seg = l5 >> 4, s16 = l5 & 15;
    L.voW[i] = r * 7168 + (seg == 0 ? 3584 : 0) + j3 * 256 + (((s16 >> 1) ^ (r & 7)) << 5) + (s16 & 1) * 16; }
}
DI void p3_issueW(const Ctx& c, const Ld3& L, int t) { const lds_t d = c.lds + (t % 3) * 49152 + 16384 + (c.wid & 3) * 8192;
#pragma unroll
  for (int i = 0; i < 8; ++i) dma<0>(L.rW, d + i * 1024, L.voW[i], t * 64 * 7168); }
DI void p3_load(Ctx& c, const Ld3& L, int i3) {
  const int lane = opaque(c.lane), lw = c.wid - 4;
  const lds_t lds = c.lds;
  constexpr int S = 3, SB = 49152;
  const int voF = lane * 16 + lw * 1024;
  auto issueA = [&](int t) { const lds_t d = lds + (t % S) * SB + lw * 1024; const int so = OFF_HF + (i3 * 16 + t) * 16384;
#pragma unroll
    for (int i = 0; i < 4; ++i) dma<LDAUX>(c.ws, d + i * 4096, voF, so + i * 4096); };
  auto issueW = [&](int t) { p3_issueW(c, L, t); };
  bar();
  issueA(0); issueA(1);
  u32x2 hv[16]; const int R = (lw & 1) * 64 + lane;
  auto gather = [&](int k) { if (lw < 2) {
      hv[2 * k] = __builtin_amdgcn_raw_buffer_load_b64(c.ws, R * 8, OFF_STH + (i3 * 16 + 2 * k) * 1024, SC_SC1);
      hv[2 * k + 1] = __builtin_amdgcn_raw_buffer_load_b64(c.ws, R * 8, OFF_STH + (i3 * 16 + 2 * k + 1) * 1024, SC_SC1); } };
  auto fin = [&]() { if (lw < 2) { float s = 0.f, qq = 0.f;
#pragma unroll
      for (int k = 0; k < 16; ++k) { s += __uint_as_float(hv[k][0]); qq += __uint_as_float(hv[k][1]); }
      s *= (1.0f / 1024.0f); qq *= (1.0f / 1024.0f);
      *(LAS float*)(lds + L_TAB3 + R * 4) = s; *(LAS float*)(lds + L_TAB3 + 512 + R * 4) = __builtin_amdgcn_rsqf(fmaxf(qq - s * s, 0.f) + 1e-5f);
      wait_lgkm0(); } };
  loader_loop16<8, true>(lw < 2, false, issueA, issueW, [](int) {}, gather, fin);
  TS(6);
}
struct Acc3 { f32x4 z2[2][4], z1[2][4]; };
DI int xf3_base(int cw) { return cw < 3 ? 16384 + cw * 32768 : 98304; }
DI void p3_mma(Ctx& c, int i3, int j3, Acc3& e) {
  const int lane = opaque(c.lane), wr = c.wid >> 1, wc = c.wid & 1, g = lane >> 4, q = (lane & 15) >> 2, p = lane & 3;
  const lds_t lds = c.lds;
  const __amdgpu_buffer_rsrc_t rWS = c.ws;
  const f32x4 Z4 = {0.f, 0.f, 0.f, 0.f};
  constexpr int S = 3, SB = 49152;
  if (wc == 1) {
    const int off = OFF_GC3 + j3 * 16 * 2048 + (128 * wr + 2 * lane) * 8; u32x4 gv[16];
    const unsigned long long t0 = __builtin_amdgcn_s_memrealtime();
    for (;;) {
      bool ok = true;
#pragma unroll
      for (int k = 0; k < 16; ++k) gv[k] = __builtin_amdgcn_raw_buffer_load_b128(rWS, off + k * 2048, 0, SC_SC1);
#pragma unroll
      for (int k = 0; k < 16; ++k) ok &= gv[k][1] == c.tag && gv[k][3] == c.tag;
      if (__all(ok) || timed_out(c, t0, 0x202u)) break;
      __builtin_amdgcn_s_sleep(2);
    }
    f32x2 cs = {0.f, 0.f};
#pragma unroll
    for (int k = 0; k < 16; ++k) { cs[0] += __uint_as_float(gv[k][0]); cs[1] += __uint_as_float(gv[k][2]); }
    *(LAS f32x2*)(lds + L_TAB3 + 1024 + (128 * wr + 2 * lane) * 4) = cs;
    wait_lgkm0();
  } else if (c.wid == 0) wait_flags(c, FG_H, i3, 16);
  f32x4 a2[4][4], a1_[4][4];
#pragma unroll
  for (int m = 0; m < 4; ++m)
#pragma unroll
    for (int n = 0; n < 4; ++n) { a2[m][n] = Z4; a1_[m][n] = Z4; }
#pragma unroll
  for (int m = 0; m < 4; ++m) asm volatile("" : "+v"(a2[m][0]), "+v"(a2[m][1]), "+v"(a2[m][2]), "+v"(a2[m][3]), "+v"(a1_[m][0]), "+v"(a1_[m][1]), "+v"(a1_[m][2]), "+v"(a1_[m][3]));
  bar();
  const int ao2 = wr * 8192 + lane * 16;
  int wo_[4];
#pragma unroll
  for (int n = 0; n < 4; ++n) wo_[n] = 16384 + (4 * g + q) * 512 + 8 * p + 32 * ((4 * wc + n) ^ (4 * (g & 1) + q));
  bf16x8 a0[4], w0[8], a1[4], w1[8];
  auto rd = [&](bf16x8(&a)[4], bf16x8(&w)[8], int sb, int ks) { const lds_t s = lds + sb;
#pragma unroll
    for (int n = 0; n < 8; ++n) w[n] = ldsT(s + wo_[n & 3] + (n >> 2) * 256 + ks * 32 * 512, 16 * 512);
#pragma unroll
    for (int m = 0; m < 4; ++m) a[m] = ldsA(s + ao2 + m * 2048 + ks * 1024); };
  auto mm = [&](bf16x8(&a)[4], bf16x8(&w)[8]) {
#pragma unroll
    for (int n = 0; n < 4; ++n)
#pragma unroll
      for (int m = 0; m < 4; ++m) a2[m][n] = mfma(a[m], w[n], a2[m][n]);
#pragma unroll
    for (int n = 0; n < 4; ++n)
#pragma unroll
      for (int m = 0; m < 4; ++m) a1_[m][n] = mfma(w[4 + n], a[m], a1_[m][n]); };
  consumer_loop<S, SB>(c, 16, 16, [&](int sb) { rd(a0, w0, sb, 0); }, [&](int sb) { rd(a1, w1, sb, 1); }, [&]() { mm(a0, w0); }, [&]() { mm(a1, w1); }, []() { interleave<32, 20>(); }, [](int) {}, [&]() { TS(17); });
  TS(8);
  const lds_t xf = lds + xf3_base(c.wid) + lane * 16;
#pragma unroll
  for (int m = 0; m < 2; ++m)
#pragma unroll
    for (int n = 0; n < 4; ++n) { *(LAS f32x4*)(xf + (m * 8 + n) * 1024) = a2[2 + m][n]; *(LAS f32x4*)(xf + (m * 8 + 4 + n) * 1024) = a1_[2 + m][n]; e.z2[m][n] = a2[m][n]; e.z1[m][n] = a1_[m][n]; }
  wait_lgkm0();
}
struct Ld4 { __amdgpu_buffer_rsrc_t rA; int voA, voF; };
DI void p4_issueW(const Ctx& c, const Ld4& L, int t) { const int lw = c.wid & 3; const lds_t d = c.lds + (t & 3) * 32768 + lw * 1024;
#pragma unroll
  for (int i = 0; i < 4; ++i) dma<0>(L.rA, d + i * 4096, L.voA, t * 128 + i * 32 * 4096); }
DI void p3_epilogue(Ctx& c, const Ld4& L, int i3, int j3, Acc3& e, u32x2 (&z1)[2][4]) {
  const int lane = opaque(c.lane), cw = c.wid & 3, hf = c.wid >> 2, wr = cw >> 1, wc = cw & 1, g = lane >> 4, i16 = lane & 15;
  const lds_t lds = c.lds, tab = lds + L_TAB3;
  const __amdgpu_buffer_rsrc_t rWS = c.ws;
  const f32x4 Z4 = {0.f, 0.f, 0.f, 0.f};
  bar();
  TS(25);
  __builtin_amdgcn_s_setprio(1);
  if (hf) { const lds_t xf = lds + xf3_base(cw) + lane * 16;
#pragma unroll
    for (int m = 0; m < 2; ++m)
#pragma unroll
      for (int n = 0; n < 4; ++n) { e.z2[m][n] = *(LAS f32x4*)(xf + (m * 8 + n) * 1024); e.z1[m][n] = *(LAS f32x4*)(xf + (m * 8 + 4 + n) * 1024); } }
  {
    float cs[4];
#pragma unroll
    for (int n = 0; n < 4; ++n) cs[n] = *(LAS float*)(tab + 1024 + (64 * wc + 16 * n + i16) * 4);
#pragma unroll
    for (int m = 0; m < 2; ++m) {
      const int R = 64 * wr + 32 * hf + 16 * m + 4 * g;
      const f32x4 mu = *(LAS f32x4*)(tab + R * 4), rs = *(LAS f32x4*)(tab + 512 + R * 4), nm = -mu * rs;
      f32x4 s = Z4, qq = Z4;
#pragma unroll
      for (int n = 0; n < 4; ++n) { const f32x4 y = gelu4(e.z2[m][n] * rs + nm * cs[n]); e.z2[m][n] = y; s += y; qq += y * y; }
      sum_row16x8(s, qq);
      if (i16 < 4) { const float sv = i16 == 0 ? s[0] : i16 == 1 ? s[1] : i16 == 2 ? s[2] : s[3], qv = i16 == 0 ? qq[0] : i16 == 1 ? qq[1] : i16 == 2 ? qq[2] : qq[3];
        __builtin_amdgcn_raw_buffer_store_b128((u32x4){__float_as_uint(sv), c.tag, __float_as_uint(qv), c.tag}, rWS, OFF_ST2 + ((i3 * 14 + j3) * 2 + wc) * 2048 + (R + i16) * 16, 0, SC_SC1); }
    }
  }
  TS(9);
  __builtin_amdgcn_s_setprio(0);
  if (!hf) {
#pragma unroll
    for (int s = 0; s < 3; ++s) p4_issueW(c, L, s);
  }
  {
    const int R0 = 64 * wr + 32 * hf, off = OFF_ST2 + (i3 * 28 + (lane >> 5)) * 2048 + (R0 + (lane & 31)) * 16;
    u32x4 v[14];
    const unsigned long long t0 = __builtin_amdgcn_s_memrealtime();
    for (;;) {
      bool ok = true;
#pragma unroll
      for (int k = 0; k < 14; ++k) v[k] = __builtin_amdgcn_raw_buffer_load_b128(rWS, off + k * 4096, 0, SC_SC1);
#pragma unroll
      for (int k = 0; k < 14; ++k) ok &= v[k][1] == c.tag && v[k][3] == c.tag;
      if (__all(ok) || timed_out(c, t0, 0x203u)) break;
      __builtin_amdgcn_s_sleep(PSLEEP);
    }
    __builtin_amdgcn_s_setprio(1);
    float s = 0.f, qq = 0.f;
#pragma unroll
    for (int k = 0; k < 14; ++k) { s += __uint_as_float(v[k][0]); qq += __uint_as_float(v[k][2]); }
    { auto a = __builtin_amdgcn_permlane32_swap(__float_as_uint(s), __float_as_uint(s), false, false); s = (__uint_as_float(a[0]) + __uint_as_float(a[1])) * (1.0f / 1792.0f);
      auto b = __builtin_amdgcn_permlane32_swap(__float_as_uint(qq), __float_as_uint(qq), false, false); qq = (__uint_as_float(b[0]) + __uint_as_float(b[1])) * (1.0f / 1792.0f); }
    const float rsv = __builtin_amdgcn_rsqf(fmaxf(qq - s * s, 0.f) + 1e-5f);
    const lds_t ft = lds + L_FIN + c.wid * 256;
    if (lane < 32) { *(LAS float*)(ft + lane * 4) = rsv; *(LAS float*)(ft + 128 + lane * 4) = -s * rsv; }
    asm volatile("s_waitcnt lgkmcnt(0)" ::: "memory");
    TS(31);
    f32x4 rs[2], nm[2];
#pragma unroll
    for (int m = 0; m < 2; ++m) { rs[m] = *(LAS f32x4*)(ft + (16 * m + 4 * g) * 4); nm[m] = *(LAS f32x4*)(ft + 128 + (16 * m + 4 * g) * 4); }
#pragma unroll
    for (int n = 0; n < 4; ++n)
      st16(c, pk8(e.z2[0][n] * rs[0] + nm[0], e.z2[1][n] * rs[1] + nm[1]), lane * 16 + (OFF_Z2 + (j3 * 32 + 2 * i3 + wr) * 16384 + ((4 * wc + n) * 2 + hf) * 1024));
  }
  __builtin_amdgcn_s_setprio(0);
  publish<8>(c, 4, FG_Z2, j3, i3);
  TS(11);
#pragma unroll
  for (int m = 0; m < 2; ++m) asm volatile("" : "+v"(e.z1[m][0]), "+v"(e.z1[m][1]), "+v"(e.z1[m][2]), "+v"(e.z1[m][3]));
#pragma unroll
  for (int m = 0; m < 2; ++m) {
    const int R = 64 * wr + 32 * hf + 16 * m + i16;
    const float mu = *(LAS float*)(tab + R * 4), rs = *(LAS float*)(tab + 512 + R * 4), nm = -mu * rs;
#pragma unroll
    for (int n = 0; n < 4; ++n) { const f32x4 cs = *(LAS f32x4*)(tab + 1024 + (128 + 64 * wc + 16 * n + 4 * g) * 4); z1[m][n] = pk4(gelu4(e.z1[m][n] * rs + cs * nm)); }
    asm volatile("" : "+v"(z1[m][0]), "+v"(z1[m][1]), "+v"(z1[m][2]), "+v"(z1[m][3]));
  }
  TS(10);
}
DI void p4_load(Ctx& c, const Ld4& L, int j3) {
  const int lw = c.wid - 4;
  auto issueW = [&](int t) { p4_issueW(c, L, t); };
  auto issueA = [&](int t) { const lds_t d = c.lds + (t & 3) * 32768 + 16384 + lw * 1024; const int so = OFF_Z2 + (j3 * 32 + t) * 16384;
#pragma unroll
    for (int i = 0; i < 4; ++i) dma<LDAUX>(c.ws, d + i * 4096, L.voF, so + i * 4096); };
  bar();
  if (c.b == 0 && lw == 0 && c.lane == 0) __hip_atomic_store((unsigned*)(c.wsp + OFF_EPOCH), c.tag, __ATOMIC_RELAXED, __HIP_MEMORY_SCOPE_AGENT);
#pragma unroll
  for (int s = 0; s < 3; ++s) issueA(s);
  loader_loop<4, 4>(32, true, issueA, issueW, [](int) {}, [](int) {});
}
DI void p4_mma(Ctx& c, int j3, f32x4 (&e)[2][4]) {
  const int lane = opaque(c.lane), wr = c.wid >> 1, wc = c.wid & 1, g = lane >> 4, i16 = lane & 15;
  const lds_t lds = c.lds;
  constexpr int S = 4, SB = 32768;
  f32x4 acc[4][4];
#pragma unroll
  for (int m = 0; m < 4; ++m)
#pragma unroll
    for (int n = 0; n < 4; ++n) { acc[m][n] = (f32x4){0.f, 0.f, 0.f, 0.f}; asm volatile("" : "+v"(acc[m][n])); }
  const int ao0 = (wr * 64 + i16) * 128 + ((g ^ (i16 >> 1)) << 4), ao1 = ao0 ^ 64, zo = 16384 + wc * 8192 + lane * 16;
  bf16x8 a0[4], w0[4], a1[4], w1[4];
  auto rd = [&](bf16x8(&a)[4], bf16x8(&w)[4], int sb, int ks) { const lds_t s = lds + sb;
#pragma unroll
    for (int n = 0; n < 4; ++n) { w[n] = ldsA(s + zo + n * 2048 + ks * 1024); a[n] = ldsA(s + (ks ? ao1 : ao0) + n * 2048); } };
  auto mm = [&](bf16x8(&a)[4], bf16x8(&w)[4]) {
#pragma unroll
    for (int n = 0; n < 4; ++n)
#pragma unroll
      for (int m = 0; m < 4; ++m) acc[m][n] = mfma(w[n], a[m], acc[m][n]); };
  if (c.wid == 0) wait_flags(c, FG_Z2, j3, 16);
  bar();
  consumer_loop<S, SB>(c, 32, 32, [&](int sb) { rd(a0, w0, sb, 0); }, [&](int sb) { rd(a1, w1, sb, 1); }, [&]() { mm(a0, w0); }, [&]() { mm(a1, w1); }, []() { interleave<16, 8>(); }, [](int) {}, [&]() { TS(18); });
  TS(12);
  const lds_t xf = lds + c.wid * 8192 + lane * 16;
#pragma unroll
  for (int m = 0; m < 2; ++m)
#pragma unroll
    for (int n = 0; n < 4; ++n) { *(LAS f32x4*)(xf + (m * 4 + n) * 1024) = acc[2 + m][n]; e[m][n] = acc[m][n]; }
  wait_lgkm0();
}
DI void p5_issueW(const Ctx& c, const Args& A, int j2);
DI void p4_epilogue(Ctx& c, const Args& A, int i3, int j3, int j2, f32x4 (&e)[2][4], const u32x2 (&z1)[2][4]) {
  const int lane = opaque(c.lane), cw = c.wid & 3, hf = c.wid >> 2, wr = cw >> 1, wc = cw & 1, i16 = lane & 15;
  float bs[2];
#pragma unroll
  for (int m = 0; m < 2; ++m) bs[m] = bflo((unsigned)A.bsp[i3 * 128 + true_row(4 * wr + 2 * hf + m, i16)]);
  bar();
  if (hf) { const lds_t xf = c.lds + cw * 8192 + lane * 16;
#pragma unroll
    for (int m = 0; m < 2; ++m)
#pragma unroll
      for (int n = 0; n < 4; ++n) e[m][n] = *(LAS f32x4*)(xf + (m * 4 + n) * 1024); }
#pragma unroll
  for (int m = 0; m < 2; ++m)
#pragma unroll
    for (int u = 0; u < 2; ++u)
      st16(c, pk8(up4(z1[m][2 * u]) * (e[m][2 * u] + bs[m]), up4(z1[m][2 * u + 1]) * (e[m][2 * u + 1] + bs[m])),
           lane * 16 + (OFF_UF + (i3 * 28 + 2 * j3 + wc) * 16384 + ((4 * wr + 2 * hf + m) * 2 + u) * 1024));
  wait_lgkm0(); bar();
  publish<8>(c, 5, FG_U, i3, j3);
  if (hf) p5_issueW(c, A, j2);
  TS(13);
}

DI void p5_issueW(const Ctx& c, const Args& A, int j2) {
  const int lane = opaque(c.lane), lw = c.wid - 4;
  const __amdgpu_buffer_rsrc_t rW = __builtin_amdgcn_make_buffer_rsrc((void*)A.wout, 0, 1792 * 2048, 0x00020000);
  int vo[2];
#pragma unroll
  for (int i = 0; i < 2; ++i) { const int r = (2 * lw + i) * 8 + (lane >> 3), s16 = lane & 7; vo[i] = r * 2048 + j2 * 128 + (((s16 >> 1) ^ ((r >> 1) & 3)) << 5) + (s16 & 1) * 16; }
#pragma unroll
  for (int t = 0; t < 18; ++t)
#pragma unroll
    for (int i = 0; i < 2; ++i) dma<0>(rW, c.lds + ((t & 1) * 9 + (t >> 1)) * 8192 + (lw * 2 + i) * 1024, vo[i], ((t & 1) * 14 + (t >> 1)) * 64 * 2048);
}

constexpr int L_RED = 98304;
#define STG1 0
#define STG2 2
#define R5 6
#define R2 6
template <int NSH, int R, bool REFILL>
DI void gemm_split(Ctx& c, f32x4 (&res)[4], int aoff, __amdgpu_buffer_rsrc_t rW, int j2) {
  const int ln = opaque(c.lane), g = ln >> 4, q = (ln & 15) >> 2, p = ln & 3, rw = c.wid & 3, kh = c.wid >> 2;
  const lds_t lds = c.lds;
  const int sw2 = (2 * g + (q >> 1)) & 3, wl2 = (4 * g + q) * 128 + 8 * p + kh * (REFILL ? 9 : 8) * 8192, va = ln * 16 + rw * 4096;
  const int ao = aoff + kh * (NSH / 2) * 16384;
  int wo2[4];
#pragma unroll
  for (int n = 0; n < 4; ++n) wo2[n] = wl2 + 32 * (n ^ sw2);
  f32x4 acc[2][4];
#pragma unroll
  for (int n = 0; n < 4; ++n) { acc[0][n] = (f32x4){0.f, 0.f, 0.f, 0.f}; acc[1][n] = acc[0][n]; }
  u32x4 a[R][2]; bf16x8 w0[4], w1[4];
  auto lda = [&](int s) {
#pragma unroll
    for (int m = 0; m < 2; ++m) a[s % R][m] = __builtin_amdgcn_raw_buffer_load_b128(c.ws, va + m * 2048, ao + (s >> 1) * 16384 + (s & 1) * 1024, LDAUX); };
  auto ldw = [&](bf16x8(&d)[4], int s) { const int lt = s >> 1, slot = (REFILL && lt >= 9) ? lt - 9 : lt;
#pragma unroll
    for (int n = 0; n < 4; ++n) d[n] = ldsT(lds + wo2[n] + slot * 8192 + (s & 1) * 4096, 16 * 128); };
  TS(REFILL ? 19 : 24);
#pragma unroll
  for (int s = 0; s < R; ++s) { lda(s); if (s == STG1 || s == STG2) bar(); }
  if (!REFILL && kh) {
#pragma unroll
    for (int i = 0; i < 2; ++i) { const int r = (2 * rw + i) * 8 + (ln >> 3), s16 = ln & 7, vo = r * 2048 + j2 * 128 + (((s16 >> 1) ^ ((r >> 1) & 3)) << 5) + (s16 & 1) * 16;
#pragma unroll
      for (int t = 4; t < 8; ++t) dma<0>(rW, lds + t * 8192 + (rw * 2 + i) * 1024, vo, t * 64 * 2048); }
  }
  ldw(w0, 0);
#pragma unroll
  for (int s = 0; s < NSH; ++s) {
    if (s == 5) {
      if (kh) { if (REFILL) wait_vm<2 * R + 10>(); else wait_vm<10>(); }
      bar(); __builtin_amdgcn_sched_barrier(0);
    }
    if (REFILL && s == 10) {
      wait_lgkm0(); bar();
      const int r = 8 * c.wid + (ln >> 3), s16 = ln & 7, vo = r * 2048 + j2 * 128 + (((s16 >> 1) ^ ((r >> 1) & 3)) << 5) + (s16 & 1) * 16;
#pragma unroll
      for (int k = 0; k < 10; ++k) dma<0>(rW, lds + ((k / 5) * 9 + (k % 5)) * 8192 + c.wid * 1024, vo, ((k / 5) * 14 + 9 + (k % 5)) * 64 * 2048);
      __builtin_amdgcn_sched_barrier(0);
    }
    if (REFILL && s == 17) { wait_vm<2 * ((NSH - R < 17 ? NSH - R : 17) - 10)>(); wait_lgkm0(); bar(); __builtin_amdgcn_sched_barrier(0); }
    if (s + 1 < NSH) { if (s & 1) ldw(w0, s + 1); else ldw(w1, s + 1); }
#pragma unroll
    for (int n = 0; n < 4; ++n)
#pragma unroll
      for (int m = 0; m < 2; ++m) acc[m][n] = mfma((s & 1) ? w1[n] : w0[n], __builtin_bit_cast(bf16x8, a[s % R][m]), acc[m][n]);
    if (s + R < NSH) lda(s + R);
    interleave<8, 8>();
    __builtin_amdgcn_sched_barrier(0);
  }
  TS(REFILL ? 29 : 28);
  wait_lgkm0(); bar();
#pragma unroll
  for (int n = 0; n < 4; ++n) *(LAS f32x4*)(lds + L_RED + c.wid * 4096 + n * 1024 + ln * 16) = kh ? acc[0][n] : acc[1][n];
  wait_lgkm0(); bar();
#pragma unroll
  for (int n = 0; n < 4; ++n) res[n] = (kh ? acc[1][n] : acc[0][n]) + *(LAS f32x4*)(lds + L_RED + (c.wid ^ 4) * 4096 + n * 1024 + ln * 16);
}

template <class PRE> DI void phase2(Ctx& c, const Args& A, int i2, int j2, bool pre_on, PRE pre) {
  const __amdgpu_buffer_rsrc_t rW2 = __builtin_amdgcn_make_buffer_rsrc((void*)A.wo, 0, 1024 * 2048, 0x00020000);
  const int lane = opaque(c.lane), g = lane >> 4, i16 = lane & 15, mt = 2 * (c.wid & 3) + (c.wid >> 2);
  u32x2 xr[4];
#pragma unroll
  for (int n = 0; n < 4; ++n) xr[n] = *(const u32x2*)(A.x + (size_t)(i2 * 128 + true_row(mt, i16)) * 1024 + j2 * 64 + 16 * n + 4 * g);
  wait_lgkm0(); bar();
  f32x4 acc[4];
  gemm_split<16, R2, false>(c, acc, OFF_OF + i2 * 262144, rW2, j2);
  TS(6);
  float s = 0.f, qq = 0.f;
#pragma unroll
  for (int u = 0; u < 2; ++u) {
    f32x4 h0 = acc[2 * u], h1 = acc[2 * u + 1]; const u32x2 x0 = xr[2 * u], x1 = xr[2 * u + 1];
    h0[0] += bflo(x0[0]); h0[1] += bfhi(x0[0]); h0[2] += bflo(x0[1]); h0[3] += bfhi(x0[1]);
    h1[0] += bflo(x1[0]); h1[1] += bfhi(x1[0]); h1[2] += bflo(x1[1]); h1[3] += bfhi(x1[1]);
    const bf16x8 hb = pk8(h0, h1);
    st16(c, hb, lane * 16 + (OFF_HF + (i2 * 16 + j2) * 16384 + (mt * 2 + u) * 1024));
    acc_row(s, qq, __builtin_bit_cast(u32x4, hb));
  }
  s = sum_g(s); qq = sum_g(qq);
  if (lane < 16) __builtin_amdgcn_raw_buffer_store_b64((u32x2){__float_as_uint(s), __float_as_uint(qq)}, c.ws, OFF_STH + (i2 * 16 + j2) * 1024 + (mt * 16 + lane) * 8, 0, SC_SC1);
  publish<8>(c, 2, FG_H, i2, j2);
  if (pre_on) pre();
  TS(7);
}

DI void phase5(Ctx& c, const Args& A, int i2, int j2) {
  const int lane = opaque(c.lane), g = lane >> 4, i16 = lane & 15, mt = 2 * (c.wid & 3) + (c.wid >> 2);
  u32x4 hr[2];
#pragma unroll
  for (int u = 0; u < 2; ++u) hr[u] = __builtin_amdgcn_raw_buffer_load_b128(c.ws, lane * 16, OFF_HF + (i2 * 16 + j2) * 16384 + (mt * 2 + u) * 1024, SC_SC1);
  const __amdgpu_buffer_rsrc_t rW = __builtin_amdgcn_make_buffer_rsrc((void*)A.wout, 0, 1792 * 2048, 0x00020000);
  if (c.wid == 0) wait_flags(c, FG_U, i2, 14);
  if (c.wid >= 4) wait_vm<24>();
  bar();
  f32x4 acc[4];
  gemm_split<28, R5, true>(c, acc, OFF_UF + i2 * 458752, rW, j2);
  TS(14);
  const lds_t ot = c.lds + c.wid * 2048;
#pragma unroll
  for (int n = 0; n < 4; ++n) { const unsigned r0 = hr[n >> 1][2 * (n & 1)], r1 = hr[n >> 1][2 * (n & 1) + 1];
    *(LAS u32x2*)(ot + i16 * 128 + (((2 * n + (g >> 1)) ^ (i16 & 7)) << 4) + (g & 1) * 8) =
        (u32x2){pk2(acc[n][0] + bflo(r0), acc[n][1] + bfhi(r0)), pk2(acc[n][2] + bflo(r1), acc[n][3] + bfhi(r1))}; }
  asm volatile("s_waitcnt lgkmcnt(0)" ::: "memory");
  const __amdgpu_buffer_rsrc_t rO = __builtin_amdgcn_make_buffer_rsrc((void*)A.out, 0, 2048 * 2048, 0x00020000);
#pragma unroll
  for (int u = 0; u < 2; ++u) { const int r = 8 * u + (lane >> 3), ch = lane & 7;
    const u32x4 v = *(LAS u32x4*)(ot + r * 128 + ((ch ^ (r & 7)) << 4));
    __builtin_amdgcn_raw_buffer_store_b128(v, rO, (i2 * 128 + true_row(mt, r)) * 2048 + j2 * 128 + ch * 16, 0, OUTAUX); }
  TS(15);
}

#define LAST_PHASE 5
__global__ __launch_bounds__(512, 2) void fused_fwd(const u16* __restrict__ x, const u16* __restrict__ wqkv, const u16* __restrict__ wphi, const u16* __restrict__ wo,
                                                      const u16* __restrict__ win, const u16* __restrict__ wsp, const u16* __restrict__ bsp, const u16* __restrict__ wout,
                                                      u16* __restrict__ out, char* __restrict__ ws) {
  extern __shared__ __attribute__((aligned(16))) char shm[];
  Ctx c;
  c.lds = (lds_t)shm; c.wsp = ws; c.lane = threadIdx.x & 63; c.wid = __builtin_amdgcn_readfirstlane(threadIdx.x >> 6); c.b = blockIdx.x;
  c.ws = __builtin_amdgcn_make_buffer_rsrc((void*)ws, 0, WS_BYTES, 0x00020000);
  c.tag = 0u; c.ts = 0u;
  if (threadIdx.x < 16) *(LAS unsigned*)(c.lds + L_CNT + threadIdx.x * 4) = 0u;
  const int xcd = c.b & 7, jj = c.b >> 3;
  Map M;
  M.i1 = 8 * (xcd & 1) + (jj >> 2); M.j1 = 4 * (xcd >> 1) + (jj & 3);
  M.i2 = 4 * (xcd >> 1) + (jj >> 3); M.j2 = 8 * (xcd & 1) + (jj & 7);
  M.act3 = jj < 28;
  M.i3 = 4 * (xcd >> 1) + (M.act3 ? jj / 7 : 0); M.j3 = 7 * (xcd & 1) + (M.act3 ? jj % 7 : 0);
  const Args A{x, wqkv, wphi, wo, win, wsp, bsp, wout, out, ws};
  constexpr int last = LAST_PHASE;
  const bool ld = c.wid >= 4;
  {
    Acc1 e;
    if (ld) p1_load(c, A, M.i1, M.j1);
    else { __builtin_amdgcn_s_setprio(1); p1_mma(c, A, M.i1, M.j1, e); __builtin_amdgcn_s_setprio(0); }
    p1_epilogue(c, A, M, e, last);
  }
  if (last < 2) return;
  Ld3 L3;
  p3_setup(c, A, M.j3, L3);
  phase2(c, A, M.i2, M.j2, ld && M.act3, [&]() { p3_issueW(c, L3, 0); p3_issueW(c, L3, 1); });
  if (last < 3) return;
  if (M.act3) {
    u32x2 z1[2][4];
    Ld4 L;
    L.rA = __builtin_amdgcn_make_buffer_rsrc((void*)(A.wsp + (size_t)M.i3 * 128 * 2048), 0, 128 * 4096, 0x00020000);
    {
      Acc3 e;
      if (ld) p3_load(c, L3, M.i3);
      else { __builtin_amdgcn_s_setprio(1); p3_mma(c, M.i3, M.j3, e); __builtin_amdgcn_s_setprio(0); }
      const int ln = opaque(c.lane);
      L.voA = rowmajor_voff(c.wid & 3, ln, 4096); L.voF = ln * 16 + (c.wid & 3) * 1024;
      p3_epilogue(c, L, M.i3, M.j3, e, z1);
    }
    if (last < 4) return;
    f32x4 e4[2][4];
    if (ld) p4_load(c, L, M.j3);
    else { __builtin_amdgcn_s_setprio(1); p4_mma(c, M.j3, e4); __builtin_amdgcn_s_setprio(0); }
    p4_epilogue(c, A, M.i3, M.j3, M.j2, e4, z1);
  } else {
    bar();
    if (!ld) { const int wv = ((c.b & 7) * 4 + ((c.b >> 3) - 28)) * 4 + c.wid;
#pragma unroll 1
      for (int k = 0; k < 12; ++k) { const int Li = (k * 128 + wv) * 64 + c.lane;
        touch(c.lds, Li < 65536 ? (const char*)A.wsp + (size_t)Li * 128 : (const char*)A.wout + (size_t)((Li < 94208 ? Li : 94207) - 65536) * 128); } }
    if (ld) p5_issueW(c, A, M.j2);
  }
  if (last < 5) return;
  phase5(c, A, M.i2, M.j2);
}

extern "C" void kernel_launch(void* const* d_in, const int* in_sizes, int n_in, void* d_out, int out_size, void* d_ws, size_t ws_size, hipStream_t stream) {
  (void)in_sizes; (void)n_in; (void)out_size; (void)ws_size;
  (void)hipFuncSetAttribute((const void*)fused_fwd, hipFuncAttributeMaxDynamicSharedMemorySize, LDS_BYTES);
  fused_fwd<<<256, 512, LDS_BYTES, stream>>>((const u16*)d_in[0], (const u16*)d_in[1], (const u16*)d_in[2], (const u16*)d_in[3], (const u16*)d_in[5], (const u16*)d_in[6],
                                             (const u16*)d_in[7], (const u16*)d_in[8], (u16*)d_out, (char*)d_ws);
}
```

```cpp
#include <hip/hip_runtime.h>
#include <hip/hip_bf16.h>
#include <stdint.h>
#include <type_traits>

#define LAS __attribute__((address_space(3)))
#define DI __device__ __forceinline__
typedef unsigned short u16;
typedef short bf16x8 __attribute__((ext_vector_type(8)));
typedef short s16x4 __attribute__((ext_vector_type(4)));
typedef float f32x4 __attribute__((ext_vector_type(4)));
typedef float f32x2 __attribute__((ext_vector_type(2)));
typedef unsigned u32x4 __attribute__((ext_vector_type(4)));
typedef unsigned u32x2 __attribute__((ext_vector_type(2)));
typedef __bf16 bf2 __attribute__((ext_vector_type(2)));
typedef LAS char* lds_t;

constexpr unsigned OFF_EPOCH = 0, OFF_ERR = 128, OFF_FLAG = 4096;
constexpr unsigned OFF_KVP = 262144;
constexpr unsigned OFF_ZP = OFF_KVP + 2097152;
constexpr unsigned OFF_ST2 = 27u << 20 | 0;
constexpr unsigned OFF_STH = OFF_ZP + 131072 + 262144;
constexpr unsigned OFF_GX = OFF_STH + 262144;
constexpr unsigned OFF_GC1 = OFF_GX + 65536;
constexpr unsigned OFF_GC3 = OFF_GC1 + 393216;
constexpr unsigned OFF_OF = 4u << 20, OFF_HF = 8u << 20, OFF_Z2 = 12u << 20, OFF_UF = 20u << 20, OFF_TS = 28u << 20;
constexpr unsigned WS_BYTES = 32u << 20;
enum { FG_KV = 0, FG_O = 1, FG_H = 2, FG_ST = 3, FG_Z2 = 4, FG_U = 5 };
constexpr int LDS_BYTES = 163840;
constexpr int SC1 = 122880, L_WPHI = SC1, L_TAB1 = SC1 + 4096, L_XBUF = SC1 + 22528;
constexpr int L_XF1 = 65536, L_TB = 32768, L_KR = 65536, L_ZR = 131072;
constexpr int SC3 = 147456, L_TAB3 = SC3, L_FIN = SC3 + 4096;
constexpr int L_CNT = LDS_BYTES - 64, L_DUMMY = LDS_BYTES - 512;
constexpr int SC_SC1 = 16;
#define PSLEEP 4
#define OUTAUX 16
#define LDAUX 16

DI void bar() { asm volatile("" ::: "memory"); __builtin_amdgcn_s_barrier(); asm volatile("" ::: "memory"); }
template <int N> DI void wait_vm() { asm volatile("s_waitcnt vmcnt(%0)" ::"n"(N) : "memory"); }
DI int opaque(int v) { asm volatile("" : "+v"(v)); return v; }
DI void wait_lgkm0() { __builtin_amdgcn_s_waitcnt(0xc07f); }
DI unsigned pk2(float lo, float hi) { f32x2 v = {lo, hi}; return __builtin_bit_cast(unsigned, __builtin_convertvector(v, bf2)); }
DI bf16x8 pk8(const f32x4& a, const f32x4& b) { u32x4 r = {pk2(a[0], a[1]), pk2(a[2], a[3]), pk2(b[0], b[1]), pk2(b[2], b[3])}; return __builtin_bit_cast(bf16x8, r); }
DI float bflo(unsigned w) { return __uint_as_float(w << 16); }
DI float bfhi(unsigned w) { return __uint_as_float(w & 0xffff0000u); }
DI f32x4 mfma(bf16x8 a, bf16x8 b, f32x4 c) { return __builtin_amdgcn_mfma_f32_16x16x32_bf16(a, b, c, 0, 0, 0); }
DI bf16x8 ldsA(lds_t p) { return *(LAS bf16x8*)p; }
DI bf16x8 ldsT(lds_t p, int d) {
  s16x4 lo = __builtin_amdgcn_ds_read_tr16_b64_v4i16((LAS s16x4*)p), hi = __builtin_amdgcn_ds_read_tr16_b64_v4i16((LAS s16x4*)(p + d));
  return __builtin_shufflevector(lo, hi, 0, 1, 2, 3, 4, 5, 6, 7);
}
template <int D1, int D2> DI void tr4_issue(unsigned addr, u32x2 (&r)[4]) {
  asm volatile("ds_read_b64_tr_b16 %0, %4\n\tds_read_b64_tr_b16 %1, %4 offset:%5\n\tds_read_b64_tr_b16 %2, %4 offset:%6\n\tds_read_b64_tr_b16 %3, %4 offset:%7"
               : "=&v"(r[0]), "=&v"(r[1]), "=&v"(r[2]), "=&v"(r[3]) : "v"(addr), "n"(D1), "n"(D2), "n"(D1 + D2) : "memory");
}
DI void tr4_wait(u32x2 (&r)[4]) { asm volatile("s_waitcnt lgkmcnt(0)" : "+v"(r[0]), "+v"(r[1]), "+v"(r[2]), "+v"(r[3])::"memory"); }
DI float sum_g(float x) {
  auto a = __builtin_amdgcn_permlane16_swap(__float_as_uint(x), __float_as_uint(x), false, false);
  x = __uint_as_float(a[0]) + __uint_as_float(a[1]);
  auto b = __builtin_amdgcn_permlane32_swap(__float_as_uint(x), __float_as_uint(x), false, false);
  return __uint_as_float(b[0]) + __uint_as_float(b[1]);
}
DI float max_g(float x) {
  auto a = __builtin_amdgcn_permlane16_swap(__float_as_uint(x), __float_as_uint(x), false, false);
  x = fmaxf(__uint_as_float(a[0]), __uint_as_float(a[1]));
  auto b = __builtin_amdgcn_permlane32_swap(__float_as_uint(x), __float_as_uint(x), false, false);
  return fmaxf(__uint_as_float(b[0]), __uint_as_float(b[1]));
}
template <int CTRL> DI float dpp_add(float x) { return x + __int_as_float(__builtin_amdgcn_update_dpp(0, __float_as_int(x), CTRL, 0xf, 0xf, true)); }
DI float sum_row16(float x) { x = dpp_add<0x128>(x); x = dpp_add<0x124>(x); x = dpp_add<0x122>(x); return dpp_add<0x121>(x); }
DI void sum_row16x8(f32x4& a, f32x4& b) {
#define RR(n) "v_add_f32_dpp %0, %0, %0 row_ror:" n " row_mask:0xf bank_mask:0xf\n\tv_add_f32_dpp %1, %1, %1 row_ror:" n " row_mask:0xf bank_mask:0xf\n\t" \
  "v_add_f32_dpp %2, %2, %2 row_ror:" n " row_mask:0xf bank_mask:0xf\n\tv_add_f32_dpp %3, %3, %3 row_ror:" n " row_mask:0xf bank_mask:0xf\n\t" \
  "v_add_f32_dpp %4, %4, %4 row_ror:" n " row_mask:0xf bank_mask:0xf\n\tv_add_f32_dpp %5, %5, %5 row_ror:" n " row_mask:0xf bank_mask:0xf\n\t" \
  "v_add_f32_dpp %6, %6, %6 row_ror:" n " row_mask:0xf bank_mask:0xf\n\tv_add_f32_dpp %7, %7, %7 row_ror:" n " row_mask:0xf bank_mask:0xf\n\t"
  float a0 = a[0], a1 = a[1], a2 = a[2], a3 = a[3], b0 = b[0], b1 = b[1], b2 = b[2], b3 = b[3];
  asm volatile("s_nop 1\n\t" RR("8") RR("4") RR("2") RR("1") "s_nop 0" : "+v"(a0), "+v"(a1), "+v"(a2), "+v"(a3), "+v"(b0), "+v"(b1), "+v"(b2), "+v"(b3));
#undef RR
  a = (f32x4){a0, a1, a2, a3}; b = (f32x4){b0, b1, b2, b3};
}
DI float dot2(unsigned a, unsigned b, float c) { return __builtin_amdgcn_fdot2_f32_bf16(__builtin_bit_cast(bf2, a), __builtin_bit_cast(bf2, b), c, false); }
constexpr unsigned ONES2 = 0x3f803f80u;
DI f32x4 gelu4(f32x4 x) {
  const f32x4 t = x * (x * x * -0.1029432397f + -2.302208198f);
  f32x4 d, r;
#pragma unroll
  for (int i = 0; i < 4; ++i) d[i] = __builtin_amdgcn_exp2f(t[i]);
  d += 1.0f;
#pragma unroll
  for (int i = 0; i < 4; ++i) r[i] = __builtin_amdgcn_rcpf(d[i]);
  return x * r;
}
DI u32x2 pk4(const f32x4& a) { return (u32x2){pk2(a[0], a[1]), pk2(a[2], a[3])}; }
DI f32x4 up4(u32x2 w) { return (f32x4){bflo(w[0]), bfhi(w[0]), bflo(w[1]), bfhi(w[1])}; }
DI float gelu(float x) { const float t = x * fmaf(x * x, -0.1029432397f, -2.302208198f); return x * __builtin_amdgcn_rcpf(1.0f + __builtin_amdgcn_exp2f(t)); }

struct Ctx {
  __amdgpu_buffer_rsrc_t ws;
  char* wsp; lds_t lds; unsigned tag; int lane, wid, b; unsigned ts;
};
DI unsigned* flag_ptr(const Ctx& c, int g, int a, int bb) { return (unsigned*)(c.wsp + OFF_FLAG + ((g * 16 + a) * 16 + bb) * 128); }
template <unsigned NWAVES = 4, int YOUNGER = 0> DI void publish(const Ctx& c, int cnt, int g, int a, int bb) {
  wait_vm<YOUNGER>();
  if (c.lane == 0) {
    unsigned old = __hip_atomic_fetch_add((LAS unsigned*)(c.lds + L_CNT + cnt * 4), 1u, __ATOMIC_RELAXED, __HIP_MEMORY_SCOPE_WORKGROUP);
    if (old == NWAVES - 1u) __hip_atomic_store(flag_ptr(c, g, a, bb), c.tag, __ATOMIC_RELAXED, __HIP_MEMORY_SCOPE_AGENT);
  }
}
DI void wait_flags(const Ctx& c, int g, int a, int n) {
  const unsigned* p = flag_ptr(c, g, a, c.lane < n ? c.lane : 0);
  const unsigned long long t0 = __builtin_amdgcn_s_memrealtime();
  for (;;) {
    const unsigned v = __hip_atomic_load(p, __ATOMIC_RELAXED, __HIP_MEMORY_SCOPE_AGENT);
    if (__all(v == c.tag)) break;
    if (__builtin_amdgcn_s_memrealtime() - t0 > 300000ull) { if (c.lane == 0) __hip_atomic_store((unsigned*)(c.wsp + OFF_ERR), 0x100u + (unsigned)g, __ATOMIC_RELAXED, __HIP_MEMORY_SCOPE_AGENT); break; }
    __builtin_amdgcn_s_sleep(1);
  }
  asm volatile("" ::: "memory");
}
#define STEP(t) do {} while (0)
#define TS(k) do {} while (0)

DI void st16(const Ctx& c, bf16x8 v, int off) { __builtin_amdgcn_raw_buffer_store_b128(__builtin_bit_cast(u32x4, v), c.ws, off, 0, SC_SC1); }
template <int AUX> DI void dma(__amdgpu_buffer_rsrc_t r, lds_t dst, int voff, int soff) {
  if (AUX) asm volatile("s_mov_b32 m0, %0\n\ts_nop 0\n\tbuffer_load_dwordx4 %1, %2, %3 offen sc1 lds" ::"s"((unsigned)(size_t)dst), "v"(voff), "s"(r), "s"(soff) : "memory");
  else asm volatile("s_mov_b32 m0, %0\n\ts_nop 0\n\tbuffer_load_dwordx4 %1, %2, %3 offen lds" ::"s"((unsigned)(size_t)dst), "v"(voff), "s"(r), "s"(soff) : "memory");
}

#define EXP 0
template <int S, int NW, class IA, class IW, class MID, class POST>
DI void loader_loop(int nt, bool gated_prologue, IA issueA, IW issueW, MID mid, POST post) {
  constexpr int PPW = 4 + NW;
  if (gated_prologue) wait_vm<4 * (S - 2)>(); else wait_vm<PPW * (S - 2)>();
  bar();
  int t = 0;
#pragma unroll
  for (int p = 0; p < S - 2; ++p, ++t) {
    mid(t); issueA(t + S - 1); issueW(t + S - 1); post(t);
    if (gated_prologue) {
      if (p == 0) wait_vm<4 * (S - 3 > 0 ? S - 3 : 0) + PPW * 1>();
      else if (p == 1) wait_vm<4 * (S - 4 > 0 ? S - 4 : 0) + PPW * 2>();
      else wait_vm<4 * (S - 5 > 0 ? S - 5 : 0) + PPW * 3>();
    } else wait_vm<PPW * (S - 2)>();
    bar();
  }
#pragma unroll 1
  for (; t + S - 1 < nt; ++t) { mid(t); if (!(EXP & 1)) { issueA(t + S - 1); issueW(t + S - 1); } wait_vm<PPW * (S - 2)>(); bar(); }
#pragma unroll
  for (int r = S - 2; r >= 0; --r, ++t) {
    if (r >= 4) wait_vm<PPW * 3>(); else if (r == 3) wait_vm<PPW * 2>(); else if (r == 2) wait_vm<PPW * 1>(); else wait_vm<0>();
    bar();
  }
}
template <int NW, bool GATED, class IA, class IW, class POST, class GA, class FIN>
DI void loader_loop16(bool full, bool once, IA issueA, IW issueW, POST post, GA gather, FIN fin) {
  constexpr int PPW = 4 + NW;
  if (GATED) wait_vm<4>(); else wait_vm<PPW>();
  bar();
  issueA(2); issueW(2); post(0); wait_vm<PPW>(); bar();
#pragma unroll 1
  for (int t = 1; t < 5; ++t) { if (!(EXP & 1)) { issueA(t + 2); issueW(t + 2); } post(t); wait_vm<PPW>(); bar(); }
#pragma unroll
  for (int t = 5; t < 13; ++t) {
    issueA(t + 2); issueW(t + 2); gather(t - 5);
    if (full) { if (t == 5) wait_vm<PPW + 2>(); else wait_vm<PPW + 4>(); }
    else if (once && t <= 6) wait_vm<PPW + 2>();
    else wait_vm<PPW>();
    bar();
  }
  issueA(15); issueW(15); if (full) wait_vm<PPW + 2>(); else wait_vm<PPW>(); bar();
  wait_vm<0>(); fin(); bar();
  bar();
}
template <int S, int SB, class RD0, class RD1, class MM0, class MM1, class SCH, class HK, class ST>
DI void consumer_loop(Ctx& c, int sbase, int nt, RD0 rd0, RD1 rd1, MM0 mm0, MM1 mm1, SCH sched, HK hook, ST stamp) {
  bar();
  stamp();
  rd0(0);
  int sb = 0;
#pragma unroll 1
  for (int t = 0; t < nt; ++t) {
    hook(t); STEP(t);
    __builtin_amdgcn_sched_barrier(0);
    if (!(EXP & 4)) rd1(sb); if (!(EXP & 2)) mm0(); sched();
    __builtin_amdgcn_sched_barrier(0);
    wait_lgkm0(); bar();
    __builtin_amdgcn_sched_barrier(0);
    sb = (sb == (S - 1) * SB) ? 0 : sb + SB;
    if (!(EXP & 4)) rd0(sb); if (!(EXP & 2)) mm1(); sched();
    __builtin_amdgcn_sched_barrier(0);
  }
}
#define TOUCH(name, mod) DI void name(lds_t lds, const void* p) { \
  asm volatile("s_mov_b32 m0, %1\n\ts_nop 0\n\tglobal_load_lds_dword %0, off" mod ::"v"(p), "s"((unsigned)(size_t)(lds + L_DUMMY)) : "memory"); }
TOUCH(touch, "") TOUCH(touch_nt, " nt") TOUCH(touch_sc1, " sc1")
template <int NM, int NR> DI void interleave() {
#pragma unroll
  for (int i = 0; i < NR; ++i) { __builtin_amdgcn_sched_group_barrier(0x008, 1, 0); __builtin_amdgcn_sched_group_barrier(0x100, 1, 0); }
  __builtin_amdgcn_sched_group_barrier(0x008, NM - NR, 0);
}
template <int NM, int NR> DI void interleave2() {
#pragma unroll
  for (int i = 0; i < NR / 2; ++i) { __builtin_amdgcn_sched_group_barrier(0x008, 1, 0); __builtin_amdgcn_sched_group_barrier(0x100, 2, 0); }
  __builtin_amdgcn_sched_group_barrier(0x008, NM - NR / 2, 0);
}

DI int rowmajor_voff(int lw, int lane, int pitch) {
  const int rr = lane >> 3, slot = 8 * (lw & 1) + rr;
  const int row = 8 * (slot >> 2) + 4 * (lw >> 1) + (slot & 3);
  return row * pitch + (((lane & 7) ^ (slot >> 1)) << 4);
}
DI int true_row(int mt, int i) { return 32 * (mt >> 1) + 8 * (i >> 2) + 4 * (mt & 1) + (i & 3); }

DI void acc_row(float& s, float& q, u32x4 w) {
#pragma unroll
  for (int i = 0; i < 4; ++i) { s = dot2(w[i], ONES2, s); q = dot2(w[i], w[i], q); } }
DI float col_sum(const u32x2 (&r)[4]) { float s0 = 0.f, s1 = 0.f;
#pragma unroll
  for (int i = 0; i < 4; ++i) { s0 = dot2(r[i][0], ONES2, s0); s1 = dot2(r[i][1], ONES2, s1); }
  return sum_g(s0 + s1); }
DI void st_granule(const Ctx& c, float v, int off) { __builtin_amdgcn_raw_buffer_store_b64((u32x2){__float_as_uint(v), c.tag}, c.ws, off, 0, SC_SC1); }
DI bool timed_out(const Ctx& c, unsigned long long t0, unsigned code) {
  if (__builtin_amdgcn_s_memrealtime() - t0 <= 300000ull) return false;
  if (c.lane == 0) __hip_atomic_store((unsigned*)(c.wsp + OFF_ERR), code, __ATOMIC_RELAXED, __HIP_MEMORY_SCOPE_AGENT);
  return true;
}
DI float sum_granules16(const Ctx& c, u32x2 (&v)[16], int off, int stride) {
  const unsigned long long t0 = __builtin_amdgcn_s_memrealtime();
  for (;;) {
    bool ok = true;
#pragma unroll
    for (int k = 0; k < 16; ++k) ok &= v[k][1] == c.tag;
    if (__all(ok) || timed_out(c, t0, 0x200u)) break;
    __builtin_amdgcn_s_sleep(2);
#pragma unroll
    for (int k = 0; k < 16; ++k) v[k] = __builtin_amdgcn_raw_buffer_load_b64(c.ws, off + k * stride, 0, SC_SC1);
  }
  float s = 0.f;
#pragma unroll
  for (int k = 0; k < 16; ++k) s += __uint_as_float(v[k][0]);
  return s;
}

#define ROTM 0
#define ROT2(j) (ROTM * 2 * ((j) & 7))
#define ROT5(j) (ROTM * ((7 * ((j) & 7)) >> 1))
#define P4D 0
#define TOUCHW touch
#define PF_T0 2
#define PF_DT 3

struct Args { const u16 *x, *wqkv, *wphi, *wo, *win, *wsp, *bsp, *wout; u16* out; char* ws; };

DI void get_tag(Ctx& c) { unsigned e; asm volatile("s_load_dword %0, %1, 0x0\n\ts_waitcnt lgkmcnt(0)" : "=&s"(e) : "s"(c.wsp) : "memory"); c.tag = e + 1u; }
DI void kv_gather(const Ctx& c, int j1) {
  const int lane = c.lane, g = lane >> 4, i16 = lane & 15, tid = c.wid * 64 + lane;
  f32x4 a0 = {0, 0, 0, 0}, a1 = {0, 0, 0, 0};
  u32x4 v[16];
  const unsigned f0 = __hip_atomic_load(flag_ptr(c, FG_KV, j1, i16), __ATOMIC_RELAXED, __HIP_MEMORY_SCOPE_AGENT);
  const unsigned ready = __builtin_amdgcn_readfirstlane((unsigned)(__ballot(f0 == c.tag) & 0xffffull));
  asm volatile("" ::: "memory");
#pragma unroll
  for (int k = 0; k < 16; ++k) if ((ready >> k) & 1u) v[k] = __builtin_amdgcn_raw_buffer_load_b128(c.ws, tid * 16, OFF_KVP + (j1 * 16 + k) * 8192, SC_SC1);
  if (ready != 0xffffu) {
    wait_flags(c, FG_KV, j1, 16);
#pragma unroll
    for (int k = 0; k < 16; ++k) if (!((ready >> k) & 1u)) v[k] = __builtin_amdgcn_raw_buffer_load_b128(c.ws, tid * 16, OFF_KVP + (j1 * 16 + k) * 8192, SC_SC1);
  }
  float zs = 0.f;
  if (c.wid < 2) { unsigned zv[16];
#pragma unroll
    for (int k = 0; k < 16; ++k) zv[k] = __builtin_amdgcn_raw_buffer_load_b32(c.ws, lane * 4, OFF_ZP + ((j1 * 16 + k) * 2 + c.wid) * 256, SC_SC1);
#pragma unroll
    for (int k = 0; k < 16; ++k) zs += __uint_as_float(zv[k]); }
#pragma unroll
  for (int k = 0; k < 16; ++k) { a0[0] += bflo(v[k][0]); a0[1] += bfhi(v[k][0]); a0[2] += bflo(v[k][1]); a0[3] += bfhi(v[k][1]); a1[0] += bflo(v[k][2]); a1[1] += bfhi(v[k][2]); a1[2] += bflo(v[k][3]); a1[3] += bfhi(v[k][3]); }
  const int w = c.wid, hd = w >> 2, dt = (w >> 1) & 1, fp = w & 1;
  const lds_t tb = c.lds + L_XBUF + hd * 8192 + (32 * fp + i16) * 128 + (16 * dt + 4 * g) * 2;
  *(LAS u32x2*)tb = (u32x2){pk2(a0[0], a0[1]), pk2(a0[2], a0[3])};
  *(LAS u32x2*)(tb + 16 * 128) = (u32x2){pk2(a1[0], a1[1]), pk2(a1[2], a1[3])};
  if (c.wid < 2) { const unsigned z2 = pk2(zs, zs); const lds_t t3 = c.lds + L_XBUF + c.wid * 8192 + lane * 128 + 64;
    *(LAS u32x4*)t3 = (u32x4){z2, z2, z2, z2}; *(LAS u32x4*)(t3 + 16) = (u32x4){z2, z2, z2, z2}; }
  wait_lgkm0();
}
struct Map { int i1, j1, i2, j2, i3, j3; bool act3; };
DI void p1_load(Ctx& c, const Args& A, int i1, int j1) {
  const int lane = c.lane, lw = c.wid - 4, g = lane >> 4, q = (lane & 15) >> 2, p = lane & 3;
  const lds_t lds = c.lds;
  const __amdgpu_buffer_rsrc_t rWS = c.ws;
  constexpr int S = 3, SB = 40960, NW = 6;
  const __amdgpu_buffer_rsrc_t rA = __builtin_amdgcn_make_buffer_rsrc((void*)(A.x + (size_t)i1 * 128 * 1024), 0, 128 * 2048, 0x00020000);
  const __amdgpu_buffer_rsrc_t rW = __builtin_amdgcn_make_buffer_rsrc((void*)A.wqkv, 0, 1024 * 6144, 0x00020000);
  const __amdgpu_buffer_rsrc_t rP = __builtin_amdgcn_make_buffer_rsrc((void*)A.wphi, 0, 65536, 0x00020000);
  const int voA = rowmajor_voff(lw, lane, 2048);
  int voW[NW];
#pragma unroll
  for (int i = 0; i < NW; ++i) { const int o = (NW * lw + i) * 1024 + lane * 16, r = o / 384, rem = o % 384, seg = rem >> 7, s16 = (rem & 127) >> 4;
    const int c32 = (s16 >> 1) ^ ((r >> 1) & 3), r5 = r & 31, k = (r & 32) + 8 * ((r5 >> 2) & 3) + 4 * (r5 >> 4) + (r5 & 3);
    voW[i] = k * 6144 + seg * 2048 + j1 * 128 + c32 * 32 + (s16 & 1) * 16; }
#define ROTMODE 1
  const int rot = ROTMODE == 2 ? (2 * (i1 & 7) + (i1 >> 3) + 4 * (j1 & 3)) & 15 : ROTMODE == 1 ? 4 * (i1 & 3) : ROTMODE == 3 ? 8 * (i1 & 1) : 0;
  auto issueA = [&](int t) { const lds_t d = lds + (t % S) * SB + lw * 1024; const int so = ((t + rot) & 15) * 128;
#pragma unroll
    for (int i = 0; i < 4; ++i) dma<0>(rA, d + i * 4096, voA, so + i * 32 * 2048); };
  auto issueW = [&](int t) { const lds_t d = lds + (t % S) * SB + 16384 + lw * NW * 1024; const int so = ((t + rot) & 15) * 64 * 6144;
#pragma unroll
    for (int i = 0; i < NW; ++i) dma<0>(rW, d + i * 1024, voW[i], so); };
  issueA(0); issueW(0); issueA(1); issueW(1);
  dma<0>(rP, lds + L_WPHI + lw * 1024, lane * 16 + lw * 1024, j1 * 4096);
  get_tag(c);
  TS(0);
  u32x2 gv[16]; u32x4 gx[2];
  const int goff = OFF_GC1 + j1 * 16 * 1536 + (lw * 64 + lane) * 8, xoff = OFF_GX + (i1 * 128 + lane) * 16;
  const int tau = ROTMODE == 2 ? 0 : ROTMODE == 1 ? i1 >> 2 : ROTMODE == 3 ? 0 : 0;
  auto post = [&](int t) {
    if (t != tau) return;
    const int sw = (2 * g + (q >> 1)) & 3; u32x2 cr[3][4];
#pragma unroll
    for (int n = 0; n < 3; ++n) { const int nu = 3 * lw + n;
      tr4_issue<16 * 384, 32 * 384>((unsigned)(size_t)(lds + (t % S) * SB + 16384 + (4 * g + q) * 384 + 8 * p + (nu >> 2) * 128 + 32 * ((nu & 3) ^ sw)), cr[n]); }
#pragma unroll
    for (int n = 0; n < 3; ++n) { tr4_wait(cr[n]); const float s = col_sum(cr[n]);
      if (lane < 16) st_granule(c, s, OFF_GC1 + (j1 * 16 + rot + tau) * 1536 + ((3 * lw + n) * 16 + lane) * 8); }
  };
  auto gather = [&](int k) {
    if (lw < 3) { gv[2 * k] = __builtin_amdgcn_raw_buffer_load_b64(rWS, goff + 2 * k * 1536, 0, SC_SC1); gv[2 * k + 1] = __builtin_amdgcn_raw_buffer_load_b64(rWS, goff + (2 * k + 1) * 1536, 0, SC_SC1); }
    else if (k == 0) { gx[0] = __builtin_amdgcn_raw_buffer_load_b128(rWS, xoff, 0, SC_SC1); gx[1] = __builtin_amdgcn_raw_buffer_load_b128(rWS, xoff + 1024, 0, SC_SC1); }
  };
  auto fin = [&]() {
    if (lw < 3) *(LAS float*)(lds + L_TAB1 + 1024 + (lw * 64 + lane) * 4) = sum_granules16(c, gv, goff, 1536);
    else {
      const unsigned long long t0 = __builtin_amdgcn_s_memrealtime();
      for (;;) {
        if (__all(gx[0][1] == c.tag && gx[0][3] == c.tag && gx[1][1] == c.tag && gx[1][3] == c.tag) || timed_out(c, t0, 0x201u)) break;
        __builtin_amdgcn_s_sleep(2);
        gx[0] = __builtin_amdgcn_raw_buffer_load_b128(rWS, xoff, 0, SC_SC1); gx[1] = __builtin_amdgcn_raw_buffer_load_b128(rWS, xoff + 1024, 0, SC_SC1);
      }
#pragma unroll
      for (int e = 0; e < 2; ++e) { *(LAS unsigned*)(lds + L_TAB1 + (lane + 64 * e) * 4) = gx[e][0]; *(LAS unsigned*)(lds + L_TAB1 + 512 + (lane + 64 * e) * 4) = gx[e][2]; }
    }
    wait_lgkm0();
    __builtin_amdgcn_s_waitcnt(0x0f70);
  {
    const int gw = c.b * 4 + lw;
    if (lane < 56) touch(lds, (const char*)A.win + (size_t)(gw * 56 + lane) * 128);
    if (lane < 16) touch(lds, (const char*)A.wo + (size_t)(gw * 16 + lane) * 128);
  }
  };
  loader_loop16<NW, false>(lw < 3, lw == 3, issueA, issueW, post, gather, fin);
  TS(1);
}

struct Acc1 { f32x4 q[2][2], k[2][2], v[2][2]; };
DI void p1_mma(Ctx& c, const Args& A, int i1, int j1, Acc1& e) {
  const int lane = c.lane, wr = c.wid >> 1, wc = c.wid & 1, g = lane >> 4, q = (lane & 15) >> 2, p = lane & 3, i16 = lane & 15;
  const lds_t lds = c.lds;
  const f32x4 Z4 = {0.f, 0.f, 0.f, 0.f};
  constexpr int S = 3, SB = 40960;
  f32x4 aq[4][2], ak[4][2], av[4][2];
#pragma unroll
  for (int m = 0; m < 4; ++m)
#pragma unroll
    for (int n = 0; n < 2; ++n) { aq[m][n] = Z4; ak[m][n] = Z4; av[m][n] = Z4; asm volatile("" : "+v"(aq[m][n]), "+v"(ak[m][n]), "+v"(av[m][n])); }
  const int sw = (2 * g + (q >> 1)) & 3;
  const int ao0 = (wr * 64 + i16) * 128 + ((g ^ (i16 >> 1)) << 4), ao1 = ao0 ^ 64;
  const int wl = 16384 + (4 * g + q) * 384 + 8 * p;
  const int wo0 = wl + 32 * ((2 * wc) ^ sw), wo1 = wl + 32 * ((2 * wc + 1) ^ sw);
  bf16x8 a0[4], w0[6], a1[4], w1[6];
  auto rd = [&](bf16x8(&a)[4], bf16x8(&w)[6], int sb, int ks) { const lds_t s = lds + sb;
#pragma unroll
    for (int sg = 0; sg < 3; ++sg) { w[2 * sg] = ldsT(s + wo0 + sg * 128 + ks * 32 * 384, 16 * 384); w[2 * sg + 1] = ldsT(s + wo1 + sg * 128 + ks * 32 * 384, 16 * 384); }
#pragma unroll
    for (int m = 0; m < 4; ++m) a[m] = ldsA(s + (ks ? ao1 : ao0) + m * 2048); };
  auto mm = [&](bf16x8(&a)[4], bf16x8(&w)[6]) {
#pragma unroll
    for (int n = 0; n < 2; ++n)
#pragma unroll
      for (int m = 0; m < 4; ++m) aq[m][n] = mfma(w[n], a[m], aq[m][n]);
#pragma unroll
    for (int n = 0; n < 2; ++n)
#pragma unroll
      for (int m = 0; m < 4; ++m) ak[m][n] = mfma(w[2 + n], a[m], ak[m][n]);
#pragma unroll
    for (int n = 0; n < 2; ++n)
#pragma unroll
      for (int m = 0; m < 4; ++m) av[m][n] = mfma(a[m], w[4 + n], av[m][n]); };
  TS(0);
  {
    u32x4 xv[2][2];
#pragma unroll
    for (int e2 = 0; e2 < 2; ++e2) { const int R = 8 * j1 + 2 * c.wid + e2; const u16* xr = A.x + (size_t)(i1 * 128 + true_row(R >> 4, R & 15)) * 1024 + lane * 16;
      xv[e2][0] = *(const u32x4*)xr; xv[e2][1] = *(const u32x4*)(xr + 8); }
    get_tag(c);
#pragma unroll
    for (int e2 = 0; e2 < 2; ++e2) { float s = 0.f, qq = 0.f; acc_row(s, qq, xv[e2][0]); acc_row(s, qq, xv[e2][1]);
      s = sum_g(sum_row16(s)) * (1.0f / 1024.0f); qq = sum_g(sum_row16(qq)) * (1.0f / 1024.0f);
      const float rs = __builtin_amdgcn_rsqf(fmaxf(qq - s * s, 0.f) + 1e-5f);
      if (lane == 0) __builtin_amdgcn_raw_buffer_store_b128((u32x4){__float_as_uint(s), c.tag, __float_as_uint(rs), c.tag}, c.ws, OFF_GX + (i1 * 128 + 8 * j1 + 2 * c.wid + e2) * 16, 0, SC_SC1); }
  }
  consumer_loop<S, SB>(c, 0, 16, [&](int sb) { rd(a0, w0, sb, 0); }, [&](int sb) { rd(a1, w1, sb, 1); }, [&]() { mm(a0, w0); }, [&]() { mm(a1, w1); }, []() { interleave<24, 16>(); }, [](int) {}, [&]() { TS(16); });
  TS(1);
  const lds_t xf = lds + L_XF1 + c.wid * 12288 + lane * 16;
#pragma unroll
  for (int m = 0; m < 2; ++m)
#pragma unroll
    for (int n = 0; n < 2; ++n) {
      *(LAS f32x4*)(xf + (m * 6 + n) * 1024) = aq[2 + m][n]; *(LAS f32x4*)(xf + (m * 6 + 2 + n) * 1024) = ak[2 + m][n]; *(LAS f32x4*)(xf + (m * 6 + 4 + n) * 1024) = av[2 + m][n];
      e.q[m][n] = aq[m][n]; e.k[m][n] = ak[m][n]; e.v[m][n] = av[m][n];
    }
  wait_lgkm0();
}

DI void both_g_max(float& a, float& b) {
  auto r = __builtin_amdgcn_permlane16_swap(__float_as_uint(a), __float_as_uint(b), false, false);
  const float x = fmaxf(__uint_as_float(r[0]), __uint_as_float(r[1]));
  auto s = __builtin_amdgcn_permlane32_swap(__float_as_uint(x), __float_as_uint(x), false, false);
  const float y = fmaxf(__uint_as_float(s[0]), __uint_as_float(s[1]));
  auto t = __builtin_amdgcn_permlane16_swap(__float_as_uint(y), __float_as_uint(y), false, false);
  a = __uint_as_float(t[0]); b = __uint_as_float(t[1]);
}
DI void both_g_sum(float& a, float& b) {
  auto r = __builtin_amdgcn_permlane16_swap(__float_as_uint(a), __float_as_uint(b), false, false);
  const float x = __uint_as_float(r[0]) + __uint_as_float(r[1]);
  auto s = __builtin_amdgcn_permlane32_swap(__float_as_uint(x), __float_as_uint(x), false, false);
  const float y = __uint_as_float(s[0]) + __uint_as_float(s[1]);
  auto t = __builtin_amdgcn_permlane16_swap(__float_as_uint(y), __float_as_uint(y), false, false);
  a = __uint_as_float(t[0]); b = __uint_as_float(t[1]);
}
DI void feat(const f32x4& p0, const f32x4& p1, f32x4& e0, f32x4& e1, f32x4& n0, f32x4& n1) {
  constexpr float L2E = 1.4426950408889634f;
  const f32x4 t0 = p0 * L2E, t1 = p1 * L2E;
  float mx = fmaxf(fmaxf(fmaxf(t0[0], t0[1]), t0[2]), fmaxf(fmaxf(fmaxf(t0[3], t1[0]), t1[1]), fmaxf(t1[2], t1[3])));
  float mn = -fminf(fminf(fminf(t0[0], t0[1]), t0[2]), fminf(fminf(fminf(t0[3], t1[0]), t1[1]), fminf(t1[2], t1[3])));
  both_g_max(mx, mn);
  const f32x4 a0 = t0 - mx, a1 = t1 - mx, b0 = -t0 - mn, b1 = -t1 - mn;
#pragma unroll
  for (int r = 0; r < 4; ++r) { e0[r] = __builtin_amdgcn_exp2f(a0[r]); e1[r] = __builtin_amdgcn_exp2f(a1[r]); n0[r] = __builtin_amdgcn_exp2f(b0[r]); n1[r] = __builtin_amdgcn_exp2f(b1[r]); }
  const f32x4 se = e0 + e1, sn4 = n0 + n1;
  float sp = (se[0] + se[1]) + (se[2] + se[3]), sn = (sn4[0] + sn4[1]) + (sn4[2] + sn4[3]);
  both_g_sum(sp, sn);
  sp = __builtin_amdgcn_rcpf(sp); sn = __builtin_amdgcn_rcpf(sn);
  e0 *= sp; e1 *= sp; n0 *= sn; n1 *= sn;
}

DI void p1_epilogue(Ctx& c, const Args& A, const Map& M, Acc1& e, int last) {
  const int lane = opaque(c.lane), cw = c.wid & 3, hf = c.wid >> 2, wr = cw >> 1, wc = cw & 1, g = lane >> 4, q = (lane & 15) >> 2, p = lane & 3, i16 = lane & 15;
  const int lw = cw, i1 = M.i1, j1 = M.j1;
  const lds_t lds = c.lds;
  const f32x4 Z4 = {0.f, 0.f, 0.f, 0.f};
  bar();
  TS(20);
  if (hf) { const lds_t xf = lds + L_XF1 + cw * 12288 + lane * 16;
#pragma unroll
    for (int m = 0; m < 2; ++m)
#pragma unroll
      for (int n = 0; n < 2; ++n) { e.q[m][n] = *(LAS f32x4*)(xf + (m * 6 + n) * 1024); e.k[m][n] = *(LAS f32x4*)(xf + (m * 6 + 2 + n) * 1024); e.v[m][n] = *(LAS f32x4*)(xf + (m * 6 + 4 + n) * 1024); } }
  const lds_t tab = lds + L_TAB1;
  bf16x8 qf[2], kf[2], vf[2], wT[2];
  {
    f32x4 vv[2][2];
#pragma unroll
    for (int m = 0; m < 2; ++m) {
      const int R = 64 * wr + 32 * hf + 16 * m;
      const float mu = *(LAS float*)(tab + (R + i16) * 4), rs = *(LAS float*)(tab + 512 + (R + i16) * 4), nm = -mu * rs;
      const f32x4 mu4 = *(LAS f32x4*)(tab + (R + 4 * g) * 4), rs4 = *(LAS f32x4*)(tab + 512 + (R + 4 * g) * 4), nm4 = -mu4 * rs4;
      f32x4 qq[2], kk[2];
#pragma unroll
      for (int n = 0; n < 2; ++n) {
        const f32x4 cq = *(LAS f32x4*)(tab + 1024 + (32 * wc + 16 * n + 4 * g) * 4), ck = *(LAS f32x4*)(tab + 1024 + (64 + 32 * wc + 16 * n + 4 * g) * 4);
        const float cv = *(LAS float*)(tab + 1024 + (128 + 32 * wc + 16 * n + i16) * 4);
        qq[n] = e.q[m][n] * rs + cq * nm; kk[n] = e.k[m][n] * rs + ck * nm; vv[m][n] = e.v[m][n] * rs4 + nm4 * cv;
      }
      qf[m] = pk8(qq[0], qq[1]); kf[m] = pk8(kk[0], kk[1]);
    }
    vf[0] = pk8(vv[0][0], vv[1][0]); vf[1] = pk8(vv[0][1], vv[1][1]);
#pragma unroll
    for (int ft = 0; ft < 2; ++ft) wT[ft] = ldsT(lds + L_WPHI + wc * 2048 + (4 * g + q) * 64 + ft * 32 + 8 * p, 16 * 64);
  }
  wait_lgkm0(); bar();
  TS(21);
  {
    const lds_t tb = lds + L_TB + c.wid * 4096;
#pragma unroll
    for (int m = 0; m < 2; ++m) {
      const f32x4 p0 = mfma(wT[0], kf[m], Z4), p1 = mfma(wT[1], kf[m], Z4);
      f32x4 e0, e1, n0, n1; feat(p0, p1, e0, e1, n0, n1);
      const lds_t rw = tb + (16 * m + i16) * 128;
      *(LAS u32x2*)(rw + 8 * ((0 + g) ^ i16)) = pk4(e0); *(LAS u32x2*)(rw + 8 * ((4 + g) ^ i16)) = pk4(e1);
      *(LAS u32x2*)(rw + 8 * ((8 + g) ^ i16)) = pk4(n0); *(LAS u32x2*)(rw + 8 * ((12 + g) ^ i16)) = pk4(n1);
    }
    asm volatile("s_waitcnt lgkmcnt(0)" ::: "memory");
    const bf16x8 ones = __builtin_bit_cast(bf16x8, (u32x4){ONES2, ONES2, ONES2, ONES2});
    const lds_t kr = lds + L_KR + c.wid * 8192 + lane * 16;
    float zf[4];
#pragma unroll
    for (int f = 0; f < 4; ++f) {
      const int r0 = 4 * g + q;
      const s16x4 lo = __builtin_amdgcn_ds_read_tr16_b64_v4i16((LAS s16x4*)(tb + r0 * 128 + 8 * ((4 * f + p) ^ r0)));
      const s16x4 hi = __builtin_amdgcn_ds_read_tr16_b64_v4i16((LAS s16x4*)(tb + (16 + r0) * 128 + 8 * ((4 * f + p) ^ r0)));
      const bf16x8 pf = __builtin_shufflevector(lo, hi, 0, 1, 2, 3, 4, 5, 6, 7);
      *(LAS f32x4*)(kr + f * 1024) = mfma(vf[0], pf, Z4); *(LAS f32x4*)(kr + (4 + f) * 1024) = mfma(vf[1], pf, Z4);
      zf[f] = mfma(ones, pf, Z4)[0];
    }
    *(LAS float*)(lds + L_ZR + c.wid * 256 + lane * 4) = g == 0 ? zf[0] : g == 1 ? zf[1] : g == 2 ? zf[2] : zf[3];
    TS(22);
    wait_lgkm0(); bar();
    f32x4 k0 = Z4, k1 = Z4;
#pragma unroll
    for (int w = 0; w < 4; ++w) { const lds_t s = lds + L_KR + (wc + 2 * w) * 8192 + (4 * wr + 2 * hf) * 1024 + lane * 16; k0 += *(LAS f32x4*)s; k1 += *(LAS f32x4*)(s + 1024); }
    st16(c, pk8(k0, k1), lane * 16 + (OFF_KVP + (j1 * 16 + i1) * 8192 + wc * 4096 + wr * 2048 + hf * 1024));
    if (c.wid < 2) { float zt = 0.f;
#pragma unroll
      for (int w = 0; w < 4; ++w) zt += *(LAS float*)(lds + L_ZR + (wc + 2 * w) * 256 + lane * 4);
      __builtin_amdgcn_raw_buffer_store_b32(__float_as_uint(zt), c.ws, lane * 4, OFF_ZP + ((j1 * 16 + i1) * 2 + wc) * 256, SC_SC1); }
    wait_lgkm0(); bar();
    TS(23);
    publish<8>(c, 0, FG_KV, j1, i1);
  }
  TS(2);
  const __amdgpu_buffer_rsrc_t rW2 = __builtin_amdgcn_make_buffer_rsrc((void*)A.wo, 0, 1024 * 2048, 0x00020000);
  int voW2[2];
#pragma unroll
  for (int i = 0; i < 2; ++i) { const int r = (2 * lw + i) * 8 + (lane >> 3), s16 = lane & 7; voW2[i] = r * 2048 + M.j2 * 128 + (((s16 >> 1) ^ ((r >> 1) & 3)) << 5) + (s16 & 1) * 16; }
  auto issueW2 = [&](int t) {
#pragma unroll
    for (int i = 0; i < 2; ++i) dma<0>(rW2, lds + t * 8192 + (lw * 2 + i) * 1024, voW2[i], t * 64 * 2048); };
  if (hf) {
    if (M.act3) {
      const __amdgpu_buffer_rsrc_t rW3 = __builtin_amdgcn_make_buffer_rsrc((void*)A.win, 0, 1024 * 7168, 0x00020000);
#pragma unroll
      for (int i = 0; i < 8; ++i) { const int r = (8 * lw + i) * 2 + (lane >> 5), l5 = lane & 31, seg = l5 >> 4, s16 = l5 & 15;
        dma<0>(rW3, lds + 32768 + (8 * lw + i) * 1024, r * 7168 + (seg == 0 ? 3584 : 0) + M.j3 * 256 + (((s16 >> 1) ^ (r & 7)) << 5) + (s16 & 1) * 16, M.i3 * 64 * 7168); }
    }
#pragma unroll
    for (int t = 0; t < 3; ++t) { issueW2(t); issueW2(8 + t); }
  }
  bf16x8 ph[2][2];
#pragma unroll
  for (int m = 0; m < 2; ++m) {
    const f32x4 p0 = mfma(wT[0], qf[m], Z4), p1 = mfma(wT[1], qf[m], Z4);
    f32x4 e0, e1, n0, n1; feat(p0, p1, e0, e1, n0, n1);
    ph[m][0] = pk8(e0, e1); ph[m][1] = pk8(n0, n1);
  }
  asm volatile("" : "+v"(ph[0][0]), "+v"(ph[0][1]), "+v"(ph[1][0]), "+v"(ph[1][1]));
  TS(3);
  kv_gather(c, j1);
  bar();
  TS(4);
  {
    bf16x8 kt[3][2];
#pragma unroll
    for (int d = 0; d < 3; ++d)
#pragma unroll
      for (int s = 0; s < 2; ++s) kt[d][s] = ldsT(lds + L_XBUF + wc * 8192 + (32 * s + 4 * g + q) * 128 + 32 * d + 8 * p, 16 * 128);
#pragma unroll
    for (int m = 0; m < 2; ++m) {
      const f32x4 n0 = mfma(kt[0][1], ph[m][1], mfma(kt[0][0], ph[m][0], Z4)), n1 = mfma(kt[1][1], ph[m][1], mfma(kt[1][0], ph[m][0], Z4));
      const f32x4 dn = mfma(kt[2][1], ph[m][1], mfma(kt[2][0], ph[m][0], Z4));
      const float r = __builtin_amdgcn_rcpf(dn[0] + 1e-6f);
      st16(c, pk8(n0 * r, n1 * r), lane * 16 + (OFF_OF + (i1 * 16 + j1) * 16384 + ((4 * wr + 2 * hf + m) * 2 + wc) * 1024));
    }
  }
  publish<8>(c, 1, FG_O, i1, j1);
  TS(5);
  if (hf && M.act3) {
    u32x2 cr[4][4];
#pragma unroll
    for (int n = 0; n < 4; ++n) tr4_issue<16 * 512, 32 * 512>((unsigned)(size_t)(lds + 32768 + (4 * g + q) * 512 + 8 * p + (lw >> 1) * 256 + 32 * ((4 * (lw & 1) + n) ^ (4 * (g & 1) + q))), cr[n]);
#pragma unroll
    for (int n = 0; n < 4; ++n) { tr4_wait(cr[n]); const float s = col_sum(cr[n]);
      if (lane < 16) st_granule(c, s, OFF_GC3 + (M.j3 * 16 + M.i3) * 2048 + ((4 * lw + n) * 16 + lane) * 8); }
  }
  if (hf) {
    issueW2(3);
#pragma unroll
    for (int t = 11; t < 16; ++t) issueW2(t);
  } else if (c.wid == 0 && last >= 2) wait_flags(c, FG_O, M.i2, 16);
}

struct Ld3 { __amdgpu_buffer_rsrc_t rW; int voW[8]; };
DI void p3_setup(const Ctx& c, const Args& A, int j3, Ld3& L) {
  const int lane = opaque(c.lane), lw = c.wid & 3;
  L.rW = __builtin_amdgcn_make_buffer_rsrc((void*)A.win, 0, 1024 * 7168, 0x00020000);
#pragma unroll
  for (int i = 0; i < 8; ++i) { const int r = (8 * lw + i) * 2 + (lane >> 5), l5 = lane & 31, seg = l5 >> 4, s16 = l5 & 15;
    L.voW[i] = r * 7168 + (seg == 0 ? 3584 : 0) + j3 * 256 + (((s16 >> 1) ^ (r & 7)) << 5) + (s16 & 1) * 16; }
}
DI void p3_issueW(const Ctx& c, const Ld3& L, int t) { const lds_t d = c.lds + (t % 3) * 49152 + 16384 + (c.wid & 3) * 8192;
#pragma unroll
  for (int i = 0; i < 8; ++i) dma<0>(L.rW, d + i * 1024, L.voW[i], t * 64 * 7168); }
DI void p3_load(Ctx& c, const Ld3& L, int i3) {
  const int lane = opaque(c.lane), lw = c.wid - 4;
  const lds_t lds = c.lds;
  constexpr int S = 3, SB = 49152;
  const int voF = lane * 16 + lw * 1024;
  auto issueA = [&](int t) { const lds_t d = lds + (t % S) * SB + lw * 1024; const int so = OFF_HF + (i3 * 16 + t) * 16384;
#pragma unroll
    for (int i = 0; i < 4; ++i) dma<LDAUX>(c.ws, d + i * 4096, voF, so + i * 4096); };
  auto issueW = [&](int t) { p3_issueW(c, L, t); };
  bar();
  issueA(0); issueA(1);
  u32x2 hv[16]; const int R = (lw & 1) * 64 + lane;
  auto gather = [&](int k) { if (lw < 2) {
      hv[2 * k] = __builtin_amdgcn_raw_buffer_load_b64(c.ws, R * 8, OFF_STH + (i3 * 16 + 2 * k) * 1024, SC_SC1);
      hv[2 * k + 1] = __builtin_amdgcn_raw_buffer_load_b64(c.ws, R * 8, OFF_STH + (i3 * 16 + 2 * k + 1) * 1024, SC_SC1); } };
  auto fin = [&]() { if (lw < 2) { float s = 0.f, qq = 0.f;
#pragma unroll
      for (int k = 0; k < 16; ++k) { s += __uint_as_float(hv[k][0]); qq += __uint_as_float(hv[k][1]); }
      s *= (1.0f / 1024.0f); qq *= (1.0f / 1024.0f);
      *(LAS float*)(lds + L_TAB3 + R * 4) = s; *(LAS float*)(lds + L_TAB3 + 512 + R * 4) = __builtin_amdgcn_rsqf(fmaxf(qq - s * s, 0.f) + 1e-5f);
      wait_lgkm0(); } };
  loader_loop16<8, true>(lw < 2, false, issueA, issueW, [](int) {}, gather, fin);
  TS(6);
}
struct Acc3 { f32x4 z2[2][4], z1[2][4]; };
DI int xf3_base(int cw) { return cw < 3 ? 16384 + cw * 32768 : 98304; }
DI void p3_mma(Ctx& c, int i3, int j3, Acc3& e) {
  const int lane = opaque(c.lane), wr = c.wid >> 1, wc = c.wid & 1, g = lane >> 4, q = (lane & 15) >> 2, p = lane & 3;
  const lds_t lds = c.lds;
  const __amdgpu_buffer_rsrc_t rWS = c.ws;
  const f32x4 Z4 = {0.f, 0.f, 0.f, 0.f};
  constexpr int S = 3, SB = 49152;
  if (wc == 1) {
    const int off = OFF_GC3 + j3 * 16 * 2048 + (128 * wr + 2 * lane) * 8; u32x4 gv[16];
    const unsigned long long t0 = __builtin_amdgcn_s_memrealtime();
    for (;;) {
      bool ok = true;
#pragma unroll
      for (int k = 0; k < 16; ++k) gv[k] = __builtin_amdgcn_raw_buffer_load_b128(rWS, off + k * 2048, 0, SC_SC1);
#pragma unroll
      for (int k = 0; k < 16; ++k) ok &= gv[k][1] == c.tag && gv[k][3] == c.tag;
      if (__all(ok) || timed_out(c, t0, 0x202u)) break;
      __builtin_amdgcn_s_sleep(2);
    }
    f32x2 cs = {0.f, 0.f};
#pragma unroll
    for (int k = 0; k < 16; ++k) { cs[0] += __uint_as_float(gv[k][0]); cs[1] += __uint_as_float(gv[k][2]); }
    *(LAS f32x2*)(lds + L_TAB3 + 1024 + (128 * wr + 2 * lane) * 4) = cs;
    wait_lgkm0();
  } else if (c.wid == 0) wait_flags(c, FG_H, i3, 16);
  f32x4 a2[4][4], a1_[4][4];
#pragma unroll
  for (int m = 0; m < 4; ++m)
#pragma unroll
    for (int n = 0; n < 4; ++n) { a2[m][n] = Z4; a1_[m][n] = Z4; }
#pragma unroll
  for (int m = 0; m < 4; ++m) asm volatile("" : "+v"(a2[m][0]), "+v"(a2[m][1]), "+v"(a2[m][2]), "+v"(a2[m][3]), "+v"(a1_[m][0]), "+v"(a1_[m][1]), "+v"(a1_[m][2]), "+v"(a1_[m][3]));
  bar();
  const int ao2 = wr * 8192 + lane * 16;
  int wo_[4];
#pragma unroll
  for (int n = 0; n < 4; ++n) wo_[n] = 16384 + (4 * g + q) * 512 + 8 * p + 32 * ((4 * wc + n) ^ (4 * (g & 1) + q));
  bf16x8 a0[4], w0[8], a1[4], w1[8];
  auto rd = [&](bf16x8(&a)[4], bf16x8(&w)[8], int sb, int ks) { const lds_t s = lds + sb;
#pragma unroll
    for (int n = 0; n < 8; ++n) w[n] = ldsT(s + wo_[n & 3] + (n >> 2) * 256 + ks * 32 * 512, 16 * 512);
#pragma unroll
    for (int m = 0; m < 4; ++m) a[m] = ldsA(s + ao2 + m * 2048 + ks * 1024); };
  auto mm = [&](bf16x8(&a)[4], bf16x8(&w)[8]) {
#pragma unroll
    for (int n = 0; n < 4; ++n)
#pragma unroll
      for (int m = 0; m < 4; ++m) a2[m][n] = mfma(a[m], w[n], a2[m][n]);
#pragma unroll
    for (int n = 0; n < 4; ++n)
#pragma unroll
      for (int m = 0; m < 4; ++m) a1_[m][n] = mfma(w[4 + n], a[m], a1_[m][n]); };
  consumer_loop<S, SB>(c, 16, 16, [&](int sb) { rd(a0, w0, sb, 0); }, [&](int sb) { rd(a1, w1, sb, 1); }, [&]() { mm(a0, w0); }, [&]() { mm(a1, w1); }, []() { interleave<32, 20>(); }, [](int) {}, [&]() { TS(17); });
  TS(8);
  const lds_t xf = lds + xf3_base(c.wid) + lane * 16;
#pragma unroll
  for (int m = 0; m < 2; ++m)
#pragma unroll
    for (int n = 0; n < 4; ++n) { *(LAS f32x4*)(xf + (m * 8 + n) * 1024) = a2[2 + m][n]; *(LAS f32x4*)(xf + (m * 8 + 4 + n) * 1024) = a1_[2 + m][n]; e.z2[m][n] = a2[m][n]; e.z1[m][n] = a1_[m][n]; }
  wait_lgkm0();
}
struct Ld4 { __amdgpu_buffer_rsrc_t rA; int voA, voF; };
DI void p4_issueW(const Ctx& c, const Ld4& L, int t) { const int lw = c.wid & 3; const lds_t d = c.lds + (t & 3) * 32768 + lw * 1024;
#pragma unroll
  for (int i = 0; i < 4; ++i) dma<0>(L.rA, d + i * 4096, L.voA, t * 128 + i * 32 * 4096); }
DI void p3_epilogue(Ctx& c, const Ld4& L, int i3, int j3, Acc3& e, u32x2 (&z1)[2][4]) {
  const int lane = opaque(c.lane), cw = c.wid & 3, hf = c.wid >> 2, wr = cw >> 1, wc = cw & 1, g = lane >> 4, i16 = lane & 15;
  const lds_t lds = c.lds, tab = lds + L_TAB3;
  const __amdgpu_buffer_rsrc_t rWS = c.ws;
  const f32x4 Z4 = {0.f, 0.f, 0.f, 0.f};
  bar();
  TS(25);
  __builtin_amdgcn_s_setprio(1);
  if (hf) { const lds_t xf = lds + xf3_base(cw) + lane * 16;
#pragma unroll
    for (int m = 0; m < 2; ++m)
#pragma unroll
      for (int n = 0; n < 4; ++n) { e.z2[m][n] = *(LAS f32x4*)(xf + (m * 8 + n) * 1024); e.z1[m][n] = *(LAS f32x4*)(xf + (m * 8 + 4 + n) * 1024); } }
  {
    float cs[4];
#pragma unroll
    for (int n = 0; n < 4; ++n) cs[n] = *(LAS float*)(tab + 1024 + (64 * wc + 16 * n + i16) * 4);
#pragma unroll
    for (int m = 0; m < 2; ++m) {
      const int R = 64 * wr + 32 * hf + 16 * m + 4 * g;
      const f32x4 mu = *(LAS f32x4*)(tab + R * 4), rs = *(LAS f32x4*)(tab + 512 + R * 4), nm = -mu * rs;
      f32x4 s = Z4, qq = Z4;
#pragma unroll
      for (int n = 0; n < 4; ++n) { const f32x4 y = gelu4(e.z2[m][n] * rs + nm * cs[n]); e.z2[m][n] = y; s += y; qq += y * y; }
      sum_row16x8(s, qq);
      if (i16 < 4) { const float sv = i16 == 0 ? s[0] : i16 == 1 ? s[1] : i16 == 2 ? s[2] : s[3], qv = i16 == 0 ? qq[0] : i16 == 1 ? qq[1] : i16 == 2 ? qq[2] : qq[3];
        __builtin_amdgcn_raw_buffer_store_b128((u32x4){__float_as_uint(sv), c.tag, __float_as_uint(qv), c.tag}, rWS, OFF_ST2 + ((i3 * 14 + j3) * 2 + wc) * 2048 + (R + i16) * 16, 0, SC_SC1); }
    }
  }
  TS(9);
  __builtin_amdgcn_s_setprio(0);
  if (!hf) {
#pragma unroll
    for (int s = 0; s < 3; ++s) p4_issueW(c, L, s);
  }
  {
    const int R0 = 64 * wr + 32 * hf, off = OFF_ST2 + (i3 * 28 + (lane >> 5)) * 2048 + (R0 + (lane & 31)) * 16;
    u32x4 v[14];
    const unsigned long long t0 = __builtin_amdgcn_s_memrealtime();
    for (;;) {
      bool ok = true;
#pragma unroll
      for (int k = 0; k < 14; ++k) v[k] = __builtin_amdgcn_raw_buffer_load_b128(rWS, off + k * 4096, 0, SC_SC1);
#pragma unroll
      for (int k = 0; k < 14; ++k) ok &= v[k][1] == c.tag && v[k][3] == c.tag;
      if (__all(ok) || timed_out(c, t0, 0x203u)) break;
      __builtin_amdgcn_s_sleep(PSLEEP);
    }
    __builtin_amdgcn_s_setprio(1);
    float s = 0.f, qq = 0.f;
#pragma unroll
    for (int k = 0; k < 14; ++k) { s += __uint_as_float(v[k][0]); qq += __uint_as_float(v[k][2]); }
    { auto a = __builtin_amdgcn_permlane32_swap(__float_as_uint(s), __float_as_uint(s), false, false); s = (__uint_as_float(a[0]) + __uint_as_float(a[1])) * (1.0f / 1792.0f);
      auto b = __builtin_amdgcn_permlane32_swap(__float_as_uint(qq), __float_as_uint(qq), false, false); qq = (__uint_as_float(b[0]) + __uint_as_float(b[1])) * (1.0f / 1792.0f); }
    const float rsv = __builtin_amdgcn_rsqf(fmaxf(qq - s * s, 0.f) + 1e-5f);
    const lds_t ft = lds + L_FIN + c.wid * 256;
    if (lane < 32) { *(LAS float*)(ft + lane * 4) = rsv; *(LAS float*)(ft + 128 + lane * 4) = -s * rsv; }
    asm volatile("s_waitcnt lgkmcnt(0)" ::: "memory");
    TS(31);
    f32x4 rs[2], nm[2];
#pragma unroll
    for (int m = 0; m < 2; ++m) { rs[m] = *(LAS f32x4*)(ft + (16 * m + 4 * g) * 4); nm[m] = *(LAS f32x4*)(ft + 128 + (16 * m + 4 * g) * 4); }
#pragma unroll
    for (int n = 0; n < 4; ++n)
      st16(c, pk8(e.z2[0][n] * rs[0] + nm[0], e.z2[1][n] * rs[1] + nm[1]), lane * 16 + (OFF_Z2 + (j3 * 32 + 2 * i3 + wr) * 16384 + ((4 * wc + n) * 2 + hf) * 1024));
  }
  __builtin_amdgcn_s_setprio(0);
  publish<8>(c, 4, FG_Z2, j3, i3);
  TS(11);
#pragma unroll
  for (int m = 0; m < 2; ++m) asm volatile("" : "+v"(e.z1[m][0]), "+v"(e.z1[m][1]), "+v"(e.z1[m][2]), "+v"(e.z1[m][3]));
#pragma unroll
  for (int m = 0; m < 2; ++m) {
    const int R = 64 * wr + 32 * hf + 16 * m + i16;
    const float mu = *(LAS float*)(tab + R * 4), rs = *(LAS float*)(tab + 512 + R * 4), nm = -mu * rs;
#pragma unroll
    for (int n = 0; n < 4; ++n) { const f32x4 cs = *(LAS f32x4*)(tab + 1024 + (128 + 64 * wc + 16 * n + 4 * g) * 4); z1[m][n] = pk4(gelu4(e.z1[m][n] * rs + cs * nm)); }
    asm volatile("" : "+v"(z1[m][0]), "+v"(z1[m][1]), "+v"(z1[m][2]), "+v"(z1[m][3]));
  }
  TS(10);
}
DI void p4_load(Ctx& c, const Ld4& L, int j3) {
  const int lw = c.wid - 4;
  auto issueW = [&](int t) { p4_issueW(c, L, t); };
  auto issueA = [&](int t) { const lds_t d = c.lds + (t & 3) * 32768 + 16384 + lw * 1024; const int so = OFF_Z2 + (j3 * 32 + t) * 16384;
#pragma unroll
    for (int i = 0; i < 4; ++i) dma<LDAUX>(c.ws, d + i * 4096, L.voF, so + i * 4096); };
  bar();
  if (c.b == 0 && lw == 0 && c.lane == 0) __hip_atomic_store((unsigned*)(c.wsp + OFF_EPOCH), c.tag, __ATOMIC_RELAXED, __HIP_MEMORY_SCOPE_AGENT);
#pragma unroll
  for (int s = 0; s < 3; ++s) issueA(s);
  loader_loop<4, 4>(32, true, issueA, issueW, [](int) {}, [](int) {});
}
DI void p4_mma(Ctx& c, int j3, f32x4 (&e)[2][4]) {
  const int lane = opaque(c.lane), wr = c.wid >> 1, wc = c.wid & 1, g = lane >> 4, i16 = lane & 15;
  const lds_t lds = c.lds;
  constexpr int S = 4, SB = 32768;
  f32x4 acc[4][4];
#pragma unroll
  for (int m = 0; m < 4; ++m)
#pragma unroll
    for (int n = 0; n < 4; ++n) { acc[m][n] = (f32x4){0.f, 0.f, 0.f, 0.f}; asm volatile("" : "+v"(acc[m][n])); }
  const int ao0 = (wr * 64 + i16) * 128 + ((g ^ (i16 >> 1)) << 4), ao1 = ao0 ^ 64, zo = 16384 + wc * 8192 + lane * 16;
  bf16x8 a0[4], w0[4], a1[4], w1[4];
  auto rd = [&](bf16x8(&a)[4], bf16x8(&w)[4], int sb, int ks) { const lds_t s = lds + sb;
#pragma unroll
    for (int n = 0; n < 4; ++n) { w[n] = ldsA(s + zo + n * 2048 + ks * 1024); a[n] = ldsA(s + (ks ? ao1 : ao0) + n * 2048); } };
  auto mm = [&](bf16x8(&a)[4], bf16x8(&w)[4]) {
#pragma unroll
    for (int n = 0; n < 4; ++n)
#pragma unroll
      for (int m = 0; m < 4; ++m) acc[m][n] = mfma(w[n], a[m], acc[m][n]); };
  if (c.wid == 0) wait_flags(c, FG_Z2, j3, 16);
  bar();
  consumer_loop<S, SB>(c, 32, 32, [&](int sb) { rd(a0, w0, sb, 0); }, [&](int sb) { rd(a1, w1, sb, 1); }, [&]() { mm(a0, w0); }, [&]() { mm(a1, w1); }, []() { interleave<16, 8>(); }, [](int) {}, [&]() { TS(18); });
  TS(12);
  const lds_t xf = lds + c.wid * 8192 + lane * 16;
#pragma unroll
  for (int m = 0; m < 2; ++m)
#pragma unroll
    for (int n = 0; n < 4; ++n) { *(LAS f32x4*)(xf + (m * 4 + n) * 1024) = acc[2 + m][n]; e[m][n] = acc[m][n]; }
  wait_lgkm0();
}
DI void p5_issueW(const Ctx& c, const Args& A, int j2);
DI void p4_epilogue(Ctx& c, const Args& A, int i3, int j3, int j2, f32x4 (&e)[2][4], const u32x2 (&z1)[2][4]) {
  const int lane = opaque(c.lane), cw = c.wid & 3, hf = c.wid >> 2, wr = cw >> 1, wc = cw & 1, i16 = lane & 15;
  float bs[2];
#pragma unroll
  for (int m = 0; m < 2; ++m) bs[m] = bflo((unsigned)A.bsp[i3 * 128 + true_row(4 * wr + 2 * hf + m, i16)]);
  bar();
  if (hf) { const lds_t xf = c.lds + cw * 8192 + lane * 16;
#pragma unroll
    for (int m = 0; m < 2; ++m)
#pragma unroll
      for (int n = 0; n < 4; ++n) e[m][n] = *(LAS f32x4*)(xf + (m * 4 + n) * 1024); }
#pragma unroll
  for (int m = 0; m < 2; ++m)
#pragma unroll
    for (int u = 0; u < 2; ++u)
      st16(c, pk8(up4(z1[m][2 * u]) * (e[m][2 * u] + bs[m]), up4(z1[m][2 * u + 1]) * (e[m][2 * u + 1] + bs[m])),
           lane * 16 + (OFF_UF + (i3 * 28 + 2 * j3 + wc) * 16384 + ((4 * wr + 2 * hf + m) * 2 + u) * 1024));
  wait_lgkm0(); bar();
  publish<8>(c, 5, FG_U, i3, j3);
  if (hf) p5_issueW(c, A, j2);
  TS(13);
}

DI void p5_issueW(const Ctx& c, const Args& A, int j2) {
  const int lane = opaque(c.lane), lw = c.wid - 4;
  const __amdgpu_buffer_rsrc_t rW = __builtin_amdgcn_make_buffer_rsrc((void*)A.wout, 0, 1792 * 2048, 0x00020000);
  int vo[2];
#pragma unroll
  for (int i = 0; i < 2; ++i) { const int r = (2 * lw + i) * 8 + (lane >> 3), s16 = lane & 7; vo[i] = r * 2048 + j2 * 128 + (((s16 >> 1) ^ ((r >> 1) & 3)) << 5) + (s16 & 1) * 16; }
#pragma unroll
  for (int t = 0; t < 18; ++t)
#pragma unroll
    for (int i = 0; i < 2; ++i) dma<0>(rW, c.lds + ((t & 1) * 9 + (t >> 1)) * 8192 + (lw * 2 + i) * 1024, vo[i], ((t & 1) * 14 + (t >> 1)) * 64 * 2048);
}

constexpr int L_RED = 98304;
#define STG1 0
#define STG2 2
#define R5 6
#define R2 6
template <int NSH, int R, bool REFILL>
DI void gemm_split(Ctx& c, f32x4 (&res)[4], int aoff, __amdgpu_buffer_rsrc_t rW, int j2) {
  const int ln = opaque(c.lane), g = ln >> 4, q = (ln & 15) >> 2, p = ln & 3, rw = c.wid & 3, kh = c.wid >> 2;
  const lds_t lds = c.lds;
  const int sw2 = (2 * g + (q >> 1)) & 3, wl2 = (4 * g + q) * 128 + 8 * p + kh * (REFILL ? 9 : 8) * 8192, va = ln * 16 + rw * 4096;
  const int ao = aoff + kh * (NSH / 2) * 16384;
  int wo2[4];
#pragma unroll
  for (int n = 0; n < 4; ++n) wo2[n] = wl2 + 32 * (n ^ sw2);
  f32x4 acc[2][4];
#pragma unroll
  for (int n = 0; n < 4; ++n) { acc[0][n] = (f32x4){0.f, 0.f, 0.f, 0.f}; acc[1][n] = acc[0][n]; }
  u32x4 a[R][2]; bf16x8 w0[4], w1[4];
  auto lda = [&](int s) {
#pragma unroll
    for (int m = 0; m < 2; ++m) a[s % R][m] = __builtin_amdgcn_raw_buffer_load_b128(c.ws, va + m * 2048, ao + (s >> 1) * 16384 + (s & 1) * 1024, LDAUX); };
  auto ldw = [&](bf16x8(&d)[4], int s) { const int lt = s >> 1, slot = (REFILL && lt >= 9) ? lt - 9 : lt;
#pragma unroll
    for (int n = 0; n < 4; ++n) d[n] = ldsT(lds + wo2[n] + slot * 8192 + (s & 1) * 4096, 16 * 128); };
  TS(REFILL ? 19 : 24);
#pragma unroll
  for (int s = 0; s < R; ++s) { lda(s); if (s == STG1 || s == STG2) bar(); }
  if (!REFILL && kh) {
#pragma unroll
    for (int i = 0; i < 2; ++i) { const int r = (2 * rw + i) * 8 + (ln >> 3), s16 = ln & 7, vo = r * 2048 + j2 * 128 + (((s16 >> 1) ^ ((r >> 1) & 3)) << 5) + (s16 & 1) * 16;
#pragma unroll
      for (int t = 4; t < 8; ++t) dma<0>(rW, lds + t * 8192 + (rw * 2 + i) * 1024, vo, t * 64 * 2048); }
  }
  ldw(w0, 0);
#pragma unroll
  for (int s = 0; s < NSH; ++s) {
    if (s == 5) {
      if (kh) { if (REFILL) wait_vm<2 * R + 10>(); else wait_vm<10>(); }
      bar(); __builtin_amdgcn_sched_barrier(0);
    }
    if (REFILL && s == 10) {
      wait_lgkm0(); bar();
      const int r = 8 * c.wid + (ln >> 3), s16 = ln & 7, vo = r * 2048 + j2 * 128 + (((s16 >> 1) ^ ((r >> 1) & 3)) << 5) + (s16 & 1) * 16;
#pragma unroll
      for (int k = 0; k < 10; ++k) dma<0>(rW, lds + ((k / 5) * 9 + (k % 5)) * 8192 + c.wid * 1024, vo, ((k / 5) * 14 + 9 + (k % 5)) * 64 * 2048);
      __builtin_amdgcn_sched_barrier(0);
    }
    if (REFILL && s == 17) { wait_vm<2 * ((NSH - R < 17 ? NSH - R : 17) - 10)>(); wait_lgkm0(); bar(); __builtin_amdgcn_sched_barrier(0); }
    if (s + 1 < NSH) { if (s & 1) ldw(w0, s + 1); else ldw(w1, s + 1); }
#pragma unroll
    for (int n = 0; n < 4; ++n)
#pragma unroll
      for (int m = 0; m < 2; ++m) acc[m][n] = mfma((s & 1) ? w1[n] : w0[n], __builtin_bit_cast(bf16x8, a[s % R][m]), acc[m][n]);
    if (s + R < NSH) lda(s + R);
    interleave<8, 8>();
    __builtin_amdgcn_sched_barrier(0);
  }
  TS(REFILL ? 29 : 28);
  wait_lgkm0(); bar();
#pragma unroll
  for (int n = 0; n < 4; ++n) *(LAS f32x4*)(lds + L_RED + c.wid * 4096 + n * 1024 + ln * 16) = kh ? acc[0][n] : acc[1][n];
  wait_lgkm0(); bar();
#pragma unroll
  for (int n = 0; n < 4; ++n) res[n] = (kh ? acc[1][n] : acc[0][n]) + *(LAS f32x4*)(lds + L_RED + (c.wid ^ 4) * 4096 + n * 1024 + ln * 16);
}

template <class PRE> DI void phase2(Ctx& c, const Args& A, int i2, int j2, bool pre_on, PRE pre) {
  const __amdgpu_buffer_rsrc_t rW2 = __builtin_amdgcn_make_buffer_rsrc((void*)A.wo, 0, 1024 * 2048, 0x00020000);
  const int lane = opaque(c.lane), g = lane >> 4, i16 = lane & 15, mt = 2 * (c.wid & 3) + (c.wid >> 2);
  u32x2 xr[4];
#pragma unroll
  for (int n = 0; n < 4; ++n) xr[n] = *(const u32x2*)(A.x + (size_t)(i2 * 128 + true_row(mt, i16)) * 1024 + j2 * 64 + 16 * n + 4 * g);
  wait_lgkm0(); bar();
  f32x4 acc[4];
  gemm_split<16, R2, false>(c, acc, OFF_OF + i2 * 262144, rW2, j2);
  TS(6);
  float s = 0.f, qq = 0.f;
#pragma unroll
  for (int u = 0; u < 2; ++u) {
    f32x4 h0 = acc[2 * u], h1 = acc[2 * u + 1]; const u32x2 x0 = xr[2 * u], x1 = xr[2 * u + 1];
    h0[0] += bflo(x0[0]); h0[1] += bfhi(x0[0]); h0[2] += bflo(x0[1]); h0[3] += bfhi(x0[1]);
    h1[0] += bflo(x1[0]); h1[1] += bfhi(x1[0]); h1[2] += bflo(x1[1]); h1[3] += bfhi(x1[1]);
    const bf16x8 hb = pk8(h0, h1);
    st16(c, hb, lane * 16 + (OFF_HF + (i2 * 16 + j2) * 16384 + (mt * 2 + u) * 1024));
    acc_row(s, qq, __builtin_bit_cast(u32x4, hb));
  }
  s = sum_g(s); qq = sum_g(qq);
  if (lane < 16) __builtin_amdgcn_raw_buffer_store_b64((u32x2){__float_as_uint(s), __float_as_uint(qq)}, c.ws, OFF_STH + (i2 * 16 + j2) * 1024 + (mt * 16 + lane) * 8, 0, SC_SC1);
  publish<8>(c, 2, FG_H, i2, j2);
  if (pre_on) pre();
  TS(7);
}

DI void phase5(Ctx& c, const Args& A, int i2, int j2) {
  const int lane = opaque(c.lane), g = lane >> 4, i16 = lane & 15, mt = 2 * (c.wid & 3) + (c.wid >> 2);
  u32x4 hr[2];
#pragma unroll
  for (int u = 0; u < 2; ++u) hr[u] = __builtin_amdgcn_raw_buffer_load_b128(c.ws, lane * 16, OFF_HF + (i2 * 16 + j2) * 16384 + (mt * 2 + u) * 1024, SC_SC1);
  const __amdgpu_buffer_rsrc_t rW = __builtin_amdgcn_make_buffer_rsrc((void*)A.wout, 0, 1792 * 2048, 0x00020000);
  if (c.wid == 0) wait_flags(c, FG_U, i2, 14);
  if (c.wid >= 4) wait_vm<24>();
  bar();
  f32x4 acc[4];
  gemm_split<28, R5, true>(c, acc, OFF_UF + i2 * 458752, rW, j2);
  TS(14);
  const lds_t ot = c.lds + c.wid * 2048;
#pragma unroll
  for (int n = 0; n < 4; ++n) { const unsigned r0 = hr[n >> 1][2 * (n & 1)], r1 = hr[n >> 1][2 * (n & 1) + 1];
    *(LAS u32x2*)(ot + i16 * 128 + (((2 * n + (g >> 1)) ^ (i16 & 7)) << 4) + (g & 1) * 8) =
        (u32x2){pk2(acc[n][0] + bflo(r0), acc[n][1] + bfhi(r0)), pk2(acc[n][2] + bflo(r1), acc[n][3] + bfhi(r1))}; }
  asm volatile("s_waitcnt lgkmcnt(0)" ::: "memory");
  const __amdgpu_buffer_rsrc_t rO = __builtin_amdgcn_make_buffer_rsrc((void*)A.out, 0, 2048 * 2048, 0x00020000);
#pragma unroll
  for (int u = 0; u < 2; ++u) { const int r = 8 * u + (lane >> 3), ch = lane & 7;
    const u32x4 v = *(LAS u32x4*)(ot + r * 128 + ((ch ^ (r & 7)) << 4));
    __builtin_amdgcn_raw_buffer_store_b128(v, rO, (i2 * 128 + true_row(mt, r)) * 2048 + j2 * 128 + ch * 16, 0, OUTAUX); }
  TS(15);
}

#define LAST_PHASE 5
__global__ __launch_bounds__(512, 2) void fused_fwd(const u16* __restrict__ x, const u16* __restrict__ wqkv, const u16* __restrict__ wphi, const u16* __restrict__ wo,
                                                      const u16* __restrict__ win, const u16* __restrict__ wsp, const u16* __restrict__ bsp, const u16* __restrict__ wout,
                                                      u16* __restrict__ out, char* __restrict__ ws) {
  extern __shared__ __attribute__((aligned(16))) char shm[];
  Ctx c;
  c.lds = (lds_t)shm; c.wsp = ws; c.lane = threadIdx.x & 63; c.wid = __builtin_amdgcn_readfirstlane(threadIdx.x >> 6); c.b = blockIdx.x;
  c.ws = __builtin_amdgcn_make_buffer_rsrc((void*)ws, 0, WS_BYTES, 0x00020000);
  c.tag = 0u; c.ts = 0u;
  const unsigned long long tstart = __builtin_amdgcn_s_memrealtime();
  if (threadIdx.x < 16) *(LAS unsigned*)(c.lds + L_CNT + threadIdx.x * 4) = 0u;
  const int xcd = c.b & 7, jj = c.b >> 3;
  Map M;
  M.i1 = 8 * (xcd & 1) + (jj >> 2); M.j1 = 4 * (xcd >> 1) + (jj & 3);
  M.i2 = 4 * (xcd >> 1) + (jj >> 3); M.j2 = 8 * (xcd & 1) + (jj & 7);
  M.act3 = jj < 28;
  M.i3 = 4 * (xcd >> 1) + (M.act3 ? jj / 7 : 0); M.j3 = 7 * (xcd & 1) + (M.act3 ? jj % 7 : 0);
  const Args A{x, wqkv, wphi, wo, win, wsp, bsp, wout, out, ws};
  constexpr int last = LAST_PHASE;
  const bool ld = c.wid >= 4;
  {
    Acc1 e;
    if (ld) p1_load(c, A, M.i1, M.j1);
    else { __builtin_amdgcn_s_setprio(1); p1_mma(c, A, M.i1, M.j1, e); __builtin_amdgcn_s_setprio(0); }
    p1_epilogue(c, A, M, e, last);
  }
  if (c.b == 0 && threadIdx.x == 0) { __hip_atomic_store((unsigned long long*)(c.wsp + 512), tstart, __ATOMIC_RELAXED, __HIP_MEMORY_SCOPE_AGENT); asm volatile("s_waitcnt vmcnt(0)" ::: "memory"); }
  if (last < 2) return;
  Ld3 L3;
  p3_setup(c, A, M.j3, L3);
  phase2(c, A, M.i2, M.j2, ld && M.act3, [&]() { p3_issueW(c, L3, 0); p3_issueW(c, L3, 1); });
  if (last < 3) return;
  if (M.act3) {
    u32x2 z1[2][4];
    Ld4 L;
    L.rA = __builtin_amdgcn_make_buffer_rsrc((void*)(A.wsp + (size_t)M.i3 * 128 * 2048), 0, 128 * 4096, 0x00020000);
    {
      Acc3 e;
      if (ld) p3_load(c, L3, M.i3);
      else { __builtin_amdgcn_s_setprio(1); p3_mma(c, M.i3, M.j3, e); __builtin_amdgcn_s_setprio(0); }
      const int ln = opaque(c.lane);
      L.voA = rowmajor_voff(c.wid & 3, ln, 4096); L.voF = ln * 16 + (c.wid & 3) * 1024;
      p3_epilogue(c, L, M.i3, M.j3, e, z1);
    }
    if (last < 4) return;
    f32x4 e4[2][4];
    if (ld) p4_load(c, L, M.j3);
    else { __builtin_amdgcn_s_setprio(1); p4_mma(c, M.j3, e4); __builtin_amdgcn_s_setprio(0); }
    p4_epilogue(c, A, M.i3, M.j3, M.j2, e4, z1);
  } else {
    bar();
    if (!ld) { const int wv = ((c.b & 7) * 4 + ((c.b >> 3) - 28)) * 4 + c.wid;
#pragma unroll 1
      for (int k = 0; k < 12; ++k) { const int Li = (k * 128 + wv) * 64 + c.lane;
        touch(c.lds, Li < 65536 ? (const char*)A.wsp + (size_t)Li * 128 : (const char*)A.wout + (size_t)((Li < 94208 ? Li : 94207) - 65536) * 128); } }
    if (ld) p5_issueW(c, A, M.j2);
  }
  if (last < 5) return;
  phase5(c, A, M.i2, M.j2);
  {
    asm volatile("s_waitcnt vmcnt(0)" ::: "memory");
    const unsigned long long t0 = __hip_atomic_load((unsigned long long*)(c.wsp + 512), __ATOMIC_RELAXED, __HIP_MEMORY_SCOPE_AGENT);
    if (c.b == 0 && c.wid == 0) while (__builtin_amdgcn_s_memrealtime() - t0 < 10000ull) __builtin_amdgcn_s_sleep(1);
  }
}

extern "C" void kernel_launch(void* const* d_in, const int* in_sizes, int n_in, void* d_out, int out_size, void* d_ws, size_t ws_size, hipStream_t stream) {
  (void)in_sizes; (void)n_in; (void)out_size; (void)ws_size;
  (void)hipFuncSetAttribute((const void*)fused_fwd, hipFuncAttributeMaxDynamicSharedMemorySize, LDS_BYTES);
  fused_fwd<<<256, 512, LDS_BYTES, stream>>>((const u16*)d_in[0], (const u16*)d_in[1], (const u16*)d_in[2], (const u16*)d_in[3], (const u16*)d_in[5], (const u16*)d_in[6],
                                             (const u16*)d_in[7], (const u16*)d_in[8], (u16*)d_out, (char*)d_ws);
}
```

```cpp
#include <hip/hip_runtime.h>
#include <hip/hip_bf16.h>
#include <stdint.h>
#include <type_traits>

#define LAS __attribute__((address_space(3)))
#define DI __device__ __forceinline__
typedef unsigned short u16;
typedef short bf16x8 __attribute__((ext_vector_type(8)));
typedef short s16x4 __attribute__((ext_vector_type(4)));
typedef float f32x4 __attribute__((ext_vector_type(4)));
typedef float f32x2 __attribute__((ext_vector_type(2)));
typedef unsigned u32x4 __attribute__((ext_vector_type(4)));
typedef unsigned u32x2 __attribute__((ext_vector_type(2)));
typedef __bf16 bf2 __attribute__((ext_vector_type(2)));
typedef LAS char* lds_t;

constexpr unsigned OFF_EPOCH = 0, OFF_ERR = 128, OFF_FLAG = 4096;
constexpr unsigned OFF_KVP = 262144;
constexpr unsigned OFF_ZP = OFF_KVP + 2097152;
constexpr unsigned OFF_ST2 = 27u << 20 | 0;
constexpr unsigned OFF_STH = OFF_ZP + 131072 + 262144;
constexpr unsigned OFF_GX = OFF_STH + 262144;
constexpr unsigned OFF_GC1 = OFF_GX + 65536;
constexpr unsigned OFF_GC3 = OFF_GC1 + 393216;
constexpr unsigned OFF_OF = 4u << 20, OFF_HF = 8u << 20, OFF_Z2 = 12u << 20, OFF_UF = 20u << 20, OFF_TS = 28u << 20;
constexpr unsigned WS_BYTES = 32u << 20;
enum { FG_KV = 0, FG_O = 1, FG_H = 2, FG_ST = 3, FG_Z2 = 4, FG_U = 5 };
constexpr int LDS_BYTES = 163840;
constexpr int SC1 = 122880, L_WPHI = SC1, L_TAB1 = SC1 + 4096, L_XBUF = SC1 + 22528;
constexpr int L_XF1 = 65536, L_TB = 32768, L_KR = 65536, L_ZR = 131072;
constexpr int SC3 = 147456, L_TAB3 = SC3, L_FIN = SC3 + 4096;
constexpr int L_CNT = LDS_BYTES - 64, L_DUMMY = LDS_BYTES - 512;
constexpr int SC_SC1 = 16;
#define GPRIO 0
#define PSLEEP 4
#define OUTAUX 16
#define LDAUX 16

DI void bar() { asm volatile("" ::: "memory"); __builtin_amdgcn_s_barrier(); asm volatile("" ::: "memory"); }
template <int N> DI void wait_vm() { asm volatile("s_waitcnt vmcnt(%0)" ::"n"(N) : "memory"); }
DI int opaque(int v) { asm volatile("" : "+v"(v)); return v; }
DI void wait_lgkm0() { __builtin_amdgcn_s_waitcnt(0xc07f); }
DI unsigned pk2(float lo, float hi) { f32x2 v = {lo, hi}; return __builtin_bit_cast(unsigned, __builtin_convertvector(v, bf2)); }
DI bf16x8 pk8(const f32x4& a, const f32x4& b) { u32x4 r = {pk2(a[0], a[1]), pk2(a[2], a[3]), pk2(b[0], b[1]), pk2(b[2], b[3])}; return __builtin_bit_cast(bf16x8, r); }
DI float bflo(unsigned w) { return __uint_as_float(w << 16); }
DI float bfhi(unsigned w) { return __uint_as_float(w & 0xffff0000u); }
DI f32x4 mfma(bf16x8 a, bf16x8 b, f32x4 c) { return __builtin_amdgcn_mfma_f32_16x16x32_bf16(a, b, c, 0, 0, 0); }
DI bf16x8 ldsA(lds_t p) { return *(LAS bf16x8*)p; }
DI bf16x8 ldsT(lds_t p, int d) {
  s16x4 lo = __builtin_amdgcn_ds_read_tr16_b64_v4i16((LAS s16x4*)p), hi = __builtin_amdgcn_ds_read_tr16_b64_v4i16((LAS s16x4*)(p + d));
  return __builtin_shufflevector(lo, hi, 0, 1, 2, 3, 4, 5, 6, 7);
}
template <int D1, int D2> DI void tr4_issue(unsigned addr, u32x2 (&r)[4]) {
  asm volatile("ds_read_b64_tr_b16 %0, %4\n\tds_read_b64_tr_b16 %1, %4 offset:%5\n\tds_read_b64_tr_b16 %2, %4 offset:%6\n\tds_read_b64_tr_b16 %3, %4 offset:%7"
               : "=&v"(r[0]), "=&v"(r[1]), "=&v"(r[2]), "=&v"(r[3]) : "v"(addr), "n"(D1), "n"(D2), "n"(D1 + D2) : "memory");
}
DI void tr4_wait(u32x2 (&r)[4]) { asm volatile("s_waitcnt lgkmcnt(0)" : "+v"(r[0]), "+v"(r[1]), "+v"(r[2]), "+v"(r[3])::"memory"); }
DI float sum_g(float x) {
  auto a = __builtin_amdgcn_permlane16_swap(__float_as_uint(x), __float_as_uint(x), false, false);
  x = __uint_as_float(a[0]) + __uint_as_float(a[1]);
  auto b = __builtin_amdgcn_permlane32_swap(__float_as_uint(x), __float_as_uint(x), false, false);
  return __uint_as_float(b[0]) + __uint_as_float(b[1]);
}
DI float max_g(float x) {
  auto a = __builtin_amdgcn_permlane16_swap(__float_as_uint(x), __float_as_uint(x), false, false);
  x = fmaxf(__uint_as_float(a[0]), __uint_as_float(a[1]));
  auto b = __builtin_amdgcn_permlane32_swap(__float_as_uint(x), __float_as_uint(x), false, false);
  return fmaxf(__uint_as_float(b[0]), __uint_as_float(b[1]));
}
template <int CTRL> DI float dpp_add(float x) { return x + __int_as_float(__builtin_amdgcn_update_dpp(0, __float_as_int(x), CTRL, 0xf, 0xf, true)); }
DI float sum_row16(float x) { x = dpp_add<0x128>(x); x = dpp_add<0x124>(x); x = dpp_add<0x122>(x); return dpp_add<0x121>(x); }
DI void sum_row16x8(f32x4& a, f32x4& b) {
#define RR(n) "v_add_f32_dpp %0, %0, %0 row_ror:" n " row_mask:0xf bank_mask:0xf\n\tv_add_f32_dpp %1, %1, %1 row_ror:" n " row_mask:0xf bank_mask:0xf\n\t" \
  "v_add_f32_dpp %2, %2, %2 row_ror:" n " row_mask:0xf bank_mask:0xf\n\tv_add_f32_dpp %3, %3, %3 row_ror:" n " row_mask:0xf bank_mask:0xf\n\t" \
  "v_add_f32_dpp %4, %4, %4 row_ror:" n " row_mask:0xf bank_mask:0xf\n\tv_add_f32_dpp %5, %5, %5 row_ror:" n " row_mask:0xf bank_mask:0xf\n\t" \
  "v_add_f32_dpp %6, %6, %6 row_ror:" n " row_mask:0xf bank_mask:0xf\n\tv_add_f32_dpp %7, %7, %7 row_ror:" n " row_mask:0xf bank_mask:0xf\n\t"
  float a0 = a[0], a1 = a[1], a2 = a[2], a3 = a[3], b0 = b[0], b1 = b[1], b2 = b[2], b3 = b[3];
  asm volatile("s_nop 1\n\t" RR("8") RR("4") RR("2") RR("1") "s_nop 0" : "+v"(a0), "+v"(a1), "+v"(a2), "+v"(a3), "+v"(b0), "+v"(b1), "+v"(b2), "+v"(b3));
#undef RR
  a = (f32x4){a0, a1, a2, a3}; b = (f32x4){b0, b1, b2, b3};
}
DI float dot2(unsigned a, unsigned b, float c) { return __builtin_amdgcn_fdot2_f32_bf16(__builtin_bit_cast(bf2, a), __builtin_bit_cast(bf2, b), c, false); }
constexpr unsigned ONES2 = 0x3f803f80u;
DI f32x4 gelu4(f32x4 x) {
  const f32x4 t = x * (x * x * -0.1029432397f + -2.302208198f);
  f32x4 d, r;
#pragma unroll
  for (int i = 0; i < 4; ++i) d[i] = __builtin_amdgcn_exp2f(t[i]);
  d += 1.0f;
#pragma unroll
  for (int i = 0; i < 4; ++i) r[i] = __builtin_amdgcn_rcpf(d[i]);
  return x * r;
}
DI u32x2 pk4(const f32x4& a) { return (u32x2){pk2(a[0], a[1]), pk2(a[2], a[3])}; }
DI f32x4 up4(u32x2 w) { return (f32x4){bflo(w[0]), bfhi(w[0]), bflo(w[1]), bfhi(w[1])}; }
DI float gelu(float x) { const float t = x * fmaf(x * x, -0.1029432397f, -2.302208198f); return x * __builtin_amdgcn_rcpf(1.0f + __builtin_amdgcn_exp2f(t)); }

struct Ctx {
  __amdgpu_buffer_rsrc_t ws;
  char* wsp; lds_t lds; unsigned tag; int lane, wid, b; unsigned ts;
};
DI unsigned* flag_ptr(const Ctx& c, int g, int a, int bb) { return (unsigned*)(c.wsp + OFF_FLAG + ((g * 16 + a) * 16 + bb) * 128); }
template <unsigned NWAVES = 4, int YOUNGER = 0> DI void publish(const Ctx& c, int cnt, int g, int a, int bb) {
  wait_vm<YOUNGER>();
  if (c.lane == 0) {
    unsigned old = __hip_atomic_fetch_add((LAS unsigned*)(c.lds + L_CNT + cnt * 4), 1u, __ATOMIC_RELAXED, __HIP_MEMORY_SCOPE_WORKGROUP);
    if (old == NWAVES - 1u) __hip_atomic_store(flag_ptr(c, g, a, bb), c.tag, __ATOMIC_RELAXED, __HIP_MEMORY_SCOPE_AGENT);
  }
}
DI void wait_flags(const Ctx& c, int g, int a, int n) {
  const unsigned* p = flag_ptr(c, g, a, c.lane < n ? c.lane : 0);
  const unsigned long long t0 = __builtin_amdgcn_s_memrealtime();
  for (;;) {
    const unsigned v = __hip_atomic_load(p, __ATOMIC_RELAXED, __HIP_MEMORY_SCOPE_AGENT);
    if (__all(v == c.tag)) break;
    if (__builtin_amdgcn_s_memrealtime() - t0 > 300000ull) { if (c.lane == 0) __hip_atomic_store((unsigned*)(c.wsp + OFF_ERR), 0x100u + (unsigned)g, __ATOMIC_RELAXED, __HIP_MEMORY_SCOPE_AGENT); break; }
    __builtin_amdgcn_s_sleep(1);
  }
  asm volatile("" ::: "memory");
}
#define STEP(t) do {} while (0)
#define TS(k) do {} while (0)

DI void st16(const Ctx& c, bf16x8 v, int off) { __builtin_amdgcn_raw_buffer_store_b128(__builtin_bit_cast(u32x4, v), c.ws, off, 0, SC_SC1); }
template <int AUX> DI void dma(__amdgpu_buffer_rsrc_t r, lds_t dst, int voff, int soff) {
  if (AUX) asm volatile("s_mov_b32 m0, %0\n\ts_nop 3\n\tbuffer_load_dwordx4 %1, %2, %3 offen sc1 lds" ::"s"((unsigned)(size_t)dst), "v"(voff), "s"(r), "s"(soff) : "memory");
  else asm volatile("s_mov_b32 m0, %0\n\ts_nop 3\n\tbuffer_load_dwordx4 %1, %2, %3 offen lds" ::"s"((unsigned)(size_t)dst), "v"(voff), "s"(r), "s"(soff) : "memory");
}

#define EXP 0
template <int S, int NW, class IA, class IW, class MID, class POST>
DI void loader_loop(int nt, bool gated_prologue, IA issueA, IW issueW, MID mid, POST post) {
  constexpr int PPW = 4 + NW;
  if (gated_prologue) wait_vm<4 * (S - 2)>(); else wait_vm<PPW * (S - 2)>();
  bar();
  int t = 0;
#pragma unroll
  for (int p = 0; p < S - 2; ++p, ++t) {
    mid(t); issueA(t + S - 1); issueW(t + S - 1); post(t);
    if (gated_prologue) {
      if (p == 0) wait_vm<4 * (S - 3 > 0 ? S - 3 : 0) + PPW * 1>();
      else if (p == 1) wait_vm<4 * (S - 4 > 0 ? S - 4 : 0) + PPW * 2>();
      else wait_vm<4 * (S - 5 > 0 ? S - 5 : 0) + PPW * 3>();
    } else wait_vm<PPW * (S - 2)>();
    bar();
  }
#pragma unroll 1
  for (; t + S - 1 < nt; ++t) { mid(t); if (!(EXP & 1)) { issueA(t + S - 1); issueW(t + S - 1); } wait_vm<PPW * (S - 2)>(); bar(); }
#pragma unroll
  for (int r = S - 2; r >= 0; --r, ++t) {
    if (r >= 4) wait_vm<PPW * 3>(); else if (r == 3) wait_vm<PPW * 2>(); else if (r == 2) wait_vm<PPW * 1>(); else wait_vm<0>();
    bar();
  }
}
template <int NW, bool GATED, class IA, class IW, class POST, class GA, class FIN>
DI void loader_loop16(bool full, bool once, IA issueA, IW issueW, POST post, GA gather, FIN fin) {
  constexpr int PPW = 4 + NW;
  if (GATED) wait_vm<4>(); else wait_vm<PPW>();
  bar();
  issueA(2); issueW(2); post(0); wait_vm<PPW>(); bar();
#pragma unroll 1
  for (int t = 1; t < 5; ++t) { if (!(EXP & 1)) { issueA(t + 2); issueW(t + 2); } post(t); wait_vm<PPW>(); bar(); }
#pragma unroll
  for (int t = 5; t < 13; ++t) {
    issueA(t + 2); issueW(t + 2); gather(t - 5);
    if (full) { if (t == 5) wait_vm<PPW + 2>(); else wait_vm<PPW + 4>(); }
    else if (once && t <= 6) wait_vm<PPW + 2>();
    else wait_vm<PPW>();
    bar();
  }
  issueA(15); issueW(15); if (full) wait_vm<PPW + 2>(); else wait_vm<PPW>(); bar();
  wait_vm<0>(); fin(); bar();
  bar();
}
template <int S, int SB, class RD0, class RD1, class MM0, class MM1, class SCH, class HK, class ST>
DI void consumer_loop(Ctx& c, int sbase, int nt, RD0 rd0, RD1 rd1, MM0 mm0, MM1 mm1, SCH sched, HK hook, ST stamp) {
  bar();
  stamp();
  rd0(0);
  int sb = 0;
#pragma unroll 2
  for (int t = 0; t < nt; ++t) {
    hook(t); STEP(t);
    __builtin_amdgcn_sched_barrier(0);
    if (!(EXP & 4)) rd1(sb); if (!(EXP & 2)) mm0(); sched();
    __builtin_amdgcn_sched_barrier(0);
    wait_lgkm0(); bar();
    __builtin_amdgcn_sched_barrier(0);
    sb = (sb == (S - 1) * SB) ? 0 : sb + SB;
    if (!(EXP & 4)) rd0(sb); if (!(EXP & 2)) mm1(); sched();
    __builtin_amdgcn_sched_barrier(0);
  }
}
#define TOUCH(name, mod) DI void name(lds_t lds, const void* p) { \
  asm volatile("s_mov_b32 m0, %1\n\ts_nop 0\n\tglobal_load_lds_dword %0, off" mod ::"v"(p), "s"((unsigned)(size_t)(lds + L_DUMMY)) : "memory"); }
TOUCH(touch, "") TOUCH(touch_nt, " nt") TOUCH(touch_sc1, " sc1")
template <int NM, int NR> DI void interleave() {
#pragma unroll
  for (int i = 0; i < NR; ++i) { __builtin_amdgcn_sched_group_barrier(0x008, 1, 0); __builtin_amdgcn_sched_group_barrier(0x100, 1, 0); }
  __builtin_amdgcn_sched_group_barrier(0x008, NM - NR, 0);
}
template <int NM, int NR> DI void interleave2() {
#pragma unroll
  for (int i = 0; i < NR / 2; ++i) { __builtin_amdgcn_sched_group_barrier(0x008, 1, 0); __builtin_amdgcn_sched_group_barrier(0x100, 2, 0); }
  __builtin_amdgcn_sched_group_barrier(0x008, NM - NR / 2, 0);
}

DI int rowmajor_voff(int lw, int lane, int pitch) {
  const int rr = lane >> 3, slot = 8 * (lw & 1) + rr;
  const int row = 8 * (slot >> 2) + 4 * (lw >> 1) + (slot & 3);
  return row * pitch + (((lane & 7) ^ (slot >> 1)) << 4);
}
DI int true_row(int mt, int i) { return 32 * (mt >> 1) + 8 * (i >> 2) + 4 * (mt & 1) + (i & 3); }

DI void acc_row(float& s, float& q, u32x4 w) {
#pragma unroll
  for (int i = 0; i < 4; ++i) { s = dot2(w[i], ONES2, s); q = dot2(w[i], w[i], q); } }
DI float col_sum(const u32x2 (&r)[4]) { float s0 = 0.f, s1 = 0.f;
#pragma unroll
  for (int i = 0; i < 4; ++i) { s0 = dot2(r[i][0], ONES2, s0); s1 = dot2(r[i][1], ONES2, s1); }
  return sum_g(s0 + s1); }
DI void st_granule(const Ctx& c, float v, int off) { __builtin_amdgcn_raw_buffer_store_b64((u32x2){__float_as_uint(v), c.tag}, c.ws, off, 0, SC_SC1); }
DI bool timed_out(const Ctx& c, unsigned long long t0, unsigned code) {
  if (__builtin_amdgcn_s_memrealtime() - t0 <= 300000ull) return false;
  if (c.lane == 0) __hip_atomic_store((unsigned*)(c.wsp + OFF_ERR), code, __ATOMIC_RELAXED, __HIP_MEMORY_SCOPE_AGENT);
  return true;
}
DI float sum_granules16(const Ctx& c, u32x2 (&v)[16], int off, int stride) {
  const unsigned long long t0 = __builtin_amdgcn_s_memrealtime();
  for (;;) {
    bool ok = true;
#pragma unroll
    for (int k = 0; k < 16; ++k) ok &= v[k][1] == c.tag;
    if (__all(ok) || timed_out(c, t0, 0x200u)) break;
    __builtin_amdgcn_s_sleep(2);
#pragma unroll
    for (int k = 0; k < 16; ++k) v[k] = __builtin_amdgcn_raw_buffer_load_b64(c.ws, off + k * stride, 0, SC_SC1);
  }
  float s = 0.f;
#pragma unroll
  for (int k = 0; k < 16; ++k) s += __uint_as_float(v[k][0]);
  return s;
}

#define ROTM 0
#define ROT2(j) (ROTM * 2 * ((j) & 7))
#define ROT5(j) (ROTM * ((7 * ((j) & 7)) >> 1))
#define P4D 0
#define TOUCHW touch
#define PF_T0 2
#define PF_DT 3

struct Args { const u16 *x, *wqkv, *wphi, *wo, *win, *wsp, *bsp, *wout; u16* out; char* ws; };

DI void get_tag(Ctx& c) { unsigned e; asm volatile("s_load_dword %0, %1, 0x0\n\ts_waitcnt lgkmcnt(0)" : "=&s"(e) : "s"(c.wsp) : "memory"); c.tag = e + 1u; }
DI void kv_gather(const Ctx& c, int j1) {
  const int lane = c.lane, g = lane >> 4, i16 = lane & 15, tid = c.wid * 64 + lane;
  f32x4 a0 = {0, 0, 0, 0}, a1 = {0, 0, 0, 0};
  u32x4 v[16];
  const unsigned f0 = __hip_atomic_load(flag_ptr(c, FG_KV, j1, i16), __ATOMIC_RELAXED, __HIP_MEMORY_SCOPE_AGENT);
  const unsigned ready = __builtin_amdgcn_readfirstlane((unsigned)(__ballot(f0 == c.tag) & 0xffffull));
  asm volatile("" ::: "memory");
#pragma unroll
  for (int k = 0; k < 16; ++k) if ((ready >> k) & 1u) v[k] = __builtin_amdgcn_raw_buffer_load_b128(c.ws, tid * 16, OFF_KVP + (j1 * 16 + k) * 8192, SC_SC1);
  if (ready != 0xffffu) {
    wait_flags(c, FG_KV, j1, 16);
#pragma unroll
    for (int k = 0; k < 16; ++k) if (!((ready >> k) & 1u)) v[k] = __builtin_amdgcn_raw_buffer_load_b128(c.ws, tid * 16, OFF_KVP + (j1 * 16 + k) * 8192, SC_SC1);
  }
  float zs = 0.f;
  if (c.wid < 2) { unsigned zv[16];
#pragma unroll
    for (int k = 0; k < 16; ++k) zv[k] = __builtin_amdgcn_raw_buffer_load_b32(c.ws, lane * 4, OFF_ZP + ((j1 * 16 + k) * 2 + c.wid) * 256, SC_SC1);
#pragma unroll
    for (int k = 0; k < 16; ++k) zs += __uint_as_float(zv[k]); }
#pragma unroll
  for (int k = 0; k < 16; ++k) { a0[0] += bflo(v[k][0]); a0[1] += bfhi(v[k][0]); a0[2] += bflo(v[k][1]); a0[3] += bfhi(v[k][1]); a1[0] += bflo(v[k][2]); a1[1] += bfhi(v[k][2]); a1[2] += bflo(v[k][3]); a1[3] += bfhi(v[k][3]); }
  const int w = c.wid, hd = w >> 2, dt = (w >> 1) & 1, fp = w & 1;
  const lds_t tb = c.lds + L_XBUF + hd * 8192 + (32 * fp + i16) * 128 + (16 * dt + 4 * g) * 2;
  *(LAS u32x2*)tb = (u32x2){pk2(a0[0], a0[1]), pk2(a0[2], a0[3])};
  *(LAS u32x2*)(tb + 16 * 128) = (u32x2){pk2(a1[0], a1[1]), pk2(a1[2], a1[3])};
  if (c.wid < 2) { const unsigned z2 = pk2(zs, zs); const lds_t t3 = c.lds + L_XBUF + c.wid * 8192 + lane * 128 + 64;
    *(LAS u32x4*)t3 = (u32x4){z2, z2, z2, z2}; *(LAS u32x4*)(t3 + 16) = (u32x4){z2, z2, z2, z2}; }
  wait_lgkm0();
}
struct Map { int i1, j1, i2, j2, i3, j3; bool act3; };
DI void p1_load(Ctx& c, const Args& A, int i1, int j1) {
  const int lane = c.lane, lw = c.wid - 4, g = lane >> 4, q = (lane & 15) >> 2, p = lane & 3;
  const lds_t lds = c.lds;
  const __amdgpu_buffer_rsrc_t rWS = c.ws;
  constexpr int S = 3, SB = 40960, NW = 6;
  const __amdgpu_buffer_rsrc_t rA = __builtin_amdgcn_make_buffer_rsrc((void*)(A.x + (size_t)i1 * 128 * 1024), 0, 128 * 2048, 0x00020000);
  const __amdgpu_buffer_rsrc_t rW = __builtin_amdgcn_make_buffer_rsrc((void*)A.wqkv, 0, 1024 * 6144, 0x00020000);
  const __amdgpu_buffer_rsrc_t rP = __builtin_amdgcn_make_buffer_rsrc((void*)A.wphi, 0, 65536, 0x00020000);
  const int voA = rowmajor_voff(lw, lane, 2048);
#define ROTMODE 1
  const int rot = ROTMODE == 2 ? (2 * (i1 & 7) + (i1 >> 3) + 4 * (j1 & 3)) & 15 : 4 * (i1 & 3);
  auto issueA = [&](int t) { const lds_t d = lds + (t % S) * SB + lw * 1024; const int so = ((t + rot) & 15) * 128;
#pragma unroll
    for (int i = 0; i < 4; ++i) dma<0>(rA, d + i * 4096, voA, so + i * 32 * 2048); };
  issueA(0);
  int voW[NW];
  { const int ln = opaque(lane);
#pragma unroll
    for (int i = 0; i < 3; ++i) { const int o = (NW * lw + i) * 1024 + ln * 16, r = o / 384, rem = o % 384, seg = rem >> 7, s16 = (rem & 127) >> 4;
      const int c32 = (s16 >> 1) ^ ((r >> 1) & 3), r5 = r & 31, k = (r & 32) + 8 * ((r5 >> 2) & 3) + 4 * (r5 >> 4) + (r5 & 3);
      voW[i] = k * 6144 + seg * 2048 + j1 * 128 + c32 * 32 + (s16 & 1) * 16;
      voW[i + 3] = voW[i] + 16 * 6144; }
  }
  auto issueW = [&](int t) { const lds_t d = lds + (t % S) * SB + 16384 + lw * NW * 1024; const int so = ((t + rot) & 15) * 64 * 6144;
#pragma unroll
    for (int i = 0; i < NW; ++i) dma<0>(rW, d + i * 1024, voW[i], so); };
  issueW(0); issueA(1); issueW(1);
  dma<0>(rP, lds + L_WPHI + lw * 1024, lane * 16 + lw * 1024, j1 * 4096);
  get_tag(c);
  TS(0);
  u32x2 gv[16]; u32x4 gx[2];
  const int goff = OFF_GC1 + j1 * 16 * 1536 + (lw * 64 + lane) * 8, xoff = OFF_GX + (i1 * 128 + lane) * 16;
  const int tau = ROTMODE == 2 ? 0 : ROTMODE == 1 ? i1 >> 2 : ROTMODE == 3 ? 0 : 0;
  auto post = [&](int t) {
    if (t != tau) return;
    const int sw = (2 * g + (q >> 1)) & 3; u32x2 cr[3][4];
#pragma unroll
    for (int n = 0; n < 3; ++n) { const int nu = 3 * lw + n;
      tr4_issue<16 * 384, 32 * 384>((unsigned)(size_t)(lds + (t % S) * SB + 16384 + (4 * g + q) * 384 + 8 * p + (nu >> 2) * 128 + 32 * ((nu & 3) ^ sw)), cr[n]); }
#pragma unroll
    for (int n = 0; n < 3; ++n) { tr4_wait(cr[n]); const float s = col_sum(cr[n]);
      if (lane < 16) st_granule(c, s, OFF_GC1 + (j1 * 16 + rot + tau) * 1536 + ((3 * lw + n) * 16 + lane) * 8); }
  };
  auto gather = [&](int k) {
    if (lw < 3) { gv[2 * k] = __builtin_amdgcn_raw_buffer_load_b64(rWS, goff + 2 * k * 1536, 0, SC_SC1); gv[2 * k + 1] = __builtin_amdgcn_raw_buffer_load_b64(rWS, goff + (2 * k + 1) * 1536, 0, SC_SC1); }
    else if (k == 0) { gx[0] = __builtin_amdgcn_raw_buffer_load_b128(rWS, xoff, 0, SC_SC1); gx[1] = __builtin_amdgcn_raw_buffer_load_b128(rWS, xoff + 1024, 0, SC_SC1); }
  };
  auto fin = [&]() {
    if (lw < 3) *(LAS float*)(lds + L_TAB1 + 1024 + (lw * 64 + lane) * 4) = sum_granules16(c, gv, goff, 1536);
    else {
      const unsigned long long t0 = __builtin_amdgcn_s_memrealtime();
      for (;;) {
        if (__all(gx[0][1] == c.tag && gx[0][3] == c.tag && gx[1][1] == c.tag && gx[1][3] == c.tag) || timed_out(c, t0, 0x201u)) break;
        __builtin_amdgcn_s_sleep(2);
        gx[0] = __builtin_amdgcn_raw_buffer_load_b128(rWS, xoff, 0, SC_SC1); gx[1] = __builtin_amdgcn_raw_buffer_load_b128(rWS, xoff + 1024, 0, SC_SC1);
      }
#pragma unroll
      for (int e = 0; e < 2; ++e) { *(LAS unsigned*)(lds + L_TAB1 + (lane + 64 * e) * 4) = gx[e][0]; *(LAS unsigned*)(lds + L_TAB1 + 512 + (lane + 64 * e) * 4) = gx[e][2]; }
    }
    wait_lgkm0();
    __builtin_amdgcn_s_waitcnt(0x0f70);
  {
    const int gw = c.b * 4 + lw;
    if (lane < 56) touch(lds, (const char*)A.win + (size_t)(gw * 56 + lane) * 128);
    if (lane < 16) touch(lds, (const char*)A.wo + (size_t)(gw * 16 + lane) * 128);
  }
  };
  loader_loop16<NW, false>(lw < 3, lw == 3, issueA, issueW, post, gather, fin);
  TS(1);
}

struct Acc1 { f32x4 q[2][2], k[2][2], v[2][2]; };
DI void p1_mma(Ctx& c, const Args& A, int i1, int j1, Acc1& e) {
  const int lane = c.lane, wr = c.wid >> 1, wc = c.wid & 1, g = lane >> 4, q = (lane & 15) >> 2, p = lane & 3, i16 = lane & 15;
  const lds_t lds = c.lds;
  const f32x4 Z4 = {0.f, 0.f, 0.f, 0.f};
  constexpr int S = 3, SB = 40960;
  f32x4 aq[4][2], ak[4][2], av[4][2];
#pragma unroll
  for (int m = 0; m < 4; ++m)
#pragma unroll
    for (int n = 0; n < 2; ++n) { aq[m][n] = Z4; ak[m][n] = Z4; av[m][n] = Z4; asm volatile("" : "+v"(aq[m][n]), "+v"(ak[m][n]), "+v"(av[m][n])); }
  const int sw = (2 * g + (q >> 1)) & 3;
  const int ao0 = (wr * 64 + i16) * 128 + ((g ^ (i16 >> 1)) << 4), ao1 = ao0 ^ 64;
  const int wl = 16384 + (4 * g + q) * 384 + 8 * p;
  const int wo0 = wl + 32 * ((2 * wc) ^ sw), wo1 = wl + 32 * ((2 * wc + 1) ^ sw);
  bf16x8 a0[4], w0[6], a1[4], w1[6];
  auto rd = [&](bf16x8(&a)[4], bf16x8(&w)[6], int sb, int ks) { const lds_t s = lds + sb;
#pragma unroll
    for (int sg = 0; sg < 3; ++sg) { w[2 * sg] = ldsT(s + wo0 + sg * 128 + ks * 32 * 384, 16 * 384); w[2 * sg + 1] = ldsT(s + wo1 + sg * 128 + ks * 32 * 384, 16 * 384); }
#pragma unroll
    for (int m = 0; m < 4; ++m) a[m] = ldsA(s + (ks ? ao1 : ao0) + m * 2048); };
  auto mm = [&](bf16x8(&a)[4], bf16x8(&w)[6]) {
#pragma unroll
    for (int n = 0; n < 2; ++n)
#pragma unroll
      for (int m = 0; m < 4; ++m) aq[m][n] = mfma(w[n], a[m], aq[m][n]);
#pragma unroll
    for (int n = 0; n < 2; ++n)
#pragma unroll
      for (int m = 0; m < 4; ++m) ak[m][n] = mfma(w[2 + n], a[m], ak[m][n]);
#pragma unroll
    for (int n = 0; n < 2; ++n)
#pragma unroll
      for (int m = 0; m < 4; ++m) av[m][n] = mfma(a[m], w[4 + n], av[m][n]); };
  TS(0);
  {
    u32x4 xv[2][2];
#pragma unroll
    for (int e2 = 0; e2 < 2; ++e2) { const int R = 8 * j1 + 2 * c.wid + e2; const u16* xr = A.x + (size_t)(i1 * 128 + true_row(R >> 4, R & 15)) * 1024 + lane * 16;
      xv[e2][0] = *(const u32x4*)xr; xv[e2][1] = *(const u32x4*)(xr + 8); }
    get_tag(c);
#pragma unroll
    for (int e2 = 0; e2 < 2; ++e2) { float s = 0.f, qq = 0.f; acc_row(s, qq, xv[e2][0]); acc_row(s, qq, xv[e2][1]);
      s = sum_g(sum_row16(s)) * (1.0f / 1024.0f); qq = sum_g(sum_row16(qq)) * (1.0f / 1024.0f);
      const float rs = __builtin_amdgcn_rsqf(fmaxf(qq - s * s, 0.f) + 1e-5f);
      if (lane == 0) __builtin_amdgcn_raw_buffer_store_b128((u32x4){__float_as_uint(s), c.tag, __float_as_uint(rs), c.tag}, c.ws, OFF_GX + (i1 * 128 + 8 * j1 + 2 * c.wid + e2) * 16, 0, SC_SC1); }
  }
  consumer_loop<S, SB>(c, 0, 16, [&](int sb) { rd(a0, w0, sb, 0); }, [&](int sb) { rd(a1, w1, sb, 1); }, [&]() { mm(a0, w0); }, [&]() { mm(a1, w1); }, []() { interleave<24, 16>(); }, [](int) {}, [&]() { TS(16); });
  TS(1);
  const lds_t xf = lds + L_XF1 + c.wid * 12288 + lane * 16;
#pragma unroll
  for (int m = 0; m < 2; ++m)
#pragma unroll
    for (int n = 0; n < 2; ++n) {
      *(LAS f32x4*)(xf + (m * 6 + n) * 1024) = aq[2 + m][n]; *(LAS f32x4*)(xf + (m * 6 + 2 + n) * 1024) = ak[2 + m][n]; *(LAS f32x4*)(xf + (m * 6 + 4 + n) * 1024) = av[2 + m][n];
      e.q[m][n] = aq[m][n]; e.k[m][n] = ak[m][n]; e.v[m][n] = av[m][n];
    }
  wait_lgkm0();
}

DI void both_g_max(float& a, float& b) {
  auto r = __builtin_amdgcn_permlane16_swap(__float_as_uint(a), __float_as_uint(b), false, false);
  const float x = fmaxf(__uint_as_float(r[0]), __uint_as_float(r[1]));
  auto s = __builtin_amdgcn_permlane32_swap(__float_as_uint(x), __float_as_uint(x), false, false);
  const float y = fmaxf(__uint_as_float(s[0]), __uint_as_float(s[1]));
  auto t = __builtin_amdgcn_permlane16_swap(__float_as_uint(y), __float_as_uint(y), false, false);
  a = __uint_as_float(t[0]); b = __uint_as_float(t[1]);
}
DI void both_g_sum(float& a, float& b) {
  auto r = __builtin_amdgcn_permlane16_swap(__float_as_uint(a), __float_as_uint(b), false, false);
  const float x = __uint_as_float(r[0]) + __uint_as_float(r[1]);
  auto s = __builtin_amdgcn_permlane32_swap(__float_as_uint(x), __float_as_uint(x), false, false);
  const float y = __uint_as_float(s[0]) + __uint_as_float(s[1]);
  auto t = __builtin_amdgcn_permlane16_swap(__float_as_uint(y), __float_as_uint(y), false, false);
  a = __uint_as_float(t[0]); b = __uint_as_float(t[1]);
}
DI void feat(const f32x4& p0, const f32x4& p1, f32x4& e0, f32x4& e1, f32x4& n0, f32x4& n1) {
  constexpr float L2E = 1.4426950408889634f;
  const f32x4 t0 = p0 * L2E, t1 = p1 * L2E;
  float mx = fmaxf(fmaxf(fmaxf(t0[0], t0[1]), t0[2]), fmaxf(fmaxf(fmaxf(t0[3], t1[0]), t1[1]), fmaxf(t1[2], t1[3])));
  float mn = -fminf(fminf(fminf(t0[0], t0[1]), t0[2]), fminf(fminf(fminf(t0[3], t1[0]), t1[1]), fminf(t1[2], t1[3])));
  both_g_max(mx, mn);
  const f32x4 a0 = t0 - mx, a1 = t1 - mx, b0 = -t0 - mn, b1 = -t1 - mn;
#pragma unroll
  for (int r = 0; r < 4; ++r) { e0[r] = __builtin_amdgcn_exp2f(a0[r]); e1[r] = __builtin_amdgcn_exp2f(a1[r]); n0[r] = __builtin_amdgcn_exp2f(b0[r]); n1[r] = __builtin_amdgcn_exp2f(b1[r]); }
  const f32x4 se = e0 + e1, sn4 = n0 + n1;
  float sp = (se[0] + se[1]) + (se[2] + se[3]), sn = (sn4[0] + sn4[1]) + (sn4[2] + sn4[3]);
  both_g_sum(sp, sn);
  sp = __builtin_amdgcn_rcpf(sp); sn = __builtin_amdgcn_rcpf(sn);
  e0 *= sp; e1 *= sp; n0 *= sn; n1 *= sn;
}

DI void p1_epilogue(Ctx& c, const Args& A, const Map& M, Acc1& e, int last) {
  const int lane = opaque(c.lane), cw = c.wid & 3, hf = c.wid >> 2, wr = cw >> 1, wc = cw & 1, g = lane >> 4, q = (lane & 15) >> 2, p = lane & 3, i16 = lane & 15;
  const int lw = cw, i1 = M.i1, j1 = M.j1;
  const lds_t lds = c.lds;
  const f32x4 Z4 = {0.f, 0.f, 0.f, 0.f};
  bar();
  TS(20);
  if (hf) { const lds_t xf = lds + L_XF1 + cw * 12288 + lane * 16;
#pragma unroll
    for (int m = 0; m < 2; ++m)
#pragma unroll
      for (int n = 0; n < 2; ++n) { e.q[m][n] = *(LAS f32x4*)(xf + (m * 6 + n) * 1024); e.k[m][n] = *(LAS f32x4*)(xf + (m * 6 + 2 + n) * 1024); e.v[m][n] = *(LAS f32x4*)(xf + (m * 6 + 4 + n) * 1024); } }
  const lds_t tab = lds + L_TAB1;
  bf16x8 qf[2], kf[2], vf[2], wT[2];
  {
    f32x4 vv[2][2];
#pragma unroll
    for (int m = 0; m < 2; ++m) {
      const int R = 64 * wr + 32 * hf + 16 * m;
      const float mu = *(LAS float*)(tab + (R + i16) * 4), rs = *(LAS float*)(tab + 512 + (R + i16) * 4), nm = -mu * rs;
      const f32x4 mu4 = *(LAS f32x4*)(tab + (R + 4 * g) * 4), rs4 = *(LAS f32x4*)(tab + 512 + (R + 4 * g) * 4), nm4 = -mu4 * rs4;
      f32x4 qq[2], kk[2];
#pragma unroll
      for (int n = 0; n < 2; ++n) {
        const f32x4 cq = *(LAS f32x4*)(tab + 1024 + (32 * wc + 16 * n + 4 * g) * 4), ck = *(LAS f32x4*)(tab + 1024 + (64 + 32 * wc + 16 * n + 4 * g) * 4);
        const float cv = *(LAS float*)(tab + 1024 + (128 + 32 * wc + 16 * n + i16) * 4);
        qq[n] = e.q[m][n] * rs + cq * nm; kk[n] = e.k[m][n] * rs + ck * nm; vv[m][n] = e.v[m][n] * rs4 + nm4 * cv;
      }
      qf[m] = pk8(qq[0], qq[1]); kf[m] = pk8(kk[0], kk[1]);
    }
    vf[0] = pk8(vv[0][0], vv[1][0]); vf[1] = pk8(vv[0][1], vv[1][1]);
#pragma unroll
    for (int ft = 0; ft < 2; ++ft) wT[ft] = ldsT(lds + L_WPHI + wc * 2048 + (4 * g + q) * 64 + ft * 32 + 8 * p, 16 * 64);
  }
  wait_lgkm0(); bar();
  TS(21);
  {
    const lds_t tb = lds + L_TB + c.wid * 4096;
#pragma unroll
    for (int m = 0; m < 2; ++m) {
      const f32x4 p0 = mfma(wT[0], kf[m], Z4), p1 = mfma(wT[1], kf[m], Z4);
      f32x4 e0, e1, n0, n1; feat(p0, p1, e0, e1, n0, n1);
      const lds_t rw = tb + (16 * m + i16) * 128;
      *(LAS u32x2*)(rw + 8 * ((0 + g) ^ i16)) = pk4(e0); *(LAS u32x2*)(rw + 8 * ((4 + g) ^ i16)) = pk4(e1);
      *(LAS u32x2*)(rw + 8 * ((8 + g) ^ i16)) = pk4(n0); *(LAS u32x2*)(rw + 8 * ((12 + g) ^ i16)) = pk4(n1);
    }
    asm volatile("s_waitcnt lgkmcnt(0)" ::: "memory");
    const bf16x8 ones = __builtin_bit_cast(bf16x8, (u32x4){ONES2, ONES2, ONES2, ONES2});
    const lds_t kr = lds + L_KR + c.wid * 8192 + lane * 16;
    float zf[4];
#pragma unroll
    for (int f = 0; f < 4; ++f) {
      const int r0 = 4 * g + q;
      const s16x4 lo = __builtin_amdgcn_ds_read_tr16_b64_v4i16((LAS s16x4*)(tb + r0 * 128 + 8 * ((4 * f + p) ^ r0)));
      const s16x4 hi = __builtin_amdgcn_ds_read_tr16_b64_v4i16((LAS s16x4*)(tb + (16 + r0) * 128 + 8 * ((4 * f + p) ^ r0)));
      const bf16x8 pf = __builtin_shufflevector(lo, hi, 0, 1, 2, 3, 4, 5, 6, 7);
      *(LAS f32x4*)(kr + f * 1024) = mfma(vf[0], pf, Z4); *(LAS f32x4*)(kr + (4 + f) * 1024) = mfma(vf[1], pf, Z4);
      zf[f] = mfma(ones, pf, Z4)[0];
    }
    *(LAS float*)(lds + L_ZR + c.wid * 256 + lane * 4) = g == 0 ? zf[0] : g == 1 ? zf[1] : g == 2 ? zf[2] : zf[3];
    TS(22);
    wait_lgkm0(); bar();
    f32x4 k0 = Z4, k1 = Z4;
#pragma unroll
    for (int w = 0; w < 4; ++w) { const lds_t s = lds + L_KR + (wc + 2 * w) * 8192 + (4 * wr + 2 * hf) * 1024 + lane * 16; k0 += *(LAS f32x4*)s; k1 += *(LAS f32x4*)(s + 1024); }
    st16(c, pk8(k0, k1), lane * 16 + (OFF_KVP + (j1 * 16 + i1) * 8192 + wc * 4096 + wr * 2048 + hf * 1024));
    if (c.wid < 2) { float zt = 0.f;
#pragma unroll
      for (int w = 0; w < 4; ++w) zt += *(LAS float*)(lds + L_ZR + (wc + 2 * w) * 256 + lane * 4);
      __builtin_amdgcn_raw_buffer_store_b32(__float_as_uint(zt), c.ws, lane * 4, OFF_ZP + ((j1 * 16 + i1) * 2 + wc) * 256, SC_SC1); }
    wait_lgkm0(); bar();
    TS(23);
    publish<8>(c, 0, FG_KV, j1, i1);
  }
  TS(2);
  const __amdgpu_buffer_rsrc_t rW2 = __builtin_amdgcn_make_buffer_rsrc((void*)A.wo, 0, 1024 * 2048, 0x00020000);
  int voW2[2];
#pragma unroll
  for (int i = 0; i < 2; ++i) { const int r = (2 * lw + i) * 8 + (lane >> 3), s16 = lane & 7; voW2[i] = r * 2048 + M.j2 * 128 + (((s16 >> 1) ^ ((r >> 1) & 3)) << 5) + (s16 & 1) * 16; }
  auto issueW2 = [&](int t) {
#pragma unroll
    for (int i = 0; i < 2; ++i) dma<0>(rW2, lds + t * 8192 + (lw * 2 + i) * 1024, voW2[i], t * 64 * 2048); };
  if (hf) {
    if (M.act3) {
      const __amdgpu_buffer_rsrc_t rW3 = __builtin_amdgcn_make_buffer_rsrc((void*)A.win, 0, 1024 * 7168, 0x00020000);
#pragma unroll
      for (int i = 0; i < 8; ++i) { const int r = (8 * lw + i) * 2 + (lane >> 5), l5 = lane & 31, seg = l5 >> 4, s16 = l5 & 15;
        dma<0>(rW3, lds + 32768 + (8 * lw + i) * 1024, r * 7168 + (seg == 0 ? 3584 : 0) + M.j3 * 256 + (((s16 >> 1) ^ (r & 7)) << 5) + (s16 & 1) * 16, M.i3 * 64 * 7168); }
    }
#pragma unroll
    for (int t = 0; t < 3; ++t) { issueW2(t); issueW2(8 + t); }
  }
  bf16x8 ph[2][2];
#pragma unroll
  for (int m = 0; m < 2; ++m) {
    const f32x4 p0 = mfma(wT[0], qf[m], Z4), p1 = mfma(wT[1], qf[m], Z4);
    f32x4 e0, e1, n0, n1; feat(p0, p1, e0, e1, n0, n1);
    ph[m][0] = pk8(e0, e1); ph[m][1] = pk8(n0, n1);
  }
  asm volatile("" : "+v"(ph[0][0]), "+v"(ph[0][1]), "+v"(ph[1][0]), "+v"(ph[1][1]));
  TS(3);
  kv_gather(c, j1);
  bar();
  TS(4);
  {
    bf16x8 kt[3][2];
#pragma unroll
    for (int d = 0; d < 3; ++d)
#pragma unroll
      for (int s = 0; s < 2; ++s) kt[d][s] = ldsT(lds + L_XBUF + wc * 8192 + (32 * s + 4 * g + q) * 128 + 32 * d + 8 * p, 16 * 128);
#pragma unroll
    for (int m = 0; m < 2; ++m) {
      const f32x4 n0 = mfma(kt[0][1], ph[m][1], mfma(kt[0][0], ph[m][0], Z4)), n1 = mfma(kt[1][1], ph[m][1], mfma(kt[1][0], ph[m][0], Z4));
      const f32x4 dn = mfma(kt[2][1], ph[m][1], mfma(kt[2][0], ph[m][0], Z4));
      const float r = __builtin_amdgcn_rcpf(dn[0] + 1e-6f);
      st16(c, pk8(n0 * r, n1 * r), lane * 16 + (OFF_OF + (i1 * 16 + j1) * 16384 + ((4 * wr + 2 * hf + m) * 2 + wc) * 1024));
    }
  }
  publish<8>(c, 1, FG_O, i1, j1);
  TS(5);
  if (hf && M.act3) {
    u32x2 cr[4][4];
#pragma unroll
    for (int n = 0; n < 4; ++n) tr4_issue<16 * 512, 32 * 512>((unsigned)(size_t)(lds + 32768 + (4 * g + q) * 512 + 8 * p + (lw >> 1) * 256 + 32 * ((4 * (lw & 1) + n) ^ (4 * (g & 1) + q))), cr[n]);
#pragma unroll
    for (int n = 0; n < 4; ++n) { tr4_wait(cr[n]); const float s = col_sum(cr[n]);
      if (lane < 16) st_granule(c, s, OFF_GC3 + (M.j3 * 16 + M.i3) * 2048 + ((4 * lw + n) * 16 + lane) * 8); }
  }
  if (hf) {
    issueW2(3);
#pragma unroll
    for (int t = 11; t < 16; ++t) issueW2(t);
  } else if (c.wid == 0 && last >= 2) wait_flags(c, FG_O, M.i2, 16);
}

struct Ld3 { __amdgpu_buffer_rsrc_t rW; int voW[8]; };
DI void p3_setup(const Ctx& c, const Args& A, int j3, Ld3& L) {
  const int lane = opaque(c.lane), lw = c.wid & 3;
  L.rW = __builtin_amdgcn_make_buffer_rsrc((void*)A.win, 0, 1024 * 7168, 0x00020000);
#pragma unroll
  for (int i = 0; i < 8; ++i) { const int r = (8 * lw + i) * 2 + (lane >> 5), l5 = lane & 31, seg = l5 >> 4, s16 = l5 & 15;
    L.voW[i] = r * 7168 + (seg == 0 ? 3584 : 0) + j3 * 256 + (((s16 >> 1) ^ (r & 7)) << 5) + (s16 & 1) * 16; }
}
DI void p3_issueW(const Ctx& c, const Ld3& L, int t) { const lds_t d = c.lds + (t % 3) * 49152 + 16384 + (c.wid & 3) * 8192;
#pragma unroll
  for (int i = 0; i < 8; ++i) dma<0>(L.rW, d + i * 1024, L.voW[i], t * 64 * 7168); }
DI void p3_load(Ctx& c, const Ld3& L, int i3) {
  const int lane = opaque(c.lane), lw = c.wid - 4;
  const lds_t lds = c.lds;
  constexpr int S = 3, SB = 49152;
  const int voF = lane * 16 + lw * 1024;
  auto issueA = [&](int t) { const lds_t d = lds + (t % S) * SB + lw * 1024; const int so = OFF_HF + (i3 * 16 + t) * 16384;
#pragma unroll
    for (int i = 0; i < 4; ++i) dma<LDAUX>(c.ws, d + i * 4096, voF, so + i * 4096); };
  auto issueW = [&](int t) { p3_issueW(c, L, t); };
  bar();
  issueA(0); issueA(1);
  u32x2 hv[16]; const int R = (lw & 1) * 64 + lane;
  auto gather = [&](int k) { if (lw < 2) {
      hv[2 * k] = __builtin_amdgcn_raw_buffer_load_b64(c.ws, R * 8, OFF_STH + (i3 * 16 + 2 * k) * 1024, SC_SC1);
      hv[2 * k + 1] = __builtin_amdgcn_raw_buffer_load_b64(c.ws, R * 8, OFF_STH + (i3 * 16 + 2 * k + 1) * 1024, SC_SC1); } };
  auto fin = [&]() { if (lw < 2) { float s = 0.f, qq = 0.f;
#pragma unroll
      for (int k = 0; k < 16; ++k) { s += __uint_as_float(hv[k][0]); qq += __uint_as_float(hv[k][1]); }
      s *= (1.0f / 1024.0f); qq *= (1.0f / 1024.0f);
      *(LAS float*)(lds + L_TAB3 + R * 4) = s; *(LAS float*)(lds + L_TAB3 + 512 + R * 4) = __builtin_amdgcn_rsqf(fmaxf(qq - s * s, 0.f) + 1e-5f);
      wait_lgkm0(); } };
  loader_loop16<8, true>(lw < 2, false, issueA, issueW, [](int) {}, gather, fin);
  TS(6);
}
struct Acc3 { f32x4 z2[2][4], z1[2][4]; };
DI int xf3_base(int cw) { return cw < 3 ? 16384 + cw * 32768 : 98304; }
DI void p3_mma(Ctx& c, int i3, int j3, Acc3& e) {
  const int lane = opaque(c.lane), wr = c.wid >> 1, wc = c.wid & 1, g = lane >> 4, q = (lane & 15) >> 2, p = lane & 3;
  const lds_t lds = c.lds;
  const __amdgpu_buffer_rsrc_t rWS = c.ws;
  const f32x4 Z4 = {0.f, 0.f, 0.f, 0.f};
  constexpr int S = 3, SB = 49152;
  if (wc == 1) {
    const int off = OFF_GC3 + j3 * 16 * 2048 + (128 * wr + 2 * lane) * 8; u32x4 gv[16];
    const unsigned long long t0 = __builtin_amdgcn_s_memrealtime();
    for (;;) {
      bool ok = true;
#pragma unroll
      for (int k = 0; k < 16; ++k) gv[k] = __builtin_amdgcn_raw_buffer_load_b128(rWS, off + k * 2048, 0, SC_SC1);
#pragma unroll
      for (int k = 0; k < 16; ++k) ok &= gv[k][1] == c.tag && gv[k][3] == c.tag;
      if (__all(ok) || timed_out(c, t0, 0x202u)) break;
      __builtin_amdgcn_s_sleep(2);
    }
    f32x2 cs = {0.f, 0.f};
#pragma unroll
    for (int k = 0; k < 16; ++k) { cs[0] += __uint_as_float(gv[k][0]); cs[1] += __uint_as_float(gv[k][2]); }
    *(LAS f32x2*)(lds + L_TAB3 + 1024 + (128 * wr + 2 * lane) * 4) = cs;
    wait_lgkm0();
  } else if (c.wid == 0) wait_flags(c, FG_H, i3, 16);
  f32x4 a2[4][4], a1_[4][4];
#pragma unroll
  for (int m = 0; m < 4; ++m)
#pragma unroll
    for (int n = 0; n < 4; ++n) { a2[m][n] = Z4; a1_[m][n] = Z4; }
#pragma unroll
  for (int m = 0; m < 4; ++m) asm volatile("" : "+v"(a2[m][0]), "+v"(a2[m][1]), "+v"(a2[m][2]), "+v"(a2[m][3]), "+v"(a1_[m][0]), "+v"(a1_[m][1]), "+v"(a1_[m][2]), "+v"(a1_[m][3]));
  bar();
  const int ao2 = wr * 8192 + lane * 16;
  int wo_[4];
#pragma unroll
  for (int n = 0; n < 4; ++n) wo_[n] = 16384 + (4 * g + q) * 512 + 8 * p + 32 * ((4 * wc + n) ^ (4 * (g & 1) + q));
  bf16x8 a0[4], w0[8], a1[4], w1[8];
  auto rd = [&](bf16x8(&a)[4], bf16x8(&w)[8], int sb, int ks) { const lds_t s = lds + sb;
#pragma unroll
    for (int n = 0; n < 8; ++n) w[n] = ldsT(s + wo_[n & 3] + (n >> 2) * 256 + ks * 32 * 512, 16 * 512);
#pragma unroll
    for (int m = 0; m < 4; ++m) a[m] = ldsA(s + ao2 + m * 2048 + ks * 1024); };
  auto mm = [&](bf16x8(&a)[4], bf16x8(&w)[8]) {
#pragma unroll
    for (int n = 0; n < 4; ++n)
#pragma unroll
      for (int m = 0; m < 4; ++m) a2[m][n] = mfma(a[m], w[n], a2[m][n]);
#pragma unroll
    for (int n = 0; n < 4; ++n)
#pragma unroll
      for (int m = 0; m < 4; ++m) a1_[m][n] = mfma(w[4 + n], a[m], a1_[m][n]); };
  consumer_loop<S, SB>(c, 16, 16, [&](int sb) { rd(a0, w0, sb, 0); }, [&](int sb) { rd(a1, w1, sb, 1); }, [&]() { mm(a0, w0); }, [&]() { mm(a1, w1); }, []() { interleave<32, 20>(); }, [](int) {}, [&]() { TS(17); });
  TS(8);
  const lds_t xf = lds + xf3_base(c.wid) + lane * 16;
#pragma unroll
  for (int m = 0; m < 2; ++m)
#pragma unroll
    for (int n = 0; n < 4; ++n) { *(LAS f32x4*)(xf + (m * 8 + n) * 1024) = a2[2 + m][n]; *(LAS f32x4*)(xf + (m * 8 + 4 + n) * 1024) = a1_[2 + m][n]; e.z2[m][n] = a2[m][n]; e.z1[m][n] = a1_[m][n]; }
  wait_lgkm0();
}
struct Ld4 { __amdgpu_buffer_rsrc_t rA; int voA, voF; };
DI void p4_issueW(const Ctx& c, const Ld4& L, int t) { const int lw = c.wid & 3; const lds_t d = c.lds + (t & 3) * 32768 + lw * 1024;
#pragma unroll
  for (int i = 0; i < 4; ++i) dma<0>(L.rA, d + i * 4096, L.voA, t * 128 + i * 32 * 4096); }
DI void p3_epilogue(Ctx& c, const Ld4& L, int i3, int j3, Acc3& e, u32x2 (&z1)[2][4]) {
  const int lane = opaque(c.lane), cw = c.wid & 3, hf = c.wid >> 2, wr = cw >> 1, wc = cw & 1, g = lane >> 4, i16 = lane & 15;
  const lds_t lds = c.lds, tab = lds + L_TAB3;
  const __amdgpu_buffer_rsrc_t rWS = c.ws;
  const f32x4 Z4 = {0.f, 0.f, 0.f, 0.f};
  bar();
  TS(25);
  __builtin_amdgcn_s_setprio(1);
  if (hf) { const lds_t xf = lds + xf3_base(cw) + lane * 16;
#pragma unroll
    for (int m = 0; m < 2; ++m)
#pragma unroll
      for (int n = 0; n < 4; ++n) { e.z2[m][n] = *(LAS f32x4*)(xf + (m * 8 + n) * 1024); e.z1[m][n] = *(LAS f32x4*)(xf + (m * 8 + 4 + n) * 1024); } }
  {
    float cs[4];
#pragma unroll
    for (int n = 0; n < 4; ++n) cs[n] = *(LAS float*)(tab + 1024 + (64 * wc + 16 * n + i16) * 4);
#pragma unroll
    for (int m = 0; m < 2; ++m) {
      const int R = 64 * wr + 32 * hf + 16 * m + 4 * g;
      const f32x4 mu = *(LAS f32x4*)(tab + R * 4), rs = *(LAS f32x4*)(tab + 512 + R * 4), nm = -mu * rs;
      f32x4 s = Z4, qq = Z4;
#pragma unroll
      for (int n = 0; n < 4; ++n) { const f32x4 y = gelu4(e.z2[m][n] * rs + nm * cs[n]); e.z2[m][n] = y; s += y; qq += y * y; }
      sum_row16x8(s, qq);
      if (i16 < 4) { const float sv = i16 == 0 ? s[0] : i16 == 1 ? s[1] : i16 == 2 ? s[2] : s[3], qv = i16 == 0 ? qq[0] : i16 == 1 ? qq[1] : i16 == 2 ? qq[2] : qq[3];
        __builtin_amdgcn_raw_buffer_store_b128((u32x4){__float_as_uint(sv), c.tag, __float_as_uint(qv), c.tag}, rWS, OFF_ST2 + ((i3 * 14 + j3) * 2 + wc) * 2048 + (R + i16) * 16, 0, SC_SC1); }
    }
  }
  TS(9);
  __builtin_amdgcn_s_setprio(0);
  if (!hf) {
#pragma unroll
    for (int s = 0; s < 3; ++s) p4_issueW(c, L, s);
  }
  {
    const int R0 = 64 * wr + 32 * hf, off = OFF_ST2 + (i3 * 28 + (lane >> 5)) * 2048 + (R0 + (lane & 31)) * 16;
    u32x4 v[14];
    const unsigned long long t0 = __builtin_amdgcn_s_memrealtime();
    for (;;) {
      bool ok = true;
#pragma unroll
      for (int k = 0; k < 14; ++k) v[k] = __builtin_amdgcn_raw_buffer_load_b128(rWS, off + k * 4096, 0, SC_SC1);
#pragma unroll
      for (int k = 0; k < 14; ++k) ok &= v[k][1] == c.tag && v[k][3] == c.tag;
      if (__all(ok) || timed_out(c, t0, 0x203u)) break;
      __builtin_amdgcn_s_sleep(PSLEEP);
    }
    __builtin_amdgcn_s_setprio(1);
    float s = 0.f, qq = 0.f;
#pragma unroll
    for (int k = 0; k < 14; ++k) { s += __uint_as_float(v[k][0]); qq += __uint_as_float(v[k][2]); }
    { auto a = __builtin_amdgcn_permlane32_swap(__float_as_uint(s), __float_as_uint(s), false, false); s = (__uint_as_float(a[0]) + __uint_as_float(a[1])) * (1.0f / 1792.0f);
      auto b = __builtin_amdgcn_permlane32_swap(__float_as_uint(qq), __float_as_uint(qq), false, false); qq = (__uint_as_float(b[0]) + __uint_as_float(b[1])) * (1.0f / 1792.0f); }
    const float rsv = __builtin_amdgcn_rsqf(fmaxf(qq - s * s, 0.f) + 1e-5f);
    const lds_t ft = lds + L_FIN + c.wid * 256;
    if (lane < 32) { *(LAS float*)(ft + lane * 4) = rsv; *(LAS float*)(ft + 128 + lane * 4) = -s * rsv; }
    asm volatile("s_waitcnt lgkmcnt(0)" ::: "memory");
    TS(31);
    f32x4 rs[2], nm[2];
#pragma unroll
    for (int m = 0; m < 2; ++m) { rs[m] = *(LAS f32x4*)(ft + (16 * m + 4 * g) * 4); nm[m] = *(LAS f32x4*)(ft + 128 + (16 * m + 4 * g) * 4); }
#pragma unroll
    for (int n = 0; n < 4; ++n)
      st16(c, pk8(e.z2[0][n] * rs[0] + nm[0], e.z2[1][n] * rs[1] + nm[1]), lane * 16 + (OFF_Z2 + (j3 * 32 + 2 * i3 + wr) * 16384 + ((4 * wc + n) * 2 + hf) * 1024));
  }
  __builtin_amdgcn_s_setprio(0);
  publish<8>(c, 4, FG_Z2, j3, i3);
  TS(11);
#pragma unroll
  for (int m = 0; m < 2; ++m) asm volatile("" : "+v"(e.z1[m][0]), "+v"(e.z1[m][1]), "+v"(e.z1[m][2]), "+v"(e.z1[m][3]));
#pragma unroll
  for (int m = 0; m < 2; ++m) {
    const int R = 64 * wr + 32 * hf + 16 * m + i16;
    const float mu = *(LAS float*)(tab + R * 4), rs = *(LAS float*)(tab + 512 + R * 4), nm = -mu * rs;
#pragma unroll
    for (int n = 0; n < 4; ++n) { const f32x4 cs = *(LAS f32x4*)(tab + 1024 + (128 + 64 * wc + 16 * n + 4 * g) * 4); z1[m][n] = pk4(gelu4(e.z1[m][n] * rs + cs * nm)); }
    asm volatile("" : "+v"(z1[m][0]), "+v"(z1[m][1]), "+v"(z1[m][2]), "+v"(z1[m][3]));
  }
  TS(10);
}
DI void p4_load(Ctx& c, const Ld4& L, int j3) {
  const int lw = c.wid - 4;
  auto issueW = [&](int t) { p4_issueW(c, L, t); };
  auto issueA = [&](int t) { const lds_t d = c.lds + (t & 3) * 32768 + 16384 + lw * 1024; const int so = OFF_Z2 + (j3 * 32 + t) * 16384;
#pragma unroll
    for (int i = 0; i < 4; ++i) dma<LDAUX>(c.ws, d + i * 4096, L.voF, so + i * 4096); };
  bar();
  if (c.b == 0 && lw == 0 && c.lane == 0) __hip_atomic_store((unsigned*)(c.wsp + OFF_EPOCH), c.tag, __ATOMIC_RELAXED, __HIP_MEMORY_SCOPE_AGENT);
#pragma unroll
  for (int s = 0; s < 3; ++s) issueA(s);
  loader_loop<4, 4>(32, true, issueA, issueW, [](int) {}, [](int) {});
}
DI void p4_mma(Ctx& c, int j3, f32x4 (&e)[2][4]) {
  const int lane = opaque(c.lane), wr = c.wid >> 1, wc = c.wid & 1, g = lane >> 4, i16 = lane & 15;
  const lds_t lds = c.lds;
  constexpr int S = 4, SB = 32768;
  f32x4 acc[4][4];
#pragma unroll
  for (int m = 0; m < 4; ++m)
#pragma unroll
    for (int n = 0; n < 4; ++n) { acc[m][n] = (f32x4){0.f, 0.f, 0.f, 0.f}; asm volatile("" : "+v"(acc[m][n])); }
  const int ao0 = (wr * 64 + i16) * 128 + ((g ^ (i16 >> 1)) << 4), ao1 = ao0 ^ 64, zo = 16384 + wc * 8192 + lane * 16;
  bf16x8 a0[4], w0[4], a1[4], w1[4];
  auto rd = [&](bf16x8(&a)[4], bf16x8(&w)[4], int sb, int ks) { const lds_t s = lds + sb;
#pragma unroll
    for (int n = 0; n < 4; ++n) { w[n] = ldsA(s + zo + n * 2048 + ks * 1024); a[n] = ldsA(s + (ks ? ao1 : ao0) + n * 2048); } };
  auto mm = [&](bf16x8(&a)[4], bf16x8(&w)[4]) {
#pragma unroll
    for (int n = 0; n < 4; ++n)
#pragma unroll
      for (int m = 0; m < 4; ++m) acc[m][n] = mfma(w[n], a[m], acc[m][n]); };
  if (c.wid == 0) wait_flags(c, FG_Z2, j3, 16);
  bar();
  consumer_loop<S, SB>(c, 32, 32, [&](int sb) { rd(a0, w0, sb, 0); }, [&](int sb) { rd(a1, w1, sb, 1); }, [&]() { mm(a0, w0); }, [&]() { mm(a1, w1); }, []() { interleave<16, 8>(); }, [](int) {}, [&]() { TS(18); });
  TS(12);
  const lds_t xf = lds + c.wid * 8192 + lane * 16;
#pragma unroll
  for (int m = 0; m < 2; ++m)
#pragma unroll
    for (int n = 0; n < 4; ++n) { *(LAS f32x4*)(xf + (m * 4 + n) * 1024) = acc[2 + m][n]; e[m][n] = acc[m][n]; }
  wait_lgkm0();
}
DI void p5_issueW(const Ctx& c, const Args& A, int j2);
DI void p4_epilogue(Ctx& c, const Args& A, int i3, int j3, int j2, f32x4 (&e)[2][4], const u32x2 (&z1)[2][4]) {
  const int lane = opaque(c.lane), cw = c.wid & 3, hf = c.wid >> 2, wr = cw >> 1, wc = cw & 1, i16 = lane & 15;
  float bs[2];
#pragma unroll
  for (int m = 0; m < 2; ++m) bs[m] = bflo((unsigned)A.bsp[i3 * 128 + true_row(4 * wr + 2 * hf + m, i16)]);
  bar();
  if (hf) { const lds_t xf = c.lds + cw * 8192 + lane * 16;
#pragma unroll
    for (int m = 0; m < 2; ++m)
#pragma unroll
      for (int n = 0; n < 4; ++n) e[m][n] = *(LAS f32x4*)(xf + (m * 4 + n) * 1024); }
#pragma unroll
  for (int m = 0; m < 2; ++m)
#pragma unroll
    for (int u = 0; u < 2; ++u)
      st16(c, pk8(up4(z1[m][2 * u]) * (e[m][2 * u] + bs[m]), up4(z1[m][2 * u + 1]) * (e[m][2 * u + 1] + bs[m])),
           lane * 16 + (OFF_UF + (i3 * 28 + 2 * j3 + wc) * 16384 + ((4 * wr + 2 * hf + m) * 2 + u) * 1024));
  wait_lgkm0(); bar();
  publish<8>(c, 5, FG_U, i3, j3);
  if (hf) p5_issueW(c, A, j2);
  TS(13);
}

DI void p5_issueW(const Ctx& c, const Args& A, int j2) {
  const int lane = opaque(c.lane), lw = c.wid - 4;
  const __amdgpu_buffer_rsrc_t rW = __builtin_amdgcn_make_buffer_rsrc((void*)A.wout, 0, 1792 * 2048, 0x00020000);
  int vo[2];
#pragma unroll
  for (int i = 0; i < 2; ++i) { const int r = (2 * lw + i) * 8 + (lane >> 3), s16 = lane & 7; vo[i] = r * 2048 + j2 * 128 + (((s16 >> 1) ^ ((r >> 1) & 3)) << 5) + (s16 & 1) * 16; }
#pragma unroll
  for (int t = 0; t < 18; ++t)
#pragma unroll
    for (int i = 0; i < 2; ++i) dma<0>(rW, c.lds + ((t & 1) * 9 + (t >> 1)) * 8192 + (lw * 2 + i) * 1024, vo[i], ((t & 1) * 14 + (t >> 1)) * 64 * 2048);
}

constexpr int L_RED = 98304;
#define STG1 0
#define STG2 2
#define R5 6
#define R2 6
template <int NSH, int R, bool REFILL>
DI void gemm_split(Ctx& c, f32x4 (&res)[4], int aoff, __amdgpu_buffer_rsrc_t rW, int j2) {
  const int ln = opaque(c.lane), g = ln >> 4, q = (ln & 15) >> 2, p = ln & 3, rw = c.wid & 3, kh = c.wid >> 2;
  const lds_t lds = c.lds;
  const int sw2 = (2 * g + (q >> 1)) & 3, wl2 = (4 * g + q) * 128 + 8 * p + kh * (REFILL ? 9 : 8) * 8192, va = ln * 16 + rw * 4096;
  const int ao = aoff + kh * (NSH / 2) * 16384;
  int wo2[4];
#pragma unroll
  for (int n = 0; n < 4; ++n) wo2[n] = wl2 + 32 * (n ^ sw2);
  f32x4 acc[2][4];
#pragma unroll
  for (int n = 0; n < 4; ++n) { acc[0][n] = (f32x4){0.f, 0.f, 0.f, 0.f}; acc[1][n] = acc[0][n]; }
  u32x4 a[R][2]; bf16x8 w0[4], w1[4];
  auto lda = [&](int s) {
#pragma unroll
    for (int m = 0; m < 2; ++m) a[s % R][m] = __builtin_amdgcn_raw_buffer_load_b128(c.ws, va + m * 2048, ao + (s >> 1) * 16384 + (s & 1) * 1024, LDAUX); };
  auto ldw = [&](bf16x8(&d)[4], int s) { const int lt = s >> 1, slot = (REFILL && lt >= 9) ? lt - 9 : lt;
#pragma unroll
    for (int n = 0; n < 4; ++n) d[n] = ldsT(lds + wo2[n] + slot * 8192 + (s & 1) * 4096, 16 * 128); };
  TS(REFILL ? 19 : 24);
#pragma unroll
  for (int s = 0; s < R; ++s) { lda(s); if (s == STG1 || s == STG2) bar(); }
  if (!REFILL && kh) {
#pragma unroll
    for (int i = 0; i < 2; ++i) { const int r = (2 * rw + i) * 8 + (ln >> 3), s16 = ln & 7, vo = r * 2048 + j2 * 128 + (((s16 >> 1) ^ ((r >> 1) & 3)) << 5) + (s16 & 1) * 16;
#pragma unroll
      for (int t = 4; t < 8; ++t) dma<0>(rW, lds + t * 8192 + (rw * 2 + i) * 1024, vo, t * 64 * 2048); }
  }
  ldw(w0, 0);
#pragma unroll
  for (int s = 0; s < NSH; ++s) {
    if (s == 5) {
      if (kh) { if (REFILL) wait_vm<2 * R + 10>(); else wait_vm<10>(); }
      bar(); __builtin_amdgcn_sched_barrier(0);
    }
    if (REFILL && s == 10) {
      wait_lgkm0(); bar();
      const int r = 8 * c.wid + (ln >> 3), s16 = ln & 7, vo = r * 2048 + j2 * 128 + (((s16 >> 1) ^ ((r >> 1) & 3)) << 5) + (s16 & 1) * 16;
#pragma unroll
      for (int k = 0; k < 10; ++k) dma<0>(rW, lds + ((k / 5) * 9 + (k % 5)) * 8192 + c.wid * 1024, vo, ((k / 5) * 14 + 9 + (k % 5)) * 64 * 2048);
      __builtin_amdgcn_sched_barrier(0);
    }
    if (REFILL && s == 17) { wait_vm<2 * ((NSH - R < 17 ? NSH - R : 17) - 10)>(); wait_lgkm0(); bar(); __builtin_amdgcn_sched_barrier(0); }
    if (s + 1 < NSH) { if (s & 1) ldw(w0, s + 1); else ldw(w1, s + 1); }
#pragma unroll
    for (int n = 0; n < 4; ++n)
#pragma unroll
      for (int m = 0; m < 2; ++m) acc[m][n] = mfma((s & 1) ? w1[n] : w0[n], __builtin_bit_cast(bf16x8, a[s % R][m]), acc[m][n]);
    if (s + R < NSH) lda(s + R);
    interleave<8, 8>();
    __builtin_amdgcn_sched_barrier(0);
  }
  __builtin_amdgcn_s_setprio(0);
  TS(REFILL ? 29 : 28);
  wait_lgkm0(); bar();
#pragma unroll
  for (int n = 0; n < 4; ++n) *(LAS f32x4*)(lds + L_RED + c.wid * 4096 + n * 1024 + ln * 16) = kh ? acc[0][n] : acc[1][n];
  wait_lgkm0(); bar();
#pragma unroll
  for (int n = 0; n < 4; ++n) res[n] = (kh ? acc[1][n] : acc[0][n]) + *(LAS f32x4*)(lds + L_RED + (c.wid ^ 4) * 4096 + n * 1024 + ln * 16);
}

template <class PRE> DI void phase2(Ctx& c, const Args& A, int i2, int j2, bool pre_on, PRE pre) {
  const __amdgpu_buffer_rsrc_t rW2 = __builtin_amdgcn_make_buffer_rsrc((void*)A.wo, 0, 1024 * 2048, 0x00020000);
  const int lane = opaque(c.lane), g = lane >> 4, i16 = lane & 15, mt = 2 * (c.wid & 3) + (c.wid >> 2);
  u32x2 xr[4];
#pragma unroll
  for (int n = 0; n < 4; ++n) xr[n] = *(const u32x2*)(A.x + (size_t)(i2 * 128 + true_row(mt, i16)) * 1024 + j2 * 64 + 16 * n + 4 * g);
  wait_lgkm0(); bar();
  f32x4 acc[4];
  gemm_split<16, R2, false>(c, acc, OFF_OF + i2 * 262144, rW2, j2);
  TS(6);
  float s = 0.f, qq = 0.f;
#pragma unroll
  for (int u = 0; u < 2; ++u) {
    f32x4 h0 = acc[2 * u], h1 = acc[2 * u + 1]; const u32x2 x0 = xr[2 * u], x1 = xr[2 * u + 1];
    h0[0] += bflo(x0[0]); h0[1] += bfhi(x0[0]); h0[2] += bflo(x0[1]); h0[3] += bfhi(x0[1]);
    h1[0] += bflo(x1[0]); h1[1] += bfhi(x1[0]); h1[2] += bflo(x1[1]); h1[3] += bfhi(x1[1]);
    const bf16x8 hb = pk8(h0, h1);
    st16(c, hb, lane * 16 + (OFF_HF + (i2 * 16 + j2) * 16384 + (mt * 2 + u) * 1024));
    acc_row(s, qq, __builtin_bit_cast(u32x4, hb));
  }
  s = sum_g(s); qq = sum_g(qq);
  if (lane < 16) __builtin_amdgcn_raw_buffer_store_b64((u32x2){__float_as_uint(s), __float_as_uint(qq)}, c.ws, OFF_STH + (i2 * 16 + j2) * 1024 + (mt * 16 + lane) * 8, 0, SC_SC1);
  publish<8>(c, 2, FG_H, i2, j2);
  if (pre_on) pre();
  TS(7);
}

DI void phase5(Ctx& c, const Args& A, int i2, int j2) {
  const int lane = opaque(c.lane), g = lane >> 4, i16 = lane & 15, mt = 2 * (c.wid & 3) + (c.wid >> 2);
  u32x4 hr[2];
#pragma unroll
  for (int u = 0; u < 2; ++u) hr[u] = __builtin_amdgcn_raw_buffer_load_b128(c.ws, lane * 16, OFF_HF + (i2 * 16 + j2) * 16384 + (mt * 2 + u) * 1024, SC_SC1);
  const __amdgpu_buffer_rsrc_t rW = __builtin_amdgcn_make_buffer_rsrc((void*)A.wout, 0, 1792 * 2048, 0x00020000);
  if (c.wid == 0) wait_flags(c, FG_U, i2, 14);
  if (c.wid >= 4) wait_vm<24>();
  bar();
  f32x4 acc[4];
  gemm_split<28, R5, true>(c, acc, OFF_UF + i2 * 458752, rW, j2);
  TS(14);
  const lds_t ot = c.lds + c.wid * 2048;
#pragma unroll
  for (int n = 0; n < 4; ++n) { const unsigned r0 = hr[n >> 1][2 * (n & 1)], r1 = hr[n >> 1][2 * (n & 1) + 1];
    *(LAS u32x2*)(ot + i16 * 128 + (((2 * n + (g >> 1)) ^ (i16 & 7)) << 4) + (g & 1) * 8) =
        (u32x2){pk2(acc[n][0] + bflo(r0), acc[n][1] + bfhi(r0)), pk2(acc[n][2] + bflo(r1), acc[n][3] + bfhi(r1))}; }
  asm volatile("s_waitcnt lgkmcnt(0)" ::: "memory");
  const __amdgpu_buffer_rsrc_t rO = __builtin_amdgcn_make_buffer_rsrc((void*)A.out, 0, 2048 * 2048, 0x00020000);
#pragma unroll
  for (int u = 0; u < 2; ++u) { const int r = 8 * u + (lane >> 3), ch = lane & 7;
    const u32x4 v = *(LAS u32x4*)(ot + r * 128 + ((ch ^ (r & 7)) << 4));
    __builtin_amdgcn_raw_buffer_store_b128(v, rO, (i2 * 128 + true_row(mt, r)) * 2048 + j2 * 128 + ch * 16, 0, OUTAUX); }
  TS(15);
}

#define LAST_PHASE 5
__global__ __launch_bounds__(512, 2) void fused_fwd(const u16* __restrict__ x, const u16* __restrict__ wqkv, const u16* __restrict__ wphi, const u16* __restrict__ wo,
                                                      const u16* __restrict__ win, const u16* __restrict__ wsp, const u16* __restrict__ bsp, const u16* __restrict__ wout,
                                                      u16* __restrict__ out, char* __restrict__ ws) {
  extern __shared__ __attribute__((aligned(16))) char shm[];
  Ctx c;
  c.lds = (lds_t)shm; c.wsp = ws; c.lane = threadIdx.x & 63; c.wid = __builtin_amdgcn_readfirstlane(threadIdx.x >> 6); c.b = blockIdx.x;
  c.ws = __builtin_amdgcn_make_buffer_rsrc((void*)ws, 0, WS_BYTES, 0x00020000);
  c.tag = 0u; c.ts = 0u;
  if (threadIdx.x < 16) *(LAS unsigned*)(c.lds + L_CNT + threadIdx.x * 4) = 0u;
  const int xcd = c.b & 7, jj = c.b >> 3;
  Map M;
  M.i1 = 8 * (xcd & 1) + (jj >> 2); M.j1 = 4 * (xcd >> 1) + (jj & 3);
  M.i2 = 4 * (xcd >> 1) + (jj >> 3); M.j2 = 8 * (xcd & 1) + (jj & 7);
  M.act3 = jj < 28;
  M.i3 = 4 * (xcd >> 1) + (M.act3 ? jj / 7 : 0); M.j3 = 7 * (xcd & 1) + (M.act3 ? jj % 7 : 0);
  const Args A{x, wqkv, wphi, wo, win, wsp, bsp, wout, out, ws};
  constexpr int last = LAST_PHASE;
  const bool ld = c.wid >= 4;
  {
    Acc1 e;
    if (ld) p1_load(c, A, M.i1, M.j1);
    else { __builtin_amdgcn_s_setprio(1); p1_mma(c, A, M.i1, M.j1, e); __builtin_amdgcn_s_setprio(0); }
    p1_epilogue(c, A, M, e, last);
  }
  if (last < 2) return;
  Ld3 L3;
  p3_setup(c, A, M.j3, L3);
  phase2(c, A, M.i2, M.j2, ld && M.act3, [&]() { p3_issueW(c, L3, 0); p3_issueW(c, L3, 1); });
  if (last < 3) return;
  if (M.act3) {
    u32x2 z1[2][4];
    Ld4 L;
    L.rA = __builtin_amdgcn_make_buffer_rsrc((void*)(A.wsp + (size_t)M.i3 * 128 * 2048), 0, 128 * 4096, 0x00020000);
    {
      Acc3 e;
      if (ld) p3_load(c, L3, M.i3);
      else { __builtin_amdgcn_s_setprio(1); p3_mma(c, M.i3, M.j3, e); __builtin_amdgcn_s_setprio(0); }
      const int ln = opaque(c.lane);
      L.voA = rowmajor_voff(c.wid & 3, ln, 4096); L.voF = ln * 16 + (c.wid & 3) * 1024;
      p3_epilogue(c, L, M.i3, M.j3, e, z1);
    }
    if (last < 4) return;
    f32x4 e4[2][4];
    if (ld) p4_load(c, L, M.j3);
    else { __builtin_amdgcn_s_setprio(1); p4_mma(c, M.j3, e4); __builtin_amdgcn_s_setprio(0); }
    p4_epilogue(c, A, M.i3, M.j3, M.j2, e4, z1);
  } else {
    bar();
    if (!ld) { const int wv = ((c.b & 7) * 4 + ((c.b >> 3) - 28)) * 4 + c.wid;
#pragma unroll 1
      for (int k = 0; k < 12; ++k) { const int Li = (k * 128 + wv) * 64 + c.lane;
        touch(c.lds, Li < 65536 ? (const char*)A.wsp + (size_t)Li * 128 : (const char*)A.wout + (size_t)((Li < 94208 ? Li : 94207) - 65536) * 128); } }
    if (ld) p5_issueW(c, A, M.j2);
  }
  if (last < 5) return;
  phase5(c, A, M.i2, M.j2);
}

extern "C" void kernel_launch(void* const* d_in, const int* in_sizes, int n_in, void* d_out, int out_size, void* d_ws, size_t ws_size, hipStream_t stream) {
  (void)in_sizes; (void)n_in; (void)out_size; (void)ws_size;
  (void)hipFuncSetAttribute((const void*)fused_fwd, hipFuncAttributeMaxDynamicSharedMemorySize, LDS_BYTES);
  fused_fwd<<<256, 512, LDS_BYTES, stream>>>((const u16*)d_in[0], (const u16*)d_in[1], (const u16*)d_in[2], (const u16*)d_in[3], (const u16*)d_in[5], (const u16*)d_in[6],
                                             (const u16*)d_in[7], (const u16*)d_in[8], (u16*)d_out, (char*)d_ws);
}
```

```cpp
#include <hip/hip_runtime.h>
#include <hip/hip_bf16.h>
#include <stdint.h>
#include <type_traits>

#define LAS __attribute__((address_space(3)))
#define DI __device__ __forceinline__
typedef unsigned short u16;
typedef short bf16x8 __attribute__((ext_vector_type(8)));
typedef short s16x4 __attribute__((ext_vector_type(4)));
typedef float f32x4 __attribute__((ext_vector_type(4)));
typedef float f32x2 __attribute__((ext_vector_type(2)));
typedef unsigned u32x4 __attribute__((ext_vector_type(4)));
typedef unsigned u32x2 __attribute__((ext_vector_type(2)));
typedef __bf16 bf2 __attribute__((ext_vector_type(2)));
typedef LAS char* lds_t;

constexpr unsigned OFF_EPOCH = 0, OFF_ERR = 128, OFF_FLAG = 4096;
constexpr unsigned OFF_KVP = 262144;
constexpr unsigned OFF_ZP = OFF_KVP + 2097152;
constexpr unsigned OFF_ST2 = 27u << 20;
constexpr unsigned OFF_STH = OFF_ZP + 131072 + 262144;
constexpr unsigned OFF_GX = OFF_STH + 262144;
constexpr unsigned OFF_GC1 = OFF_GX + 65536;
constexpr unsigned OFF_GC3 = OFF_GC1 + 393216;
constexpr unsigned OFF_OF = 4u << 20, OFF_HF = 8u << 20, OFF_Z2 = 12u << 20, OFF_UF = 20u << 20;
constexpr unsigned WS_BYTES = 32u << 20;
enum { FG_KV = 0, FG_O = 1, FG_H = 2, FG_Z2 = 3, FG_U = 4 };
constexpr int LDS_BYTES = 163840;
constexpr int SC1 = 122880, L_WPHI = SC1, L_TAB1 = SC1 + 4096, L_XBUF = SC1 + 22528;
constexpr int L_XF1 = 65536, L_TB = 32768, L_KR = 65536, L_ZR = 131072;
constexpr int SC3 = 147456, L_TAB3 = SC3, L_FIN = SC3 + 4096;
constexpr int L_CNT = LDS_BYTES - 64, L_DUMMY = LDS_BYTES - 512;
constexpr int SC_SC1 = 16;

DI void bar() { asm volatile("" ::: "memory"); __builtin_amdgcn_s_barrier(); asm volatile("" ::: "memory"); }
template <int N> DI void wait_vm() { asm volatile("s_waitcnt vmcnt(%0)" ::"n"(N) : "memory"); }
DI int opaque(int v) { asm volatile("" : "+v"(v)); return v; }
DI void wait_lgkm0() { __builtin_amdgcn_s_waitcnt(0xc07f); }
DI unsigned pk2(float lo, float hi) { f32x2 v = {lo, hi}; return __builtin_bit_cast(unsigned, __builtin_convertvector(v, bf2)); }
DI bf16x8 pk8(const f32x4& a, const f32x4& b) { u32x4 r = {pk2(a[0], a[1]), pk2(a[2], a[3]), pk2(b[0], b[1]), pk2(b[2], b[3])}; return __builtin_bit_cast(bf16x8, r); }
DI float bflo(unsigned w) { return __uint_as_float(w << 16); }
DI float bfhi(unsigned w) { return __uint_as_float(w & 0xffff0000u); }
DI f32x4 mfma(bf16x8 a, bf16x8 b, f32x4 c) { return __builtin_amdgcn_mfma_f32_16x16x32_bf16(a, b, c, 0, 0, 0); }
DI bf16x8 ldsA(lds_t p) { return *(LAS bf16x8*)p; }
DI bf16x8 ldsT(lds_t p, int d) {
  s16x4 lo = __builtin_amdgcn_ds_read_tr16_b64_v4i16((LAS s16x4*)p), hi = __builtin_amdgcn_ds_read_tr16_b64_v4i16((LAS s16x4*)(p + d));
  return __builtin_shufflevector(lo, hi, 0, 1, 2, 3, 4, 5, 6, 7);
}
template <int D1, int D2> DI void tr4_issue(unsigned addr, u32x2 (&r)[4]) {
  asm volatile("ds_read_b64_tr_b16 %0, %4\n\tds_read_b64_tr_b16 %1, %4 offset:%5\n\tds_read_b64_tr_b16 %2, %4 offset:%6\n\tds_read_b64_tr_b16 %3, %4 offset:%7"
               : "=&v"(r[0]), "=&v"(r[1]), "=&v"(r[2]), "=&v"(r[3]) : "v"(addr), "n"(D1), "n"(D2), "n"(D1 + D2) : "memory");
}
DI void tr4_wait(u32x2 (&r)[4]) { asm volatile("s_waitcnt lgkmcnt(0)" : "+v"(r[0]), "+v"(r[1]), "+v"(r[2]), "+v"(r[3])::"memory"); }
DI float sum_g(float x) {
  auto a = __builtin_amdgcn_permlane16_swap(__float_as_uint(x), __float_as_uint(x), false, false);
  x = __uint_as_float(a[0]) + __uint_as_float(a[1]);
  auto b = __builtin_amdgcn_permlane32_swap(__float_as_uint(x), __float_as_uint(x), false, false);
  return __uint_as_float(b[0]) + __uint_as_float(b[1]);
}
template <int CTRL> DI float dpp_add(float x) { return x + __int_as_float(__builtin_amdgcn_update_dpp(0, __float_as_int(x), CTRL, 0xf, 0xf, true)); }
DI float sum_row16(float x) { x = dpp_add<0x128>(x); x = dpp_add<0x124>(x); x = dpp_add<0x122>(x); return dpp_add<0x121>(x); }
DI void sum_row16x8(f32x4& a, f32x4& b) {
#define RR(n) "v_add_f32_dpp %0, %0, %0 row_ror:" n " row_mask:0xf bank_mask:0xf\n\tv_add_f32_dpp %1, %1, %1 row_ror:" n " row_mask:0xf bank_mask:0xf\n\t" \
  "v_add_f32_dpp %2, %2, %2 row_ror:" n " row_mask:0xf bank_mask:0xf\n\tv_add_f32_dpp %3, %3, %3 row_ror:" n " row_mask:0xf bank_mask:0xf\n\t" \
  "v_add_f32_dpp %4, %4, %4 row_ror:" n " row_mask:0xf bank_mask:0xf\n\tv_add_f32_dpp %5, %5, %5 row_ror:" n " row_mask:0xf bank_mask:0xf\n\t" \
  "v_add_f32_dpp %6, %6, %6 row_ror:" n " row_mask:0xf bank_mask:0xf\n\tv_add_f32_dpp %7, %7, %7 row_ror:" n " row_mask:0xf bank_mask:0xf\n\t"
  float a0 = a[0], a1 = a[1], a2 = a[2], a3 = a[3], b0 = b[0], b1 = b[1], b2 = b[2], b3 = b[3];
  asm volatile("s_nop 1\n\t" RR("8") RR("4") RR("2") RR("1") "s_nop 0" : "+v"(a0), "+v"(a1), "+v"(a2), "+v"(a3), "+v"(b0), "+v"(b1), "+v"(b2), "+v"(b3));
#undef RR
  a = (f32x4){a0, a1, a2, a3}; b = (f32x4){b0, b1, b2, b3};
}
DI float dot2(unsigned a, unsigned b, float c) { return __builtin_amdgcn_fdot2_f32_bf16(__builtin_bit_cast(bf2, a), __builtin_bit_cast(bf2, b), c, false); }
constexpr unsigned ONES2 = 0x3f803f80u;
DI f32x4 gelu4(f32x4 x) {
  const f32x4 t = x * (x * x * -0.1029432397f + -2.302208198f);
  f32x4 d, r;
#pragma unroll
  for (int i = 0; i < 4; ++i) d[i] = __builtin_amdgcn_exp2f(t[i]);
  d += 1.0f;
#pragma unroll
  for (int i = 0; i < 4; ++i) r[i] = __builtin_amdgcn_rcpf(d[i]);
  return x * r;
}
DI u32x2 pk4(const f32x4& a) { return (u32x2){pk2(a[0], a[1]), pk2(a[2], a[3])}; }
DI f32x4 up4(u32x2 w) { return (f32x4){bflo(w[0]), bfhi(w[0]), bflo(w[1]), bfhi(w[1])}; }

struct Ctx {
  __amdgpu_buffer_rsrc_t ws;
  char* wsp; lds_t lds; unsigned tag; int lane, wid, b;
};
DI unsigned* flag_ptr(const Ctx& c, int g, int a, int bb) { return (unsigned*)(c.wsp + OFF_FLAG + ((g * 16 + a) * 16 + bb) * 128); }
DI void publish(const Ctx& c, int g, int a, int bb) {
  wait_vm<0>();
  if (c.lane == 0) {
    unsigned old = __hip_atomic_fetch_add((LAS unsigned*)(c.lds + L_CNT + g * 4), 1u, __ATOMIC_RELAXED, __HIP_MEMORY_SCOPE_WORKGROUP);
    if (old == 7u) __hip_atomic_store(flag_ptr(c, g, a, bb), c.tag, __ATOMIC_RELAXED, __HIP_MEMORY_SCOPE_AGENT);
  }
}
DI bool gave_up(const Ctx& c, unsigned spins, unsigned code) {
  if (spins < 40000u) return false;
  if (c.lane == 0) __hip_atomic_store((unsigned*)(c.wsp + OFF_ERR), code, __ATOMIC_RELAXED, __HIP_MEMORY_SCOPE_AGENT);
  return true;
}
DI void wait_flags(const Ctx& c, int g, int a, int n) {
  const unsigned* p = flag_ptr(c, g, a, c.lane < n ? c.lane : 0);
  for (unsigned spins = 0;; ++spins) {
    const unsigned v = __hip_atomic_load(p, __ATOMIC_RELAXED, __HIP_MEMORY_SCOPE_AGENT);
    if (__all(v == c.tag) || gave_up(c, spins, 0x100u + (unsigned)g)) break;
    __builtin_amdgcn_s_sleep(1);
  }
  asm volatile("" ::: "memory");
}

DI void st16(const Ctx& c, bf16x8 v, int off) { __builtin_amdgcn_raw_buffer_store_b128(__builtin_bit_cast(u32x4, v), c.ws, off, 0, SC_SC1); }
template <int AUX> DI void dma(__amdgpu_buffer_rsrc_t r, lds_t dst, int voff, int soff) {
  if (AUX) asm volatile("s_mov_b32 m0, %0\n\ts_nop 3\n\tbuffer_load_dwordx4 %1, %2, %3 offen sc1 lds" ::"s"((unsigned)(size_t)dst), "v"(voff), "s"(r), "s"(soff) : "memory");
  else asm volatile("s_mov_b32 m0, %0\n\ts_nop 3\n\tbuffer_load_dwordx4 %1, %2, %3 offen lds" ::"s"((unsigned)(size_t)dst), "v"(voff), "s"(r), "s"(soff) : "memory");
}

template <class IA, class IW>
DI void loader_loop32(IA issueA, IW issueW) {
  wait_vm<8>(); bar();
  issueA(3); issueW(3); wait_vm<4 + 8>(); bar();
  issueA(4); issueW(4); wait_vm<16>(); bar();
#pragma unroll 1
  for (int t = 2; t < 29; ++t) { issueA(t + 3); issueW(t + 3); wait_vm<16>(); bar(); }
  wait_vm<8>(); bar();
  wait_vm<0>(); bar();
  bar();
}
template <int NW, bool GATED, class IA, class IW, class POST, class GA, class FIN>
DI void loader_loop16(bool full, bool once, IA issueA, IW issueW, POST post, GA gather, FIN fin) {
  constexpr int PPW = 4 + NW;
  if (GATED) wait_vm<4>(); else wait_vm<PPW>();
  bar();
  issueA(2); issueW(2); post(0); wait_vm<PPW>(); bar();
#pragma unroll 1
  for (int t = 1; t < 5; ++t) { issueA(t + 2); issueW(t + 2); post(t); wait_vm<PPW>(); bar(); }
#pragma unroll
  for (int t = 5; t < 13; ++t) {
    issueA(t + 2); issueW(t + 2); gather(t - 5);
    if (full) { if (t == 5) wait_vm<PPW + 2>(); else wait_vm<PPW + 4>(); }
    else if (once && t <= 6) wait_vm<PPW + 2>();
    else wait_vm<PPW>();
    bar();
  }
  issueA(15); issueW(15); if (full) wait_vm<PPW + 2>(); else wait_vm<PPW>(); bar();
  wait_vm<0>(); fin(); bar();
  bar();
}
template <int S, int SB, class RD0, class RD1, class MM0, class MM1, class SCH>
DI void consumer_loop(int nt, RD0 rd0, RD1 rd1, MM0 mm0, MM1 mm1, SCH sched) {
  bar();
  rd0(0);
  int sb = 0;
#pragma unroll 2
  for (int t = 0; t < nt; ++t) {
    __builtin_amdgcn_sched_barrier(0);
    rd1(sb); mm0(); sched();
    __builtin_amdgcn_sched_barrier(0);
    wait_lgkm0(); bar();
    __builtin_amdgcn_sched_barrier(0);
    sb = (sb == (S - 1) * SB) ? 0 : sb + SB;
    rd0(sb); mm1(); sched();
    __builtin_amdgcn_sched_barrier(0);
  }
}
DI void touch(lds_t lds, const void* p) {
  asm volatile("s_mov_b32 m0, %1\n\ts_nop 0\n\tglobal_load_lds_dword %0, off" ::"v"(p), "s"((unsigned)(size_t)(lds + L_DUMMY)) : "memory"); }
template <int NM, int NR> DI void interleave() {
#pragma unroll
  for (int i = 0; i < NR; ++i) { __builtin_amdgcn_sched_group_barrier(0x008, 1, 0); __builtin_amdgcn_sched_group_barrier(0x100, 1, 0); }
  __builtin_amdgcn_sched_group_barrier(0x008, NM - NR, 0);
}

DI int rowmajor_voff(int lw, int lane, int pitch) {
  const int rr = lane >> 3, slot = 8 * (lw & 1) + rr;
  const int row = 8 * (slot >> 2) + 4 * (lw >> 1) + (slot & 3);
  return row * pitch + (((lane & 7) ^ (slot >> 1)) << 4);
}
DI int true_row(int mt, int i) { return 32 * (mt >> 1) + 8 * (i >> 2) + 4 * (mt & 1) + (i & 3); }

DI void acc_row(float& s, float& q, u32x4 w) {
#pragma unroll
  for (int i = 0; i < 4; ++i) { s = dot2(w[i], ONES2, s); q = dot2(w[i], w[i], q); } }
DI float col_sum(const u32x2 (&r)[4]) { float s0 = 0.f, s1 = 0.f;
#pragma unroll
  for (int i = 0; i < 4; ++i) { s0 = dot2(r[i][0], ONES2, s0); s1 = dot2(r[i][1], ONES2, s1); }
  return sum_g(s0 + s1); }
DI void st_granule(const Ctx& c, float v, int off) { __builtin_amdgcn_raw_buffer_store_b64((u32x2){__float_as_uint(v), c.tag}, c.ws, off, 0, SC_SC1); }
DI float sum_granules16(const Ctx& c, u32x2 (&v)[16], int off, int stride) {
  for (unsigned spins = 0;; ++spins) {
    bool ok = true;
#pragma unroll
    for (int k = 0; k < 16; ++k) ok &= v[k][1] == c.tag;
    if (__all(ok) || gave_up(c, spins, 0x200u)) break;
    __builtin_amdgcn_s_sleep(2);
#pragma unroll
    for (int k = 0; k < 16; ++k) v[k] = __builtin_amdgcn_raw_buffer_load_b64(c.ws, off + k * stride, 0, SC_SC1);
  }
  float s = 0.f;
#pragma unroll
  for (int k = 0; k < 16; ++k) s += __uint_as_float(v[k][0]);
  return s;
}

struct Args { const u16 *x, *wqkv, *wphi, *wo, *win, *wsp, *bsp, *wout; u16* out; char* ws; };

DI void get_tag(Ctx& c) { unsigned e; asm volatile("s_load_dword %0, %1, 0x0\n\ts_waitcnt lgkmcnt(0)" : "=&s"(e) : "s"(c.wsp) : "memory"); c.tag = e + 1u; }
DI void kv_gather(const Ctx& c, int j1) {
  const int lane = c.lane, g = lane >> 4, i16 = lane & 15, tid = c.wid * 64 + lane;
  f32x4 a0 = {0, 0, 0, 0}, a1 = {0, 0, 0, 0};
  u32x4 v[16];
  const unsigned f0 = __hip_atomic_load(flag_ptr(c, FG_KV, j1, i16), __ATOMIC_RELAXED, __HIP_MEMORY_SCOPE_AGENT);
  const unsigned ready = __builtin_amdgcn_readfirstlane((unsigned)(__ballot(f0 == c.tag) & 0xffffull));
  asm volatile("" ::: "memory");
#pragma unroll
  for (int k = 0; k < 16; ++k) if ((ready >> k) & 1u) v[k] = __builtin_amdgcn_raw_buffer_load_b128(c.ws, tid * 16, OFF_KVP + (j1 * 16 + k) * 8192, SC_SC1);
  if (ready != 0xffffu) {
    wait_flags(c, FG_KV, j1, 16);
#pragma unroll
    for (int k = 0; k < 16; ++k) if (!((ready >> k) & 1u)) v[k] = __builtin_amdgcn_raw_buffer_load_b128(c.ws, tid * 16, OFF_KVP + (j1 * 16 + k) * 8192, SC_SC1);
  }
  float zs = 0.f;
  if (c.wid < 2) { unsigned zv[16];
#pragma unroll
    for (int k = 0; k < 16; ++k) zv[k] = __builtin_amdgcn_raw_buffer_load_b32(c.ws, lane * 4, OFF_ZP + ((j1 * 16 + k) * 2 + c.wid) * 256, SC_SC1);
#pragma unroll
    for (int k = 0; k < 16; ++k) zs += __uint_as_float(zv[k]); }
#pragma unroll
  for (int k = 0; k < 16; ++k) { a0[0] += bflo(v[k][0]); a0[1] += bfhi(v[k][0]); a0[2] += bflo(v[k][1]); a0[3] += bfhi(v[k][1]); a1[0] += bflo(v[k][2]); a1[1] += bfhi(v[k][2]); a1[2] += bflo(v[k][3]); a1[3] += bfhi(v[k][3]); }
  const int w = c.wid, hd = w >> 2, dt = (w >> 1) & 1, fp = w & 1;
  const lds_t tb = c.lds + L_XBUF + hd * 8192 + (32 * fp + i16) * 128 + (16 * dt + 4 * g) * 2;
  *(LAS u32x2*)tb = (u32x2){pk2(a0[0], a0[1]), pk2(a0[2], a0[3])};
  *(LAS u32x2*)(tb + 16 * 128) = (u32x2){pk2(a1[0], a1[1]), pk2(a1[2], a1[3])};
  if (c.wid < 2) { const unsigned z2 = pk2(zs, zs); const lds_t t3 = c.lds + L_XBUF + c.wid * 8192 + lane * 128 + 64;
    *(LAS u32x4*)t3 = (u32x4){z2, z2, z2, z2}; *(LAS u32x4*)(t3 + 16) = (u32x4){z2, z2, z2, z2}; }
  wait_lgkm0();
}
struct Map { int i1, j1, i2, j2, i3, j3; bool act3; };
DI void p1_load(Ctx& c, const Args& A, int i1, int j1) {
  const int lane = c.lane, lw = c.wid - 4, g = lane >> 4, q = (lane & 15) >> 2, p = lane & 3;
  const lds_t lds = c.lds;
  const __amdgpu_buffer_rsrc_t rWS = c.ws;
  constexpr int S = 3, SB = 40960, NW = 6;
  const __amdgpu_buffer_rsrc_t rA = __builtin_amdgcn_make_buffer_rsrc((void*)(A.x + (size_t)i1 * 128 * 1024), 0, 128 * 2048, 0x00020000);
  const __amdgpu_buffer_rsrc_t rW = __builtin_amdgcn_make_buffer_rsrc((void*)A.wqkv, 0, 1024 * 6144, 0x00020000);
  const __amdgpu_buffer_rsrc_t rP = __builtin_amdgcn_make_buffer_rsrc((void*)A.wphi, 0, 65536, 0x00020000);
  const int voA = rowmajor_voff(lw, lane, 2048);
  const int rot = 4 * (i1 & 3);
  auto issueA = [&](int t) { const lds_t d = lds + (t % S) * SB + lw * 1024; const int so = ((t + rot) & 15) * 128;
#pragma unroll
    for (int i = 0; i < 4; ++i) dma<0>(rA, d + i * 4096, voA, so + i * 32 * 2048); };
  issueA(0);
  int voW[NW];
  { const int ln = opaque(lane);
#pragma unroll
    for (int i = 0; i < 3; ++i) { const int o = (NW * lw + i) * 1024 + ln * 16, r = o / 384, rem = o % 384, seg = rem >> 7, s16 = (rem & 127) >> 4;
      const int c32 = (s16 >> 1) ^ ((r >> 1) & 3), r5 = r & 31, k = (r & 32) + 8 * ((r5 >> 2) & 3) + 4 * (r5 >> 4) + (r5 & 3);
      voW[i] = k * 6144 + seg * 2048 + j1 * 128 + c32 * 32 + (s16 & 1) * 16;
      voW[i + 3] = voW[i] + 16 * 6144; }
  }
  auto issueW = [&](int t) { const lds_t d = lds + (t % S) * SB + 16384 + lw * NW * 1024; const int so = ((t + rot) & 15) * 64 * 6144;
#pragma unroll
    for (int i = 0; i < NW; ++i) dma<0>(rW, d + i * 1024, voW[i], so); };
  issueW(0); issueA(1); issueW(1);
  dma<0>(rP, lds + L_WPHI + lw * 1024, lane * 16 + lw * 1024, j1 * 4096);
  get_tag(c);

  u32x2 gv[16]; u32x4 gx[2];
  const int goff = OFF_GC1 + j1 * 16 * 1536 + (lw * 64 + lane) * 8, xoff = OFF_GX + (i1 * 128 + lane) * 16;
  const int tau = i1 >> 2;
  auto post = [&](int t) {
    if (t != tau) return;
    const int sw = (2 * g + (q >> 1)) & 3; u32x2 cr[3][4];
#pragma unroll
    for (int n = 0; n < 3; ++n) { const int nu = 3 * lw + n;
      tr4_issue<16 * 384, 32 * 384>((unsigned)(size_t)(lds + (t % S) * SB + 16384 + (4 * g + q) * 384 + 8 * p + (nu >> 2) * 128 + 32 * ((nu & 3) ^ sw)), cr[n]); }
#pragma unroll
    for (int n = 0; n < 3; ++n) { tr4_wait(cr[n]); const float s = col_sum(cr[n]);
      if (lane < 16) st_granule(c, s, OFF_GC1 + (j1 * 16 + rot + tau) * 1536 + ((3 * lw + n) * 16 + lane) * 8); }
  };
  auto gather = [&](int k) {
    if (lw < 3) { gv[2 * k] = __builtin_amdgcn_raw_buffer_load_b64(rWS, goff + 2 * k * 1536, 0, SC_SC1); gv[2 * k + 1] = __builtin_amdgcn_raw_buffer_load_b64(rWS, goff + (2 * k + 1) * 1536, 0, SC_SC1); }
    else if (k == 0) { gx[0] = __builtin_amdgcn_raw_buffer_load_b128(rWS, xoff, 0, SC_SC1); gx[1] = __builtin_amdgcn_raw_buffer_load_b128(rWS, xoff + 1024, 0, SC_SC1); }
  };
  auto fin = [&]() {
    if (lw < 3) *(LAS float*)(lds + L_TAB1 + 1024 + (lw * 64 + lane) * 4) = sum_granules16(c, gv, goff, 1536);
    else {
      for (unsigned spins = 0;; ++spins) {
        if (__all(gx[0][1] == c.tag && gx[0][3] == c.tag && gx[1][1] == c.tag && gx[1][3] == c.tag) || gave_up(c, spins, 0x201u)) break;
        __builtin_amdgcn_s_sleep(2);
        gx[0] = __builtin_amdgcn_raw_buffer_load_b128(rWS, xoff, 0, SC_SC1); gx[1] = __builtin_amdgcn_raw_buffer_load_b128(rWS, xoff + 1024, 0, SC_SC1);
      }
#pragma unroll
      for (int e = 0; e < 2; ++e) { *(LAS unsigned*)(lds + L_TAB1 + (lane + 64 * e) * 4) = gx[e][0]; *(LAS unsigned*)(lds + L_TAB1 + 512 + (lane + 64 * e) * 4) = gx[e][2]; }
    }
    wait_lgkm0();
    __builtin_amdgcn_s_waitcnt(0x0f70);
    const int gw = c.b * 4 + lw;
    if (lane < 56) touch(lds, (const char*)A.win + (size_t)(gw * 56 + lane) * 128);
    if (lane < 16) touch(lds, (const char*)A.wo + (size_t)(gw * 16 + lane) * 128);
  };
  loader_loop16<NW, false>(lw < 3, lw == 3, issueA, issueW, post, gather, fin);
}

struct Acc1 { f32x4 q[2][2], k[2][2], v[2][2]; };
DI void p1_mma(Ctx& c, const Args& A, int i1, int j1, Acc1& e) {
  const int lane = c.lane, wr = c.wid >> 1, wc = c.wid & 1, g = lane >> 4, q = (lane & 15) >> 2, p = lane & 3, i16 = lane & 15;
  const lds_t lds = c.lds;
  const f32x4 Z4 = {0.f, 0.f, 0.f, 0.f};
  constexpr int S = 3, SB = 40960;
  f32x4 aq[4][2], ak[4][2], av[4][2];
#pragma unroll
  for (int m = 0; m < 4; ++m)
#pragma unroll
    for (int n = 0; n < 2; ++n) { aq[m][n] = Z4; ak[m][n] = Z4; av[m][n] = Z4; asm volatile("" : "+v"(aq[m][n]), "+v"(ak[m][n]), "+v"(av[m][n])); }
  const int sw = (2 * g + (q >> 1)) & 3;
  const int ao0 = (wr * 64 + i16) * 128 + ((g ^ (i16 >> 1)) << 4), ao1 = ao0 ^ 64;
  const int wl = 16384 + (4 * g + q) * 384 + 8 * p;
  const int wo0 = wl + 32 * ((2 * wc) ^ sw), wo1 = wl + 32 * ((2 * wc + 1) ^ sw);
  bf16x8 a0[4], w0[6], a1[4], w1[6];
  auto rd = [&](bf16x8(&a)[4], bf16x8(&w)[6], int sb, int ks) { const lds_t s = lds + sb;
#pragma unroll
    for (int sg = 0; sg < 3; ++sg) { w[2 * sg] = ldsT(s + wo0 + sg * 128 + ks * 32 * 384, 16 * 384); w[2 * sg + 1] = ldsT(s + wo1 + sg * 128 + ks * 32 * 384, 16 * 384); }
#pragma unroll
    for (int m = 0; m < 4; ++m) a[m] = ldsA(s + (ks ? ao1 : ao0) + m * 2048); };
  auto mm = [&](bf16x8(&a)[4], bf16x8(&w)[6]) {
#pragma unroll
    for (int n = 0; n < 2; ++n)
#pragma unroll
      for (int m = 0; m < 4; ++m) aq[m][n] = mfma(w[n], a[m], aq[m][n]);
#pragma unroll
    for (int n = 0; n < 2; ++n)
#pragma unroll
      for (int m = 0; m < 4; ++m) ak[m][n] = mfma(w[2 + n], a[m], ak[m][n]);
#pragma unroll
    for (int n = 0; n < 2; ++n)
#pragma unroll
      for (int m = 0; m < 4; ++m) av[m][n] = mfma(a[m], w[4 + n], av[m][n]); };

  {
    u32x4 xv[2][2];
#pragma unroll
    for (int e2 = 0; e2 < 2; ++e2) { const int R = 8 * j1 + 2 * c.wid + e2; const u16* xr = A.x + (size_t)(i1 * 128 + true_row(R >> 4, R & 15)) * 1024 + lane * 16;
      xv[e2][0] = *(const u32x4*)xr; xv[e2][1] = *(const u32x4*)(xr + 8); }
    get_tag(c);
#pragma unroll
    for (int e2 = 0; e2 < 2; ++e2) { float s = 0.f, qq = 0.f; acc_row(s, qq, xv[e2][0]); acc_row(s, qq, xv[e2][1]);
      s = sum_g(sum_row16(s)) * (1.0f / 1024.0f); qq = sum_g(sum_row16(qq)) * (1.0f / 1024.0f);
      const float rs = __builtin_amdgcn_rsqf(fmaxf(qq - s * s, 0.f) + 1e-5f);
      if (lane == 0) __builtin_amdgcn_raw_buffer_store_b128((u32x4){__float_as_uint(s), c.tag, __float_as_uint(rs), c.tag}, c.ws, OFF_GX + (i1 * 128 + 8 * j1 + 2 * c.wid + e2) * 16, 0, SC_SC1); }
  }
  consumer_loop<S, SB>(16, [&](int sb) { rd(a0, w0, sb, 0); }, [&](int sb) { rd(a1, w1, sb, 1); }, [&]() { mm(a0, w0); }, [&]() { mm(a1, w1); }, []() { interleave<24, 16>(); });

  const lds_t xf = lds + L_XF1 + c.wid * 12288 + lane * 16;
#pragma unroll
  for (int m = 0; m < 2; ++m)
#pragma unroll
    for (int n = 0; n < 2; ++n) {
      *(LAS f32x4*)(xf + (m * 6 + n) * 1024) = aq[2 + m][n]; *(LAS f32x4*)(xf + (m * 6 + 2 + n) * 1024) = ak[2 + m][n]; *(LAS f32x4*)(xf + (m * 6 + 4 + n) * 1024) = av[2 + m][n];
      e.q[m][n] = aq[m][n]; e.k[m][n] = ak[m][n]; e.v[m][n] = av[m][n];
    }
  wait_lgkm0();
}

DI void both_g_max(float& a, float& b) {
  auto r = __builtin_amdgcn_permlane16_swap(__float_as_uint(a), __float_as_uint(b), false, false);
  const float x = fmaxf(__uint_as_float(r[0]), __uint_as_float(r[1]));
  auto s = __builtin_amdgcn_permlane32_swap(__float_as_uint(x), __float_as_uint(x), false, false);
  const float y = fmaxf(__uint_as_float(s[0]), __uint_as_float(s[1]));
  auto t = __builtin_amdgcn_permlane16_swap(__float_as_uint(y), __float_as_uint(y), false, false);
  a = __uint_as_float(t[0]); b = __uint_as_float(t[1]);
}
DI void both_g_sum(float& a, float& b) {
  auto r = __builtin_amdgcn_permlane16_swap(__float_as_uint(a), __float_as_uint(b), false, false);
  const float x = __uint_as_float(r[0]) + __uint_as_float(r[1]);
  auto s = __builtin_amdgcn_permlane32_swap(__float_as_uint(x), __float_as_uint(x), false, false);
  const float y = __uint_as_float(s[0]) + __uint_as_float(s[1]);
  auto t = __builtin_amdgcn_permlane16_swap(__float_as_uint(y), __float_as_uint(y), false, false);
  a = __uint_as_float(t[0]); b = __uint_as_float(t[1]);
}
DI void feat(const f32x4& p0, const f32x4& p1, f32x4& e0, f32x4& e1, f32x4& n0, f32x4& n1) {
  constexpr float L2E = 1.4426950408889634f;
  const f32x4 t0 = p0 * L2E, t1 = p1 * L2E;
  float mx = fmaxf(fmaxf(fmaxf(t0[0], t0[1]), t0[2]), fmaxf(fmaxf(fmaxf(t0[3], t1[0]), t1[1]), fmaxf(t1[2], t1[3])));
  float mn = -fminf(fminf(fminf(t0[0], t0[1]), t0[2]), fminf(fminf(fminf(t0[3], t1[0]), t1[1]), fminf(t1[2], t1[3])));
  both_g_max(mx, mn);
  const f32x4 a0 = t0 - mx, a1 = t1 - mx, b0 = -t0 - mn, b1 = -t1 - mn;
#pragma unroll
  for (int r = 0; r < 4; ++r) { e0[r] = __builtin_amdgcn_exp2f(a0[r]); e1[r] = __builtin_amdgcn_exp2f(a1[r]); n0[r] = __builtin_amdgcn_exp2f(b0[r]); n1[r] = __builtin_amdgcn_exp2f(b1[r]); }
  const f32x4 se = e0 + e1, sn4 = n0 + n1;
  float sp = (se[0] + se[1]) + (se[2] + se[3]), sn = (sn4[0] + sn4[1]) + (sn4[2] + sn4[3]);
  both_g_sum(sp, sn);
  sp = __builtin_amdgcn_rcpf(sp); sn = __builtin_amdgcn_rcpf(sn);
  e0 *= sp; e1 *= sp; n0 *= sn; n1 *= sn;
}

DI void p1_epilogue(Ctx& c, const Args& A, const Map& M, Acc1& e) {
  const int lane = opaque(c.lane), cw = c.wid & 3, hf = c.wid >> 2, wr = cw >> 1, wc = cw & 1, g = lane >> 4, q = (lane & 15) >> 2, p = lane & 3, i16 = lane & 15;
  const int lw = cw, i1 = M.i1, j1 = M.j1;
  const lds_t lds = c.lds;
  const f32x4 Z4 = {0.f, 0.f, 0.f, 0.f};
  bar();

  if (hf) { const lds_t xf = lds + L_XF1 + cw * 12288 + lane * 16;
#pragma unroll
    for (int m = 0; m < 2; ++m)
#pragma unroll
      for (int n = 0; n < 2; ++n) { e.q[m][n] = *(LAS f32x4*)(xf + (m * 6 + n) * 1024); e.k[m][n] = *(LAS f32x4*)(xf + (m * 6 + 2 + n) * 1024); e.v[m][n] = *(LAS f32x4*)(xf + (m * 6 + 4 + n) * 1024); } }
  const lds_t tab = lds + L_TAB1;
  bf16x8 qf[2], kf[2], vf[2], wT[2];
  {
    f32x4 vv[2][2];
#pragma unroll
    for (int m = 0; m < 2; ++m) {
      const int R = 64 * wr + 32 * hf + 16 * m;
      const float mu = *(LAS float*)(tab + (R + i16) * 4), rs = *(LAS float*)(tab + 512 + (R + i16) * 4), nm = -mu * rs;
      const f32x4 mu4 = *(LAS f32x4*)(tab + (R + 4 * g) * 4), rs4 = *(LAS f32x4*)(tab + 512 + (R + 4 * g) * 4), nm4 = -mu4 * rs4;
      f32x4 qq[2], kk[2];
#pragma unroll
      for (int n = 0; n < 2; ++n) {
        const f32x4 cq = *(LAS f32x4*)(tab + 1024 + (32 * wc + 16 * n + 4 * g) * 4), ck = *(LAS f32x4*)(tab + 1024 + (64 + 32 * wc + 16 * n + 4 * g) * 4);
        const float cv = *(LAS float*)(tab + 1024 + (128 + 32 * wc + 16 * n + i16) * 4);
        qq[n] = e.q[m][n] * rs + cq * nm; kk[n] = e.k[m][n] * rs + ck * nm; vv[m][n] = e.v[m][n] * rs4 + nm4 * cv;
      }
      qf[m] = pk8(qq[0], qq[1]); kf[m] = pk8(kk[0], kk[1]);
    }
    vf[0] = pk8(vv[0][0], vv[1][0]); vf[1] = pk8(vv[0][1], vv[1][1]);
#pragma unroll
    for (int ft = 0; ft < 2; ++ft) wT[ft] = ldsT(lds + L_WPHI + wc * 2048 + (4 * g + q) * 64 + ft * 32 + 8 * p, 16 * 64);
  }
  wait_lgkm0(); bar();

  {
    const lds_t tb = lds + L_TB + c.wid * 4096;
#pragma unroll
    for (int m = 0; m < 2; ++m) {
      const f32x4 p0 = mfma(wT[0], kf[m], Z4), p1 = mfma(wT[1], kf[m], Z4);
      f32x4 e0, e1, n0, n1; feat(p0, p1, e0, e1, n0, n1);
      const lds_t rw = tb + (16 * m + i16) * 128;
      *(LAS u32x2*)(rw + 8 * ((0 + g) ^ i16)) = pk4(e0); *(LAS u32x2*)(rw + 8 * ((4 + g) ^ i16)) = pk4(e1);
      *(LAS u32x2*)(rw + 8 * ((8 + g) ^ i16)) = pk4(n0); *(LAS u32x2*)(rw + 8 * ((12 + g) ^ i16)) = pk4(n1);
    }
    asm volatile("s_waitcnt lgkmcnt(0)" ::: "memory");
    const bf16x8 ones = __builtin_bit_cast(bf16x8, (u32x4){ONES2, ONES2, ONES2, ONES2});
    const lds_t kr = lds + L_KR + c.wid * 8192 + lane * 16;
    float zf[4];
#pragma unroll
    for (int f = 0; f < 4; ++f) {
      const int r0 = 4 * g + q;
      const s16x4 lo = __builtin_amdgcn_ds_read_tr16_b64_v4i16((LAS s16x4*)(tb + r0 * 128 + 8 * ((4 * f + p) ^ r0)));
      const s16x4 hi = __builtin_amdgcn_ds_read_tr16_b64_v4i16((LAS s16x4*)(tb + (16 + r0) * 128 + 8 * ((4 * f + p) ^ r0)));
      const bf16x8 pf = __builtin_shufflevector(lo, hi, 0, 1, 2, 3, 4, 5, 6, 7);
      *(LAS f32x4*)(kr + f * 1024) = mfma(vf[0], pf, Z4); *(LAS f32x4*)(kr + (4 + f) * 1024) = mfma(vf[1], pf, Z4);
      zf[f] = mfma(ones, pf, Z4)[0];
    }
    *(LAS float*)(lds + L_ZR + c.wid * 256 + lane * 4) = g == 0 ? zf[0] : g == 1 ? zf[1] : g == 2 ? zf[2] : zf[3];

    wait_lgkm0(); bar();
    f32x4 k0 = Z4, k1 = Z4;
#pragma unroll
    for (int w = 0; w < 4; ++w) { const lds_t s = lds + L_KR + (wc + 2 * w) * 8192 + (4 * wr + 2 * hf) * 1024 + lane * 16; k0 += *(LAS f32x4*)s; k1 += *(LAS f32x4*)(s + 1024); }
    st16(c, pk8(k0, k1), lane * 16 + (OFF_KVP + (j1 * 16 + i1) * 8192 + wc * 4096 + wr * 2048 + hf * 1024));
    if (c.wid < 2) { float zt = 0.f;
#pragma unroll
      for (int w = 0; w < 4; ++w) zt += *(LAS float*)(lds + L_ZR + (wc + 2 * w) * 256 + lane * 4);
      __builtin_amdgcn_raw_buffer_store_b32(__float_as_uint(zt), c.ws, lane * 4, OFF_ZP + ((j1 * 16 + i1) * 2 + wc) * 256, SC_SC1); }
    wait_lgkm0(); bar();

    publish(c, FG_KV, j1, i1);
  }

  const __amdgpu_buffer_rsrc_t rW2 = __builtin_amdgcn_make_buffer_rsrc((void*)A.wo, 0, 1024 * 2048, 0x00020000);
  int voW2[2];
#pragma unroll
  for (int i = 0; i < 2; ++i) { const int r = (2 * lw + i) * 8 + (lane >> 3), s16 = lane & 7; voW2[i] = r * 2048 + M.j2 * 128 + (((s16 >> 1) ^ ((r >> 1) & 3)) << 5) + (s16 & 1) * 16; }
  auto issueW2 = [&](int t) {
#pragma unroll
    for (int i = 0; i < 2; ++i) dma<0>(rW2, lds + t * 8192 + (lw * 2 + i) * 1024, voW2[i], t * 64 * 2048); };
  if (hf) {
    if (M.act3) {
      const __amdgpu_buffer_rsrc_t rW3 = __builtin_amdgcn_make_buffer_rsrc((void*)A.win, 0, 1024 * 7168, 0x00020000);
#pragma unroll
      for (int i = 0; i < 8; ++i) { const int r = (8 * lw + i) * 2 + (lane >> 5), l5 = lane & 31, seg = l5 >> 4, s16 = l5 & 15;
        dma<0>(rW3, lds + 32768 + (8 * lw + i) * 1024, r * 7168 + (seg == 0 ? 3584 : 0) + M.j3 * 256 + (((s16 >> 1) ^ (r & 7)) << 5) + (s16 & 1) * 16, M.i3 * 64 * 7168); }
    }
#pragma unroll
    for (int t = 0; t < 3; ++t) { issueW2(t); issueW2(8 + t); }
  }
  bf16x8 ph[2][2];
#pragma unroll
  for (int m = 0; m < 2; ++m) {
    const f32x4 p0 = mfma(wT[0], qf[m], Z4), p1 = mfma(wT[1], qf[m], Z4);
    f32x4 e0, e1, n0, n1; feat(p0, p1, e0, e1, n0, n1);
    ph[m][0] = pk8(e0, e1); ph[m][1] = pk8(n0, n1);
  }
  asm volatile("" : "+v"(ph[0][0]), "+v"(ph[0][1]), "+v"(ph[1][0]), "+v"(ph[1][1]));

  kv_gather(c, j1);
  bar();

  {
    bf16x8 kt[3][2];
#pragma unroll
    for (int d = 0; d < 3; ++d)
#pragma unroll
      for (int s = 0; s < 2; ++s) kt[d][s] = ldsT(lds + L_XBUF + wc * 8192 + (32 * s + 4 * g + q) * 128 + 32 * d + 8 * p, 16 * 128);
#pragma unroll
    for (int m = 0; m < 2; ++m) {
      const f32x4 n0 = mfma(kt[0][1], ph[m][1], mfma(kt[0][0], ph[m][0], Z4)), n1 = mfma(kt[1][1], ph[m][1], mfma(kt[1][0], ph[m][0], Z4));
      const f32x4 dn = mfma(kt[2][1], ph[m][1], mfma(kt[2][0], ph[m][0], Z4));
      const float r = __builtin_amdgcn_rcpf(dn[0] + 1e-6f);
      st16(c, pk8(n0 * r, n1 * r), lane * 16 + (OFF_OF + (i1 * 16 + j1) * 16384 + ((4 * wr + 2 * hf + m) * 2 + wc) * 1024));
    }
  }
  publish(c, FG_O, i1, j1);

  if (hf && M.act3) {
    u32x2 cr[4][4];
#pragma unroll
    for (int n = 0; n < 4; ++n) tr4_issue<16 * 512, 32 * 512>((unsigned)(size_t)(lds + 32768 + (4 * g + q) * 512 + 8 * p + (lw >> 1) * 256 + 32 * ((4 * (lw & 1) + n) ^ (4 * (g & 1) + q))), cr[n]);
#pragma unroll
    for (int n = 0; n < 4; ++n) { tr4_wait(cr[n]); const float s = col_sum(cr[n]);
      if (lane < 16) st_granule(c, s, OFF_GC3 + (M.j3 * 16 + M.i3) * 2048 + ((4 * lw + n) * 16 + lane) * 8); }
  }
  if (hf) {
    issueW2(3);
#pragma unroll
    for (int t = 11; t < 16; ++t) issueW2(t);
  } else if (c.wid == 0) wait_flags(c, FG_O, M.i2, 16);
}

struct Ld3 { __amdgpu_buffer_rsrc_t rW; int voW[8]; };
DI void p3_setup(const Ctx& c, const Args& A, int j3, Ld3& L) {
  const int lane = opaque(c.lane), lw = c.wid & 3;
  L.rW = __builtin_amdgcn_make_buffer_rsrc((void*)A.win, 0, 1024 * 7168, 0x00020000);
#pragma unroll
  for (int i = 0; i < 8; ++i) { const int r = (8 * lw + i) * 2 + (lane >> 5), l5 = lane & 31, seg = l5 >> 4, s16 = l5 & 15;
    L.voW[i] = r * 7168 + (seg == 0 ? 3584 : 0) + j3 * 256 + (((s16 >> 1) ^ (r & 7)) << 5) + (s16 & 1) * 16; }
}
DI void p3_issueW(const Ctx& c, const Ld3& L, int t) { const lds_t d = c.lds + (t % 3) * 49152 + 16384 + (c.wid & 3) * 8192;
#pragma unroll
  for (int i = 0; i < 8; ++i) dma<0>(L.rW, d + i * 1024, L.voW[i], t * 64 * 7168); }
DI void p3_load(Ctx& c, const Ld3& L, int i3) {
  const int lane = opaque(c.lane), lw = c.wid - 4;
  const lds_t lds = c.lds;
  constexpr int S = 3, SB = 49152;
  const int voF = lane * 16 + lw * 1024;
  auto issueA = [&](int t) { const lds_t d = lds + (t % S) * SB + lw * 1024; const int so = OFF_HF + (i3 * 16 + t) * 16384;
#pragma unroll
    for (int i = 0; i < 4; ++i) dma<SC_SC1>(c.ws, d + i * 4096, voF, so + i * 4096); };
  auto issueW = [&](int t) { p3_issueW(c, L, t); };
  bar();
  issueA(0); issueA(1);
  u32x2 hv[16]; const int R = (lw & 1) * 64 + lane;
  auto gather = [&](int k) { if (lw < 2) {
      hv[2 * k] = __builtin_amdgcn_raw_buffer_load_b64(c.ws, R * 8, OFF_STH + (i3 * 16 + 2 * k) * 1024, SC_SC1);
      hv[2 * k + 1] = __builtin_amdgcn_raw_buffer_load_b64(c.ws, R * 8, OFF_STH + (i3 * 16 + 2 * k + 1) * 1024, SC_SC1); } };
  auto fin = [&]() { if (lw < 2) { float s = 0.f, qq = 0.f;
#pragma unroll
      for (int k = 0; k < 16; ++k) { s += __uint_as_float(hv[k][0]); qq += __uint_as_float(hv[k][1]); }
      s *= (1.0f / 1024.0f); qq *= (1.0f / 1024.0f);
      *(LAS float*)(lds + L_TAB3 + R * 4) = s; *(LAS float*)(lds + L_TAB3 + 512 + R * 4) = __builtin_amdgcn_rsqf(fmaxf(qq - s * s, 0.f) + 1e-5f);
      wait_lgkm0(); } };
  loader_loop16<8, true>(lw < 2, false, issueA, issueW, [](int) {}, gather, fin);
}
struct Acc3 { f32x4 z2[2][4], z1[2][4]; };
DI int xf3_base(int cw) { return cw < 3 ? 16384 + cw * 32768 : 98304; }
DI void p3_mma(Ctx& c, int i3, int j3, Acc3& e) {
  const int lane = opaque(c.lane), wr = c.wid >> 1, wc = c.wid & 1, g = lane >> 4, q = (lane & 15) >> 2, p = lane & 3;
  const lds_t lds = c.lds;
  const __amdgpu_buffer_rsrc_t rWS = c.ws;
  const f32x4 Z4 = {0.f, 0.f, 0.f, 0.f};
  constexpr int S = 3, SB = 49152;
  if (wc == 1) {
    const int off = OFF_GC3 + j3 * 16 * 2048 + (128 * wr + 2 * lane) * 8; u32x4 gv[16];
    for (unsigned spins = 0;; ++spins) {
      bool ok = true;
#pragma unroll
      for (int k = 0; k < 16; ++k) gv[k] = __builtin_amdgcn_raw_buffer_load_b128(rWS, off + k * 2048, 0, SC_SC1);
#pragma unroll
      for (int k = 0; k < 16; ++k) ok &= gv[k][1] == c.tag && gv[k][3] == c.tag;
      if (__all(ok) || gave_up(c, spins, 0x202u)) break;
      __builtin_amdgcn_s_sleep(2);
    }
    f32x2 cs = {0.f, 0.f};
#pragma unroll
    for (int k = 0; k < 16; ++k) { cs[0] += __uint_as_float(gv[k][0]); cs[1] += __uint_as_float(gv[k][2]); }
    *(LAS f32x2*)(lds + L_TAB3 + 1024 + (128 * wr + 2 * lane) * 4) = cs;
    wait_lgkm0();
  } else if (c.wid == 0) wait_flags(c, FG_H, i3, 16);
  f32x4 a2[4][4], a1_[4][4];
#pragma unroll
  for (int m = 0; m < 4; ++m)
#pragma unroll
    for (int n = 0; n < 4; ++n) { a2[m][n] = Z4; a1_[m][n] = Z4; }
#pragma unroll
  for (int m = 0; m < 4; ++m) asm volatile("" : "+v"(a2[m][0]), "+v"(a2[m][1]), "+v"(a2[m][2]), "+v"(a2[m][3]), "+v"(a1_[m][0]), "+v"(a1_[m][1]), "+v"(a1_[m][2]), "+v"(a1_[m][3]));
  bar();
  const int ao2 = wr * 8192 + lane * 16;
  int wo_[4];
#pragma unroll
  for (int n = 0; n < 4; ++n) wo_[n] = 16384 + (4 * g + q) * 512 + 8 * p + 32 * ((4 * wc + n) ^ (4 * (g & 1) + q));
  bf16x8 a0[4], w0[8], a1[4], w1[8];
  auto rd = [&](bf16x8(&a)[4], bf16x8(&w)[8], int sb, int ks) { const lds_t s = lds + sb;
#pragma unroll
    for (int n = 0; n < 8; ++n) w[n] = ldsT(s + wo_[n & 3] + (n >> 2) * 256 + ks * 32 * 512, 16 * 512);
#pragma unroll
    for (int m = 0; m < 4; ++m) a[m] = ldsA(s + ao2 + m * 2048 + ks * 1024); };
  auto mm = [&](bf16x8(&a)[4], bf16x8(&w)[8]) {
#pragma unroll
    for (int n = 0; n < 4; ++n)
#pragma unroll
      for (int m = 0; m < 4; ++m) a2[m][n] = mfma(a[m], w[n], a2[m][n]);
#pragma unroll
    for (int n = 0; n < 4; ++n)
#pragma unroll
      for (int m = 0; m < 4; ++m) a1_[m][n] = mfma(w[4 + n], a[m], a1_[m][n]); };
  consumer_loop<S, SB>(16, [&](int sb) { rd(a0, w0, sb, 0); }, [&](int sb) { rd(a1, w1, sb, 1); }, [&]() { mm(a0, w0); }, [&]() { mm(a1, w1); }, []() { interleave<32, 20>(); });

  const lds_t xf = lds + xf3_base(c.wid) + lane * 16;
#pragma unroll
  for (int m = 0; m < 2; ++m)
#pragma unroll
    for (int n = 0; n < 4; ++n) { *(LAS f32x4*)(xf + (m * 8 + n) * 1024) = a2[2 + m][n]; *(LAS f32x4*)(xf + (m * 8 + 4 + n) * 1024) = a1_[2 + m][n]; e.z2[m][n] = a2[m][n]; e.z1[m][n] = a1_[m][n]; }
  wait_lgkm0();
}
struct Ld4 { __amdgpu_buffer_rsrc_t rA; int voA, voF; };
DI void p4_issueW(const Ctx& c, const Ld4& L, int t) { const int lw = c.wid & 3; const lds_t d = c.lds + (t & 3) * 32768 + lw * 1024;
#pragma unroll
  for (int i = 0; i < 4; ++i) dma<0>(L.rA, d + i * 4096, L.voA, t * 128 + i * 32 * 4096); }
DI void p3_epilogue(Ctx& c, const Ld4& L, int i3, int j3, Acc3& e, u32x2 (&z1)[2][4]) {
  const int lane = opaque(c.lane), cw = c.wid & 3, hf = c.wid >> 2, wr = cw >> 1, wc = cw & 1, g = lane >> 4, i16 = lane & 15;
  const lds_t lds = c.lds, tab = lds + L_TAB3;
  const __amdgpu_buffer_rsrc_t rWS = c.ws;
  const f32x4 Z4 = {0.f, 0.f, 0.f, 0.f};
  bar();

  __builtin_amdgcn_s_setprio(1);
  if (hf) { const lds_t xf = lds + xf3_base(cw) + lane * 16;
#pragma unroll
    for (int m = 0; m < 2; ++m)
#pragma unroll
      for (int n = 0; n < 4; ++n) { e.z2[m][n] = *(LAS f32x4*)(xf + (m * 8 + n) * 1024); e.z1[m][n] = *(LAS f32x4*)(xf + (m * 8 + 4 + n) * 1024); } }
  {
    float cs[4];
#pragma unroll
    for (int n = 0; n < 4; ++n) cs[n] = *(LAS float*)(tab + 1024 + (64 * wc + 16 * n + i16) * 4);
#pragma unroll
    for (int m = 0; m < 2; ++m) {
      const int R = 64 * wr + 32 * hf + 16 * m + 4 * g;
      const f32x4 mu = *(LAS f32x4*)(tab + R * 4), rs = *(LAS f32x4*)(tab + 512 + R * 4), nm = -mu * rs;
      f32x4 s = Z4, qq = Z4;
#pragma unroll
      for (int n = 0; n < 4; ++n) { const f32x4 y = gelu4(e.z2[m][n] * rs + nm * cs[n]); e.z2[m][n] = y; s += y; qq += y * y; }
      sum_row16x8(s, qq);
      if (i16 < 4) { const float sv = i16 == 0 ? s[0] : i16 == 1 ? s[1] : i16 == 2 ? s[2] : s[3], qv = i16 == 0 ? qq[0] : i16 == 1 ? qq[1] : i16 == 2 ? qq[2] : qq[3];
        __builtin_amdgcn_raw_buffer_store_b128((u32x4){__float_as_uint(sv), c.tag, __float_as_uint(qv), c.tag}, rWS, OFF_ST2 + ((i3 * 14 + j3) * 2 + wc) * 2048 + (R + i16) * 16, 0, SC_SC1); }
    }
  }

  __builtin_amdgcn_s_setprio(0);
  if (!hf) {
#pragma unroll
    for (int s = 0; s < 3; ++s) p4_issueW(c, L, s);
  }
  {
    const int R0 = 64 * wr + 32 * hf, off = OFF_ST2 + (i3 * 28 + (lane >> 5)) * 2048 + (R0 + (lane & 31)) * 16;
    u32x4 v[14];
    for (unsigned spins = 0;; ++spins) {
      bool ok = true;
#pragma unroll
      for (int k = 0; k < 14; ++k) v[k] = __builtin_amdgcn_raw_buffer_load_b128(rWS, off + k * 4096, 0, SC_SC1);
#pragma unroll
      for (int k = 0; k < 14; ++k) ok &= v[k][1] == c.tag && v[k][3] == c.tag;
      if (__all(ok) || gave_up(c, spins, 0x203u)) break;
      __builtin_amdgcn_s_sleep(4);
    }
    __builtin_amdgcn_s_setprio(1);
    float s = 0.f, qq = 0.f;
#pragma unroll
    for (int k = 0; k < 14; ++k) { s += __uint_as_float(v[k][0]); qq += __uint_as_float(v[k][2]); }
    { auto a = __builtin_amdgcn_permlane32_swap(__float_as_uint(s), __float_as_uint(s), false, false); s = (__uint_as_float(a[0]) + __uint_as_float(a[1])) * (1.0f / 1792.0f);
      auto b = __builtin_amdgcn_permlane32_swap(__float_as_uint(qq), __float_as_uint(qq), false, false); qq = (__uint_as_float(b[0]) + __uint_as_float(b[1])) * (1.0f / 1792.0f); }
    const float rsv = __builtin_amdgcn_rsqf(fmaxf(qq - s * s, 0.f) + 1e-5f);
    const lds_t ft = lds + L_FIN + c.wid * 256;
    if (lane < 32) { *(LAS float*)(ft + lane * 4) = rsv; *(LAS float*)(ft + 128 + lane * 4) = -s * rsv; }
    asm volatile("s_waitcnt lgkmcnt(0)" ::: "memory");

    f32x4 rs[2], nm[2];
#pragma unroll
    for (int m = 0; m < 2; ++m) { rs[m] = *(LAS f32x4*)(ft + (16 * m + 4 * g) * 4); nm[m] = *(LAS f32x4*)(ft + 128 + (16 * m + 4 * g) * 4); }
#pragma unroll
    for (int n = 0; n < 4; ++n)
      st16(c, pk8(e.z2[0][n] * rs[0] + nm[0], e.z2[1][n] * rs[1] + nm[1]), lane * 16 + (OFF_Z2 + (j3 * 32 + 2 * i3 + wr) * 16384 + ((4 * wc + n) * 2 + hf) * 1024));
  }
  __builtin_amdgcn_s_setprio(0);
  publish(c, FG_Z2, j3, i3);

#pragma unroll
  for (int m = 0; m < 2; ++m) asm volatile("" : "+v"(e.z1[m][0]), "+v"(e.z1[m][1]), "+v"(e.z1[m][2]), "+v"(e.z1[m][3]));
#pragma unroll
  for (int m = 0; m < 2; ++m) {
    const int R = 64 * wr + 32 * hf + 16 * m + i16;
    const float mu = *(LAS float*)(tab + R * 4), rs = *(LAS float*)(tab + 512 + R * 4), nm = -mu * rs;
#pragma unroll
    for (int n = 0; n < 4; ++n) { const f32x4 cs = *(LAS f32x4*)(tab + 1024 + (128 + 64 * wc + 16 * n + 4 * g) * 4); z1[m][n] = pk4(gelu4(e.z1[m][n] * rs + cs * nm)); }
    asm volatile("" : "+v"(z1[m][0]), "+v"(z1[m][1]), "+v"(z1[m][2]), "+v"(z1[m][3]));
  }
}
DI void p4_load(Ctx& c, const Ld4& L, int j3) {
  const int lw = c.wid - 4;
  auto issueW = [&](int t) { p4_issueW(c, L, t); };
  auto issueA = [&](int t) { const lds_t d = c.lds + (t & 3) * 32768 + 16384 + lw * 1024; const int so = OFF_Z2 + (j3 * 32 + t) * 16384;
#pragma unroll
    for (int i = 0; i < 4; ++i) dma<SC_SC1>(c.ws, d + i * 4096, L.voF, so + i * 4096); };
  bar();
  if (c.b == 0 && lw == 0 && c.lane == 0) __hip_atomic_store((unsigned*)(c.wsp + OFF_EPOCH), c.tag, __ATOMIC_RELAXED, __HIP_MEMORY_SCOPE_AGENT);
#pragma unroll
  for (int s = 0; s < 3; ++s) issueA(s);
  loader_loop32(issueA, issueW);
}
DI void p4_mma(Ctx& c, int j3, f32x4 (&e)[2][4]) {
  const int lane = opaque(c.lane), wr = c.wid >> 1, wc = c.wid & 1, g = lane >> 4, i16 = lane & 15;
  const lds_t lds = c.lds;
  constexpr int S = 4, SB = 32768;
  f32x4 acc[4][4];
#pragma unroll
  for (int m = 0; m < 4; ++m)
#pragma unroll
    for (int n = 0; n < 4; ++n) { acc[m][n] = (f32x4){0.f, 0.f, 0.f, 0.f}; asm volatile("" : "+v"(acc[m][n])); }
  const int ao0 = (wr * 64 + i16) * 128 + ((g ^ (i16 >> 1)) << 4), ao1 = ao0 ^ 64, zo = 16384 + wc * 8192 + lane * 16;
  bf16x8 a0[4], w0[4], a1[4], w1[4];
  auto rd = [&](bf16x8(&a)[4], bf16x8(&w)[4], int sb, int ks) { const lds_t s = lds + sb;
#pragma unroll
    for (int n = 0; n < 4; ++n) { w[n] = ldsA(s + zo + n * 2048 + ks * 1024); a[n] = ldsA(s + (ks ? ao1 : ao0) + n * 2048); } };
  auto mm = [&](bf16x8(&a)[4], bf16x8(&w)[4]) {
#pragma unroll
    for (int n = 0; n < 4; ++n)
#pragma unroll
      for (int m = 0; m < 4; ++m) acc[m][n] = mfma(w[n], a[m], acc[m][n]); };
  if (c.wid == 0) wait_flags(c, FG_Z2, j3, 16);
  bar();
  consumer_loop<S, SB>(32, [&](int sb) { rd(a0, w0, sb, 0); }, [&](int sb) { rd(a1, w1, sb, 1); }, [&]() { mm(a0, w0); }, [&]() { mm(a1, w1); }, []() { interleave<16, 8>(); });

  const lds_t xf = lds + c.wid * 8192 + lane * 16;
#pragma unroll
  for (int m = 0; m < 2; ++m)
#pragma unroll
    for (int n = 0; n < 4; ++n) { *(LAS f32x4*)(xf + (m * 4 + n) * 1024) = acc[2 + m][n]; e[m][n] = acc[m][n]; }
  wait_lgkm0();
}
DI void p5_issueW(const Ctx& c, const Args& A, int j2);
DI void p4_epilogue(Ctx& c, const Args& A, int i3, int j3, int j2, f32x4 (&e)[2][4], const u32x2 (&z1)[2][4]) {
  const int lane = opaque(c.lane), cw = c.wid & 3, hf = c.wid >> 2, wr = cw >> 1, wc = cw & 1, i16 = lane & 15;
  float bs[2];
#pragma unroll
  for (int m = 0; m < 2; ++m) bs[m] = bflo((unsigned)A.bsp[i3 * 128 + true_row(4 * wr + 2 * hf + m, i16)]);
  bar();
  if (hf) { const lds_t xf = c.lds + cw * 8192 + lane * 16;
#pragma unroll
    for (int m = 0; m < 2; ++m)
#pragma unroll
      for (int n = 0; n < 4; ++n) e[m][n] = *(LAS f32x4*)(xf + (m * 4 + n) * 1024); }
#pragma unroll
  for (int m = 0; m < 2; ++m)
#pragma unroll
    for (int u = 0; u < 2; ++u)
      st16(c, pk8(up4(z1[m][2 * u]) * (e[m][2 * u] + bs[m]), up4(z1[m][2 * u + 1]) * (e[m][2 * u + 1] + bs[m])),
           lane * 16 + (OFF_UF + (i3 * 28 + 2 * j3 + wc) * 16384 + ((4 * wr + 2 * hf + m) * 2 + u) * 1024));
  wait_lgkm0(); bar();
  publish(c, FG_U, i3, j3);
  if (hf) p5_issueW(c, A, j2);
}

DI void p5_issueW(const Ctx& c, const Args& A, int j2) {
  const int lane = opaque(c.lane), lw = c.wid - 4;
  const __amdgpu_buffer_rsrc_t rW = __builtin_amdgcn_make_buffer_rsrc((void*)A.wout, 0, 1792 * 2048, 0x00020000);
  int vo[2];
#pragma unroll
  for (int i = 0; i < 2; ++i) { const int r = (2 * lw + i) * 8 + (lane >> 3), s16 = lane & 7; vo[i] = r * 2048 + j2 * 128 + (((s16 >> 1) ^ ((r >> 1) & 3)) << 5) + (s16 & 1) * 16; }
#pragma unroll
  for (int t = 0; t < 18; ++t)
#pragma unroll
    for (int i = 0; i < 2; ++i) dma<0>(rW, c.lds + ((t & 1) * 9 + (t >> 1)) * 8192 + (lw * 2 + i) * 1024, vo[i], ((t & 1) * 14 + (t >> 1)) * 64 * 2048);
}

constexpr int L_RED = 98304;
constexpr int RING = 6;
template <int NSH, int R, bool REFILL>
DI void gemm_split(Ctx& c, f32x4 (&res)[4], int aoff, __amdgpu_buffer_rsrc_t rW, int j2) {
  const int ln = opaque(c.lane), g = ln >> 4, q = (ln & 15) >> 2, p = ln & 3, rw = c.wid & 3, kh = c.wid >> 2;
  const lds_t lds = c.lds;
  const int sw2 = (2 * g + (q >> 1)) & 3, wl2 = (4 * g + q) * 128 + 8 * p + kh * (REFILL ? 9 : 8) * 8192, va = ln * 16 + rw * 4096;
  const int ao = aoff + kh * (NSH / 2) * 16384;
  int wo2[4];
#pragma unroll
  for (int n = 0; n < 4; ++n) wo2[n] = wl2 + 32 * (n ^ sw2);
  f32x4 acc[2][4];
#pragma unroll
  for (int n = 0; n < 4; ++n) { acc[0][n] = (f32x4){0.f, 0.f, 0.f, 0.f}; acc[1][n] = acc[0][n]; }
  u32x4 a[R][2]; bf16x8 w0[4], w1[4];
  auto lda = [&](int s) {
#pragma unroll
    for (int m = 0; m < 2; ++m) a[s % R][m] = __builtin_amdgcn_raw_buffer_load_b128(c.ws, va + m * 2048, ao + (s >> 1) * 16384 + (s & 1) * 1024, SC_SC1); };
  auto ldw = [&](bf16x8(&d)[4], int s) { const int lt = s >> 1, slot = (REFILL && lt >= 9) ? lt - 9 : lt;
#pragma unroll
    for (int n = 0; n < 4; ++n) d[n] = ldsT(lds + wo2[n] + slot * 8192 + (s & 1) * 4096, 16 * 128); };

#pragma unroll
  for (int s = 0; s < R; ++s) { lda(s); if (s == 0 || s == 2) bar(); }
  if (!REFILL && kh) {
#pragma unroll
    for (int i = 0; i < 2; ++i) { const int r = (2 * rw + i) * 8 + (ln >> 3), s16 = ln & 7, vo = r * 2048 + j2 * 128 + (((s16 >> 1) ^ ((r >> 1) & 3)) << 5) + (s16 & 1) * 16;
#pragma unroll
      for (int t = 4; t < 8; ++t) dma<0>(rW, lds + t * 8192 + (rw * 2 + i) * 1024, vo, t * 64 * 2048); }
  }
  ldw(w0, 0);
#pragma unroll
  for (int s = 0; s < NSH; ++s) {
    if (s == 5) {
      if (kh) { if (REFILL) wait_vm<2 * R + 10>(); else wait_vm<10>(); }
      bar(); __builtin_amdgcn_sched_barrier(0);
    }
    if (REFILL && s == 10) {
      wait_lgkm0(); bar();
      const int r = 8 * c.wid + (ln >> 3), s16 = ln & 7, vo = r * 2048 + j2 * 128 + (((s16 >> 1) ^ ((r >> 1) & 3)) << 5) + (s16 & 1) * 16;
#pragma unroll
      for (int k = 0; k < 10; ++k) dma<0>(rW, lds + ((k / 5) * 9 + (k % 5)) * 8192 + c.wid * 1024, vo, ((k / 5) * 14 + 9 + (k % 5)) * 64 * 2048);
      __builtin_amdgcn_sched_barrier(0);
    }
    if (REFILL && s == 17) { wait_vm<2 * ((NSH - R < 17 ? NSH - R : 17) - 10)>(); wait_lgkm0(); bar(); __builtin_amdgcn_sched_barrier(0); }
    if (s + 1 < NSH) { if (s & 1) ldw(w0, s + 1); else ldw(w1, s + 1); }
#pragma unroll
    for (int n = 0; n < 4; ++n)
#pragma unroll
      for (int m = 0; m < 2; ++m) acc[m][n] = mfma((s & 1) ? w1[n] : w0[n], __builtin_bit_cast(bf16x8, a[s % R][m]), acc[m][n]);
    if (s + R < NSH) lda(s + R);
    interleave<8, 8>();
    __builtin_amdgcn_sched_barrier(0);
  }
  wait_lgkm0(); bar();
#pragma unroll
  for (int n = 0; n < 4; ++n) *(LAS f32x4*)(lds + L_RED + c.wid * 4096 + n * 1024 + ln * 16) = kh ? acc[0][n] : acc[1][n];
  wait_lgkm0(); bar();
#pragma unroll
  for (int n = 0; n < 4; ++n) res[n] = (kh ? acc[1][n] : acc[0][n]) + *(LAS f32x4*)(lds + L_RED + (c.wid ^ 4) * 4096 + n * 1024 + ln * 16);
}

template <class PRE> DI void phase2(Ctx& c, const Args& A, int i2, int j2, bool pre_on, PRE pre) {
  const __amdgpu_buffer_rsrc_t rW2 = __builtin_amdgcn_make_buffer_rsrc((void*)A.wo, 0, 1024 * 2048, 0x00020000);
  const int lane = opaque(c.lane), g = lane >> 4, i16 = lane & 15, mt = 2 * (c.wid & 3) + (c.wid >> 2);
  u32x2 xr[4];
#pragma unroll
  for (int n = 0; n < 4; ++n) xr[n] = *(const u32x2*)(A.x + (size_t)(i2 * 128 + true_row(mt, i16)) * 1024 + j2 * 64 + 16 * n + 4 * g);
  wait_lgkm0(); bar();
  f32x4 acc[4];
  gemm_split<16, RING, false>(c, acc, OFF_OF + i2 * 262144, rW2, j2);

  float s = 0.f, qq = 0.f;
#pragma unroll
  for (int u = 0; u < 2; ++u) {
    f32x4 h0 = acc[2 * u], h1 = acc[2 * u + 1]; const u32x2 x0 = xr[2 * u], x1 = xr[2 * u + 1];
    h0[0] += bflo(x0[0]); h0[1] += bfhi(x0[0]); h0[2] += bflo(x0[1]); h0[3] += bfhi(x0[1]);
    h1[0] += bflo(x1[0]); h1[1] += bfhi(x1[0]); h1[2] += bflo(x1[1]); h1[3] += bfhi(x1[1]);
    const bf16x8 hb = pk8(h0, h1);
    st16(c, hb, lane * 16 + (OFF_HF + (i2 * 16 + j2) * 16384 + (mt * 2 + u) * 1024));
    acc_row(s, qq, __builtin_bit_cast(u32x4, hb));
  }
  s = sum_g(s); qq = sum_g(qq);
  if (lane < 16) __builtin_amdgcn_raw_buffer_store_b64((u32x2){__float_as_uint(s), __float_as_uint(qq)}, c.ws, OFF_STH + (i2 * 16 + j2) * 1024 + (mt * 16 + lane) * 8, 0, SC_SC1);
  publish(c, FG_H, i2, j2);
  if (pre_on) pre();
}

DI void phase5(Ctx& c, const Args& A, int i2, int j2) {
  const int lane = opaque(c.lane), g = lane >> 4, i16 = lane & 15, mt = 2 * (c.wid & 3) + (c.wid >> 2);
  u32x4 hr[2];
#pragma unroll
  for (int u = 0; u < 2; ++u) hr[u] = __builtin_amdgcn_raw_buffer_load_b128(c.ws, lane * 16, OFF_HF + (i2 * 16 + j2) * 16384 + (mt * 2 + u) * 1024, SC_SC1);
  const __amdgpu_buffer_rsrc_t rW = __builtin_amdgcn_make_buffer_rsrc((void*)A.wout, 0, 1792 * 2048, 0x00020000);
  if (c.wid == 0) wait_flags(c, FG_U, i2, 14);
  if (c.wid >= 4) wait_vm<24>();
  bar();
  f32x4 acc[4];
  gemm_split<28, RING, true>(c, acc, OFF_UF + i2 * 458752, rW, j2);

  const lds_t ot = c.lds + c.wid * 2048;
#pragma unroll
  for (int n = 0; n < 4; ++n) { const unsigned r0 = hr[n >> 1][2 * (n & 1)], r1 = hr[n >> 1][2 * (n & 1) + 1];
    *(LAS u32x2*)(ot + i16 * 128 + (((2 * n + (g >> 1)) ^ (i16 & 7)) << 4) + (g & 1) * 8) =
        (u32x2){pk2(acc[n][0] + bflo(r0), acc[n][1] + bfhi(r0)), pk2(acc[n][2] + bflo(r1), acc[n][3] + bfhi(r1))}; }
  asm volatile("s_waitcnt lgkmcnt(0)" ::: "memory");
  const __amdgpu_buffer_rsrc_t rO = __builtin_amdgcn_make_buffer_rsrc((void*)A.out, 0, 2048 * 2048, 0x00020000);
#pragma unroll
  for (int u = 0; u < 2; ++u) { const int r = 8 * u + (lane >> 3), ch = lane & 7;
    const u32x4 v = *(LAS u32x4*)(ot + r * 128 + ((ch ^ (r & 7)) << 4));
    __builtin_amdgcn_raw_buffer_store_b128(v, rO, (i2 * 128 + true_row(mt, r)) * 2048 + j2 * 128 + ch * 16, 0, SC_SC1); }
}

__global__ __launch_bounds__(512, 2) void fused_fwd(const u16* __restrict__ x, const u16* __restrict__ wqkv, const u16* __restrict__ wphi, const u16* __restrict__ wo,
                                                      const u16* __restrict__ win, const u16* __restrict__ wsp, const u16* __restrict__ bsp, const u16* __restrict__ wout,
                                                      u16* __restrict__ out, char* __restrict__ ws) {
  extern __shared__ __attribute__((aligned(16))) char shm[];
  Ctx c;
  c.lds = (lds_t)shm; c.wsp = ws; c.lane = threadIdx.x & 63; c.wid = __builtin_amdgcn_readfirstlane(threadIdx.x >> 6); c.b = blockIdx.x;
  c.ws = __builtin_amdgcn_make_buffer_rsrc((void*)ws, 0, WS_BYTES, 0x00020000);
  c.tag = 0u;
  if (threadIdx.x < 16) *(LAS unsigned*)(c.lds + L_CNT + threadIdx.x * 4) = 0u;
  const int xcd = c.b & 7, jj = c.b >> 3;
  Map M;
  M.i1 = 8 * (xcd & 1) + (jj >> 2); M.j1 = 4 * (xcd >> 1) + (jj & 3);
  M.i2 = 4 * (xcd >> 1) + (jj >> 3); M.j2 = 8 * (xcd & 1) + (jj & 7);
  M.act3 = jj < 28;
  M.i3 = 4 * (xcd >> 1) + (M.act3 ? jj / 7 : 0); M.j3 = 7 * (xcd & 1) + (M.act3 ? jj % 7 : 0);
  const Args A{x, wqkv, wphi, wo, win, wsp, bsp, wout, out, ws};
  const bool ld = c.wid >= 4;
  {
    Acc1 e;
    if (ld) p1_load(c, A, M.i1, M.j1);
    else { __builtin_amdgcn_s_setprio(1); p1_mma(c, A, M.i1, M.j1, e); __builtin_amdgcn_s_setprio(0); }
    p1_epilogue(c, A, M, e);
  }
  Ld3 L3;
  p3_setup(c, A, M.j3, L3);
  phase2(c, A, M.i2, M.j2, ld && M.act3, [&]() { p3_issueW(c, L3, 0); p3_issueW(c, L3, 1); });
  if (M.act3) {
    u32x2 z1[2][4];
    Ld4 L;
    L.rA = __builtin_amdgcn_make_buffer_rsrc((void*)(A.wsp + (size_t)M.i3 * 128 * 2048), 0, 128 * 4096, 0x00020000);
    {
      Acc3 e;
      if (ld) p3_load(c, L3, M.i3);
      else { __builtin_amdgcn_s_setprio(1); p3_mma(c, M.i3, M.j3, e); __builtin_amdgcn_s_setprio(0); }
      const int ln = opaque(c.lane);
      L.voA = rowmajor_voff(c.wid & 3, ln, 4096); L.voF = ln * 16 + (c.wid & 3) * 1024;
      p3_epilogue(c, L, M.i3, M.j3, e, z1);
    }
    f32x4 e4[2][4];
    if (ld) p4_load(c, L, M.j3);
    else { __builtin_amdgcn_s_setprio(1); p4_mma(c, M.j3, e4); __builtin_amdgcn_s_setprio(0); }
    p4_epilogue(c, A, M.i3, M.j3, M.j2, e4, z1);
  } else {
    bar();
    if (!ld) { const int wv = ((c.b & 7) * 4 + ((c.b >> 3) - 28)) * 4 + c.wid;
#pragma unroll 1
      for (int k = 0; k < 12; ++k) { const int Li = (k * 128 + wv) * 64 + c.lane;
        touch(c.lds, Li < 65536 ? (const char*)A.wsp + (size_t)Li * 128 : (const char*)A.wout + (size_t)((Li < 94208 ? Li : 94207) - 65536) * 128); } }
    if (ld) p5_issueW(c, A, M.j2);
  }
  phase5(c, A, M.i2, M.j2);
}

extern "C" void kernel_launch(void* const* d_in, const int* in_sizes, int n_in, void* d_out, int out_size, void* d_ws, size_t ws_size, hipStream_t stream) {
  (void)in_sizes; (void)n_in; (void)out_size; (void)ws_size;
  (void)hipFuncSetAttribute((const void*)fused_fwd, hipFuncAttributeMaxDynamicSharedMemorySize, LDS_BYTES);
  fused_fwd<<<256, 512, LDS_BYTES, stream>>>((const u16*)d_in[0], (const u16*)d_in[1], (const u16*)d_in[2], (const u16*)d_in[3], (const u16*)d_in[5], (const u16*)d_in[6],
                                             (const u16*)d_in[7], (const u16*)d_in[8], (u16*)d_out, (char*)d_ws);
}
```

```cpp
#include <hip/hip_runtime.h>
#include <hip/hip_bf16.h>
#include <stdint.h>
#include <type_traits>

#define LAS __attribute__((address_space(3)))
#define DI __device__ __forceinline__
typedef unsigned short u16;
typedef short bf16x8 __attribute__((ext_vector_type(8)));
typedef short s16x4 __attribute__((ext_vector_type(4)));
typedef float f32x4 __attribute__((ext_vector_type(4)));
typedef float f32x2 __attribute__((ext_vector_type(2)));
typedef unsigned u32x4 __attribute__((ext_vector_type(4)));
typedef unsigned u32x2 __attribute__((ext_vector_type(2)));
typedef __bf16 bf2 __attribute__((ext_vector_type(2)));
typedef LAS char* lds_t;

constexpr unsigned OFF_EPOCH = 0, OFF_ERR = 128, OFF_FLAG = 4096;
constexpr unsigned OFF_KVP = 262144;
constexpr unsigned OFF_ZP = OFF_KVP + 2097152;
constexpr unsigned OFF_ST2 = 27u << 20;
constexpr unsigned OFF_STH = OFF_ZP + 131072 + 262144;
constexpr unsigned OFF_GX = OFF_STH + 262144;
constexpr unsigned OFF_GC1 = OFF_GX + 65536;
constexpr unsigned OFF_GC3 = OFF_GC1 + 393216;
constexpr unsigned OFF_OF = 4u << 20, OFF_HF = 8u << 20, OFF_Z2 = 12u << 20, OFF_UF = 20u << 20;
constexpr unsigned WS_BYTES = 32u << 20;
enum { FG_KV = 0, FG_O = 1, FG_H = 2, FG_Z2 = 3, FG_U = 4 };
constexpr int LDS_BYTES = 163840;
constexpr int SC1 = 122880, L_WPHI = SC1, L_TAB1 = SC1 + 4096, L_XBUF = SC1 + 22528;
constexpr int L_XF1 = 65536, L_TB = 32768, L_KR = 65536, L_ZR = 131072;
constexpr int SC3 = 147456, L_TAB3 = SC3, L_FIN = SC3 + 4096;
constexpr int L_CNT = LDS_BYTES - 64, L_DUMMY = LDS_BYTES - 512;
constexpr int SC_SC1 = 16;

DI void bar() { asm volatile("" ::: "memory"); __builtin_amdgcn_s_barrier(); asm volatile("" ::: "memory"); }
template <int N> DI void wait_vm() { asm volatile("s_waitcnt vmcnt(%0)" ::"n"(N) : "memory"); }
DI int opaque(int v) { asm volatile("" : "+v"(v)); return v; }
DI void wait_lgkm0() { __builtin_amdgcn_s_waitcnt(0xc07f); }
DI unsigned pk2(float lo, float hi) { f32x2 v = {lo, hi}; return __builtin_bit_cast(unsigned, __builtin_convertvector(v, bf2)); }
DI bf16x8 pk8(const f32x4& a, const f32x4& b) { u32x4 r = {pk2(a[0], a[1]), pk2(a[2], a[3]), pk2(b[0], b[1]), pk2(b[2], b[3])}; return __builtin_bit_cast(bf16x8, r); }
DI float bflo(unsigned w) { return __uint_as_float(w << 16); }
DI float bfhi(unsigned w) { return __uint_as_float(w & 0xffff0000u); }
DI f32x4 mfma(bf16x8 a, bf16x8 b, f32x4 c) { return __builtin_amdgcn_mfma_f32_16x16x32_bf16(a, b, c, 0, 0, 0); }
DI bf16x8 ldsA(lds_t p) { return *(LAS bf16x8*)p; }
DI bf16x8 ldsT(lds_t p, int d) {
  s16x4 lo = __builtin_amdgcn_ds_read_tr16_b64_v4i16((LAS s16x4*)p), hi = __builtin_amdgcn_ds_read_tr16_b64_v4i16((LAS s16x4*)(p + d));
  return __builtin_shufflevector(lo, hi, 0, 1, 2, 3, 4, 5, 6, 7);
}
template <int D1, int D2> DI void tr4_issue(unsigned addr, u32x2 (&r)[4]) {
  asm volatile("ds_read_b64_tr_b16 %0, %4\n\tds_read_b64_tr_b16 %1, %4 offset:%5\n\tds_read_b64_tr_b16 %2, %4 offset:%6\n\tds_read_b64_tr_b16 %3, %4 offset:%7"
               : "=&v"(r[0]), "=&v"(r[1]), "=&v"(r[2]), "=&v"(r[3]) : "v"(addr), "n"(D1), "n"(D2), "n"(D1 + D2) : "memory");
}
DI void tr4_wait(u32x2 (&r)[4]) { asm volatile("s_waitcnt lgkmcnt(0)" : "+v"(r[0]), "+v"(r[1]), "+v"(r[2]), "+v"(r[3])::"memory"); }
DI float sum_g(float x) {
  auto a = __builtin_amdgcn_permlane16_swap(__float_as_uint(x), __float_as_uint(x), false, false);
  x = __uint_as_float(a[0]) + __uint_as_float(a[1]);
  auto b = __builtin_amdgcn_permlane32_swap(__float_as_uint(x), __float_as_uint(x), false, false);
  return __uint_as_float(b[0]) + __uint_as_float(b[1]);
}
template <int CTRL> DI float dpp_add(float x) { return x + __int_as_float(__builtin_amdgcn_update_dpp(0, __float_as_int(x), CTRL, 0xf, 0xf, true)); }
DI float sum_row16(float x) { x = dpp_add<0x128>(x); x = dpp_add<0x124>(x); x = dpp_add<0x122>(x); return dpp_add<0x121>(x); }
DI void sum_row16x8(f32x4& a, f32x4& b) {
#define RR(n) "v_add_f32_dpp %0, %0, %0 row_ror:" n " row_mask:0xf bank_mask:0xf\n\tv_add_f32_dpp %1, %1, %1 row_ror:" n " row_mask:0xf bank_mask:0xf\n\t" \
  "v_add_f32_dpp %2, %2, %2 row_ror:" n " row_mask:0xf bank_mask:0xf\n\tv_add_f32_dpp %3, %3, %3 row_ror:" n " row_mask:0xf bank_mask:0xf\n\t" \
  "v_add_f32_dpp %4, %4, %4 row_ror:" n " row_mask:0xf bank_mask:0xf\n\tv_add_f32_dpp %5, %5, %5 row_ror:" n " row_mask:0xf bank_mask:0xf\n\t" \
  "v_add_f32_dpp %6, %6, %6 row_ror:" n " row_mask:0xf bank_mask:0xf\n\tv_add_f32_dpp %7, %7, %7 row_ror:" n " row_mask:0xf bank_mask:0xf\n\t"
  float a0 = a[0], a1 = a[1], a2 = a[2], a3 = a[3], b0 = b[0], b1 = b[1], b2 = b[2], b3 = b[3];
  asm volatile("s_nop 1\n\t" RR("8") RR("4") RR("2") RR("1") "s_nop 0" : "+v"(a0), "+v"(a1), "+v"(a2), "+v"(a3), "+v"(b0), "+v"(b1), "+v"(b2), "+v"(b3));
#undef RR
  a = (f32x4){a0, a1, a2, a3}; b = (f32x4){b0, b1, b2, b3};
}
DI float dot2(unsigned a, unsigned b, float c) { return __builtin_amdgcn_fdot2_f32_bf16(__builtin_bit_cast(bf2, a), __builtin_bit_cast(bf2, b), c, false); }
constexpr unsigned ONES2 = 0x3f803f80u;
DI f32x4 gelu4(f32x4 x) {
  const f32x4 t = x * (x * x * -0.1029432397f + -2.302208198f);
  f32x4 d, r;
#pragma unroll
  for (int i = 0; i < 4; ++i) d[i] = __builtin_amdgcn_exp2f(t[i]);
  d += 1.0f;
#pragma unroll
  for (int i = 0; i < 4; ++i) r[i] = __builtin_amdgcn_rcpf(d[i]);
  return x * r;
}
DI u32x2 pk4(const f32x4& a) { return (u32x2){pk2(a[0], a[1]), pk2(a[2], a[3])}; }
DI f32x4 up4(u32x2 w) { return (f32x4){bflo(w[0]), bfhi(w[0]), bflo(w[1]), bfhi(w[1])}; }

struct Ctx {
  __amdgpu_buffer_rsrc_t ws;
  char* wsp; lds_t lds; unsigned tag; int lane, wid, b;
};
DI unsigned* flag_ptr(const Ctx& c, int g, int a, int bb) { return (unsigned*)(c.wsp + OFF_FLAG + ((g * 16 + a) * 16 + bb) * 128); }
DI void publish(const Ctx& c, int g, int a, int bb) {
  wait_vm<0>();
  if (c.lane == 0) {
    unsigned old = __hip_atomic_fetch_add((LAS unsigned*)(c.lds + L_CNT + g * 4), 1u, __ATOMIC_RELAXED, __HIP_MEMORY_SCOPE_WORKGROUP);
    if (old == 7u) __hip_atomic_store(flag_ptr(c, g, a, bb), c.tag, __ATOMIC_RELAXED, __HIP_MEMORY_SCOPE_AGENT);
  }
}
DI bool gave_up(const Ctx& c, unsigned spins, unsigned code) {
  if (spins < 40000u) return false;
  if (c.lane == 0) __hip_atomic_store((unsigned*)(c.wsp + OFF_ERR), code, __ATOMIC_RELAXED, __HIP_MEMORY_SCOPE_AGENT);
  return true;
}
DI void wait_flags(const Ctx& c, int g, int a, int n) {
  const unsigned* p = flag_ptr(c, g, a, c.lane < n ? c.lane : 0);
  for (unsigned spins = 0;; ++spins) {
    const unsigned v = __hip_atomic_load(p, __ATOMIC_RELAXED, __HIP_MEMORY_SCOPE_AGENT);
    if (__all(v == c.tag) || gave_up(c, spins, 0x100u + (unsigned)g)) break;
    __builtin_amdgcn_s_sleep(1);
  }
  asm volatile("" ::: "memory");
}

DI void st16(const Ctx& c, bf16x8 v, int off) { __builtin_amdgcn_raw_buffer_store_b128(__builtin_bit_cast(u32x4, v), c.ws, off, 0, SC_SC1); }
template <int AUX> DI void dma(__amdgpu_buffer_rsrc_t r, lds_t dst, int voff, int soff) {
  if (AUX) asm volatile("s_mov_b32 m0, %0\n\ts_nop 3\n\tbuffer_load_dwordx4 %1, %2, %3 offen sc1 lds" ::"s"((unsigned)(size_t)dst), "v"(voff), "s"(r), "s"(soff) : "memory");
  else asm volatile("s_mov_b32 m0, %0\n\ts_nop 3\n\tbuffer_load_dwordx4 %1, %2, %3 offen lds" ::"s"((unsigned)(size_t)dst), "v"(voff), "s"(r), "s"(soff) : "memory");
}

template <class IA, class IW>
DI void loader_loop32(IA issueA, IW issueW) {
  wait_vm<8>(); bar();
  issueA(3); issueW(3); wait_vm<4 + 8>(); bar();
  issueA(4); issueW(4); wait_vm<16>(); bar();
#pragma unroll 1
  for (int t = 2; t < 29; ++t) { issueA(t + 3); issueW(t + 3); wait_vm<16>(); bar(); }
  wait_vm<8>(); bar();
  wait_vm<0>(); bar();
  bar();
}
template <int NW, bool GATED, class IA, class IW, class POST, class GA, class FIN>
DI void loader_loop16(bool full, bool once, IA issueA, IW issueW, POST post, GA gather, FIN fin) {
  constexpr int PPW = 4 + NW;
  if (GATED) wait_vm<4>(); else wait_vm<PPW>();
  bar();
  issueA(2); issueW(2); post(0); wait_vm<PPW>(); bar();
#pragma unroll 1
  for (int t = 1; t < 5; ++t) { issueA(t + 2); issueW(t + 2); post(t); wait_vm<PPW>(); bar(); }
#pragma unroll
  for (int t = 5; t < 13; ++t) {
    issueA(t + 2); issueW(t + 2); gather(t - 5);
    if (full) { if (t == 5) wait_vm<PPW + 2>(); else wait_vm<PPW + 4>(); }
    else if (once && t <= 6) wait_vm<PPW + 2>();
    else wait_vm<PPW>();
    bar();
  }
  issueA(15); issueW(15); if (full) wait_vm<PPW + 2>(); else wait_vm<PPW>(); bar();
  wait_vm<0>(); fin(); bar();
  bar();
}
template <int S, int SB, class RD0, class RD1, class MM0, class MM1, class SCH>
DI void consumer_loop(int nt, RD0 rd0, RD1 rd1, MM0 mm0, MM1 mm1, SCH sched) {
  bar();
  rd0(0);
  int sb = 0;
#pragma unroll 2
  for (int t = 0; t < nt; ++t) {
    __builtin_amdgcn_sched_barrier(0);
    rd1(sb); mm0(); sched();
    __builtin_amdgcn_sched_barrier(0);
    wait_lgkm0(); bar();
    __builtin_amdgcn_sched_barrier(0);
    sb = (sb == (S - 1) * SB) ? 0 : sb + SB;
    rd0(sb); mm1(); sched();
    __builtin_amdgcn_sched_barrier(0);
  }
}
DI void touch(lds_t lds, const void* p) {
  asm volatile("s_mov_b32 m0, %1\n\ts_nop 0\n\tglobal_load_lds_dword %0, off" ::"v"(p), "s"((unsigned)(size_t)(lds + L_DUMMY)) : "memory"); }
template <int NM, int NR> DI void interleave() {
#pragma unroll
  for (int i = 0; i < NR; ++i) { __builtin_amdgcn_sched_group_barrier(0x008, 1, 0); __builtin_amdgcn_sched_group_barrier(0x100, 1, 0); }
  __builtin_amdgcn_sched_group_barrier(0x008, NM - NR, 0);
}

DI int rowmajor_voff(int lw, int lane, int pitch) {
  const int rr = lane >> 3, slot = 8 * (lw & 1) + rr;
  const int row = 8 * (slot >> 2) + 4 * (lw >> 1) + (slot & 3);
  return row * pitch + (((lane & 7) ^ (slot >> 1)) << 4);
}
DI int true_row(int mt, int i) { return 32 * (mt >> 1) + 8 * (i >> 2) + 4 * (mt & 1) + (i & 3); }

DI void acc_row(float& s, float& q, u32x4 w) {
#pragma unroll
  for (int i = 0; i < 4; ++i) { s = dot2(w[i], ONES2, s); q = dot2(w[i], w[i], q); } }
DI float col_sum(const u32x2 (&r)[4]) { float s0 = 0.f, s1 = 0.f;
#pragma unroll
  for (int i = 0; i < 4; ++i) { s0 = dot2(r[i][0], ONES2, s0); s1 = dot2(r[i][1], ONES2, s1); }
  return sum_g(s0 + s1); }
DI void st_granule(const Ctx& c, float v, int off) { __builtin_amdgcn_raw_buffer_store_b64((u32x2){__float_as_uint(v), c.tag}, c.ws, off, 0, SC_SC1); }
DI float sum_granules16(const Ctx& c, u32x2 (&v)[16], int off, int stride) {
  for (unsigned spins = 0;; ++spins) {
    bool ok = true;
#pragma unroll
    for (int k = 0; k < 16; ++k) ok &= v[k][1] == c.tag;
    if (__all(ok) || gave_up(c, spins, 0x200u)) break;
    __builtin_amdgcn_s_sleep(2);
#pragma unroll
    for (int k = 0; k < 16; ++k) v[k] = __builtin_amdgcn_raw_buffer_load_b64(c.ws, off + k * stride, 0, SC_SC1);
  }
  float s = 0.f;
#pragma unroll
  for (int k = 0; k < 16; ++k) s += __uint_as_float(v[k][0]);
  return s;
}

struct Args { const u16 *x, *wqkv, *wphi, *wo, *win, *wsp, *bsp, *wout; u16* out; char* ws; };

DI unsigned tag_issue(const Ctx& c) { unsigned e; asm volatile("global_load_dword %0, %1, %2 sc1" : "=v"(e) : "v"(0), "s"(c.wsp) : "memory"); return e; }
template <int N> DI void tag_wait(Ctx& c, unsigned e) { asm volatile("s_waitcnt vmcnt(%1)" : "+v"(e) : "n"(N) : "memory"); c.tag = __builtin_amdgcn_readfirstlane(e) + 1u; }
DI void kv_gather(const Ctx& c, int j1) {
  const int lane = c.lane, g = lane >> 4, i16 = lane & 15, tid = c.wid * 64 + lane;
  f32x4 a0 = {0, 0, 0, 0}, a1 = {0, 0, 0, 0};
  u32x4 v[16];
  const unsigned f0 = __hip_atomic_load(flag_ptr(c, FG_KV, j1, i16), __ATOMIC_RELAXED, __HIP_MEMORY_SCOPE_AGENT);
  const unsigned ready = __builtin_amdgcn_readfirstlane((unsigned)(__ballot(f0 == c.tag) & 0xffffull));
  asm volatile("" ::: "memory");
#pragma unroll
  for (int k = 0; k < 16; ++k) if ((ready >> k) & 1u) v[k] = __builtin_amdgcn_raw_buffer_load_b128(c.ws, tid * 16, OFF_KVP + (j1 * 16 + k) * 8192, SC_SC1);
  if (ready != 0xffffu) {
    wait_flags(c, FG_KV, j1, 16);
#pragma unroll
    for (int k = 0; k < 16; ++k) if (!((ready >> k) & 1u)) v[k] = __builtin_amdgcn_raw_buffer_load_b128(c.ws, tid * 16, OFF_KVP + (j1 * 16 + k) * 8192, SC_SC1);
  }
  float zs = 0.f;
  if (c.wid < 2) { unsigned zv[16];
#pragma unroll
    for (int k = 0; k < 16; ++k) zv[k] = __builtin_amdgcn_raw_buffer_load_b32(c.ws, lane * 4, OFF_ZP + ((j1 * 16 + k) * 2 + c.wid) * 256, SC_SC1);
#pragma unroll
    for (int k = 0; k < 16; ++k) zs += __uint_as_float(zv[k]); }
#pragma unroll
  for (int k = 0; k < 16; ++k) { a0[0] += bflo(v[k][0]); a0[1] += bfhi(v[k][0]); a0[2] += bflo(v[k][1]); a0[3] += bfhi(v[k][1]); a1[0] += bflo(v[k][2]); a1[1] += bfhi(v[k][2]); a1[2] += bflo(v[k][3]); a1[3] += bfhi(v[k][3]); }
  const int w = c.wid, hd = w >> 2, dt = (w >> 1) & 1, fp = w & 1;
  const lds_t tb = c.lds + L_XBUF + hd * 8192 + (32 * fp + i16) * 128 + (16 * dt + 4 * g) * 2;
  *(LAS u32x2*)tb = (u32x2){pk2(a0[0], a0[1]), pk2(a0[2], a0[3])};
  *(LAS u32x2*)(tb + 16 * 128) = (u32x2){pk2(a1[0], a1[1]), pk2(a1[2], a1[3])};
  if (c.wid < 2) { const unsigned z2 = pk2(zs, zs); const lds_t t3 = c.lds + L_XBUF + c.wid * 8192 + lane * 128 + 64;
    *(LAS u32x4*)t3 = (u32x4){z2, z2, z2, z2}; *(LAS u32x4*)(t3 + 16) = (u32x4){z2, z2, z2, z2}; }
  wait_lgkm0();
}
struct Map { int i1, j1, i2, j2, i3, j3; bool act3; };
DI void p1_load(Ctx& c, const Args& A, int i1, int j1) {
  const int lane = c.lane, lw = c.wid - 4, g = lane >> 4, q = (lane & 15) >> 2, p = lane & 3;
  const lds_t lds = c.lds;
  const __amdgpu_buffer_rsrc_t rWS = c.ws;
  constexpr int S = 3, SB = 40960, NW = 6;
  const __amdgpu_buffer_rsrc_t rA = __builtin_amdgcn_make_buffer_rsrc((void*)(A.x + (size_t)i1 * 128 * 1024), 0, 128 * 2048, 0x00020000);
  const __amdgpu_buffer_rsrc_t rW = __builtin_amdgcn_make_buffer_rsrc((void*)A.wqkv, 0, 1024 * 6144, 0x00020000);
  const __amdgpu_buffer_rsrc_t rP = __builtin_amdgcn_make_buffer_rsrc((void*)A.wphi, 0, 65536, 0x00020000);
  const int voA = rowmajor_voff(lw, lane, 2048);
  const int rot = 4 * (i1 & 3);
  auto issueA = [&](int t) { const lds_t d = lds + (t % S) * SB + lw * 1024; const int so = ((t + rot) & 15) * 128;
#pragma unroll
    for (int i = 0; i < 4; ++i) dma<0>(rA, d + i * 4096, voA, so + i * 32 * 2048); };
  const unsigned epoch = tag_issue(c);
  issueA(0);
  int voW[NW];
  { const int ln = opaque(lane);
#pragma unroll
    for (int i = 0; i < 3; ++i) { const int o = (NW * lw + i) * 1024 + ln * 16, r = o / 384, rem = o % 384, seg = rem >> 7, s16 = (rem & 127) >> 4;
      const int c32 = (s16 >> 1) ^ ((r >> 1) & 3), r5 = r & 31, k = (r & 32) + 8 * ((r5 >> 2) & 3) + 4 * (r5 >> 4) + (r5 & 3);
      voW[i] = k * 6144 + seg * 2048 + j1 * 128 + c32 * 32 + (s16 & 1) * 16;
      voW[i + 3] = voW[i] + 16 * 6144; }
  }
  auto issueW = [&](int t) { const lds_t d = lds + (t % S) * SB + 16384 + lw * NW * 1024; const int so = ((t + rot) & 15) * 64 * 6144;
#pragma unroll
    for (int i = 0; i < NW; ++i) dma<0>(rW, d + i * 1024, voW[i], so); };
  issueW(0); issueA(1); issueW(1);
  dma<0>(rP, lds + L_WPHI + lw * 1024, lane * 16 + lw * 1024, j1 * 4096);
  tag_wait<21>(c, epoch);

  u32x2 gv[16]; u32x4 gx[2];
  const int goff = OFF_GC1 + j1 * 16 * 1536 + (lw * 64 + lane) * 8, xoff = OFF_GX + (i1 * 128 + lane) * 16;
  const int tau = i1 >> 2;
  auto post = [&](int t) {
    if (t != tau) return;
    const int sw = (2 * g + (q >> 1)) & 3; u32x2 cr[3][4];
#pragma unroll
    for (int n = 0; n < 3; ++n) { const int nu = 3 * lw + n;
      tr4_issue<16 * 384, 32 * 384>((unsigned)(size_t)(lds + (t % S) * SB + 16384 + (4 * g + q) * 384 + 8 * p + (nu >> 2) * 128 + 32 * ((nu & 3) ^ sw)), cr[n]); }
#pragma unroll
    for (int n = 0; n < 3; ++n) { tr4_wait(cr[n]); const float s = col_sum(cr[n]);
      if (lane < 16) st_granule(c, s, OFF_GC1 + (j1 * 16 + rot + tau) * 1536 + ((3 * lw + n) * 16 + lane) * 8); }
  };
  auto gather = [&](int k) {
    if (lw < 3) { gv[2 * k] = __builtin_amdgcn_raw_buffer_load_b64(rWS, goff + 2 * k * 1536, 0, SC_SC1); gv[2 * k + 1] = __builtin_amdgcn_raw_buffer_load_b64(rWS, goff + (2 * k + 1) * 1536, 0, SC_SC1); }
    else if (k == 0) { gx[0] = __builtin_amdgcn_raw_buffer_load_b128(rWS, xoff, 0, SC_SC1); gx[1] = __builtin_amdgcn_raw_buffer_load_b128(rWS, xoff + 1024, 0, SC_SC1); }
  };
  auto fin = [&]() {
    if (lw < 3) *(LAS float*)(lds + L_TAB1 + 1024 + (lw * 64 + lane) * 4) = sum_granules16(c, gv, goff, 1536);
    else {
      for (unsigned spins = 0;; ++spins) {
        if (__all(gx[0][1] == c.tag && gx[0][3] == c.tag && gx[1][1] == c.tag && gx[1][3] == c.tag) || gave_up(c, spins, 0x201u)) break;
        __builtin_amdgcn_s_sleep(2);
        gx[0] = __builtin_amdgcn_raw_buffer_load_b128(rWS, xoff, 0, SC_SC1); gx[1] = __builtin_amdgcn_raw_buffer_load_b128(rWS, xoff + 1024, 0, SC_SC1);
      }
#pragma unroll
      for (int e = 0; e < 2; ++e) { *(LAS unsigned*)(lds + L_TAB1 + (lane + 64 * e) * 4) = gx[e][0]; *(LAS unsigned*)(lds + L_TAB1 + 512 + (lane + 64 * e) * 4) = gx[e][2]; }
    }
    wait_lgkm0();
    __builtin_amdgcn_s_waitcnt(0x0f70);
    const int gw = c.b * 4 + lw;
    if (lane < 56) touch(lds, (const char*)A.win + (size_t)(gw * 56 + lane) * 128);
    if (lane < 16) touch(lds, (const char*)A.wo + (size_t)(gw * 16 + lane) * 128);
  };
  loader_loop16<NW, false>(lw < 3, lw == 3, issueA, issueW, post, gather, fin);
}

struct Acc1 { f32x4 q[2][2], k[2][2], v[2][2]; };
DI void p1_mma(Ctx& c, const Args& A, int i1, int j1, Acc1& e) {
  const int lane = c.lane, wr = c.wid >> 1, wc = c.wid & 1, g = lane >> 4, q = (lane & 15) >> 2, p = lane & 3, i16 = lane & 15;
  const lds_t lds = c.lds;
  const f32x4 Z4 = {0.f, 0.f, 0.f, 0.f};
  constexpr int S = 3, SB = 40960;
  f32x4 aq[4][2], ak[4][2], av[4][2];
#pragma unroll
  for (int m = 0; m < 4; ++m)
#pragma unroll
    for (int n = 0; n < 2; ++n) { aq[m][n] = Z4; ak[m][n] = Z4; av[m][n] = Z4; asm volatile("" : "+v"(aq[m][n]), "+v"(ak[m][n]), "+v"(av[m][n])); }
  const int sw = (2 * g + (q >> 1)) & 3;
  const int ao0 = (wr * 64 + i16) * 128 + ((g ^ (i16 >> 1)) << 4), ao1 = ao0 ^ 64;
  const int wl = 16384 + (4 * g + q) * 384 + 8 * p;
  const int wo0 = wl + 32 * ((2 * wc) ^ sw), wo1 = wl + 32 * ((2 * wc + 1) ^ sw);
  bf16x8 a0[4], w0[6], a1[4], w1[6];
  auto rd = [&](bf16x8(&a)[4], bf16x8(&w)[6], int sb, int ks) { const lds_t s = lds + sb;
#pragma unroll
    for (int sg = 0; sg < 3; ++sg) { w[2 * sg] = ldsT(s + wo0 + sg * 128 + ks * 32 * 384, 16 * 384); w[2 * sg + 1] = ldsT(s + wo1 + sg * 128 + ks * 32 * 384, 16 * 384); }
#pragma unroll
    for (int m = 0; m < 4; ++m) a[m] = ldsA(s + (ks ? ao1 : ao0) + m * 2048); };
  auto mm = [&](bf16x8(&a)[4], bf16x8(&w)[6]) {
#pragma unroll
    for (int n = 0; n < 2; ++n)
#pragma unroll
      for (int m = 0; m < 4; ++m) aq[m][n] = mfma(w[n], a[m], aq[m][n]);
#pragma unroll
    for (int n = 0; n < 2; ++n)
#pragma unroll
      for (int m = 0; m < 4; ++m) ak[m][n] = mfma(w[2 + n], a[m], ak[m][n]);
#pragma unroll
    for (int n = 0; n < 2; ++n)
#pragma unroll
      for (int m = 0; m < 4; ++m) av[m][n] = mfma(a[m], w[4 + n], av[m][n]); };

  {
    const unsigned epoch = tag_issue(c);
    u32x4 xv[2][2];
#pragma unroll
    for (int e2 = 0; e2 < 2; ++e2) { const int R = 8 * j1 + 2 * c.wid + e2; const u16* xr = A.x + (size_t)(i1 * 128 + true_row(R >> 4, R & 15)) * 1024 + lane * 16;
      xv[e2][0] = *(const u32x4*)xr; xv[e2][1] = *(const u32x4*)(xr + 8); }
    tag_wait<4>(c, epoch);
#pragma unroll
    for (int e2 = 0; e2 < 2; ++e2) { float s = 0.f, qq = 0.f; acc_row(s, qq, xv[e2][0]); acc_row(s, qq, xv[e2][1]);
      s = sum_g(sum_row16(s)) * (1.0f / 1024.0f); qq = sum_g(sum_row16(qq)) * (1.0f / 1024.0f);
      const float rs = __builtin_amdgcn_rsqf(fmaxf(qq - s * s, 0.f) + 1e-5f);
      if (lane == 0) __builtin_amdgcn_raw_buffer_store_b128((u32x4){__float_as_uint(s), c.tag, __float_as_uint(rs), c.tag}, c.ws, OFF_GX + (i1 * 128 + 8 * j1 + 2 * c.wid + e2) * 16, 0, SC_SC1); }
  }
  consumer_loop<S, SB>(16, [&](int sb) { rd(a0, w0, sb, 0); }, [&](int sb) { rd(a1, w1, sb, 1); }, [&]() { mm(a0, w0); }, [&]() { mm(a1, w1); }, []() { interleave<24, 16>(); });

  const lds_t xf = lds + L_XF1 + c.wid * 12288 + lane * 16;
#pragma unroll
  for (int m = 0; m < 2; ++m)
#pragma unroll
    for (int n = 0; n < 2; ++n) {
      *(LAS f32x4*)(xf + (m * 6 + n) * 1024) = aq[2 + m][n]; *(LAS f32x4*)(xf + (m * 6 + 2 + n) * 1024) = ak[2 + m][n]; *(LAS f32x4*)(xf + (m * 6 + 4 + n) * 1024) = av[2 + m][n];
      e.q[m][n] = aq[m][n]; e.k[m][n] = ak[m][n]; e.v[m][n] = av[m][n];
    }
  wait_lgkm0();
}

DI void both_g_max(float& a, float& b) {
  auto r = __builtin_amdgcn_permlane16_swap(__float_as_uint(a), __float_as_uint(b), false, false);
  const float x = fmaxf(__uint_as_float(r[0]), __uint_as_float(r[1]));
  auto s = __builtin_amdgcn_permlane32_swap(__float_as_uint(x), __float_as_uint(x), false, false);
  const float y = fmaxf(__uint_as_float(s[0]), __uint_as_float(s[1]));
  auto t = __builtin_amdgcn_permlane16_swap(__float_as_uint(y), __float_as_uint(y), false, false);
  a = __uint_as_float(t[0]); b = __uint_as_float(t[1]);
}
DI void both_g_sum(float& a, float& b) {
  auto r = __builtin_amdgcn_permlane16_swap(__float_as_uint(a), __float_as_uint(b), false, false);
  const float x = __uint_as_float(r[0]) + __uint_as_float(r[1]);
  auto s = __builtin_amdgcn_permlane32_swap(__float_as_uint(x), __float_as_uint(x), false, false);
  const float y = __uint_as_float(s[0]) + __uint_as_float(s[1]);
  auto t = __builtin_amdgcn_permlane16_swap(__float_as_uint(y), __float_as_uint(y), false, false);
  a = __uint_as_float(t[0]); b = __uint_as_float(t[1]);
}
DI void feat(const f32x4& p0, const f32x4& p1, f32x4& e0, f32x4& e1, f32x4& n0, f32x4& n1) {
  constexpr float L2E = 1.4426950408889634f;
  const f32x4 t0 = p0 * L2E, t1 = p1 * L2E;
  float mx = fmaxf(fmaxf(fmaxf(t0[0], t0[1]), t0[2]), fmaxf(fmaxf(fmaxf(t0[3], t1[0]), t1[1]), fmaxf(t1[2], t1[3])));
  float mn = -fminf(fminf(fminf(t0[0], t0[1]), t0[2]), fminf(fminf(fminf(t0[3], t1[0]), t1[1]), fminf(t1[2], t1[3])));
  both_g_max(mx, mn);
  const f32x4 a0 = t0 - mx, a1 = t1 - mx, b0 = -t0 - mn, b1 = -t1 - mn;
#pragma unroll
  for (int r = 0; r < 4; ++r) { e0[r] = __builtin_amdgcn_exp2f(a0[r]); e1[r] = __builtin_amdgcn_exp2f(a1[r]); n0[r] = __builtin_amdgcn_exp2f(b0[r]); n1[r] = __builtin_amdgcn_exp2f(b1[r]); }
  const f32x4 se = e0 + e1, sn4 = n0 + n1;
  float sp = (se[0] + se[1]) + (se[2] + se[3]), sn = (sn4[0] + sn4[1]) + (sn4[2] + sn4[3]);
  both_g_sum(sp, sn);
  sp = __builtin_amdgcn_rcpf(sp); sn = __builtin_amdgcn_rcpf(sn);
  e0 *= sp; e1 *= sp; n0 *= sn; n1 *= sn;
}

DI void p1_epilogue(Ctx& c, const Args& A, const Map& M, Acc1& e) {
  const int lane = opaque(c.lane), cw = c.wid & 3, hf = c.wid >> 2, wr = cw >> 1, wc = cw & 1, g = lane >> 4, q = (lane & 15) >> 2, p = lane & 3, i16 = lane & 15;
  const int lw = cw, i1 = M.i1, j1 = M.j1;
  const lds_t lds = c.lds;
  const f32x4 Z4 = {0.f, 0.f, 0.f, 0.f};
  bar();

  if (hf) { const lds_t xf = lds + L_XF1 + cw * 12288 + lane * 16;
#pragma unroll
    for (int m = 0; m < 2; ++m)
#pragma unroll
      for (int n = 0; n < 2; ++n) { e.q[m][n] = *(LAS f32x4*)(xf + (m * 6 + n) * 1024); e.k[m][n] = *(LAS f32x4*)(xf + (m * 6 + 2 + n) * 1024); e.v[m][n] = *(LAS f32x4*)(xf + (m * 6 + 4 + n) * 1024); } }
  const lds_t tab = lds + L_TAB1;
  bf16x8 qf[2], kf[2], vf[2], wT[2];
  {
    f32x4 vv[2][2];
#pragma unroll
    for (int m = 0; m < 2; ++m) {
      const int R = 64 * wr + 32 * hf + 16 * m;
      const float mu = *(LAS float*)(tab + (R + i16) * 4), rs = *(LAS float*)(tab + 512 + (R + i16) * 4), nm = -mu * rs;
      const f32x4 mu4 = *(LAS f32x4*)(tab + (R + 4 * g) * 4), rs4 = *(LAS f32x4*)(tab + 512 + (R + 4 * g) * 4), nm4 = -mu4 * rs4;
      f32x4 qq[2], kk[2];
#pragma unroll
      for (int n = 0; n < 2; ++n) {
        const f32x4 cq = *(LAS f32x4*)(tab + 1024 + (32 * wc + 16 * n + 4 * g) * 4), ck = *(LAS f32x4*)(tab + 1024 + (64 + 32 * wc + 16 * n + 4 * g) * 4);
        const float cv = *(LAS float*)(tab + 1024 + (128 + 32 * wc + 16 * n + i16) * 4);
        qq[n] = e.q[m][n] * rs + cq * nm; kk[n] = e.k[m][n] * rs + ck * nm; vv[m][n] = e.v[m][n] * rs4 + nm4 * cv;
      }
      qf[m] = pk8(qq[0], qq[1]); kf[m] = pk8(kk[0], kk[1]);
    }
    vf[0] = pk8(vv[0][0], vv[1][0]); vf[1] = pk8(vv[0][1], vv[1][1]);
#pragma unroll
    for (int ft = 0; ft < 2; ++ft) wT[ft] = ldsT(lds + L_WPHI + wc * 2048 + (4 * g + q) * 64 + ft * 32 + 8 * p, 16 * 64);
  }
  wait_lgkm0(); bar();

  {
    const lds_t tb = lds + L_TB + c.wid * 4096;
#pragma unroll
    for (int m = 0; m < 2; ++m) {
      const f32x4 p0 = mfma(wT[0], kf[m], Z4), p1 = mfma(wT[1], kf[m], Z4);
      f32x4 e0, e1, n0, n1; feat(p0, p1, e0, e1, n0, n1);
      const lds_t rw = tb + (16 * m + i16) * 128;
      *(LAS u32x2*)(rw + 8 * ((0 + g) ^ i16)) = pk4(e0); *(LAS u32x2*)(rw + 8 * ((4 + g) ^ i16)) = pk4(e1);
      *(LAS u32x2*)(rw + 8 * ((8 + g) ^ i16)) = pk4(n0); *(LAS u32x2*)(rw + 8 * ((12 + g) ^ i16)) = pk4(n1);
    }
    asm volatile("s_waitcnt lgkmcnt(0)" ::: "memory");
    const bf16x8 ones = __builtin_bit_cast(bf16x8, (u32x4){ONES2, ONES2, ONES2, ONES2});
    const lds_t kr = lds + L_KR + c.wid * 8192 + lane * 16;
    float zf[4];
#pragma unroll
    for (int f = 0; f < 4; ++f) {
      const int r0 = 4 * g + q;
      const s16x4 lo = __builtin_amdgcn_ds_read_tr16_b64_v4i16((LAS s16x4*)(tb + r0 * 128 + 8 * ((4 * f + p) ^ r0)));
      const s16x4 hi = __builtin_amdgcn_ds_read_tr16_b64_v4i16((LAS s16x4*)(tb + (16 + r0) * 128 + 8 * ((4 * f + p) ^ r0)));
      const bf16x8 pf = __builtin_shufflevector(lo, hi, 0, 1, 2, 3, 4, 5, 6, 7);
      *(LAS f32x4*)(kr + f * 1024) = mfma(vf[0], pf, Z4); *(LAS f32x4*)(kr + (4 + f) * 1024) = mfma(vf[1], pf, Z4);
      zf[f] = mfma(ones, pf, Z4)[0];
    }
    *(LAS float*)(lds + L_ZR + c.wid * 256 + lane * 4) = g == 0 ? zf[0] : g == 1 ? zf[1] : g == 2 ? zf[2] : zf[3];

    wait_lgkm0(); bar();
    f32x4 k0 = Z4, k1 = Z4;
#pragma unroll
    for (int w = 0; w < 4; ++w) { const lds_t s = lds + L_KR + (wc + 2 * w) * 8192 + (4 * wr + 2 * hf) * 1024 + lane * 16; k0 += *(LAS f32x4*)s; k1 += *(LAS f32x4*)(s + 1024); }
    st16(c, pk8(k0, k1), lane * 16 + (OFF_KVP + (j1 * 16 + i1) * 8192 + wc * 4096 + wr * 2048 + hf * 1024));
    if (c.wid < 2) { float zt = 0.f;
#pragma unroll
      for (int w = 0; w < 4; ++w) zt += *(LAS float*)(lds + L_ZR + (wc + 2 * w) * 256 + lane * 4);
      __builtin_amdgcn_raw_buffer_store_b32(__float_as_uint(zt), c.ws, lane * 4, OFF_ZP + ((j1 * 16 + i1) * 2 + wc) * 256, SC_SC1); }
    wait_lgkm0(); bar();

    publish(c, FG_KV, j1, i1);
  }

  const __amdgpu_buffer_rsrc_t rW2 = __builtin_amdgcn_make_buffer_rsrc((void*)A.wo, 0, 1024 * 2048, 0x00020000);
  int voW2[2];
#pragma unroll
  for (int i = 0; i < 2; ++i) { const int r = (2 * lw + i) * 8 + (lane >> 3), s16 = lane & 7; voW2[i] = r * 2048 + M.j2 * 128 + (((s16 >> 1) ^ ((r >> 1) & 3)) << 5) + (s16 & 1) * 16; }
  auto issueW2 = [&](int t) {
#pragma unroll
    for (int i = 0; i < 2; ++i) dma<0>(rW2, lds + t * 8192 + (lw * 2 + i) * 1024, voW2[i], t * 64 * 2048); };
  if (hf) {
    if (M.act3) {
      const __amdgpu_buffer_rsrc_t rW3 = __builtin_amdgcn_make_buffer_rsrc((void*)A.win, 0, 1024 * 7168, 0x00020000);
#pragma unroll
      for (int i = 0; i < 8; ++i) { const int r = (8 * lw + i) * 2 + (lane >> 5), l5 = lane & 31, seg = l5 >> 4, s16 = l5 & 15;
        dma<0>(rW3, lds + 32768 + (8 * lw + i) * 1024, r * 7168 + (seg == 0 ? 3584 : 0) + M.j3 * 256 + (((s16 >> 1) ^ (r & 7)) << 5) + (s16 & 1) * 16, M.i3 * 64 * 7168); }
    }
#pragma unroll
    for (int t = 0; t < 3; ++t) { issueW2(t); issueW2(8 + t); }
  }
  bf16x8 ph[2][2];
#pragma unroll
  for (int m = 0; m < 2; ++m) {
    const f32x4 p0 = mfma(wT[0], qf[m], Z4), p1 = mfma(wT[1], qf[m], Z4);
    f32x4 e0, e1, n0, n1; feat(p0, p1, e0, e1, n0, n1);
    ph[m][0] = pk8(e0, e1); ph[m][1] = pk8(n0, n1);
  }
  asm volatile("" : "+v"(ph[0][0]), "+v"(ph[0][1]), "+v"(ph[1][0]), "+v"(ph[1][1]));

  kv_gather(c, j1);
  bar();

  {
    bf16x8 kt[3][2];
#pragma unroll
    for (int d = 0; d < 3; ++d)
#pragma unroll
      for (int s = 0; s < 2; ++s) kt[d][s] = ldsT(lds + L_XBUF + wc * 8192 + (32 * s + 4 * g + q) * 128 + 32 * d + 8 * p, 16 * 128);
#pragma unroll
    for (int m = 0; m < 2; ++m) {
      const f32x4 n0 = mfma(kt[0][1], ph[m][1], mfma(kt[0][0], ph[m][0], Z4)), n1 = mfma(kt[1][1], ph[m][1], mfma(kt[1][0], ph[m][0], Z4));
      const f32x4 dn = mfma(kt[2][1], ph[m][1], mfma(kt[2][0], ph[m][0], Z4));
      const float r = __builtin_amdgcn_rcpf(dn[0] + 1e-6f);
      st16(c, pk8(n0 * r, n1 * r), lane * 16 + (OFF_OF + (i1 * 16 + j1) * 16384 + ((4 * wr + 2 * hf + m) * 2 + wc) * 1024));
    }
  }
  publish(c, FG_O, i1, j1);

  if (hf && M.act3) {
    u32x2 cr[4][4];
#pragma unroll
    for (int n = 0; n < 4; ++n) tr4_issue<16 * 512, 32 * 512>((unsigned)(size_t)(lds + 32768 + (4 * g + q) * 512 + 8 * p + (lw >> 1) * 256 + 32 * ((4 * (lw & 1) + n) ^ (4 * (g & 1) + q))), cr[n]);
#pragma unroll
    for (int n = 0; n < 4; ++n) { tr4_wait(cr[n]); const float s = col_sum(cr[n]);
      if (lane < 16) st_granule(c, s, OFF_GC3 + (M.j3 * 16 + M.i3) * 2048 + ((4 * lw + n) * 16 + lane) * 8); }
  }
  if (hf) {
    issueW2(3);
#pragma unroll
    for (int t = 11; t < 16; ++t) issueW2(t);
  } else if (c.wid == 0) wait_flags(c, FG_O, M.i2, 16);
}

struct Ld3 { __amdgpu_buffer_rsrc_t rW; int voW[8]; };
DI void p3_setup(const Ctx& c, const Args& A, int j3, Ld3& L) {
  const int lane = opaque(c.lane), lw = c.wid & 3;
  L.rW = __builtin_amdgcn_make_buffer_rsrc((void*)A.win, 0, 1024 * 7168, 0x00020000);
#pragma unroll
  for (int i = 0; i < 8; ++i) { const int r = (8 * lw + i) * 2 + (lane >> 5), l5 = lane & 31, seg = l5 >> 4, s16 = l5 & 15;
    L.voW[i] = r * 7168 + (seg == 0 ? 3584 : 0) + j3 * 256 + (((s16 >> 1) ^ (r & 7)) << 5) + (s16 & 1) * 16; }
}
DI void p3_issueW(const Ctx& c, const Ld3& L, int t) { const lds_t d = c.lds + (t % 3) * 49152 + 16384 + (c.wid & 3) * 8192;
#pragma unroll
  for (int i = 0; i < 8; ++i) dma<0>(L.rW, d + i * 1024, L.voW[i], t * 64 * 7168); }
DI void p3_load(Ctx& c, const Ld3& L, int i3) {
  const int lane = opaque(c.lane), lw = c.wid - 4;
  const lds_t lds = c.lds;
  constexpr int S = 3, SB = 49152;
  const int voF = lane * 16 + lw * 1024;
  auto issueA = [&](int t) { const lds_t d = lds + (t % S) * SB + lw * 1024; const int so = OFF_HF + (i3 * 16 + t) * 16384;
#pragma unroll
    for (int i = 0; i < 4; ++i) dma<SC_SC1>(c.ws, d + i * 4096, voF, so + i * 4096); };
  auto issueW = [&](int t) { p3_issueW(c, L, t); };
  bar();
  issueA(0); issueA(1);
  u32x2 hv[16]; const int R = (lw & 1) * 64 + lane;
  auto gather = [&](int k) { if (lw < 2) {
      hv[2 * k] = __builtin_amdgcn_raw_buffer_load_b64(c.ws, R * 8, OFF_STH + (i3 * 16 + 2 * k) * 1024, SC_SC1);
      hv[2 * k + 1] = __builtin_amdgcn_raw_buffer_load_b64(c.ws, R * 8, OFF_STH + (i3 * 16 + 2 * k + 1) * 1024, SC_SC1); } };
  auto fin = [&]() { if (lw < 2) { float s = 0.f, qq = 0.f;
#pragma unroll
      for (int k = 0; k < 16; ++k) { s += __uint_as_float(hv[k][0]); qq += __uint_as_float(hv[k][1]); }
      s *= (1.0f / 1024.0f); qq *= (1.0f / 1024.0f);
      *(LAS float*)(lds + L_TAB3 + R * 4) = s; *(LAS float*)(lds + L_TAB3 + 512 + R * 4) = __builtin_amdgcn_rsqf(fmaxf(qq - s * s, 0.f) + 1e-5f);
      wait_lgkm0(); } };
  loader_loop16<8, true>(lw < 2, false, issueA, issueW, [](int) {}, gather, fin);
}
struct Acc3 { f32x4 z2[2][4], z1[2][4]; };
DI int xf3_base(int cw) { return cw < 3 ? 16384 + cw * 32768 : 98304; }
DI void p3_mma(Ctx& c, int i3, int j3, Acc3& e) {
  const int lane = opaque(c.lane), wr = c.wid >> 1, wc = c.wid & 1, g = lane >> 4, q = (lane & 15) >> 2, p = lane & 3;
  const lds_t lds = c.lds;
  const __amdgpu_buffer_rsrc_t rWS = c.ws;
  const f32x4 Z4 = {0.f, 0.f, 0.f, 0.f};
  constexpr int S = 3, SB = 49152;
  if (wc == 1) {
    const int off = OFF_GC3 + j3 * 16 * 2048 + (128 * wr + 2 * lane) * 8; u32x4 gv[16];
    for (unsigned spins = 0;; ++spins) {
      bool ok = true;
#pragma unroll
      for (int k = 0; k < 16; ++k) gv[k] = __builtin_amdgcn_raw_buffer_load_b128(rWS, off + k * 2048, 0, SC_SC1);
#pragma unroll
      for (int k = 0; k < 16; ++k) ok &= gv[k][1] == c.tag && gv[k][3] == c.tag;
      if (__all(ok) || gave_up(c, spins, 0x202u)) break;
      __builtin_amdgcn_s_sleep(2);
    }
    f32x2 cs = {0.f, 0.f};
#pragma unroll
    for (int k = 0; k < 16; ++k) { cs[0] += __uint_as_float(gv[k][0]); cs[1] += __uint_as_float(gv[k][2]); }
    *(LAS f32x2*)(lds + L_TAB3 + 1024 + (128 * wr + 2 * lane) * 4) = cs;
    wait_lgkm0();
  } else if (c.wid == 0) wait_flags(c, FG_H, i3, 16);
  f32x4 a2[4][4], a1_[4][4];
#pragma unroll
  for (int m = 0; m < 4; ++m)
#pragma unroll
    for (int n = 0; n < 4; ++n) { a2[m][n] = Z4; a1_[m][n] = Z4; }
#pragma unroll
  for (int m = 0; m < 4; ++m) asm volatile("" : "+v"(a2[m][0]), "+v"(a2[m][1]), "+v"(a2[m][2]), "+v"(a2[m][3]), "+v"(a1_[m][0]), "+v"(a1_[m][1]), "+v"(a1_[m][2]), "+v"(a1_[m][3]));
  bar();
  const int ao2 = wr * 8192 + lane * 16;
  int wo_[4];
#pragma unroll
  for (int n = 0; n < 4; ++n) wo_[n] = 16384 + (4 * g + q) * 512 + 8 * p + 32 * ((4 * wc + n) ^ (4 * (g & 1) + q));
  bf16x8 a0[4], w0[8], a1[4], w1[8];
  auto rd = [&](bf16x8(&a)[4], bf16x8(&w)[8], int sb, int ks) { const lds_t s = lds + sb;
#pragma unroll
    for (int n = 0; n < 8; ++n) w[n] = ldsT(s + wo_[n & 3] + (n >> 2) * 256 + ks * 32 * 512, 16 * 512);
#pragma unroll
    for (int m = 0; m < 4; ++m) a[m] = ldsA(s + ao2 + m * 2048 + ks * 1024); };
  auto mm = [&](bf16x8(&a)[4], bf16x8(&w)[8]) {
#pragma unroll
    for (int n = 0; n < 4; ++n)
#pragma unroll
      for (int m = 0; m < 4; ++m) a2[m][n] = mfma(a[m], w[n], a2[m][n]);
#pragma unroll
    for (int n = 0; n < 4; ++n)
#pragma unroll
      for (int m = 0; m < 4; ++m) a1_[m][n] = mfma(w[4 + n], a[m], a1_[m][n]); };
  consumer_loop<S, SB>(16, [&](int sb) { rd(a0, w0, sb, 0); }, [&](int sb) { rd(a1, w1, sb, 1); }, [&]() { mm(a0, w0); }, [&]() { mm(a1, w1); }, []() { interleave<32, 20>(); });

  const lds_t xf = lds + xf3_base(c.wid) + lane * 16;
#pragma unroll
  for (int m = 0; m < 2; ++m)
#pragma unroll
    for (int n = 0; n < 4; ++n) { *(LAS f32x4*)(xf + (m * 8 + n) * 1024) = a2[2 + m][n]; *(LAS f32x4*)(xf + (m * 8 + 4 + n) * 1024) = a1_[2 + m][n]; e.z2[m][n] = a2[m][n]; e.z1[m][n] = a1_[m][n]; }
  wait_lgkm0();
}
struct Ld4 { __amdgpu_buffer_rsrc_t rA; int voA, voF; };
DI void p4_issueW(const Ctx& c, const Ld4& L, int t) { const int lw = c.wid & 3; const lds_t d = c.lds + (t & 3) * 32768 + lw * 1024;
#pragma unroll
  for (int i = 0; i < 4; ++i) dma<0>(L.rA, d + i * 4096, L.voA, t * 128 + i * 32 * 4096); }
DI void p3_epilogue(Ctx& c, const Ld4& L, int i3, int j3, Acc3& e, u32x2 (&z1)[2][4]) {
  const int lane = opaque(c.lane), cw = c.wid & 3, hf = c.wid >> 2, wr = cw >> 1, wc = cw & 1, g = lane >> 4, i16 = lane & 15;
  const lds_t lds = c.lds, tab = lds + L_TAB3;
  const __amdgpu_buffer_rsrc_t rWS = c.ws;
  const f32x4 Z4 = {0.f, 0.f, 0.f, 0.f};
  bar();

  __builtin_amdgcn_s_setprio(1);
  if (hf) { const lds_t xf = lds + xf3_base(cw) + lane * 16;
#pragma unroll
    for (int m = 0; m < 2; ++m)
#pragma unroll
      for (int n = 0; n < 4; ++n) { e.z2[m][n] = *(LAS f32x4*)(xf + (m * 8 + n) * 1024); e.z1[m][n] = *(LAS f32x4*)(xf + (m * 8 + 4 + n) * 1024); } }
  {
    float cs[4];
#pragma unroll
    for (int n = 0; n < 4; ++n) cs[n] = *(LAS float*)(tab + 1024 + (64 * wc + 16 * n + i16) * 4);
#pragma unroll
    for (int m = 0; m < 2; ++m) {
      const int R = 64 * wr + 32 * hf + 16 * m + 4 * g;
      const f32x4 mu = *(LAS f32x4*)(tab + R * 4), rs = *(LAS f32x4*)(tab + 512 + R * 4), nm = -mu * rs;
      f32x4 s = Z4, qq = Z4;
#pragma unroll
      for (int n = 0; n < 4; ++n) { const f32x4 y = gelu4(e.z2[m][n] * rs + nm * cs[n]); e.z2[m][n] = y; s += y; qq += y * y; }
      sum_row16x8(s, qq);
      if (i16 < 4) { const float sv = i16 == 0 ? s[0] : i16 == 1 ? s[1] : i16 == 2 ? s[2] : s[3], qv = i16 == 0 ? qq[0] : i16 == 1 ? qq[1] : i16 == 2 ? qq[2] : qq[3];
        __builtin_amdgcn_raw_buffer_store_b128((u32x4){__float_as_uint(sv), c.tag, __float_as_uint(qv), c.tag}, rWS, OFF_ST2 + ((i3 * 14 + j3) * 2 + wc) * 2048 + (R + i16) * 16, 0, SC_SC1); }
    }
  }

  __builtin_amdgcn_s_setprio(0);
  if (!hf) {
#pragma unroll
    for (int s = 0; s < 3; ++s) p4_issueW(c, L, s);
  }
  {
    const int R0 = 64 * wr + 32 * hf, off = OFF_ST2 + (i3 * 28 + (lane >> 5)) * 2048 + (R0 + (lane & 31)) * 16;
    u32x4 v[14];
    for (unsigned spins = 0;; ++spins) {
      bool ok = true;
#pragma unroll
      for (int k = 0; k < 14; ++k) v[k] = __builtin_amdgcn_raw_buffer_load_b128(rWS, off + k * 4096, 0, SC_SC1);
#pragma unroll
      for (int k = 0; k < 14; ++k) ok &= v[k][1] == c.tag && v[k][3] == c.tag;
      if (__all(ok) || gave_up(c, spins, 0x203u)) break;
      __builtin_amdgcn_s_sleep(4);
    }
    __builtin_amdgcn_s_setprio(1);
    float s = 0.f, qq = 0.f;
#pragma unroll
    for (int k = 0; k < 14; ++k) { s += __uint_as_float(v[k][0]); qq += __uint_as_float(v[k][2]); }
    { auto a = __builtin_amdgcn_permlane32_swap(__float_as_uint(s), __float_as_uint(s), false, false); s = (__uint_as_float(a[0]) + __uint_as_float(a[1])) * (1.0f / 1792.0f);
      auto b = __builtin_amdgcn_permlane32_swap(__float_as_uint(qq), __float_as_uint(qq), false, false); qq = (__uint_as_float(b[0]) + __uint_as_float(b[1])) * (1.0f / 1792.0f); }
    const float rsv = __builtin_amdgcn_rsqf(fmaxf(qq - s * s, 0.f) + 1e-5f);
    const lds_t ft = lds + L_FIN + c.wid * 256;
    if (lane < 32) { *(LAS float*)(ft + lane * 4) = rsv; *(LAS float*)(ft + 128 + lane * 4) = -s * rsv; }
    asm volatile("s_waitcnt lgkmcnt(0)" ::: "memory");

    f32x4 rs[2], nm[2];
#pragma unroll
    for (int m = 0; m < 2; ++m) { rs[m] = *(LAS f32x4*)(ft + (16 * m + 4 * g) * 4); nm[m] = *(LAS f32x4*)(ft + 128 + (16 * m + 4 * g) * 4); }
#pragma unroll
    for (int n = 0; n < 4; ++n)
      st16(c, pk8(e.z2[0][n] * rs[0] + nm[0], e.z2[1][n] * rs[1] + nm[1]), lane * 16 + (OFF_Z2 + (j3 * 32 + 2 * i3 + wr) * 16384 + ((4 * wc + n) * 2 + hf) * 1024));
  }
  __builtin_amdgcn_s_setprio(0);
  publish(c, FG_Z2, j3, i3);

#pragma unroll
  for (int m = 0; m < 2; ++m) asm volatile("" : "+v"(e.z1[m][0]), "+v"(e.z1[m][1]), "+v"(e.z1[m][2]), "+v"(e.z1[m][3]));
#pragma unroll
  for (int m = 0; m < 2; ++m) {
    const int R = 64 * wr + 32 * hf + 16 * m + i16;
    const float mu = *(LAS float*)(tab + R * 4), rs = *(LAS float*)(tab + 512 + R * 4), nm = -mu * rs;
#pragma unroll
    for (int n = 0; n < 4; ++n) { const f32x4 cs = *(LAS f32x4*)(tab + 1024 + (128 + 64 * wc + 16 * n + 4 * g) * 4); z1[m][n] = pk4(gelu4(e.z1[m][n] * rs + cs * nm)); }
    asm volatile("" : "+v"(z1[m][0]), "+v"(z1[m][1]), "+v"(z1[m][2]), "+v"(z1[m][3]));
  }
}
DI void p4_load(Ctx& c, const Ld4& L, int j3) {
  const int lw = c.wid - 4;
  auto issueW = [&](int t) { p4_issueW(c, L, t); };
  auto issueA = [&](int t) { const lds_t d = c.lds + (t & 3) * 32768 + 16384 + lw * 1024; const int so = OFF_Z2 + (j3 * 32 + t) * 16384;
#pragma unroll
    for (int i = 0; i < 4; ++i) dma<SC_SC1>(c.ws, d + i * 4096, L.voF, so + i * 4096); };
  bar();
  if (c.b == 0 && lw == 0 && c.lane == 0) __hip_atomic_store((unsigned*)(c.wsp + OFF_EPOCH), c.tag, __ATOMIC_RELAXED, __HIP_MEMORY_SCOPE_AGENT);
#pragma unroll
  for (int s = 0; s < 3; ++s) issueA(s);
  loader_loop32(issueA, issueW);
}
DI void p4_mma(Ctx& c, int j3, f32x4 (&e)[2][4]) {
  const int lane = opaque(c.lane), wr = c.wid >> 1, wc = c.wid & 1, g = lane >> 4, i16 = lane & 15;
  const lds_t lds = c.lds;
  constexpr int S = 4, SB = 32768;
  f32x4 acc[4][4];
#pragma unroll
  for (int m = 0; m < 4; ++m)
#pragma unroll
    for (int n = 0; n < 4; ++n) { acc[m][n] = (f32x4){0.f, 0.f, 0.f, 0.f}; asm volatile("" : "+v"(acc[m][n])); }
  const int ao0 = (wr * 64 + i16) * 128 + ((g ^ (i16 >> 1)) << 4), ao1 = ao0 ^ 64, zo = 16384 + wc * 8192 + lane * 16;
  bf16x8 a0[4], w0[4], a1[4], w1[4];
  auto rd = [&](bf16x8(&a)[4], bf16x8(&w)[4], int sb, int ks) { const lds_t s = lds + sb;
#pragma unroll
    for (int n = 0; n < 4; ++n) { w[n] = ldsA(s + zo + n * 2048 + ks * 1024); a[n] = ldsA(s + (ks ? ao1 : ao0) + n * 2048); } };
  auto mm = [&](bf16x8(&a)[4], bf16x8(&w)[4]) {
#pragma unroll
    for (int n = 0; n < 4; ++n)
#pragma unroll
      for (int m = 0; m < 4; ++m) acc[m][n] = mfma(w[n], a[m], acc[m][n]); };
  if (c.wid == 0) wait_flags(c, FG_Z2, j3, 16);
  bar();
  consumer_loop<S, SB>(32, [&](int sb) { rd(a0, w0, sb, 0); }, [&](int sb) { rd(a1, w1, sb, 1); }, [&]() { mm(a0, w0); }, [&]() { mm(a1, w1); }, []() { interleave<16, 8>(); });

  const lds_t xf = lds + c.wid * 8192 + lane * 16;
#pragma unroll
  for (int m = 0; m < 2; ++m)
#pragma unroll
    for (int n = 0; n < 4; ++n) { *(LAS f32x4*)(xf + (m * 4 + n) * 1024) = acc[2 + m][n]; e[m][n] = acc[m][n]; }
  wait_lgkm0();
}
DI void p5_issueW(const Ctx& c, const Args& A, int j2);
DI void p4_epilogue(Ctx& c, const Args& A, int i3, int j3, int j2, f32x4 (&e)[2][4], const u32x2 (&z1)[2][4]) {
  const int lane = opaque(c.lane), cw = c.wid & 3, hf = c.wid >> 2, wr = cw >> 1, wc = cw & 1, i16 = lane & 15;
  float bs[2];
#pragma unroll
  for (int m = 0; m < 2; ++m) bs[m] = bflo((unsigned)A.bsp[i3 * 128 + true_row(4 * wr + 2 * hf + m, i16)]);
  bar();
  if (hf) { const lds_t xf = c.lds + cw * 8192 + lane * 16;
#pragma unroll
    for (int m = 0; m < 2; ++m)
#pragma unroll
      for (int n = 0; n < 4; ++n) e[m][n] = *(LAS f32x4*)(xf + (m * 4 + n) * 1024); }
#pragma unroll
  for (int m = 0; m < 2; ++m)
#pragma unroll
    for (int u = 0; u < 2; ++u)
      st16(c, pk8(up4(z1[m][2 * u]) * (e[m][2 * u] + bs[m]), up4(z1[m][2 * u + 1]) * (e[m][2 * u + 1] + bs[m])),
           lane * 16 + (OFF_UF + (i3 * 28 + 2 * j3 + wc) * 16384 + ((4 * wr + 2 * hf + m) * 2 + u) * 1024));
  wait_lgkm0(); bar();
  publish(c, FG_U, i3, j3);
  if (hf) p5_issueW(c, A, j2);
}

DI void p5_issueW(const Ctx& c, const Args& A, int j2) {
  const int lane = opaque(c.lane), lw = c.wid - 4;
  const __amdgpu_buffer_rsrc_t rW = __builtin_amdgcn_make_buffer_rsrc((void*)A.wout, 0, 1792 * 2048, 0x00020000);
  int vo[2];
#pragma unroll
  for (int i = 0; i < 2; ++i) { const int r = (2 * lw + i) * 8 + (lane >> 3), s16 = lane & 7; vo[i] = r * 2048 + j2 * 128 + (((s16 >> 1) ^ ((r >> 1) & 3)) << 5) + (s16 & 1) * 16; }
#pragma unroll
  for (int t = 0; t < 18; ++t)
#pragma unroll
    for (int i = 0; i < 2; ++i) dma<0>(rW, c.lds + ((t & 1) * 9 + (t >> 1)) * 8192 + (lw * 2 + i) * 1024, vo[i], ((t & 1) * 14 + (t >> 1)) * 64 * 2048);
}

constexpr int L_RED = 98304;
constexpr int RING = 6;
template <int NSH, int R, bool REFILL>
DI void gemm_split(Ctx& c, f32x4 (&res)[4], int aoff, __amdgpu_buffer_rsrc_t rW, int j2) {
  const int ln = opaque(c.lane), g = ln >> 4, q = (ln & 15) >> 2, p = ln & 3, rw = c.wid & 3, kh = c.wid >> 2;
  const lds_t lds = c.lds;
  const int sw2 = (2 * g + (q >> 1)) & 3, wl2 = (4 * g + q) * 128 + 8 * p + kh * (REFILL ? 9 : 8) * 8192, va = ln * 16 + rw * 4096;
  const int ao = aoff + kh * (NSH / 2) * 16384;
  int wo2[4];
#pragma unroll
  for (int n = 0; n < 4; ++n) wo2[n] = wl2 + 32 * (n ^ sw2);
  f32x4 acc[2][4];
#pragma unroll
  for (int n = 0; n < 4; ++n) { acc[0][n] = (f32x4){0.f, 0.f, 0.f, 0.f}; acc[1][n] = acc[0][n]; }
  u32x4 a[R][2]; bf16x8 w0[4], w1[4];
  auto lda = [&](int s) {
#pragma unroll
    for (int m = 0; m < 2; ++m) a[s % R][m] = __builtin_amdgcn_raw_buffer_load_b128(c.ws, va + m * 2048, ao + (s >> 1) * 16384 + (s & 1) * 1024, SC_SC1); };
  auto ldw = [&](bf16x8(&d)[4], int s) { const int lt = s >> 1, slot = (REFILL && lt >= 9) ? lt - 9 : lt;
#pragma unroll
    for (int n = 0; n < 4; ++n) d[n] = ldsT(lds + wo2[n] + slot * 8192 + (s & 1) * 4096, 16 * 128); };

#pragma unroll
  for (int s = 0; s < R; ++s) { lda(s); if (s == 0 || s == 2) bar(); }
  if (!REFILL && kh) {
#pragma unroll
    for (int i = 0; i < 2; ++i) { const int r = (2 * rw + i) * 8 + (ln >> 3), s16 = ln & 7, vo = r * 2048 + j2 * 128 + (((s16 >> 1) ^ ((r >> 1) & 3)) << 5) + (s16 & 1) * 16;
#pragma unroll
      for (int t = 4; t < 8; ++t) dma<0>(rW, lds + t * 8192 + (rw * 2 + i) * 1024, vo, t * 64 * 2048); }
  }
  ldw(w0, 0);
#pragma unroll
  for (int s = 0; s < NSH; ++s) {
    if (s == 5) {
      if (kh) { if (REFILL) wait_vm<2 * R + 10>(); else wait_vm<10>(); }
      bar(); __builtin_amdgcn_sched_barrier(0);
    }
    if (REFILL && s == 10) {
      wait_lgkm0(); bar();
      const int r = 8 * c.wid + (ln >> 3), s16 = ln & 7, vo = r * 2048 + j2 * 128 + (((s16 >> 1) ^ ((r >> 1) & 3)) << 5) + (s16 & 1) * 16;
#pragma unroll
      for (int k = 0; k < 10; ++k) dma<0>(rW, lds + ((k / 5) * 9 + (k % 5)) * 8192 + c.wid * 1024, vo, ((k / 5) * 14 + 9 + (k % 5)) * 64 * 2048);
      __builtin_amdgcn_sched_barrier(0);
    }
    if (REFILL && s == 17) { wait_vm<2 * ((NSH - R < 17 ? NSH - R : 17) - 10)>(); wait_lgkm0(); bar(); __builtin_amdgcn_sched_barrier(0); }
    if (s + 1 < NSH) { if (s & 1) ldw(w0, s + 1); else ldw(w1, s + 1); }
#pragma unroll
    for (int n = 0; n < 4; ++n)
#pragma unroll
      for (int m = 0; m < 2; ++m) acc[m][n] = mfma((s & 1) ? w1[n] : w0[n], __builtin_bit_cast(bf16x8, a[s % R][m]), acc[m][n]);
    if (s + R < NSH) lda(s + R);
    interleave<8, 8>();
    __builtin_amdgcn_sched_barrier(0);
  }
  wait_lgkm0(); bar();
#pragma unroll
  for (int n = 0; n < 4; ++n) *(LAS f32x4*)(lds + L_RED + c.wid * 4096 + n * 1024 + ln * 16) = kh ? acc[0][n] : acc[1][n];
  wait_lgkm0(); bar();
#pragma unroll
  for (int n = 0; n < 4; ++n) res[n] = (kh ? acc[1][n] : acc[0][n]) + *(LAS f32x4*)(lds + L_RED + (c.wid ^ 4) * 4096 + n * 1024 + ln * 16);
}

template <class PRE> DI void phase2(Ctx& c, const Args& A, int i2, int j2, bool pre_on, PRE pre) {
  const __amdgpu_buffer_rsrc_t rW2 = __builtin_amdgcn_make_buffer_rsrc((void*)A.wo, 0, 1024 * 2048, 0x00020000);
  const int lane = opaque(c.lane), g = lane >> 4, i16 = lane & 15, mt = 2 * (c.wid & 3) + (c.wid >> 2);
  u32x2 xr[4];
#pragma unroll
  for (int n = 0; n < 4; ++n) xr[n] = *(const u32x2*)(A.x + (size_t)(i2 * 128 + true_row(mt, i16)) * 1024 + j2 * 64 + 16 * n + 4 * g);
  wait_lgkm0(); bar();
  f32x4 acc[4];
  gemm_split<16, RING, false>(c, acc, OFF_OF + i2 * 262144, rW2, j2);

  float s = 0.f, qq = 0.f;
#pragma unroll
  for (int u = 0; u < 2; ++u) {
    f32x4 h0 = acc[2 * u], h1 = acc[2 * u + 1]; const u32x2 x0 = xr[2 * u], x1 = xr[2 * u + 1];
    h0[0] += bflo(x0[0]); h0[1] += bfhi(x0[0]); h0[2] += bflo(x0[1]); h0[3] += bfhi(x0[1]);
    h1[0] += bflo(x1[0]); h1[1] += bfhi(x1[0]); h1[2] += bflo(x1[1]); h1[3] += bfhi(x1[1]);
    const bf16x8 hb = pk8(h0, h1);
    st16(c, hb, lane * 16 + (OFF_HF + (i2 * 16 + j2) * 16384 + (mt * 2 + u) * 1024));
    acc_row(s, qq, __builtin_bit_cast(u32x4, hb));
  }
  s = sum_g(s); qq = sum_g(qq);
  if (lane < 16) __builtin_amdgcn_raw_buffer_store_b64((u32x2){__float_as_uint(s), __float_as_uint(qq)}, c.ws, OFF_STH + (i2 * 16 + j2) * 1024 + (mt * 16 + lane) * 8, 0, SC_SC1);
  publish(c, FG_H, i2, j2);
  if (pre_on) pre();
}

DI void phase5(Ctx& c, const Args& A, int i2, int j2) {
  const int lane = opaque(c.lane), g = lane >> 4, i16 = lane & 15, mt = 2 * (c.wid & 3) + (c.wid >> 2);
  u32x4 hr[2];
#pragma unroll
  for (int u = 0; u < 2; ++u) hr[u] = __builtin_amdgcn_raw_buffer_load_b128(c.ws, lane * 16, OFF_HF + (i2 * 16 + j2) * 16384 + (mt * 2 + u) * 1024, SC_SC1);
  const __amdgpu_buffer_rsrc_t rW = __builtin_amdgcn_make_buffer_rsrc((void*)A.wout, 0, 1792 * 2048, 0x00020000);
  if (c.wid == 0) wait_flags(c, FG_U, i2, 14);
  if (c.wid >= 4) wait_vm<24>();
  bar();
  f32x4 acc[4];
  gemm_split<28, RING, true>(c, acc, OFF_UF + i2 * 458752, rW, j2);

  const lds_t ot = c.lds + c.wid * 2048;
#pragma unroll
  for (int n = 0; n < 4; ++n) { const unsigned r0 = hr[n >> 1][2 * (n & 1)], r1 = hr[n >> 1][2 * (n & 1) + 1];
    *(LAS u32x2*)(ot + i16 * 128 + (((2 * n + (g >> 1)) ^ (i16 & 7)) << 4) + (g & 1) * 8) =
        (u32x2){pk2(acc[n][0] + bflo(r0), acc[n][1] + bfhi(r0)), pk2(acc[n][2] + bflo(r1), acc[n][3] + bfhi(r1))}; }
  asm volatile("s_waitcnt lgkmcnt(0)" ::: "memory");
  const __amdgpu_buffer_rsrc_t rO = __builtin_amdgcn_make_buffer_rsrc((void*)A.out, 0, 2048 * 2048, 0x00020000);
#pragma unroll
  for (int u = 0; u < 2; ++u) { const int r = 8 * u + (lane >> 3), ch = lane & 7;
    const u32x4 v = *(LAS u32x4*)(ot + r * 128 + ((ch ^ (r & 7)) << 4));
    __builtin_amdgcn_raw_buffer_store_b128(v, rO, (i2 * 128 + true_row(mt, r)) * 2048 + j2 * 128 + ch * 16, 0, SC_SC1); }
}

__global__ __launch_bounds__(512, 2) void fused_fwd(const u16* __restrict__ x, const u16* __restrict__ wqkv, const u16* __restrict__ wphi, const u16* __restrict__ wo,
                                                      const u16* __restrict__ win, const u16* __restrict__ wsp, const u16* __restrict__ bsp, const u16* __restrict__ wout,
                                                      u16* __restrict__ out, char* __restrict__ ws) {
  extern __shared__ __attribute__((aligned(16))) char shm[];
  Ctx c;
  c.lds = (lds_t)shm; c.wsp = ws; c.lane = threadIdx.x & 63; c.wid = __builtin_amdgcn_readfirstlane(threadIdx.x >> 6); c.b = blockIdx.x;
  c.ws = __builtin_amdgcn_make_buffer_rsrc((void*)ws, 0, WS_BYTES, 0x00020000);
  c.tag = 0u;
  if (threadIdx.x < 16) *(LAS unsigned*)(c.lds + L_CNT + threadIdx.x * 4) = 0u;
  const int xcd = c.b & 7, jj = c.b >> 3;
  Map M;
  M.i1 = 8 * (xcd & 1) + (jj >> 2); M.j1 = 4 * (xcd >> 1) + (jj & 3);
  M.i2 = 4 * (xcd >> 1) + (jj >> 3); M.j2 = 8 * (xcd & 1) + (jj & 7);
  M.act3 = jj < 28;
  M.i3 = 4 * (xcd >> 1) + (M.act3 ? jj / 7 : 0); M.j3 = 7 * (xcd & 1) + (M.act3 ? jj % 7 : 0);
  const Args A{x, wqkv, wphi, wo, win, wsp, bsp, wout, out, ws};
  const bool ld = c.wid >= 4;
  {
    Acc1 e;
    if (ld) p1_load(c, A, M.i1, M.j1);
    else { __builtin_amdgcn_s_setprio(1); p1_mma(c, A, M.i1, M.j1, e); __builtin_amdgcn_s_setprio(0); }
    p1_epilogue(c, A, M, e);
  }
  Ld3 L3;
  p3_setup(c, A, M.j3, L3);
  phase2(c, A, M.i2, M.j2, ld && M.act3, [&]() { p3_issueW(c, L3, 0); p3_issueW(c, L3, 1); });
  if (M.act3) {
    u32x2 z1[2][4];
    Ld4 L;
    L.rA = __builtin_amdgcn_make_buffer_rsrc((void*)(A.wsp + (size_t)M.i3 * 128 * 2048), 0, 128 * 4096, 0x00020000);
    {
      Acc3 e;
      if (ld) p3_load(c, L3, M.i3);
      else { __builtin_amdgcn_s_setprio(1); p3_mma(c, M.i3, M.j3, e); __builtin_amdgcn_s_setprio(0); }
      const int ln = opaque(c.lane);
      L.voA = rowmajor_voff(c.wid & 3, ln, 4096); L.voF = ln * 16 + (c.wid & 3) * 1024;
      p3_epilogue(c, L, M.i3, M.j3, e, z1);
    }
    f32x4 e4[2][4];
    if (ld) p4_load(c, L, M.j3);
    else { __builtin_amdgcn_s_setprio(1); p4_mma(c, M.j3, e4); __builtin_amdgcn_s_setprio(0); }
    p4_epilogue(c, A, M.i3, M.j3, M.j2, e4, z1);
  } else {
    bar();
    if (!ld) { const int wv = ((c.b & 7) * 4 + ((c.b >> 3) - 28)) * 4 + c.wid;
#pragma unroll 1
      for (int k = 0; k < 12; ++k) { const int Li = (k * 128 + wv) * 64 + c.lane;
        touch(c.lds, Li < 65536 ? (const char*)A.wsp + (size_t)Li * 128 : (const char*)A.wout + (size_t)((Li < 94208 ? Li : 94207) - 65536) * 128); } }
    if (ld) p5_issueW(c, A, M.j2);
  }
  phase5(c, A, M.i2, M.j2);
}

extern "C" void kernel_launch(void* const* d_in, const int* in_sizes, int n_in, void* d_out, int out_size, void* d_ws, size_t ws_size, hipStream_t stream) {
  (void)in_sizes; (void)n_in; (void)out_size;
  (void)hipFuncSetAttribute((const void*)fused_fwd, hipFuncAttributeMaxDynamicSharedMemorySize, LDS_BYTES);
  fused_fwd<<<256, 512, LDS_BYTES, stream>>>((const u16*)d_in[0], (const u16*)d_in[1], (const u16*)d_in[2], (const u16*)d_in[3], (const u16*)d_in[5], (const u16*)d_in[6],
                                             (const u16*)d_in[7], (const u16*)d_in[8], (u16*)d_out, (char*)d_ws + ((ws_size - WS_BYTES) & ~(size_t)0x1fffff));
}
```

```cpp
#include <hip/hip_runtime.h>
#include <hip/hip_bf16.h>
#include <stdint.h>
#include <type_traits>

#define LAS __attribute__((address_space(3)))
#define DI __device__ __forceinline__
typedef unsigned short u16;
typedef short bf16x8 __attribute__((ext_vector_type(8)));
typedef short s16x4 __attribute__((ext_vector_type(4)));
typedef float f32x4 __attribute__((ext_vector_type(4)));
typedef float f32x2 __attribute__((ext_vector_type(2)));
typedef unsigned u32x4 __attribute__((ext_vector_type(4)));
typedef unsigned u32x2 __attribute__((ext_vector_type(2)));
typedef __bf16 bf2 __attribute__((ext_vector_type(2)));
typedef LAS char* lds_t;

constexpr unsigned OFF_EPOCH = 0, OFF_ERR = 128, OFF_FLAG = 4096;
constexpr unsigned OFF_KVP = 262144;
constexpr unsigned OFF_ZP = OFF_KVP + 2097152;
constexpr unsigned OFF_ST2 = 27u << 20;
constexpr unsigned OFF_STH = OFF_ZP + 131072 + 262144;
constexpr unsigned OFF_GX = OFF_STH + 262144;
constexpr unsigned OFF_GC1 = OFF_GX + 65536;
constexpr unsigned OFF_GC3 = OFF_GC1 + 393216;
constexpr unsigned OFF_OF = 4u << 20, OFF_HF = 8u << 20, OFF_Z2 = 12u << 20, OFF_UF = 20u << 20;
constexpr unsigned WS_BYTES = 32u << 20;
enum { FG_KV = 0, FG_O = 1, FG_H = 2, FG_Z2 = 3, FG_U = 4 };
constexpr int LDS_BYTES = 163840;
constexpr int SC1 = 122880, L_WPHI = SC1, L_TAB1 = SC1 + 4096, L_XBUF = SC1 + 22528;
constexpr int L_XF1 = 65536, L_TB = 32768, L_KR = 65536, L_ZR = 131072;
constexpr int SC3 = 147456, L_TAB3 = SC3, L_FIN = SC3 + 4096;
constexpr int L_CNT = LDS_BYTES - 64, L_DUMMY = LDS_BYTES - 512;
constexpr int SC_SC1 = 16;

DI void bar() { asm volatile("" ::: "memory"); __builtin_amdgcn_s_barrier(); asm volatile("" ::: "memory"); }
template <int N> DI void wait_vm() { asm volatile("s_waitcnt vmcnt(%0)" ::"n"(N) : "memory"); }
DI int opaque(int v) { asm volatile("" : "+v"(v)); return v; }
DI void wait_lgkm0() { __builtin_amdgcn_s_waitcnt(0xc07f); }
DI unsigned pk2(float lo, float hi) { f32x2 v = {lo, hi}; return __builtin_bit_cast(unsigned, __builtin_convertvector(v, bf2)); }
DI bf16x8 pk8(const f32x4& a, const f32x4& b) { u32x4 r = {pk2(a[0], a[1]), pk2(a[2], a[3]), pk2(b[0], b[1]), pk2(b[2], b[3])}; return __builtin_bit_cast(bf16x8, r); }
DI float bflo(unsigned w) { return __uint_as_float(w << 16); }
DI float bfhi(unsigned w) { return __uint_as_float(w & 0xffff0000u); }
DI f32x4 mfma(bf16x8 a, bf16x8 b, f32x4 c) { return __builtin_amdgcn_mfma_f32_16x16x32_bf16(a, b, c, 0, 0, 0); }
DI bf16x8 ldsA(lds_t p) { return *(LAS bf16x8*)p; }
DI bf16x8 ldsT(lds_t p, int d) {
  s16x4 lo = __builtin_amdgcn_ds_read_tr16_b64_v4i16((LAS s16x4*)p), hi = __builtin_amdgcn_ds_read_tr16_b64_v4i16((LAS s16x4*)(p + d));
  return __builtin_shufflevector(lo, hi, 0, 1, 2, 3, 4, 5, 6, 7);
}
template <int D1, int D2> DI void tr4_issue(unsigned addr, u32x2 (&r)[4]) {
  asm volatile("ds_read_b64_tr_b16 %0, %4\n\tds_read_b64_tr_b16 %1, %4 offset:%5\n\tds_read_b64_tr_b16 %2, %4 offset:%6\n\tds_read_b64_tr_b16 %3, %4 offset:%7"
               : "=&v"(r[0]), "=&v"(r[1]), "=&v"(r[2]), "=&v"(r[3]) : "v"(addr), "n"(D1), "n"(D2), "n"(D1 + D2) : "memory");
}
DI void tr4_wait(u32x2 (&r)[4]) { asm volatile("s_waitcnt lgkmcnt(0)" : "+v"(r[0]), "+v"(r[1]), "+v"(r[2]), "+v"(r[3])::"memory"); }
DI float sum_g(float x) {
  auto a = __builtin_amdgcn_permlane16_swap(__float_as_uint(x), __float_as_uint(x), false, false);
  x = __uint_as_float(a[0]) + __uint_as_float(a[1]);
  auto b = __builtin_amdgcn_permlane32_swap(__float_as_uint(x), __float_as_uint(x), false, false);
  return __uint_as_float(b[0]) + __uint_as_float(b[1]);
}
template <int CTRL> DI float dpp_add(float x) { return x + __int_as_float(__builtin_amdgcn_update_dpp(0, __float_as_int(x), CTRL, 0xf, 0xf, true)); }
DI float sum_row16(float x) { x = dpp_add<0x128>(x); x = dpp_add<0x124>(x); x = dpp_add<0x122>(x); return dpp_add<0x121>(x); }
DI void sum_row16x8(f32x4& a, f32x4& b) {
#define RR(n) "v_add_f32_dpp %0, %0, %0 row_ror:" n " row_mask:0xf bank_mask:0xf\n\tv_add_f32_dpp %1, %1, %1 row_ror:" n " row_mask:0xf bank_mask:0xf\n\t" \
  "v_add_f32_dpp %2, %2, %2 row_ror:" n " row_mask:0xf bank_mask:0xf\n\tv_add_f32_dpp %3, %3, %3 row_ror:" n " row_mask:0xf bank_mask:0xf\n\t" \
  "v_add_f32_dpp %4, %4, %4 row_ror:" n " row_mask:0xf bank_mask:0xf\n\tv_add_f32_dpp %5, %5, %5 row_ror:" n " row_mask:0xf bank_mask:0xf\n\t" \
  "v_add_f32_dpp %6, %6, %6 row_ror:" n " row_mask:0xf bank_mask:0xf\n\tv_add_f32_dpp %7, %7, %7 row_ror:" n " row_mask:0xf bank_mask:0xf\n\t"
  float a0 = a[0], a1 = a[1], a2 = a[2], a3 = a[3], b0 = b[0], b1 = b[1], b2 = b[2], b3 = b[3];
  asm volatile("s_nop 1\n\t" RR("8") RR("4") RR("2") RR("1") "s_nop 0" : "+v"(a0), "+v"(a1), "+v"(a2), "+v"(a3), "+v"(b0), "+v"(b1), "+v"(b2), "+v"(b3));
#undef RR
  a = (f32x4){a0, a1, a2, a3}; b = (f32x4){b0, b1, b2, b3};
}
DI float dot2(unsigned a, unsigned b, float c) { return __builtin_amdgcn_fdot2_f32_bf16(__builtin_bit_cast(bf2, a), __builtin_bit_cast(bf2, b), c, false); }
constexpr unsigned ONES2 = 0x3f803f80u;
DI f32x4 gelu4(f32x4 x) {
  const f32x4 t = x * (x * x * -0.1029432397f + -2.302208198f);
  f32x4 d, r;
#pragma unroll
  for (int i = 0; i < 4; ++i) d[i] = __builtin_amdgcn_exp2f(t[i]);
  d += 1.0f;
#pragma unroll
  for (int i = 0; i < 4; ++i) r[i] = __builtin_amdgcn_rcpf(d[i]);
  return x * r;
}
DI u32x2 pk4(const f32x4& a) { return (u32x2){pk2(a[0], a[1]), pk2(a[2], a[3])}; }
DI f32x4 up4(u32x2 w) { return (f32x4){bflo(w[0]), bfhi(w[0]), bflo(w[1]), bfhi(w[1])}; }

struct Ctx {
  __amdgpu_buffer_rsrc_t ws;
  char* wsp; lds_t lds; unsigned tag; int lane, wid, b;
};
DI unsigned* flag_ptr(const Ctx& c, int g, int a, int bb) { return (unsigned*)(c.wsp + OFF_FLAG + ((g * 16 + a) * 16 + bb) * 128); }
DI void publish(const Ctx& c, int g, int a, int bb) {
  wait_vm<0>();
  if (c.lane == 0) {
    unsigned old = __hip_atomic_fetch_add((LAS unsigned*)(c.lds + L_CNT + g * 4), 1u, __ATOMIC_RELAXED, __HIP_MEMORY_SCOPE_WORKGROUP);
    if (old == 7u) __hip_atomic_store(flag_ptr(c, g, a, bb), c.tag, __ATOMIC_RELAXED, __HIP_MEMORY_SCOPE_AGENT);
  }
}
DI bool gave_up(const Ctx& c, unsigned spins, unsigned code) {
  if (spins < 40000u) return false;
  if (c.lane == 0) __hip_atomic_store((unsigned*)(c.wsp + OFF_ERR), code, __ATOMIC_RELAXED, __HIP_MEMORY_SCOPE_AGENT);
  return true;
}
DI void wait_flags(const Ctx& c, int g, int a, int n) {
  const unsigned* p = flag_ptr(c, g, a, c.lane < n ? c.lane : 0);
  for (unsigned spins = 0;; ++spins) {
    const unsigned v = __hip_atomic_load(p, __ATOMIC_RELAXED, __HIP_MEMORY_SCOPE_AGENT);
    if (__all(v == c.tag) || gave_up(c, spins, 0x100u + (unsigned)g)) break;
    __builtin_amdgcn_s_sleep(1);
  }
  asm volatile("" ::: "memory");
}

DI void st16(const Ctx& c, bf16x8 v, int off) { __builtin_amdgcn_raw_buffer_store_b128(__builtin_bit_cast(u32x4, v), c.ws, off, 0, SC_SC1); }
template <int AUX> DI void dma(__amdgpu_buffer_rsrc_t r, lds_t dst, int voff, int soff) {
  if (AUX) asm volatile("s_mov_b32 m0, %0\n\ts_nop 3\n\tbuffer_load_dwordx4 %1, %2, %3 offen sc1 lds" ::"s"((unsigned)(size_t)dst), "v"(voff), "s"(r), "s"(soff) : "memory");
  else asm volatile("s_mov_b32 m0, %0\n\ts_nop 3\n\tbuffer_load_dwordx4 %1, %2, %3 offen lds" ::"s"((unsigned)(size_t)dst), "v"(voff), "s"(r), "s"(soff) : "memory");
}

template <class IA, class IW>
DI void loader_loop32(IA issueA, IW issueW) {
  wait_vm<8>(); bar();
  issueA(3); issueW(3); wait_vm<4 + 8>(); bar();
  issueA(4); issueW(4); wait_vm<16>(); bar();
#pragma unroll 1
  for (int t = 2; t < 29; ++t) { issueA(t + 3); issueW(t + 3); wait_vm<16>(); bar(); }
  wait_vm<8>(); bar();
  wait_vm<0>(); bar();
  bar();
}
template <int NW, bool GATED, class IA, class IW, class POST, class GA, class FIN>
DI void loader_loop16(bool full, bool once, IA issueA, IW issueW, POST post, GA gather, FIN fin) {
  constexpr int PPW = 4 + NW;
  if (GATED) wait_vm<4>(); else wait_vm<PPW>();
  bar();
  issueA(2); issueW(2); post(0); wait_vm<PPW>(); bar();
#pragma unroll 1
  for (int t = 1; t < 5; ++t) { issueA(t + 2); issueW(t + 2); post(t); wait_vm<PPW>(); bar(); }
#pragma unroll
  for (int t = 5; t < 13; ++t) {
    issueA(t + 2); issueW(t + 2); gather(t - 5);
    if (full) { if (t == 5) wait_vm<PPW + 2>(); else wait_vm<PPW + 4>(); }
    else if (once && t <= 6) wait_vm<PPW + 2>();
    else wait_vm<PPW>();
    bar();
  }
  issueA(15); issueW(15); if (full) wait_vm<PPW + 2>(); else wait_vm<PPW>(); bar();
  wait_vm<0>(); fin(); bar();
  bar();
}
template <int S, int SB, class RD0, class RD1, class MM0, class MM1, class SCH>
DI void consumer_loop(int nt, RD0 rd0, RD1 rd1, MM0 mm0, MM1 mm1, SCH sched) {
  bar();
  rd0(0);
  int sb = 0;
#pragma unroll 2
  for (int t = 0; t < nt; ++t) {
    __builtin_amdgcn_sched_barrier(0);
    rd1(sb); mm0(); sched();
    __builtin_amdgcn_sched_barrier(0);
    wait_lgkm0(); bar();
    __builtin_amdgcn_sched_barrier(0);
    sb = (sb == (S - 1) * SB) ? 0 : sb + SB;
    rd0(sb); mm1(); sched();
    __builtin_amdgcn_sched_barrier(0);
  }
}
DI void touch(lds_t lds, const void* p) {
  asm volatile("s_mov_b32 m0, %1\n\ts_nop 0\n\tglobal_load_lds_dword %0, off" ::"v"(p), "s"((unsigned)(size_t)(lds + L_DUMMY)) : "memory"); }
template <int NM, int NR> DI void interleave() {
#pragma unroll
  for (int i = 0; i < NR; ++i) { __builtin_amdgcn_sched_group_barrier(0x008, 1, 0); __builtin_amdgcn_sched_group_barrier(0x100, 1, 0); }
  __builtin_amdgcn_sched_group_barrier(0x008, NM - NR, 0);
}

DI int rowmajor_voff(int lw, int lane, int pitch) {
  const int rr = lane >> 3, slot = 8 * (lw & 1) + rr;
  const int row = 8 * (slot >> 2) + 4 * (lw >> 1) + (slot & 3);
  return row * pitch + (((lane & 7) ^ (slot >> 1)) << 4);
}
DI int true_row(int mt, int i) { return 32 * (mt >> 1) + 8 * (i >> 2) + 4 * (mt & 1) + (i & 3); }

DI void acc_row(float& s, float& q, u32x4 w) {
#pragma unroll
  for (int i = 0; i < 4; ++i) { s = dot2(w[i], ONES2, s); q = dot2(w[i], w[i], q); } }
DI float col_sum(const u32x2 (&r)[4]) { float s0 = 0.f, s1 = 0.f;
#pragma unroll
  for (int i = 0; i < 4; ++i) { s0 = dot2(r[i][0], ONES2, s0); s1 = dot2(r[i][1], ONES2, s1); }
  return sum_g(s0 + s1); }
DI void st_granule(const Ctx& c, float v, int off) { __builtin_amdgcn_raw_buffer_store_b64((u32x2){__float_as_uint(v), c.tag}, c.ws, off, 0, SC_SC1); }
DI float sum_granules16(const Ctx& c, u32x2 (&v)[16], int off, int stride) {
  for (unsigned spins = 0;; ++spins) {
    bool ok = true;
#pragma unroll
    for (int k = 0; k < 16; ++k) ok &= v[k][1] == c.tag;
    if (__all(ok) || gave_up(c, spins, 0x200u)) break;
    __builtin_amdgcn_s_sleep(2);
#pragma unroll
    for (int k = 0; k < 16; ++k) v[k] = __builtin_amdgcn_raw_buffer_load_b64(c.ws, off + k * stride, 0, SC_SC1);
  }
  float s = 0.f;
#pragma unroll
  for (int k = 0; k < 16; ++k) s += __uint_as_float(v[k][0]);
  return s;
}

struct Args { const u16 *x, *wqkv, *wphi, *wo, *win, *wsp, *bsp, *wout; u16* out; char* ws; };

DI unsigned tag_issue(const Ctx& c) { unsigned e; asm volatile("global_load_dword %0, %1, %2 sc1" : "=v"(e) : "v"(0), "s"(c.wsp) : "memory"); return e; }
template <int N> DI void tag_wait(Ctx& c, unsigned e) { asm volatile("s_waitcnt vmcnt(%1)" : "+v"(e) : "n"(N) : "memory"); c.tag = __builtin_amdgcn_readfirstlane(e) + 1u; }
DI void kv_gather(const Ctx& c, int j1) {
  const int lane = c.lane, g = lane >> 4, i16 = lane & 15, tid = c.wid * 64 + lane;
  f32x4 a0 = {0, 0, 0, 0}, a1 = {0, 0, 0, 0};
  u32x4 v[16];
  const unsigned f0 = __hip_atomic_load(flag_ptr(c, FG_KV, j1, i16), __ATOMIC_RELAXED, __HIP_MEMORY_SCOPE_AGENT);
  const unsigned ready = __builtin_amdgcn_readfirstlane((unsigned)(__ballot(f0 == c.tag) & 0xffffull));
  asm volatile("" ::: "memory");
#pragma unroll
  for (int k = 0; k < 16; ++k) if ((ready >> k) & 1u) v[k] = __builtin_amdgcn_raw_buffer_load_b128(c.ws, tid * 16, OFF_KVP + (j1 * 16 + k) * 8192, SC_SC1);
  if (ready != 0xffffu) {
    wait_flags(c, FG_KV, j1, 16);
#pragma unroll
    for (int k = 0; k < 16; ++k) if (!((ready >> k) & 1u)) v[k] = __builtin_amdgcn_raw_buffer_load_b128(c.ws, tid * 16, OFF_KVP + (j1 * 16 + k) * 8192, SC_SC1);
  }
  float zs = 0.f;
  if (c.wid < 2) { unsigned zv[16];
#pragma unroll
    for (int k = 0; k < 16; ++k) zv[k] = __builtin_amdgcn_raw_buffer_load_b32(c.ws, lane * 4, OFF_ZP + ((j1 * 16 + k) * 2 + c.wid) * 256, SC_SC1);
#pragma unroll
    for (int k = 0; k < 16; ++k) zs += __uint_as_float(zv[k]); }
#pragma unroll
  for (int k = 0; k < 16; ++k) { a0[0] += bflo(v[k][0]); a0[1] += bfhi(v[k][0]); a0[2] += bflo(v[k][1]); a0[3] += bfhi(v[k][1]); a1[0] += bflo(v[k][2]); a1[1] += bfhi(v[k][2]); a1[2] += bflo(v[k][3]); a1[3] += bfhi(v[k][3]); }
  const int w = c.wid, hd = w >> 2, dt = (w >> 1) & 1, fp = w & 1;
  const lds_t tb = c.lds + L_XBUF + hd * 8192 + (32 * fp + i16) * 128 + (16 * dt + 4 * g) * 2;
  *(LAS u32x2*)tb = (u32x2){pk2(a0[0], a0[1]), pk2(a0[2], a0[3])};
  *(LAS u32x2*)(tb + 16 * 128) = (u32x2){pk2(a1[0], a1[1]), pk2(a1[2], a1[3])};
  if (c.wid < 2) { const unsigned z2 = pk2(zs, zs); const lds_t t3 = c.lds + L_XBUF + c.wid * 8192 + lane * 128 + 64;
    *(LAS u32x4*)t3 = (u32x4){z2, z2, z2, z2}; *(LAS u32x4*)(t3 + 16) = (u32x4){z2, z2, z2, z2}; }
  wait_lgkm0();
}
struct Map { int i1, j1, i2, j2, i3, j3; bool act3; };
DI void p1_load(Ctx& c, const Args& A, int i1, int j1) {
  const int lane = c.lane, lw = c.wid - 4, g = lane >> 4, q = (lane & 15) >> 2, p = lane & 3;
  const lds_t lds = c.lds;
  const __amdgpu_buffer_rsrc_t rWS = c.ws;
  constexpr int S = 3, SB = 40960, NW = 6;
  const __amdgpu_buffer_rsrc_t rA = __builtin_amdgcn_make_buffer_rsrc((void*)(A.x + (size_t)i1 * 128 * 1024), 0, 128 * 2048, 0x00020000);
  const __amdgpu_buffer_rsrc_t rW = __builtin_amdgcn_make_buffer_rsrc((void*)A.wqkv, 0, 1024 * 6144, 0x00020000);
  const __amdgpu_buffer_rsrc_t rP = __builtin_amdgcn_make_buffer_rsrc((void*)A.wphi, 0, 65536, 0x00020000);
  const int voA = rowmajor_voff(lw, lane, 2048);
  const int rot = 4 * (i1 & 3);
  auto issueA = [&](int t) { const lds_t d = lds + (t % S) * SB + lw * 1024; const int so = ((t + rot) & 15) * 128;
#pragma unroll
    for (int i = 0; i < 4; ++i) dma<0>(rA, d + i * 4096, voA, so + i * 32 * 2048); };
  const unsigned epoch = tag_issue(c);
  issueA(0);
  int voW[NW];
  { const int ln = opaque(lane);
#pragma unroll
    for (int i = 0; i < 3; ++i) { const int o = (NW * lw + i) * 1024 + ln * 16, r = o / 384, rem = o % 384, seg = rem >> 7, s16 = (rem & 127) >> 4;
      const int c32 = (s16 >> 1) ^ ((r >> 1) & 3), r5 = r & 31, k = (r & 32) + 8 * ((r5 >> 2) & 3) + 4 * (r5 >> 4) + (r5 & 3);
      voW[i] = k * 6144 + seg * 2048 + j1 * 128 + c32 * 32 + (s16 & 1) * 16;
      voW[i + 3] = voW[i] + 16 * 6144; }
  }
  auto issueW = [&](int t) { const lds_t d = lds + (t % S) * SB + 16384 + lw * NW * 1024; const int so = ((t + rot) & 15) * 64 * 6144;
#pragma unroll
    for (int i = 0; i < NW; ++i) dma<0>(rW, d + i * 1024, voW[i], so); };
  issueW(0); issueA(1); issueW(1);
  dma<0>(rP, lds + L_WPHI + lw * 1024, lane * 16 + lw * 1024, j1 * 4096);
  tag_wait<21>(c, epoch);

  u32x2 gv[16]; u32x4 gx[2];
  const int goff = OFF_GC1 + j1 * 16 * 1536 + (lw * 64 + lane) * 8, xoff = OFF_GX + (i1 * 128 + lane) * 16;
  const int tau = i1 >> 2;
  auto post = [&](int t) {
    if (t != tau) return;
    const int sw = (2 * g + (q >> 1)) & 3; u32x2 cr[3][4];
#pragma unroll
    for (int n = 0; n < 3; ++n) { const int nu = 3 * lw + n;
      tr4_issue<16 * 384, 32 * 384>((unsigned)(size_t)(lds + (t % S) * SB + 16384 + (4 * g + q) * 384 + 8 * p + (nu >> 2) * 128 + 32 * ((nu & 3) ^ sw)), cr[n]); }
#pragma unroll
    for (int n = 0; n < 3; ++n) { tr4_wait(cr[n]); const float s = col_sum(cr[n]);
      if (lane < 16) st_granule(c, s, OFF_GC1 + (j1 * 16 + rot + tau) * 1536 + ((3 * lw + n) * 16 + lane) * 8); }
  };
  auto gather = [&](int k) {
    if (lw < 3) { gv[2 * k] = __builtin_amdgcn_raw_buffer_load_b64(rWS, goff + 2 * k * 1536, 0, SC_SC1); gv[2 * k + 1] = __builtin_amdgcn_raw_buffer_load_b64(rWS, goff + (2 * k + 1) * 1536, 0, SC_SC1); }
    else if (k == 0) { gx[0] = __builtin_amdgcn_raw_buffer_load_b128(rWS, xoff, 0, SC_SC1); gx[1] = __builtin_amdgcn_raw_buffer_load_b128(rWS, xoff + 1024, 0, SC_SC1); }
  };
  auto fin = [&]() {
    if (lw < 3) *(LAS float*)(lds + L_TAB1 + 1024 + (lw * 64 + lane) * 4) = sum_granules16(c, gv, goff, 1536);
    else {
      for (unsigned spins = 0;; ++spins) {
        if (__all(gx[0][1] == c.tag && gx[0][3] == c.tag && gx[1][1] == c.tag && gx[1][3] == c.tag) || gave_up(c, spins, 0x201u)) break;
        __builtin_amdgcn_s_sleep(2);
        gx[0] = __builtin_amdgcn_raw_buffer_load_b128(rWS, xoff, 0, SC_SC1); gx[1] = __builtin_amdgcn_raw_buffer_load_b128(rWS, xoff + 1024, 0, SC_SC1);
      }
#pragma unroll
      for (int e = 0; e < 2; ++e) { *(LAS unsigned*)(lds + L_TAB1 + (lane + 64 * e) * 4) = gx[e][0]; *(LAS unsigned*)(lds + L_TAB1 + 512 + (lane + 64 * e) * 4) = gx[e][2]; }
    }
    wait_lgkm0();
    __builtin_amdgcn_s_waitcnt(0x0f70);
    const int gw = c.b * 4 + lw;
    if (lane < 56) touch(lds, (const char*)A.win + (size_t)(gw * 56 + lane) * 128);
    if (lane < 16) touch(lds, (const char*)A.wo + (size_t)(gw * 16 + lane) * 128);
  };
  loader_loop16<NW, false>(lw < 3, lw == 3, issueA, issueW, post, gather, fin);
}

struct Acc1 { f32x4 q[2][2], k[2][2], v[2][2]; };
DI void p1_mma(Ctx& c, const Args& A, int i1, int j1, Acc1& e) {
  const int lane = c.lane, wr = c.wid >> 1, wc = c.wid & 1, g = lane >> 4, q = (lane & 15) >> 2, p = lane & 3, i16 = lane & 15;
  const lds_t lds = c.lds;
  const f32x4 Z4 = {0.f, 0.f, 0.f, 0.f};
  constexpr int S = 3, SB = 40960;
  f32x4 aq[4][2], ak[4][2], av[4][2];
#pragma unroll
  for (int m = 0; m < 4; ++m)
#pragma unroll
    for (int n = 0; n < 2; ++n) { aq[m][n] = Z4; ak[m][n] = Z4; av[m][n] = Z4; asm volatile("" : "+v"(aq[m][n]), "+v"(ak[m][n]), "+v"(av[m][n])); }
  const int sw = (2 * g + (q >> 1)) & 3;
  const int ao0 = (wr * 64 + i16) * 128 + ((g ^ (i16 >> 1)) << 4), ao1 = ao0 ^ 64;
  const int wl = 16384 + (4 * g + q) * 384 + 8 * p;
  const int wo0 = wl + 32 * ((2 * wc) ^ sw), wo1 = wl + 32 * ((2 * wc + 1) ^ sw);
  bf16x8 a0[4], w0[6], a1[4], w1[6];
  auto rd = [&](bf16x8(&a)[4], bf16x8(&w)[6], int sb, int ks) { const lds_t s = lds + sb;
#pragma unroll
    for (int sg = 0; sg < 3; ++sg) { w[2 * sg] = ldsT(s + wo0 + sg * 128 + ks * 32 * 384, 16 * 384); w[2 * sg + 1] = ldsT(s + wo1 + sg * 128 + ks * 32 * 384, 16 * 384); }
#pragma unroll
    for (int m = 0; m < 4; ++m) a[m] = ldsA(s + (ks ? ao1 : ao0) + m * 2048); };
  auto mm = [&](bf16x8(&a)[4], bf16x8(&w)[6]) {
#pragma unroll
    for (int n = 0; n < 2; ++n)
#pragma unroll
      for (int m = 0; m < 4; ++m) aq[m][n] = mfma(w[n], a[m], aq[m][n]);
#pragma unroll
    for (int n = 0; n < 2; ++n)
#pragma unroll
      for (int m = 0; m < 4; ++m) ak[m][n] = mfma(w[2 + n], a[m], ak[m][n]);
#pragma unroll
    for (int n = 0; n < 2; ++n)
#pragma unroll
      for (int m = 0; m < 4; ++m) av[m][n] = mfma(a[m], w[4 + n], av[m][n]); };

  {
    const unsigned epoch = tag_issue(c);
    u32x4 xv[2][2];
#pragma unroll
    for (int e2 = 0; e2 < 2; ++e2) { const int R = 8 * j1 + 2 * c.wid + e2; const u16* xr = A.x + (size_t)(i1 * 128 + true_row(R >> 4, R & 15)) * 1024 + lane * 16;
      xv[e2][0] = *(const u32x4*)xr; xv[e2][1] = *(const u32x4*)(xr + 8); }
    tag_wait<4>(c, epoch);
#pragma unroll
    for (int e2 = 0; e2 < 2; ++e2) { float s = 0.f, qq = 0.f; acc_row(s, qq, xv[e2][0]); acc_row(s, qq, xv[e2][1]);
      s = sum_g(sum_row16(s)) * (1.0f / 1024.0f); qq = sum_g(sum_row16(qq)) * (1.0f / 1024.0f);
      const float rs = __builtin_amdgcn_rsqf(fmaxf(qq - s * s, 0.f) + 1e-5f);
      if (lane == 0) __builtin_amdgcn_raw_buffer_store_b128((u32x4){__float_as_uint(s), c.tag, __float_as_uint(rs), c.tag}, c.ws, OFF_GX + (i1 * 128 + 8 * j1 + 2 * c.wid + e2) * 16, 0, SC_SC1); }
  }
  consumer_loop<S, SB>(16, [&](int sb) { rd(a0, w0, sb, 0); }, [&](int sb) { rd(a1, w1, sb, 1); }, [&]() { mm(a0, w0); }, [&]() { mm(a1, w1); }, []() { interleave<24, 16>(); });

  const lds_t xf = lds + L_XF1 + c.wid * 12288 + lane * 16;
#pragma unroll
  for (int m = 0; m < 2; ++m)
#pragma unroll
    for (int n = 0; n < 2; ++n) {
      *(LAS f32x4*)(xf + (m * 6 + n) * 1024) = aq[2 + m][n]; *(LAS f32x4*)(xf + (m * 6 + 2 + n) * 1024) = ak[2 + m][n]; *(LAS f32x4*)(xf + (m * 6 + 4 + n) * 1024) = av[2 + m][n];
      e.q[m][n] = aq[m][n]; e.k[m][n] = ak[m][n]; e.v[m][n] = av[m][n];
    }
  wait_lgkm0();
}

DI void both_g_max(float& a, float& b) {
  auto r = __builtin_amdgcn_permlane16_swap(__float_as_uint(a), __float_as_uint(b), false, false);
  const float x = fmaxf(__uint_as_float(r[0]), __uint_as_float(r[1]));
  auto s = __builtin_amdgcn_permlane32_swap(__float_as_uint(x), __float_as_uint(x), false, false);
  const float y = fmaxf(__uint_as_float(s[0]), __uint_as_float(s[1]));
  auto t = __builtin_amdgcn_permlane16_swap(__float_as_uint(y), __float_as_uint(y), false, false);
  a = __uint_as_float(t[0]); b = __uint_as_float(t[1]);
}
DI void both_g_sum(float& a, float& b) {
  auto r = __builtin_amdgcn_permlane16_swap(__float_as_uint(a), __float_as_uint(b), false, false);
  const float x = __uint_as_float(r[0]) + __uint_as_float(r[1]);
  auto s = __builtin_amdgcn_permlane32_swap(__float_as_uint(x), __float_as_uint(x), false, false);
  const float y = __uint_as_float(s[0]) + __uint_as_float(s[1]);
  auto t = __builtin_amdgcn_permlane16_swap(__float_as_uint(y), __float_as_uint(y), false, false);
  a = __uint_as_float(t[0]); b = __uint_as_float(t[1]);
}
DI void feat(const f32x4& p0, const f32x4& p1, f32x4& e0, f32x4& e1, f32x4& n0, f32x4& n1) {
  constexpr float L2E = 1.4426950408889634f;
  const f32x4 t0 = p0 * L2E, t1 = p1 * L2E;
  float mx = fmaxf(fmaxf(fmaxf(t0[0], t0[1]), t0[2]), fmaxf(fmaxf(fmaxf(t0[3], t1[0]), t1[1]), fmaxf(t1[2], t1[3])));
  float mn = -fminf(fminf(fminf(t0[0], t0[1]), t0[2]), fminf(fminf(fminf(t0[3], t1[0]), t1[1]), fminf(t1[2], t1[3])));
  both_g_max(mx, mn);
  const f32x4 a0 = t0 - mx, a1 = t1 - mx, b0 = -t0 - mn, b1 = -t1 - mn;
#pragma unroll
  for (int r = 0; r < 4; ++r) { e0[r] = __builtin_amdgcn_exp2f(a0[r]); e1[r] = __builtin_amdgcn_exp2f(a1[r]); n0[r] = __builtin_amdgcn_exp2f(b0[r]); n1[r] = __builtin_amdgcn_exp2f(b1[r]); }
  const f32x4 se = e0 + e1, sn4 = n0 + n1;
  float sp = (se[0] + se[1]) + (se[2] + se[3]), sn = (sn4[0] + sn4[1]) + (sn4[2] + sn4[3]);
  both_g_sum(sp, sn);
  sp = __builtin_amdgcn_rcpf(sp); sn = __builtin_amdgcn_rcpf(sn);
  e0 *= sp; e1 *= sp; n0 *= sn; n1 *= sn;
}

DI void p1_epilogue(Ctx& c, const Args& A, const Map& M, Acc1& e) {
  const int lane = opaque(c.lane), cw = c.wid & 3, hf = c.wid >> 2, wr = cw >> 1, wc = cw & 1, g = lane >> 4, q = (lane & 15) >> 2, p = lane & 3, i16 = lane & 15;
  const int lw = cw, i1 = M.i1, j1 = M.j1;
  const lds_t lds = c.lds;
  const f32x4 Z4 = {0.f, 0.f, 0.f, 0.f};
  bar();

  if (hf) { const lds_t xf = lds + L_XF1 + cw * 12288 + lane * 16;
#pragma unroll
    for (int m = 0; m < 2; ++m)
#pragma unroll
      for (int n = 0; n < 2; ++n) { e.q[m][n] = *(LAS f32x4*)(xf + (m * 6 + n) * 1024); e.k[m][n] = *(LAS f32x4*)(xf + (m * 6 + 2 + n) * 1024); e.v[m][n] = *(LAS f32x4*)(xf + (m * 6 + 4 + n) * 1024); } }
  const lds_t tab = lds + L_TAB1;
  bf16x8 qf[2], kf[2], vf[2], wT[2];
  {
    f32x4 vv[2][2];
#pragma unroll
    for (int m = 0; m < 2; ++m) {
      const int R = 64 * wr + 32 * hf + 16 * m;
      const float mu = *(LAS float*)(tab + (R + i16) * 4), rs = *(LAS float*)(tab + 512 + (R + i16) * 4), nm = -mu * rs;
      const f32x4 mu4 = *(LAS f32x4*)(tab + (R + 4 * g) * 4), rs4 = *(LAS f32x4*)(tab + 512 + (R + 4 * g) * 4), nm4 = -mu4 * rs4;
      f32x4 qq[2], kk[2];
#pragma unroll
      for (int n = 0; n < 2; ++n) {
        const f32x4 cq = *(LAS f32x4*)(tab + 1024 + (32 * wc + 16 * n + 4 * g) * 4), ck = *(LAS f32x4*)(tab + 1024 + (64 + 32 * wc + 16 * n + 4 * g) * 4);
        const float cv = *(LAS float*)(tab + 1024 + (128 + 32 * wc + 16 * n + i16) * 4);
        qq[n] = e.q[m][n] * rs + cq * nm; kk[n] = e.k[m][n] * rs + ck * nm; vv[m][n] = e.v[m][n] * rs4 + nm4 * cv;
      }
      qf[m] = pk8(qq[0], qq[1]); kf[m] = pk8(kk[0], kk[1]);
    }
    vf[0] = pk8(vv[0][0], vv[1][0]); vf[1] = pk8(vv[0][1], vv[1][1]);
#pragma unroll
    for (int ft = 0; ft < 2; ++ft) wT[ft] = ldsT(lds + L_WPHI + wc * 2048 + (4 * g + q) * 64 + ft * 32 + 8 * p, 16 * 64);
  }
  wait_lgkm0(); bar();

  {
    const lds_t tb = lds + L_TB + c.wid * 4096;
#pragma unroll
    for (int m = 0; m < 2; ++m) {
      const f32x4 p0 = mfma(wT[0], kf[m], Z4), p1 = mfma(wT[1], kf[m], Z4);
      f32x4 e0, e1, n0, n1; feat(p0, p1, e0, e1, n0, n1);
      const lds_t rw = tb + (16 * m + i16) * 128;
      *(LAS u32x2*)(rw + 8 * ((0 + g) ^ i16)) = pk4(e0); *(LAS u32x2*)(rw + 8 * ((4 + g) ^ i16)) = pk4(e1);
      *(LAS u32x2*)(rw + 8 * ((8 + g) ^ i16)) = pk4(n0); *(LAS u32x2*)(rw + 8 * ((12 + g) ^ i16)) = pk4(n1);
    }
    asm volatile("s_waitcnt lgkmcnt(0)" ::: "memory");
    const bf16x8 ones = __builtin_bit_cast(bf16x8, (u32x4){ONES2, ONES2, ONES2, ONES2});
    const lds_t kr = lds + L_KR + c.wid * 8192 + lane * 16;
    float zf[4];
#pragma unroll
    for (int f = 0; f < 4; ++f) {
      const int r0 = 4 * g + q;
      const s16x4 lo = __builtin_amdgcn_ds_read_tr16_b64_v4i16((LAS s16x4*)(tb + r0 * 128 + 8 * ((4 * f + p) ^ r0)));
      const s16x4 hi = __builtin_amdgcn_ds_read_tr16_b64_v4i16((LAS s16x4*)(tb + (16 + r0) * 128 + 8 * ((4 * f + p) ^ r0)));
      const bf16x8 pf = __builtin_shufflevector(lo, hi, 0, 1, 2, 3, 4, 5, 6, 7);
      *(LAS f32x4*)(kr + f * 1024) = mfma(vf[0], pf, Z4); *(LAS f32x4*)(kr + (4 + f) * 1024) = mfma(vf[1], pf, Z4);
      zf[f] = mfma(ones, pf, Z4)[0];
    }
    *(LAS float*)(lds + L_ZR + c.wid * 256 + lane * 4) = g == 0 ? zf[0] : g == 1 ? zf[1] : g == 2 ? zf[2] : zf[3];

    wait_lgkm0(); bar();
    f32x4 k0 = Z4, k1 = Z4;
#pragma unroll
    for (int w = 0; w < 4; ++w) { const lds_t s = lds + L_KR + (wc + 2 * w) * 8192 + (4 * wr + 2 * hf) * 1024 + lane * 16; k0 += *(LAS f32x4*)s; k1 += *(LAS f32x4*)(s + 1024); }
    st16(c, pk8(k0, k1), lane * 16 + (OFF_KVP + (j1 * 16 + i1) * 8192 + wc * 4096 + wr * 2048 + hf * 1024));
    if (c.wid < 2) { float zt = 0.f;
#pragma unroll
      for (int w = 0; w < 4; ++w) zt += *(LAS float*)(lds + L_ZR + (wc + 2 * w) * 256 + lane * 4);
      __builtin_amdgcn_raw_buffer_store_b32(__float_as_uint(zt), c.ws, lane * 4, OFF_ZP + ((j1 * 16 + i1) * 2 + wc) * 256, SC_SC1); }
    wait_lgkm0(); bar();

    publish(c, FG_KV, j1, i1);
  }

  const __amdgpu_buffer_rsrc_t rW2 = __builtin_amdgcn_make_buffer_rsrc((void*)A.wo, 0, 1024 * 2048, 0x00020000);
  int voW2[2];
#pragma unroll
  for (int i = 0; i < 2; ++i) { const int r = (2 * lw + i) * 8 + (lane >> 3), s16 = lane & 7; voW2[i] = r * 2048 + M.j2 * 128 + (((s16 >> 1) ^ ((r >> 1) & 3)) << 5) + (s16 & 1) * 16; }
  auto issueW2 = [&](int t) {
#pragma unroll
    for (int i = 0; i < 2; ++i) dma<0>(rW2, lds + t * 8192 + (lw * 2 + i) * 1024, voW2[i], t * 64 * 2048); };
  if (hf) {
    if (M.act3) {
      const __amdgpu_buffer_rsrc_t rW3 = __builtin_amdgcn_make_buffer_rsrc((void*)A.win, 0, 1024 * 7168, 0x00020000);
#pragma unroll
      for (int i = 0; i < 8; ++i) { const int r = (8 * lw + i) * 2 + (lane >> 5), l5 = lane & 31, seg = l5 >> 4, s16 = l5 & 15;
        dma<0>(rW3, lds + 32768 + (8 * lw + i) * 1024, r * 7168 + (seg == 0 ? 3584 : 0) + M.j3 * 256 + (((s16 >> 1) ^ (r & 7)) << 5) + (s16 & 1) * 16, M.i3 * 64 * 7168); }
    }
#pragma unroll
    for (int t = 0; t < 3; ++t) { issueW2(t); issueW2(8 + t); }
  }
  bf16x8 ph[2][2];
#pragma unroll
  for (int m = 0; m < 2; ++m) {
    const f32x4 p0 = mfma(wT[0], qf[m], Z4), p1 = mfma(wT[1], qf[m], Z4);
    f32x4 e0, e1, n0, n1; feat(p0, p1, e0, e1, n0, n1);
    ph[m][0] = pk8(e0, e1); ph[m][1] = pk8(n0, n1);
  }
  asm volatile("" : "+v"(ph[0][0]), "+v"(ph[0][1]), "+v"(ph[1][0]), "+v"(ph[1][1]));

  kv_gather(c, j1);
  bar();

  {
    bf16x8 kt[3][2];
#pragma unroll
    for (int d = 0; d < 3; ++d)
#pragma unroll
      for (int s = 0; s < 2; ++s) kt[d][s] = ldsT(lds + L_XBUF + wc * 8192 + (32 * s + 4 * g + q) * 128 + 32 * d + 8 * p, 16 * 128);
#pragma unroll
    for (int m = 0; m < 2; ++m) {
      const f32x4 n0 = mfma(kt[0][1], ph[m][1], mfma(kt[0][0], ph[m][0], Z4)), n1 = mfma(kt[1][1], ph[m][1], mfma(kt[1][0], ph[m][0], Z4));
      const f32x4 dn = mfma(kt[2][1], ph[m][1], mfma(kt[2][0], ph[m][0], Z4));
      const float r = __builtin_amdgcn_rcpf(dn[0] + 1e-6f);
      st16(c, pk8(n0 * r, n1 * r), lane * 16 + (OFF_OF + (i1 * 16 + j1) * 16384 + ((4 * wr + 2 * hf + m) * 2 + wc) * 1024));
    }
  }
  publish(c, FG_O, i1, j1);

  if (hf && M.act3) {
    u32x2 cr[4][4];
#pragma unroll
    for (int n = 0; n < 4; ++n) tr4_issue<16 * 512, 32 * 512>((unsigned)(size_t)(lds + 32768 + (4 * g + q) * 512 + 8 * p + (lw >> 1) * 256 + 32 * ((4 * (lw & 1) + n) ^ (4 * (g & 1) + q))), cr[n]);
#pragma unroll
    for (int n = 0; n < 4; ++n) { tr4_wait(cr[n]); const float s = col_sum(cr[n]);
      if (lane < 16) st_granule(c, s, OFF_GC3 + (M.j3 * 16 + M.i3) * 2048 + ((4 * lw + n) * 16 + lane) * 8); }
  }
  if (hf) {
    issueW2(3);
#pragma unroll
    for (int t = 11; t < 16; ++t) issueW2(t);
  } else if (c.wid == 0) wait_flags(c, FG_O, M.i2, 16);
}

struct Ld3 { __amdgpu_buffer_rsrc_t rW; int voW[8]; };
DI void p3_setup(const Ctx& c, const Args& A, int j3, Ld3& L) {
  const int lane = opaque(c.lane), lw = c.wid & 3;
  L.rW = __builtin_amdgcn_make_buffer_rsrc((void*)A.win, 0, 1024 * 7168, 0x00020000);
#pragma unroll
  for (int i = 0; i < 8; ++i) { const int r = (8 * lw + i) * 2 + (lane >> 5), l5 = lane & 31, seg = l5 >> 4, s16 = l5 & 15;
    L.voW[i] = r * 7168 + (seg == 0 ? 3584 : 0) + j3 * 256 + (((s16 >> 1) ^ (r & 7)) << 5) + (s16 & 1) * 16; }
}
DI void p3_issueW(const Ctx& c, const Ld3& L, int t) { const lds_t d = c.lds + (t % 3) * 49152 + 16384 + (c.wid & 3) * 8192;
#pragma unroll
  for (int i = 0; i < 8; ++i) dma<0>(L.rW, d + i * 1024, L.voW[i], t * 64 * 7168); }
DI void p3_load(Ctx& c, const Ld3& L, int i3) {
  const int lane = opaque(c.lane), lw = c.wid - 4;
  const lds_t lds = c.lds;
  constexpr int S = 3, SB = 49152;
  const int voF = lane * 16 + lw * 1024;
  auto issueA = [&](int t) { const lds_t d = lds + (t % S) * SB + lw * 1024; const int so = OFF_HF + (i3 * 16 + t) * 16384;
#pragma unroll
    for (int i = 0; i < 4; ++i) dma<SC_SC1>(c.ws, d + i * 4096, voF, so + i * 4096); };
  auto issueW = [&](int t) { p3_issueW(c, L, t); };
  bar();
  issueA(0); issueA(1);
  u32x2 hv[16]; const int R = (lw & 1) * 64 + lane;
  auto gather = [&](int k) { if (lw < 2) {
      hv[2 * k] = __builtin_amdgcn_raw_buffer_load_b64(c.ws, R * 8, OFF_STH + (i3 * 16 + 2 * k) * 1024, SC_SC1);
      hv[2 * k + 1] = __builtin_amdgcn_raw_buffer_load_b64(c.ws, R * 8, OFF_STH + (i3 * 16 + 2 * k + 1) * 1024, SC_SC1); } };
  auto fin = [&]() { if (lw < 2) { float s = 0.f, qq = 0.f;
#pragma unroll
      for (int k = 0; k < 16; ++k) { s += __uint_as_float(hv[k][0]); qq += __uint_as_float(hv[k][1]); }
      s *= (1.0f / 1024.0f); qq *= (1.0f / 1024.0f);
      *(LAS float*)(lds + L_TAB3 + R * 4) = s; *(LAS float*)(lds + L_TAB3 + 512 + R * 4) = __builtin_amdgcn_rsqf(fmaxf(qq - s * s, 0.f) + 1e-5f);
      wait_lgkm0(); } };
  loader_loop16<8, true>(lw < 2, false, issueA, issueW, [](int) {}, gather, fin);
}
struct Acc3 { f32x4 z2[2][4], z1[2][4]; };
DI int xf3_base(int cw) { return cw < 3 ? 16384 + cw * 32768 : 98304; }
DI void p3_mma(Ctx& c, int i3, int j3, Acc3& e) {
  const int lane = opaque(c.lane), wr = c.wid >> 1, wc = c.wid & 1, g = lane >> 4, q = (lane & 15) >> 2, p = lane & 3;
  const lds_t lds = c.lds;
  const __amdgpu_buffer_rsrc_t rWS = c.ws;
  const f32x4 Z4 = {0.f, 0.f, 0.f, 0.f};
  constexpr int S = 3, SB = 49152;
  if (wc == 1) {
    const int off = OFF_GC3 + j3 * 16 * 2048 + (128 * wr + 2 * lane) * 8; u32x4 gv[16];
    for (unsigned spins = 0;; ++spins) {
      bool ok = true;
#pragma unroll
      for (int k = 0; k < 16; ++k) gv[k] = __builtin_amdgcn_raw_buffer_load_b128(rWS, off + k * 2048, 0, SC_SC1);
#pragma unroll
      for (int k = 0; k < 16; ++k) ok &= gv[k][1] == c.tag && gv[k][3] == c.tag;
      if (__all(ok) || gave_up(c, spins, 0x202u)) break;
      __builtin_amdgcn_s_sleep(2);
    }
    f32x2 cs = {0.f, 0.f};
#pragma unroll
    for (int k = 0; k < 16; ++k) { cs[0] += __uint_as_float(gv[k][0]); cs[1] += __uint_as_float(gv[k][2]); }
    *(LAS f32x2*)(lds + L_TAB3 + 1024 + (128 * wr + 2 * lane) * 4) = cs;
    wait_lgkm0();
  } else if (c.wid == 0) wait_flags(c, FG_H, i3, 16);
  f32x4 a2[4][4], a1_[4][4];
#pragma unroll
  for (int m = 0; m < 4; ++m)
#pragma unroll
    for (int n = 0; n < 4; ++n) { a2[m][n] = Z4; a1_[m][n] = Z4; }
#pragma unroll
  for (int m = 0; m < 4; ++m) asm volatile("" : "+v"(a2[m][0]), "+v"(a2[m][1]), "+v"(a2[m][2]), "+v"(a2[m][3]), "+v"(a1_[m][0]), "+v"(a1_[m][1]), "+v"(a1_[m][2]), "+v"(a1_[m][3]));
  bar();
  const int ao2 = wr * 8192 + lane * 16;
  int wo_[4];
#pragma unroll
  for (int n = 0; n < 4; ++n) wo_[n] = 16384 + (4 * g + q) * 512 + 8 * p + 32 * ((4 * wc + n) ^ (4 * (g & 1) + q));
  bf16x8 a0[4], w0[8], a1[4], w1[8];
  auto rd = [&](bf16x8(&a)[4], bf16x8(&w)[8], int sb, int ks) { const lds_t s = lds + sb;
#pragma unroll
    for (int n = 0; n < 8; ++n) w[n] = ldsT(s + wo_[n & 3] + (n >> 2) * 256 + ks * 32 * 512, 16 * 512);
#pragma unroll
    for (int m = 0; m < 4; ++m) a[m] = ldsA(s + ao2 + m * 2048 + ks * 1024); };
  auto mm = [&](bf16x8(&a)[4], bf16x8(&w)[8]) {
#pragma unroll
    for (int n = 0; n < 4; ++n)
#pragma unroll
      for (int m = 0; m < 4; ++m) a2[m][n] = mfma(a[m], w[n], a2[m][n]);
#pragma unroll
    for (int n = 0; n < 4; ++n)
#pragma unroll
      for (int m = 0; m < 4; ++m) a1_[m][n] = mfma(w[4 + n], a[m], a1_[m][n]); };
  consumer_loop<S, SB>(16, [&](int sb) { rd(a0, w0, sb, 0); }, [&](int sb) { rd(a1, w1, sb, 1); }, [&]() { mm(a0, w0); }, [&]() { mm(a1, w1); }, []() { interleave<32, 20>(); });

  const lds_t xf = lds + xf3_base(c.wid) + lane * 16;
#pragma unroll
  for (int m = 0; m < 2; ++m)
#pragma unroll
    for (int n = 0; n < 4; ++n) { *(LAS f32x4*)(xf + (m * 8 + n) * 1024) = a2[2 + m][n]; *(LAS f32x4*)(xf + (m * 8 + 4 + n) * 1024) = a1_[2 + m][n]; e.z2[m][n] = a2[m][n]; e.z1[m][n] = a1_[m][n]; }
  wait_lgkm0();
}
struct Ld4 { __amdgpu_buffer_rsrc_t rA; int voA, voF; };
DI void p4_issueW(const Ctx& c, const Ld4& L, int t) { const int lw = c.wid & 3; const lds_t d = c.lds + (t & 3) * 32768 + lw * 1024;
#pragma unroll
  for (int i = 0; i < 4; ++i) dma<0>(L.rA, d + i * 4096, L.voA, t * 128 + i * 32 * 4096); }
DI void p3_epilogue(Ctx& c, const Ld4& L, int i3, int j3, Acc3& e, u32x2 (&z1)[2][4]) {
  const int lane = opaque(c.lane), cw = c.wid & 3, hf = c.wid >> 2, wr = cw >> 1, wc = cw & 1, g = lane >> 4, i16 = lane & 15;
  const lds_t lds = c.lds, tab = lds + L_TAB3;
  const __amdgpu_buffer_rsrc_t rWS = c.ws;
  const f32x4 Z4 = {0.f, 0.f, 0.f, 0.f};
  bar();

  __builtin_amdgcn_s_setprio(1);
  if (hf) { const lds_t xf = lds + xf3_base(cw) + lane * 16;
#pragma unroll
    for (int m = 0; m < 2; ++m)
#pragma unroll
      for (int n = 0; n < 4; ++n) { e.z2[m][n] = *(LAS f32x4*)(xf + (m * 8 + n) * 1024); e.z1[m][n] = *(LAS f32x4*)(xf + (m * 8 + 4 + n) * 1024); } }
  {
    float cs[4];
#pragma unroll
    for (int n = 0; n < 4; ++n) cs[n] = *(LAS float*)(tab + 1024 + (64 * wc + 16 * n + i16) * 4);
#pragma unroll
    for (int m = 0; m < 2; ++m) {
      const int R = 64 * wr + 32 * hf + 16 * m + 4 * g;
      const f32x4 mu = *(LAS f32x4*)(tab + R * 4), rs = *(LAS f32x4*)(tab + 512 + R * 4), nm = -mu * rs;
      f32x4 s = Z4, qq = Z4;
#pragma unroll
      for (int n = 0; n < 4; ++n) { const f32x4 y = gelu4(e.z2[m][n] * rs + nm * cs[n]); e.z2[m][n] = y; s += y; qq += y * y; }
      sum_row16x8(s, qq);
      if (i16 < 4) { const float sv = i16 == 0 ? s[0] : i16 == 1 ? s[1] : i16 == 2 ? s[2] : s[3], qv = i16 == 0 ? qq[0] : i16 == 1 ? qq[1] : i16 == 2 ? qq[2] : qq[3];
        __builtin_amdgcn_raw_buffer_store_b128((u32x4){__float_as_uint(sv), c.tag, __float_as_uint(qv), c.tag}, rWS, OFF_ST2 + ((i3 * 14 + j3) * 2 + wc) * 2048 + (R + i16) * 16, 0, SC_SC1); }
    }
  }

  __builtin_amdgcn_s_setprio(0);
  if (!hf) {
#pragma unroll
    for (int s = 0; s < 3; ++s) p4_issueW(c, L, s);
  }
  {
    const int R0 = 64 * wr + 32 * hf, off = OFF_ST2 + (i3 * 28 + (lane >> 5)) * 2048 + (R0 + (lane & 31)) * 16;
    u32x4 v[14];
    for (unsigned spins = 0;; ++spins) {
      bool ok = true;
#pragma unroll
      for (int k = 0; k < 14; ++k) v[k] = __builtin_amdgcn_raw_buffer_load_b128(rWS, off + k * 4096, 0, SC_SC1);
#pragma unroll
      for (int k = 0; k < 14; ++k) ok &= v[k][1] == c.tag && v[k][3] == c.tag;
      if (__all(ok) || gave_up(c, spins, 0x203u)) break;
      __builtin_amdgcn_s_sleep(4);
    }
    __builtin_amdgcn_s_setprio(1);
    float s = 0.f, qq = 0.f;
#pragma unroll
    for (int k = 0; k < 14; ++k) { s += __uint_as_float(v[k][0]); qq += __uint_as_float(v[k][2]); }
    { auto a = __builtin_amdgcn_permlane32_swap(__float_as_uint(s), __float_as_uint(s), false, false); s = (__uint_as_float(a[0]) + __uint_as_float(a[1])) * (1.0f / 1792.0f);
      auto b = __builtin_amdgcn_permlane32_swap(__float_as_uint(qq), __float_as_uint(qq), false, false); qq = (__uint_as_float(b[0]) + __uint_as_float(b[1])) * (1.0f / 1792.0f); }
    const float rsv = __builtin_amdgcn_rsqf(fmaxf(qq - s * s, 0.f) + 1e-5f);
    const lds_t ft = lds + L_FIN + c.wid * 256;
    if (lane < 32) { *(LAS float*)(ft + lane * 4) = rsv; *(LAS float*)(ft + 128 + lane * 4) = -s * rsv; }
    asm volatile("s_waitcnt lgkmcnt(0)" ::: "memory");

    f32x4 rs[2], nm[2];
#pragma unroll
    for (int m = 0; m < 2; ++m) { rs[m] = *(LAS f32x4*)(ft + (16 * m + 4 * g) * 4); nm[m] = *(LAS f32x4*)(ft + 128 + (16 * m + 4 * g) * 4); }
#pragma unroll
    for (int n = 0; n < 4; ++n)
      st16(c, pk8(e.z2[0][n] * rs[0] + nm[0], e.z2[1][n] * rs[1] + nm[1]), lane * 16 + (OFF_Z2 + (j3 * 32 + 2 * i3 + wr) * 16384 + ((4 * wc + n) * 2 + hf) * 1024));
  }
  __builtin_amdgcn_s_setprio(0);
  publish(c, FG_Z2, j3, i3);

#pragma unroll
  for (int m = 0; m < 2; ++m) asm volatile("" : "+v"(e.z1[m][0]), "+v"(e.z1[m][1]), "+v"(e.z1[m][2]), "+v"(e.z1[m][3]));
#pragma unroll
  for (int m = 0; m < 2; ++m) {
    const int R = 64 * wr + 32 * hf + 16 * m + i16;
    const float mu = *(LAS float*)(tab + R * 4), rs = *(LAS float*)(tab + 512 + R * 4), nm = -mu * rs;
#pragma unroll
    for (int n = 0; n < 4; ++n) { const f32x4 cs = *(LAS f32x4*)(tab + 1024 + (128 + 64 * wc + 16 * n + 4 * g) * 4); z1[m][n] = pk4(gelu4(e.z1[m][n] * rs + cs * nm)); }
    asm volatile("" : "+v"(z1[m][0]), "+v"(z1[m][1]), "+v"(z1[m][2]), "+v"(z1[m][3]));
  }
}
DI void p4_load(Ctx& c, const Ld4& L, int j3) {
  const int lw = c.wid - 4;
  auto issueW = [&](int t) { p4_issueW(c, L, t); };
  auto issueA = [&](int t) { const lds_t d = c.lds + (t & 3) * 32768 + 16384 + lw * 1024; const int so = OFF_Z2 + (j3 * 32 + t) * 16384;
#pragma unroll
    for (int i = 0; i < 4; ++i) dma<SC_SC1>(c.ws, d + i * 4096, L.voF, so + i * 4096); };
  bar();
  if (c.b == 0 && lw == 0 && c.lane == 0) __hip_atomic_store((unsigned*)(c.wsp + OFF_EPOCH), c.tag, __ATOMIC_RELAXED, __HIP_MEMORY_SCOPE_AGENT);
#pragma unroll
  for (int s = 0; s < 3; ++s) issueA(s);
  loader_loop32(issueA, issueW);
}
DI void p4_mma(Ctx& c, int j3, f32x4 (&e)[2][4]) {
  const int lane = opaque(c.lane), wr = c.wid >> 1, wc = c.wid & 1, g = lane >> 4, i16 = lane & 15;
  const lds_t lds = c.lds;
  constexpr int S = 4, SB = 32768;
  f32x4 acc[4][4];
#pragma unroll
  for (int m = 0; m < 4; ++m)
#pragma unroll
    for (int n = 0; n < 4; ++n) { acc[m][n] = (f32x4){0.f, 0.f, 0.f, 0.f}; asm volatile("" : "+v"(acc[m][n])); }
  const int ao0 = (wr * 64 + i16) * 128 + ((g ^ (i16 >> 1)) << 4), ao1 = ao0 ^ 64, zo = 16384 + wc * 8192 + lane * 16;
  bf16x8 a0[4], w0[4], a1[4], w1[4];
  auto rd = [&](bf16x8(&a)[4], bf16x8(&w)[4], int sb, int ks) { const lds_t s = lds + sb;
#pragma unroll
    for (int n = 0; n < 4; ++n) { w[n] = ldsA(s + zo + n * 2048 + ks * 1024); a[n] = ldsA(s + (ks ? ao1 : ao0) + n * 2048); } };
  auto mm = [&](bf16x8(&a)[4], bf16x8(&w)[4]) {
#pragma unroll
    for (int n = 0; n < 4; ++n)
#pragma unroll
      for (int m = 0; m < 4; ++m) acc[m][n] = mfma(w[n], a[m], acc[m][n]); };
  if (c.wid == 0) wait_flags(c, FG_Z2, j3, 16);
  bar();
  consumer_loop<S, SB>(32, [&](int sb) { rd(a0, w0, sb, 0); }, [&](int sb) { rd(a1, w1, sb, 1); }, [&]() { mm(a0, w0); }, [&]() { mm(a1, w1); }, []() { interleave<16, 8>(); });

  const lds_t xf = lds + c.wid * 8192 + lane * 16;
#pragma unroll
  for (int m = 0; m < 2; ++m)
#pragma unroll
    for (int n = 0; n < 4; ++n) { *(LAS f32x4*)(xf + (m * 4 + n) * 1024) = acc[2 + m][n]; e[m][n] = acc[m][n]; }
  wait_lgkm0();
}
DI void p5_issueW(const Ctx& c, const Args& A, int j2);
DI void p4_epilogue(Ctx& c, const Args& A, int i3, int j3, int j2, f32x4 (&e)[2][4], const u32x2 (&z1)[2][4]) {
  const int lane = opaque(c.lane), cw = c.wid & 3, hf = c.wid >> 2, wr = cw >> 1, wc = cw & 1, i16 = lane & 15;
  float bs[2];
#pragma unroll
  for (int m = 0; m < 2; ++m) bs[m] = bflo((unsigned)A.bsp[i3 * 128 + true_row(4 * wr + 2 * hf + m, i16)]);
  bar();
  if (hf) { const lds_t xf = c.lds + cw * 8192 + lane * 16;
#pragma unroll
    for (int m = 0; m < 2; ++m)
#pragma unroll
      for (int n = 0; n < 4; ++n) e[m][n] = *(LAS f32x4*)(xf + (m * 4 + n) * 1024); }
#pragma unroll
  for (int m = 0; m < 2; ++m)
#pragma unroll
    for (int u = 0; u < 2; ++u)
      st16(c, pk8(up4(z1[m][2 * u]) * (e[m][2 * u] + bs[m]), up4(z1[m][2 * u + 1]) * (e[m][2 * u + 1] + bs[m])),
           lane * 16 + (OFF_UF + (i3 * 28 + 2 * j3 + wc) * 16384 + ((4 * wr + 2 * hf + m) * 2 + u) * 1024));
  wait_lgkm0(); bar();
  publish(c, FG_U, i3, j3);
  if (hf) p5_issueW(c, A, j2);
}

DI void p5_issueW(const Ctx& c, const Args& A, int j2) {
  const int lane = opaque(c.lane), lw = c.wid - 4;
  const __amdgpu_buffer_rsrc_t rW = __builtin_amdgcn_make_buffer_rsrc((void*)A.wout, 0, 1792 * 2048, 0x00020000);
  int vo[2];
#pragma unroll
  for (int i = 0; i < 2; ++i) { const int r = (2 * lw + i) * 8 + (lane >> 3), s16 = lane & 7; vo[i] = r * 2048 + j2 * 128 + (((s16 >> 1) ^ ((r >> 1) & 3)) << 5) + (s16 & 1) * 16; }
#pragma unroll
  for (int t = 0; t < 18; ++t)
#pragma unroll
    for (int i = 0; i < 2; ++i) dma<0>(rW, c.lds + ((t & 1) * 9 + (t >> 1)) * 8192 + (lw * 2 + i) * 1024, vo[i], ((t & 1) * 14 + (t >> 1)) * 64 * 2048);
}

constexpr int L_RED = 98304;
constexpr int RING = 6;
template <int NSH, int R, bool REFILL>
DI void gemm_split(Ctx& c, f32x4 (&res)[4], int aoff, __amdgpu_buffer_rsrc_t rW, int j2) {
  const int ln = opaque(c.lane), g = ln >> 4, q = (ln & 15) >> 2, p = ln & 3, rw = c.wid & 3, kh = c.wid >> 2;
  const lds_t lds = c.lds;
  const int sw2 = (2 * g + (q >> 1)) & 3, wl2 = (4 * g + q) * 128 + 8 * p + kh * (REFILL ? 9 : 8) * 8192, va = ln * 16 + rw * 4096;
  const int ao = aoff + kh * (NSH / 2) * 16384;
  int wo2[4];
#pragma unroll
  for (int n = 0; n < 4; ++n) wo2[n] = wl2 + 32 * (n ^ sw2);
  f32x4 acc[2][4];
#pragma unroll
  for (int n = 0; n < 4; ++n) { acc[0][n] = (f32x4){0.f, 0.f, 0.f, 0.f}; acc[1][n] = acc[0][n]; }
  u32x4 a[R][2]; bf16x8 w0[4], w1[4];
  auto lda = [&](int s) {
#pragma unroll
    for (int m = 0; m < 2; ++m) a[s % R][m] = __builtin_amdgcn_raw_buffer_load_b128(c.ws, va + m * 2048, ao + (s >> 1) * 16384 + (s & 1) * 1024, SC_SC1); };
  auto ldw = [&](bf16x8(&d)[4], int s) { const int lt = s >> 1, slot = (REFILL && lt >= 9) ? lt - 9 : lt;
#pragma unroll
    for (int n = 0; n < 4; ++n) d[n] = ldsT(lds + wo2[n] + slot * 8192 + (s & 1) * 4096, 16 * 128); };

#pragma unroll
  for (int s = 0; s < R; ++s) { lda(s); if (s == 0 || s == 2) bar(); }
  if (!REFILL && kh) {
#pragma unroll
    for (int i = 0; i < 2; ++i) { const int r = (2 * rw + i) * 8 + (ln >> 3), s16 = ln & 7, vo = r * 2048 + j2 * 128 + (((s16 >> 1) ^ ((r >> 1) & 3)) << 5) + (s16 & 1) * 16;
#pragma unroll
      for (int t = 4; t < 8; ++t) dma<0>(rW, lds + t * 8192 + (rw * 2 + i) * 1024, vo, t * 64 * 2048); }
  }
  ldw(w0, 0);
#pragma unroll
  for (int s = 0; s < NSH; ++s) {
    if (s == 5) {
      if (kh) { if (REFILL) wait_vm<2 * R + 10>(); else wait_vm<10>(); }
      bar(); __builtin_amdgcn_sched_barrier(0);
    }
    if (REFILL && s == 10) {
      wait_lgkm0(); bar();
      const int r = 8 * c.wid + (ln >> 3), s16 = ln & 7, vo = r * 2048 + j2 * 128 + (((s16 >> 1) ^ ((r >> 1) & 3)) << 5) + (s16 & 1) * 16;
#pragma unroll
      for (int k = 0; k < 10; ++k) dma<0>(rW, lds + ((k / 5) * 9 + (k % 5)) * 8192 + c.wid * 1024, vo, ((k / 5) * 14 + 9 + (k % 5)) * 64 * 2048);
      __builtin_amdgcn_sched_barrier(0);
    }
    if (REFILL && s == 17) { wait_vm<2 * ((NSH - R < 17 ? NSH - R : 17) - 10)>(); wait_lgkm0(); bar(); __builtin_amdgcn_sched_barrier(0); }
    if (s + 1 < NSH) { if (s & 1) ldw(w0, s + 1); else ldw(w1, s + 1); }
#pragma unroll
    for (int n = 0; n < 4; ++n)
#pragma unroll
      for (int m = 0; m < 2; ++m) acc[m][n] = mfma((s & 1) ? w1[n] : w0[n], __builtin_bit_cast(bf16x8, a[s % R][m]), acc[m][n]);
    if (s + R < NSH) lda(s + R);
    interleave<8, 8>();
    __builtin_amdgcn_sched_barrier(0);
  }
  wait_lgkm0(); bar();
#pragma unroll
  for (int n = 0; n < 4; ++n) *(LAS f32x4*)(lds + L_RED + c.wid * 4096 + n * 1024 + ln * 16) = kh ? acc[0][n] : acc[1][n];
  wait_lgkm0(); bar();
#pragma unroll
  for (int n = 0; n < 4; ++n) res[n] = (kh ? acc[1][n] : acc[0][n]) + *(LAS f32x4*)(lds + L_RED + (c.wid ^ 4) * 4096 + n * 1024 + ln * 16);
}

template <class PRE> DI void phase2(Ctx& c, const Args& A, int i2, int j2, bool pre_on, PRE pre) {
  const __amdgpu_buffer_rsrc_t rW2 = __builtin_amdgcn_make_buffer_rsrc((void*)A.wo, 0, 1024 * 2048, 0x00020000);
  const int lane = opaque(c.lane), g = lane >> 4, i16 = lane & 15, mt = 2 * (c.wid & 3) + (c.wid >> 2);
  u32x2 xr[4];
#pragma unroll
  for (int n = 0; n < 4; ++n) xr[n] = *(const u32x2*)(A.x + (size_t)(i2 * 128 + true_row(mt, i16)) * 1024 + j2 * 64 + 16 * n + 4 * g);
  wait_lgkm0(); bar();
  f32x4 acc[4];
  gemm_split<16, RING, false>(c, acc, OFF_OF + i2 * 262144, rW2, j2);

  float s = 0.f, qq = 0.f;
#pragma unroll
  for (int u = 0; u < 2; ++u) {
    f32x4 h0 = acc[2 * u], h1 = acc[2 * u + 1]; const u32x2 x0 = xr[2 * u], x1 = xr[2 * u + 1];
    h0[0] += bflo(x0[0]); h0[1] += bfhi(x0[0]); h0[2] += bflo(x0[1]); h0[3] += bfhi(x0[1]);
    h1[0] += bflo(x1[0]); h1[1] += bfhi(x1[0]); h1[2] += bflo(x1[1]); h1[3] += bfhi(x1[1]);
    const bf16x8 hb = pk8(h0, h1);
    st16(c, hb, lane * 16 + (OFF_HF + (i2 * 16 + j2) * 16384 + (mt * 2 + u) * 1024));
    acc_row(s, qq, __builtin_bit_cast(u32x4, hb));
  }
  s = sum_g(s); qq = sum_g(qq);
  if (lane < 16) __builtin_amdgcn_raw_buffer_store_b64((u32x2){__float_as_uint(s), __float_as_uint(qq)}, c.ws, OFF_STH + (i2 * 16 + j2) * 1024 + (mt * 16 + lane) * 8, 0, SC_SC1);
  publish(c, FG_H, i2, j2);
  if (pre_on) pre();
}

DI void phase5(Ctx& c, const Args& A, int i2, int j2) {
  const int lane = opaque(c.lane), g = lane >> 4, i16 = lane & 15, mt = 2 * (c.wid & 3) + (c.wid >> 2);
  u32x4 hr[2];
#pragma unroll
  for (int u = 0; u < 2; ++u) hr[u] = __builtin_amdgcn_raw_buffer_load_b128(c.ws, lane * 16, OFF_HF + (i2 * 16 + j2) * 16384 + (mt * 2 + u) * 1024, SC_SC1);
  const __amdgpu_buffer_rsrc_t rW = __builtin_amdgcn_make_buffer_rsrc((void*)A.wout, 0, 1792 * 2048, 0x00020000);
  if (c.wid == 0) wait_flags(c, FG_U, i2, 14);
  if (c.wid >= 4) wait_vm<24>();
  bar();
  f32x4 acc[4];
  gemm_split<28, RING, true>(c, acc, OFF_UF + i2 * 458752, rW, j2);

  const lds_t ot = c.lds + c.wid * 2048;
#pragma unroll
  for (int n = 0; n < 4; ++n) { const unsigned r0 = hr[n >> 1][2 * (n & 1)], r1 = hr[n >> 1][2 * (n & 1) + 1];
    *(LAS u32x2*)(ot + i16 * 128 + (((2 * n + (g >> 1)) ^ (i16 & 7)) << 4) + (g & 1) * 8) =
        (u32x2){pk2(acc[n][0] + bflo(r0), acc[n][1] + bfhi(r0)), pk2(acc[n][2] + bflo(r1), acc[n][3] + bfhi(r1))}; }
  asm volatile("s_waitcnt lgkmcnt(0)" ::: "memory");
  const __amdgpu_buffer_rsrc_t rO = __builtin_amdgcn_make_buffer_rsrc((void*)A.out, 0, 2048 * 2048, 0x00020000);
#pragma unroll
  for (int u = 0; u < 2; ++u) { const int r = 8 * u + (lane >> 3), ch = lane & 7;
    const u32x4 v = *(LAS u32x4*)(ot + r * 128 + ((ch ^ (r & 7)) << 4));
    __builtin_amdgcn_raw_buffer_store_b128(v, rO, (i2 * 128 + true_row(mt, r)) * 2048 + j2 * 128 + ch * 16, 0, SC_SC1); }
}

__global__ __launch_bounds__(512, 2) void fused_fwd(const u16* __restrict__ x, const u16* __restrict__ wqkv, const u16* __restrict__ wphi, const u16* __restrict__ wo,
                                                      const u16* __restrict__ win, const u16* __restrict__ wsp, const u16* __restrict__ bsp, const u16* __restrict__ wout,
                                                      u16* __restrict__ out, char* __restrict__ ws) {
  extern __shared__ __attribute__((aligned(16))) char shm[];
  Ctx c;
  c.lds = (lds_t)shm; c.wsp = ws; c.lane = threadIdx.x & 63; c.wid = __builtin_amdgcn_readfirstlane(threadIdx.x >> 6); c.b = blockIdx.x;
  c.ws = __builtin_amdgcn_make_buffer_rsrc((void*)ws, 0, WS_BYTES, 0x00020000);
  c.tag = 0u;
  if (threadIdx.x < 16) *(LAS unsigned*)(c.lds + L_CNT + threadIdx.x * 4) = 0u;
  const int xcd = c.b & 7, jj = c.b >> 3;
  Map M;
  M.i1 = 8 * (xcd & 1) + (jj >> 2); M.j1 = 4 * (xcd >> 1) + (jj & 3);
  M.i2 = 4 * (xcd >> 1) + (jj >> 3); M.j2 = 8 * (xcd & 1) + (jj & 7);
  M.act3 = jj < 28;
  M.i3 = 4 * (xcd >> 1) + (M.act3 ? jj / 7 : 0); M.j3 = 7 * (xcd & 1) + (M.act3 ? jj % 7 : 0);
  const Args A{x, wqkv, wphi, wo, win, wsp, bsp, wout, out, ws};
  const bool ld = c.wid >= 4;
  {
    Acc1 e;
    if (ld) p1_load(c, A, M.i1, M.j1);
    else { __builtin_amdgcn_s_setprio(1); p1_mma(c, A, M.i1, M.j1, e); __builtin_amdgcn_s_setprio(0); }
    p1_epilogue(c, A, M, e);
  }
  Ld3 L3;
  p3_setup(c, A, M.j3, L3);
  phase2(c, A, M.i2, M.j2, ld && M.act3, [&]() { p3_issueW(c, L3, 0); p3_issueW(c, L3, 1); });
  if (M.act3) {
    u32x2 z1[2][4];
    Ld4 L;
    L.rA = __builtin_amdgcn_make_buffer_rsrc((void*)(A.wsp + (size_t)M.i3 * 128 * 2048), 0, 128 * 4096, 0x00020000);
    {
      Acc3 e;
      if (ld) p3_load(c, L3, M.i3);
      else { __builtin_amdgcn_s_setprio(1); p3_mma(c, M.i3, M.j3, e); __builtin_amdgcn_s_setprio(0); }
      const int ln = opaque(c.lane);
      L.voA = rowmajor_voff(c.wid & 3, ln, 4096); L.voF = ln * 16 + (c.wid & 3) * 1024;
      p3_epilogue(c, L, M.i3, M.j3, e, z1);
    }
    f32x4 e4[2][4];
    if (ld) p4_load(c, L, M.j3);
    else { __builtin_amdgcn_s_setprio(1); p4_mma(c, M.j3, e4); __builtin_amdgcn_s_setprio(0); }
    p4_epilogue(c, A, M.i3, M.j3, M.j2, e4, z1);
  } else {
    bar();
    if (!ld) { const int wv = ((c.b & 7) * 4 + ((c.b >> 3) - 28)) * 4 + c.wid;
#pragma unroll 1
      for (int k = 0; k < 12; ++k) { const int Li = (k * 128 + wv) * 64 + c.lane;
        touch(c.lds, Li < 65536 ? (const char*)A.wsp + (size_t)Li * 128 : (const char*)A.wout + (size_t)((Li < 94208 ? Li : 94207) - 65536) * 128); } }
    if (ld) p5_issueW(c, A, M.j2);
  }
  phase5(c, A, M.i2, M.j2);
}

extern "C" void kernel_launch(void* const* d_in, const int* in_sizes, int n_in, void* d_out, int out_size, void* d_ws, size_t ws_size, hipStream_t stream) {
  (void)in_sizes; (void)n_in; (void)out_size; (void)ws_size;
  (void)hipFuncSetAttribute((const void*)fused_fwd, hipFuncAttributeMaxDynamicSharedMemorySize, LDS_BYTES);
  fused_fwd<<<256, 512, LDS_BYTES, stream>>>((const u16*)d_in[0], (const u16*)d_in[1], (const u16*)d_in[2], (const u16*)d_in[3], (const u16*)d_in[5], (const u16*)d_in[6],
                                             (const u16*)d_in[7], (const u16*)d_in[8], (u16*)d_out, (char*)d_ws);
}
```
